```python
import torch

def _softmax(x, axis=-1):
    return torch.softmax(x, dim=axis)

def _gelu(x):
    return torch.nn.functional.gelu(x, approximate='tanh')

def _silu(x):
    return torch.nn.functional.silu(x)

def _sigmoid(x):
    return torch.sigmoid(x)

def _layernorm(x, axis=-1, eps=1e-05):
    m = x.mean(dim=axis, keepdim=True)
    v = x.var(dim=axis, keepdim=True, unbiased=False)
    return (x - m) / torch.sqrt(v + eps)

def _scatter_add(tgt, idx, src, axis=0):
    return tgt.clone().index_add_(axis, idx, src)

def _segment_sum(src, seg, n):
    out = torch.zeros((n,) + src.shape[1:], dtype=src.dtype)
    return out.index_add_(0, seg, src)

def _pad(x, pw):
    flat = tuple((v for pair in reversed(pw) for v in pair))
    return torch.nn.functional.pad(x, flat)
kernel_name = 'standalone__conformer_family_e_branchformer__speech'

def setup_inputs():

    _w = lambda a: (lambda t: t.to(torch.bfloat16) if t.is_floating_point() else t)(torch.as_tensor(a))
    B, T, D, K = (2, 4000, 512, 31)
    return {'x': _w((lambda *s: torch.randn(*s, dtype=torch.bfloat16) * 0.05)(B, T, D)), 'w_qkv': _w((lambda *s: torch.randn(*s, dtype=torch.bfloat16) * 0.05)(D, 3 * D)), 'w_att_out': _w((lambda *s: torch.randn(*s, dtype=torch.bfloat16) * 0.05)(D, D)), 'w_mlp_in': _w((lambda *s: torch.randn(*s, dtype=torch.bfloat16) * 0.05)(D, 2 * D)), 'dw_csgu': _w((lambda *s: torch.randn(*s, dtype=torch.bfloat16) * 0.05)(K, D)), 'w_mlp_out': _w((lambda *s: torch.randn(*s, dtype=torch.bfloat16) * 0.05)(D, D)), 'dw_merge': _w((lambda *s: torch.randn(*s, dtype=torch.bfloat16) * 0.05)(K, 2 * D)), 'w_merge': _w((lambda *s: torch.randn(*s, dtype=torch.bfloat16) * 0.05)(2 * D, D))}

def reference(x, w_qkv, w_att_out, w_mlp_in, dw_csgu, w_mlp_out, dw_merge, w_merge):
    B, T, D = x.shape
    K = dw_csgu.shape[0]
    pad = K // 2
    xa = _layernorm(x, axis=-1)
    qkv = torch.einsum('btd,de->bte', xa, w_qkv)
    q, k, v = (qkv[..., :D].clone(), qkv[..., D:2 * D].clone(), qkv[..., 2 * D:].clone())
    scores = torch.einsum('btd,bsd->bts', q, k) * D ** (-0.5)
    p = _softmax(scores, axis=-1)
    attn = torch.einsum('bts,bsd->btd', p, v)
    glob = torch.einsum('btd,de->bte', attn, w_att_out)
    xc = _layernorm(x, axis=-1)
    h = _gelu(torch.einsum('btd,de->bte', xc, w_mlp_in))
    u, g = (h[..., :D].clone(), h[..., D:].clone())
    g = _layernorm(g, axis=-1)
    gp = _pad(g, ((0, 0), (pad, pad), (0, 0)))
    gc = torch.zeros_like(g)
    for i in range(K):
        gc = gc + gp[:, i:i + T, :] * dw_csgu[i][None, None, :]
    loc = torch.einsum('btd,de->bte', u * gc, w_mlp_out)
    m = torch.concatenate([glob, loc], axis=-1)
    mp = _pad(m, ((0, 0), (pad, pad), (0, 0)))
    mc = torch.zeros_like(m)
    for i in range(K):
        mc = mc + mp[:, i:i + T, :] * dw_merge[i][None, None, :]
    m = m + mc
    return x + torch.einsum('btc,cd->btd', m, w_merge)
```

```cpp
#include <hip/hip_runtime.h>
#include <hip/hip_bf16.h>
typedef unsigned short u16;
__device__ __forceinline__ float b2f(u16 v){ return __uint_as_float(((unsigned)v)<<16); }
__device__ __forceinline__ u16 f2b(float f){ unsigned u=__float_as_uint(f); u += 0x7fff + ((u>>16)&1); return (u16)(u>>16); }

constexpr int B=2,T=4000,D=512,KW=31,M=B*T;

__global__ void ln_kernel(const u16* in, int ld_in, u16* out, int ld_out){
  int row=blockIdx.x; int t=threadIdx.x;
  float v[8]; float s=0;
  for(int j=0;j<8;j++){ v[j]=b2f(in[(size_t)row*ld_in+t*8+j]); s+=v[j]; }
  for(int o=32;o;o>>=1) s+=__shfl_xor(s,o);
  float m=s/512.f; float q=0;
  for(int j=0;j<8;j++){ float d=v[j]-m; q+=d*d; }
  for(int o=32;o;o>>=1) q+=__shfl_xor(q,o);
  float r=rsqrtf(q/512.f+1e-5f);
  for(int j=0;j<8;j++) out[(size_t)row*ld_out+t*8+j]=f2b((v[j]-m)*r);
}
template<int MODE,bool TB>
__global__ void gemm(const u16* A,int lda,size_t sA,const u16* Bm,int ldb,size_t sB,void* C,int ldc,size_t sC,int Mm,int N,int K,float scale,const u16* R){
  __shared__ float As[32][33], Bs[32][33];
  int bz=blockIdx.z; A+=bz*sA; Bm+=bz*sB;
  int tx=threadIdx.x&31, ty=threadIdx.x>>5;
  int m0=blockIdx.y*32,n0=blockIdx.x*32;
  float acc[4]={0,0,0,0};
  for(int k0=0;k0<K;k0+=32){
    for(int i=0;i<4;i++){ int r=ty+8*i;
      As[r][tx]=(m0+r<Mm)?b2f(A[(size_t)(m0+r)*lda+k0+tx]):0.f;
      if(TB) Bs[tx][r]=(n0+r<N)?b2f(Bm[(size_t)(n0+r)*ldb+k0+tx]):0.f;
      else Bs[r][tx]=(n0+tx<N)?b2f(Bm[(size_t)(k0+r)*ldb+n0+tx]):0.f;
    }
    __syncthreads();
    for(int k=0;k<32;k++){ float b=Bs[k][tx]; for(int i=0;i<4;i++) acc[i]+=As[ty+8*i][k]*b; }
    __syncthreads();
  }
  for(int i=0;i<4;i++){ int r=m0+ty+8*i,c=n0+tx; if(r<Mm&&c<N){ float v=acc[i]*scale;
    if(MODE==1){ float z=0.7978845608f*(v+0.044715f*v*v*v); v=0.5f*v*(1.f+tanhf(z)); }
    if(MODE==3) v+=b2f(R[(size_t)r*ldc+c]);
    if(MODE==2) ((float*)C)[bz*sC+(size_t)r*ldc+c]=v; else ((u16*)C)[bz*sC+(size_t)r*ldc+c]=f2b(v);
  }}
}
__global__ void softmax_k(const float* S,u16* P){
  int row=blockIdx.x; const float* s=S+(size_t)row*T; u16* p=P+(size_t)row*T; int t=threadIdx.x;
  float m=-1e30f; for(int i=t;i<T;i+=64) m=fmaxf(m,s[i]);
  for(int o=32;o;o>>=1) m=fmaxf(m,__shfl_xor(m,o));
  float l=0; for(int i=t;i<T;i+=64) l+=__expf(s[i]-m);
  for(int o=32;o;o>>=1) l+=__shfl_xor(l,o);
  for(int i=t;i<T;i+=64) p[i]=f2b(__expf(s[i]-m)/l);
}
__global__ void conv_k(const u16* in,int ldi,const u16* w,int C,const u16* mul,int ldm,u16* out,int ldo,int addself){
  int row=blockIdx.x; int b=row/T,t=row%T;
  for(int c=threadIdx.x;c<C;c+=blockDim.x){
    float a=0; for(int i=0;i<KW;i++){ int tt=t+i-15; if(tt>=0&&tt<T) a+=b2f(in[(size_t)(b*T+tt)*ldi+c])*b2f(w[i*C+c]); }
    if(addself) a+=b2f(in[(size_t)row*ldi+c]);
    if(mul) a*=b2f(mul[(size_t)row*ldm+c]);
    out[(size_t)row*ldo+c]=f2b(a);
  }
}
extern "C" void kernel_launch(void* const* d_in, const int* in_sizes, int n_in, void* d_out, int out_size, void* d_ws, size_t ws_size, hipStream_t stream){
  const u16* x=(const u16*)d_in[0]; const u16* wqkv=(const u16*)d_in[1]; const u16* wo=(const u16*)d_in[2]; const u16* win=(const u16*)d_in[3];
  const u16* dwc=(const u16*)d_in[4]; const u16* wout=(const u16*)d_in[5]; const u16* dwm=(const u16*)d_in[6]; const u16* wm=(const u16*)d_in[7];
  char* ws=(char*)d_ws; size_t o=0; auto al=[&](size_t n){ void* p=ws+o; o+=(n+255)&~(size_t)255; return p; };
  float* S=(float*)al((size_t)M*T*4); u16* P=(u16*)al((size_t)M*T*2);
  u16* xa=(u16*)al(M*512*2); u16* wqk=(u16*)al(512*512*2); u16* wvo=(u16*)al(512*512*2);
  u16* q=(u16*)al(M*512*2); u16* h=(u16*)al(M*1024*2); u16* mm=(u16*)al(M*1024*2);
  u16* O=(u16*)al(M*512*2);
  u16* g=(u16*)S; u16* w=g+M*512; u16* m2=w+M*512;
  ln_kernel<<<M,64,0,stream>>>(x,512,xa,512);
  gemm<0,true><<<dim3(16,16,1),256,0,stream>>>(wqkv,1536,0,wqkv+512,1536,0,wqk,512,0,512,512,512,1.f,nullptr);
  gemm<0,false><<<dim3(16,16,1),256,0,stream>>>(wqkv+1024,1536,0,wo,512,0,wvo,512,0,512,512,512,1.f,nullptr);
  gemm<0,false><<<dim3(16,250,1),256,0,stream>>>(xa,512,0,wqk,512,0,q,512,0,M,512,512,0.04419417382f,nullptr);
  gemm<2,true><<<dim3(125,125,2),256,0,stream>>>(q,512,(size_t)T*512,xa,512,(size_t)T*512,S,T,(size_t)T*T,T,T,512,1.f,nullptr);
  softmax_k<<<M,64,0,stream>>>(S,P);
  gemm<0,false><<<dim3(16,125,2),256,0,stream>>>(P,T,(size_t)T*T,xa,512,(size_t)T*512,O,512,(size_t)T*512,T,512,T,1.f,nullptr);
  gemm<0,false><<<dim3(16,250,1),256,0,stream>>>(O,512,0,wvo,512,0,mm,1024,0,M,512,512,1.f,nullptr);
  gemm<1,false><<<dim3(32,250,1),256,0,stream>>>(xa,512,0,win,1024,0,h,1024,0,M,1024,512,1.f,nullptr);
  ln_kernel<<<M,64,0,stream>>>(h+512,1024,g,512);
  conv_k<<<M,256,0,stream>>>(g,512,dwc,512,h,1024,w,512,0);
  gemm<0,false><<<dim3(16,250,1),256,0,stream>>>(w,512,0,wout,512,0,mm+512,1024,0,M,512,512,1.f,nullptr);
  conv_k<<<M,256,0,stream>>>(mm,1024,dwm,1024,nullptr,0,m2,1024,1);
  gemm<3,false><<<dim3(16,250,1),256,0,stream>>>(m2,1024,0,wm,512,0,d_out,512,0,M,512,1024,1.f,x);
}
```

```cpp
#include <hip/hip_runtime.h>
typedef unsigned short u16;
typedef unsigned u32;
typedef unsigned long long u64;
typedef unsigned u32x2 __attribute__((ext_vector_type(2)));
typedef unsigned u32x4 __attribute__((ext_vector_type(4)));
typedef float f32x2 __attribute__((ext_vector_type(2)));
typedef float f32x4 __attribute__((ext_vector_type(4)));
typedef __bf16 bf16x2 __attribute__((ext_vector_type(2)));
typedef __bf16 bf16x8 __attribute__((ext_vector_type(8)));
#define DI __device__ __forceinline__
#define RSRC(p, n) __builtin_amdgcn_make_buffer_rsrc((void*)(p), (short)0, (int)(n), 0x00020000)
#define SC1 16

constexpr int T = 4000, NT32 = 250;
constexpr size_t MBy = 1u << 20;
constexpr size_t WS_XA = 0, WS_QI = 10 * MBy, WS_GU = 18 * MBy, WS_ML = 34 * MBy, WS_ST = 35 * MBy, WS_FL = 35 * MBy + 512 * 1024, WS_W1 = 36 * MBy, WS_W4 = 38 * MBy, WS_W5 = 40 * MBy,
                 WS_TZ = WS_W4 + 16 * 69632, WS_W = 44 * MBy, WS_M2 = 56 * MBy, WS_DBG = 76 * MBy, WS_PART = 80 * MBy;
constexpr u32 XA_T = 36864, PT_Q = 20480, PT_S = 500 * PT_Q, W_T = 20480, M2_T = 266240, W1_S = 102400, W4_S = 69632, W5_S = 69632;

DI float lo16(u32 v) { return __uint_as_float(v << 16); }
DI float hi16(u32 v) { return __uint_as_float(v & 0xffff0000u); }
DI u32 pk(float a, float b) { f32x2 v = {a, b}; return __builtin_bit_cast(u32, __builtin_convertvector(v, bf16x2)); }
DI u32 rfl(u32 x) { return __builtin_amdgcn_readfirstlane(x); }
template <int CTRL> DI float dpp(float x) { return __uint_as_float(__builtin_amdgcn_update_dpp(0u, __float_as_uint(x), CTRL, 0xf, 0xf, true)); }
DI u32x2 tr_read(u32 addr) { u32x2 r; asm volatile("ds_read_b64_tr_b16 %0, %1" : "=v"(r) : "v"(addr) : "memory"); return r; }
#define LGKM0(...) asm volatile("s_waitcnt lgkmcnt(0)" : __VA_ARGS__ :: "memory")
#define SB() __builtin_amdgcn_sched_barrier(0)
#define STAMP(i)

__global__ __launch_bounds__(256, 1) void ebf_block(const u16* __restrict__ x, const u16* __restrict__ wqkv, const u16* __restrict__ wo, const u16* __restrict__ win, const u16* __restrict__ dwc,
                                                    const u16* __restrict__ wout, const u16* __restrict__ dwm, const u16* __restrict__ wm, char* __restrict__ ws, u16* __restrict__ out) {
  extern __shared__ __attribute__((aligned(16))) char lds[];
  const u32 tid = threadIdx.x, lane = tid & 63, w = rfl(tid >> 6);
  const u32 bid = blockIdx.x, xcd = bid & 7, idx = bid >> 3, n = xcd * 32 + idx, b = xcd >> 2;
  u32 dg = 0, pfd;
  STAMP(50);
  asm volatile("s_getpc_b64 s[64:65]\n EBF_BEG:\n s_and_b32 s65, s65, 0xffff\n s_mov_b32 s66, EBF_K5_END-EBF_BEG\n s_mov_b32 s67, 0x20000\n buffer_load_dword %0, %1, s[64:67], 0 offen"
               : "=&v"(pfd) : "v"((w * 32 + idx) * 896 + min(lane, 6u) * 128) : "s64", "s65", "s66", "s67", "scc", "memory");
  asm volatile("" :: "s"(x), "s"(wqkv), "s"(wo), "s"(win), "s"(dwc), "s"(wout), "s"(dwm), "s"(wm), "s"(ws), "s"(out));
  const __amdgpu_buffer_rsrc_t rw = RSRC(ws, 128 << 20);
  u32x2 ep;
  asm volatile("buffer_load_dwordx2 %0, %1, %2, 0 offen sc1" : "=&{v[254:255]}"(ep) : "v"((u32)WS_FL + 6 * 32768), "s"(rw) : "memory");
  const int i = lane & 15, g = lane >> 4, lr = lane >> 3, lc = lane & 7;
  const int which = idx & 1, ks = idx >> 1, ng = xcd, grp = (int)xcd * 12 + ks;
  const u32 oob = w ? 0x40000000u : 0u;
  const u32 f1 = (u32)WS_FL + 5 * 32768, sw0 = (lc ^ (lr >> 1)) * 16;
  const u32 oa = i * 128 + ((g ^ (i >> 1)) * 16);
  u32 tagl, tagh;
  u32x4 t2[4] = {};
#define TAGS(cnt) \
    asm volatile("s_waitcnt vmcnt(" #cnt ")\n v_readfirstlane_b32 %0, v254\n v_readfirstlane_b32 %1, v255\n s_nop 0\n s_add_u32 %0, %0, 1\n s_addc_u32 %1, %1, 0" \
                 : "=&s"(tagl), "=&s"(tagh) : "{v[254:255]}"(ep) : "scc", "memory"); \
      \
    __builtin_amdgcn_raw_buffer_store_b32(~tagl, rw, tid < 6 ? (u32)WS_FL + min(tid, 4u) * 32768 + n * 128 + (tid == 5 ? 64 : 0) : 0x40000000u, 0, SC1)
  u32 pv = 0, pv2 = 0, lv[10];
  auto params = [&]() {
  const u32 rt = n >> 2, qo = idx & 3, br = qo >> 1, ct = qo & 1;
  const int ri = rt & 31, tile0 = 4 * ri - 1;
  const u32 P_br = br, P_dbg = (u32)WS_DBG + (bid * 4 + w) * 256;
  asm volatile("s_nop 1\nv_writelane_b32 %0, %1, 34\n" : "+v"(pv) : "s"(rfl(P_br)));
  const u32 P_b4off = (u32)WS_W4 + (qo * 4 + w) * W4_S, P_tz4off = (u32)WS_TZ + (32 + qo * 16 + 4 * w) * 5120;
  asm volatile("s_nop 1\nv_writelane_b32 %0, %1, 20\nv_writelane_b32 %0, %2, 33\n" : "+v"(pv) : "s"(rfl(P_b4off)), "s"(rfl(P_tz4off)));
  const u32 P_tl0 = b * 125 + max(tile0, 0), P_tl1 = b * 125 + tile0 + 1, P_tl2 = b * 125 + min(tile0 + 2, 124), P_tl3 = b * 125 + min(tile0 + 3, 124), P_tl4 = b * 125 + min(tile0 + 4, 124);
  asm volatile("s_nop 1\nv_writelane_b32 %0, %1, 28\nv_writelane_b32 %0, %2, 29\nv_writelane_b32 %0, %3, 30\nv_writelane_b32 %0, %4, 31\nv_writelane_b32 %0, %5, 32\n" : "+v"(pv) : "s"(rfl(P_tl0)), "s"(rfl(P_tl1)), "s"(rfl(P_tl2)), "s"(rfl(P_tl3)), "s"(rfl(P_tl4)));
  const u32 P_tval = ri == 0 ? 30u : ri == 31 ? 3u : 31u;
  asm volatile("s_nop 1\nv_writelane_b32 %0, %1, 37\n" : "+v"(pv) : "s"(rfl(P_tval)));
  const u32 P_mloff = (u32)WS_ML + (w & 1) * 256, P_pwlds = (w & 1) * 16384 + (w >> 1) * 1024, P_poff = (u32)WS_PART + (w & 1) * PT_Q + (w >> 1) * 1024;
  asm volatile("s_nop 1\nv_writelane_b32 %0, %1, 26\nv_writelane_b32 %0, %2, 36\nv_writelane_b32 %0, %3, 27\n" : "+v"(pv) : "s"(rfl(P_mloff)), "s"(rfl(P_pwlds)), "s"(rfl(P_poff)));
  const u32 P_alds = (w & 1) * 5 * 16384 + (w >> 1) * 1024, P_aglb = (u32)WS_W + (u32)((int)b * 250 + 2 * tile0 + (int)(w & 1) * 5) * W_T + (w >> 1) * 1024;
  asm volatile("s_nop 1\nv_writelane_b32 %0, %1, 45\nv_writelane_b32 %0, %2, 46\n" : "+v"(pv) : "s"(rfl(P_alds)), "s"(rfl(P_aglb)));
  const u32 P_sclds = 65536 + w * 21504, P_m2lds = w * 16384, P_m2off = (u32)WS_M2 + rt * M2_T + qo * 65536 + w * 16384;
  asm volatile("s_nop 1\nv_writelane_b32 %0, %1, 42\nv_writelane_b32 %0, %2, 43\nv_writelane_b32 %0, %3, 44\n" : "+v"(pv) : "s"(rfl(P_sclds)), "s"(rfl(P_m2lds)), "s"(rfl(P_m2off)));
  const u32 b5 = (u32)WS_W5 + (qo * 4 + w) * W5_S, P_b5q0 = b5 + qo * 16384, P_b5q1 = b5 + ((qo + 1) & 3) * 16384, P_b5q2 = b5 + ((qo + 2) & 3) * 16384, P_b5q3 = b5 + ((qo + 3) & 3) * 16384;
  asm volatile("s_nop 1\nv_writelane_b32 %0, %1, 38\nv_writelane_b32 %0, %2, 39\nv_writelane_b32 %0, %3, 40\nv_writelane_b32 %0, %4, 41\n" : "+v"(pv) : "s"(rfl(P_b5q0)), "s"(rfl(P_b5q1)), "s"(rfl(P_b5q2)), "s"(rfl(P_b5q3)));
  const u32 a5 = (u32)WS_M2 + rt * M2_T + w * 2048;
  const u32 P_a5g0 = a5 + ((qo + 1) & 3) * 65536, P_a5g1 = a5 + ((qo + 2) & 3) * 65536, P_a5g2 = a5 + ((qo + 3) & 3) * 65536, P_a5lds = w * 2048;
  asm volatile("s_nop 1\nv_writelane_b32 %0, %1, 48\nv_writelane_b32 %0, %2, 49\nv_writelane_b32 %0, %3, 50\nv_writelane_b32 %0, %4, 51\n" : "+v"(pv) : "s"(rfl(P_a5g0)), "s"(rfl(P_a5g1)), "s"(rfl(P_a5g2)), "s"(rfl(P_a5lds)));
  const u32 P_pollm = w ? 0u : ~0u;
  asm volatile("s_nop 1\nv_writelane_b32 %0, %1, 0\n" : "+v"(pv) : "s"(rfl(P_pollm)));
  const u32 P_f4own = oob + (u32)WS_FL + 4 * 32768 + n * 128, P_f4base = oob + (u32)WS_FL + 4 * 32768 + rt * 512;
  asm volatile("s_nop 1\nv_writelane_b32 %0, %1, 52\nv_writelane_b32 %0, %2, 53\n" : "+v"(pv) : "s"(rfl(P_f4own)), "s"(rfl(P_f4base)));
  const u32 P_x5off = (u32)(128 * ri - 16 + 32 * (int)w) * 1024 + qo * 256, P_x5lds = 131072 + w * 8192, P_w4 = 4 * w;
  asm volatile("s_nop 1\nv_writelane_b32 %0, %1, 54\nv_writelane_b32 %0, %2, 57\nv_writelane_b32 %0, %3, 55\n" : "+v"(pv) : "s"(rfl(P_x5off)), "s"(rfl(P_x5lds)), "s"(rfl(P_w4)));
  const u32 P_o5off = (u32)(128 * ri - 16) * 1024 + (qo * 4 + w) * 64;
  asm volatile("s_nop 1\nv_writelane_b32 %0, %1, 56\n" : "+v"(pv) : "s"(rfl(P_o5off)));
  const u32 P_bval = ((ri == 0 ? 0x3fcu : ri == 31 ? 0xfu : 0x3ffu) >> ((w & 1) * 5)) & 31;
  asm volatile("s_nop 1\nv_writelane_b32 %0, %1, 47\n" : "+v"(pv) : "s"(rfl(P_bval)));

  const u32 j2 = qo * 4 + w, gt2 = b * 125 + 4 * ri, P_v2lim = ri == 31 ? 0x40000000u : 0u;
  asm volatile("s_nop 1\nv_writelane_b32 %0, %1, 10\n" : "+v"(pv2) : "s"(rfl(P_v2lim)));
  const u32 P_b2off = (u32)WS_W1 + j2 * W1_S, P_a2lds = w * 32768, P_a2glb = (u32)WS_XA + (b * 125 + min(4 * ri + (int)w, 124)) * XA_T;
  asm volatile("s_nop 1\nv_writelane_b32 %0, %1, 0\nv_writelane_b32 %0, %2, 1\nv_writelane_b32 %0, %3, 2\n" : "+v"(pv2) : "s"(rfl(P_b2off)), "s"(rfl(P_a2lds)), "s"(rfl(P_a2glb)));
  const u32 P_q2off = (u32)WS_QI + gt2 * 32768 + j2 * 2048, P_gu2off = (u32)WS_GU + 2 * j2 * (NT32 * 2048) + gt2 * 2048, P_sw2 = (2 * ri) & 3;
  asm volatile("s_nop 1\nv_writelane_b32 %0, %1, 3\nv_writelane_b32 %0, %2, 4\nv_writelane_b32 %0, %3, 5\n" : "+v"(pv2) : "s"(rfl(P_q2off)), "s"(rfl(P_gu2off)), "s"(rfl(P_sw2)));
  const u32 P_st2lds = 65536 + w * 4096, P_st2off = (w ? P_v2lim : 0u) + (u32)WS_ST + gt2 * 1024 + qo * 8;
  asm volatile("s_nop 1\nv_writelane_b32 %0, %1, 6\nv_writelane_b32 %0, %2, 7\n" : "+v"(pv2) : "s"(rfl(P_st2lds)), "s"(rfl(P_st2off)));
  const u32 P_f2own = oob + (u32)WS_FL + n * 128, P_f2base = oob + (u32)WS_FL + rt * 512, P_f2b = oob + (u32)WS_FL + 32768 + n * 128;
  asm volatile("s_nop 1\nv_writelane_b32 %0, %1, 8\nv_writelane_b32 %0, %2, 9\n" : "+v"(pv2) : "s"(rfl(P_f2own)), "s"(rfl(P_f2base)));
  asm volatile("s_nop 1\nv_writelane_b32 %0, %1, 18\n" : "+v"(pv) : "s"(rfl(P_f2b)));
  const u32 P_f2wbase = (u32)WS_FL + 32768 + w * 8192;
  asm volatile("s_nop 1\nv_writelane_b32 %0, %1, 19\n" : "+v"(pv) : "s"(rfl(P_f2wbase)));
  const u32 P_f1all = (u32)WS_FL + 5 * 32768 + w * 8192, P_epo = (n | w) ? 0x40000000u : (u32)WS_FL + 6 * 32768;
  asm volatile("s_nop 1\nv_writelane_b32 %0, %1, 12\nv_writelane_b32 %0, %2, 13\n" : "+v"(pv2) : "s"(rfl(P_f1all)), "s"(rfl(P_epo)));
  const u32 P_qxw = (w >> 1) * 81920 + 32768 + (w & 1) * 16384, P_qxr = ((w >> 1) ^ 1) * 81920 + 32768 + (w & 1) * 16384;
  asm volatile("s_nop 1\nv_writelane_b32 %0, %1, 14\nv_writelane_b32 %0, %2, 15\n" : "+v"(pv2) : "s"(rfl(P_qxw)), "s"(rfl(P_qxr)));

  const u32 qi = w & 1, kh = w >> 1, qb = (n & 127) >> 1, split = idx & 1, strm = 2 * split + kh;
  const u32 nt = qb == 63 ? 11 : strm ? 31 : 32, ntp = qb == 63 ? 11 : strm == 1 ? 32 : 31, nt0 = qb == 63 ? 11 : 32 - split, t0 = strm * 31 + (strm ? 1 : 0), xbar = nt < ntp;
  const u32 qraw = qb * 2 + qi, qtile = b * 125 + min(qraw, 124u), ldsb = kh * 81920;
  const u32 P_goff = (u32)WS_XA + (b * 125 + t0) * XA_T + kh * 16384 + qi * 8192, P_qoff = (u32)WS_QI + qtile * 32768 + kh * 16384, P_inc1 = kh ? 0u - 16384u : 16384u;
  asm volatile("s_nop 1\nv_writelane_b32 %0, %1, 2\nv_writelane_b32 %0, %2, 4\nv_writelane_b32 %0, %3, 8\n" : "+v"(pv) : "s"(rfl(P_goff)), "s"(rfl(P_qoff)), "s"(rfl(P_inc1)));
  const int wi = (n & 127) >> 3, wt0 = 8 * wi - 1, gt0 = (int)b * 125 + wt0;
  const u32 cgp = (idx & 7) * 4 + w;
  const u32 P_woff = (u32)WS_GU + cgp * (NT32 * 2048) + gt0 * 2048, P_toff = (u32)WS_TZ + cgp * 5120, P_soff = (u32)WS_ST + gt0 * 1024 + w * 1024;
  asm volatile("s_nop 1\nv_writelane_b32 %0, %1, 5\nv_writelane_b32 %0, %2, 6\nv_writelane_b32 %0, %3, 7\n" : "+v"(pv) : "s"(rfl(P_woff)), "s"(rfl(P_toff)), "s"(rfl(P_soff)));
  const u32 vQ = 1024 * (lane >> 5) + 32 * (lane & 15) + 16 * ((lane >> 4) & 1), vKb = ldsb + vQ, vVb = ldsb + 8 * lane;
#define UNIT(len, k) ((((len) == 32 ? 0x104u : 0x432u) >> (4 * (k)) & 7u) * 16384u)
  const u32 pbase = (kh ^ 1) * 81920 + qi * 8192, P_wlds = ldsb + qi * 8192;
  asm volatile("s_nop 1\nv_writelane_b32 %0, %1, 3\n" : "+v"(pv) : "s"(rfl(P_wlds)));
  const u32 P_c0 = P_wlds + UNIT(nt, 0), P_c1 = P_wlds + UNIT(nt, 1), c2 = P_wlds + UNIT(nt, 2);
  asm volatile("s_nop 1\nv_writelane_b32 %0, %1, 10\nv_writelane_b32 %0, %2, 11\n" : "+v"(pv) : "s"(rfl(P_c0)), "s"(rfl(P_c1)));
  const u32 tab = UNIT(nt0, 2) + 2048;
  const u32 cq = lane >> 2, q = lane & 3, drop = qraw < 125 ? 0u : 0x40000000u;
  u32 vT[3];
#pragma unroll
  for (int k = 0; k < 3; ++k) { const u32 tl = 2 * q + k; vT[k] = (tl < 4 ? P_c0 : tl < 8 ? P_c1 : c2) + (tl & 3) * 2048 + cq * 64 + (((u32)(wt0 + (int)tl) >> 1) & 3) * 16; }
  const u32 vTabR = tab + q * 272, vTabW = tab + ((lane & 1) * 416 + 32 * w + 4 * (w >> 1) + (lane >> 1)) * 4;
  const u32 vMl = (drop | (kh << 30)) + (u32)WS_ML + (qtile * 32 + (lane & 15)) * 16 + split * 8;
  const int tw = 32 * wt0 + 16 + 64 * (int)q;
  const u32 P_wrow = 32 * wt0 + 16, P_waddr = (u32)WS_W + (((int)b * T + 32 * wt0 + 16) >> 4) * W_T + cgp * 512, P_gmask = br ? 2u : w ? 0u : 1u;
  asm volatile("s_nop 1\nv_writelane_b32 %0, %1, 23\nv_writelane_b32 %0, %2, 24\nv_writelane_b32 %0, %3, 25\n" : "+v"(pv) : "s"(rfl(P_wrow)), "s"(rfl(P_waddr)), "s"(rfl(P_gmask)));
  const u32 tval = ((u32)(wt0 + (int)w) < 125u) | (((u32)(wt0 + (int)w + 4) < 125u) << 1) | (((u32)(wt0 + (int)w + 8) < 125u) << 2);
  const u32 P_flags = nt | (xbar << 8) | (tval << 9) | (w << 16) | (idx << 18);
  asm volatile("s_nop 1\nv_writelane_b32 %0, %1, 9\n" : "+v"(pv) : "s"(rfl(P_flags)));
  const u32 P_p0 = pbase + UNIT(ntp, 0), P_p1 = pbase + UNIT(ntp, 1), P_mlo = c2 + 6144, P_mlp = pbase + UNIT(ntp, 2) + 6144;
  asm volatile("s_nop 1\nv_writelane_b32 %0, %1, 12\nv_writelane_b32 %0, %2, 13\nv_writelane_b32 %0, %3, 14\nv_writelane_b32 %0, %4, 15\n" : "+v"(pv) : "s"(rfl(P_p0)), "s"(rfl(P_p1)), "s"(rfl(P_mlo)), "s"(rfl(P_mlp)));
  const u32 P_ov = drop + (u32)WS_PART + split * PT_S + qtile * (2 * PT_Q) + kh * 8192;
  asm volatile("s_nop 1\nv_writelane_b32 %0, %1, 16\n" : "+v"(pv) : "s"(rfl(P_ov)));
  const u32 P_f3o = oob + (u32)WS_FL + 2 * 32768 + n * 128, P_f3w = oob + (u32)WS_FL + 3 * 32768 + n * 128;
  asm volatile("s_nop 1\nv_writelane_b32 %0, %1, 17\nv_writelane_b32 %0, %2, 35\n" : "+v"(pv) : "s"(rfl(P_f3o)), "s"(rfl(P_f3w)));
  const u32 f3lo = br == 0 ? 4 * rt - (ri ? 2 : 0) : (n & ~7u) - ((ri & 1) == 0 && ri ? 8 : 0), f3hi = br == 0 ? 4 * rt + 4 : (n & ~7u) + ((ri & 1) && ri != 31 ? 16 : 8);
  const u32 P_f3base = oob + (u32)WS_FL + (2 + br) * 32768 + f3lo * 128, P_f3cnt = (f3hi - f3lo - 1) * 128;
  asm volatile("s_nop 1\nv_writelane_b32 %0, %1, 21\nv_writelane_b32 %0, %2, 22\n" : "+v"(pv) : "s"(rfl(P_f3base)), "s"(rfl(P_f3cnt)));

  lv[0] = vQ; lv[1] = vKb; lv[2] = vVb; lv[3] = vT[0]; lv[4] = vT[1]; lv[5] = vT[2]; lv[6] = vTabR; lv[7] = vTabW; lv[8] = vMl; lv[9] = tw;
  asm volatile("" : "+v"(lv[0]), "+v"(lv[1]), "+v"(lv[2]), "+v"(lv[3]), "+v"(lv[4]), "+v"(lv[5]), "+v"(lv[6]), "+v"(lv[7]), "+v"(lv[8]), "+v"(lv[9]));
  SB();
  };
  if (which) {
    const u32 pos = 8 * w + lr;
    u32x4 xv[2][8];
#pragma unroll
    for (int h = 0; h < 2; ++h) {
      const u16* xr = x + (size_t)(b * T + min((n & 126) + h, 124u) * 32 + 8 * ((pos >> 2) & 3) + 4 * (pos >> 4) + (pos & 3)) * 512 + lc * 8;
#pragma unroll
      for (int k = 0; k < 8; ++k) xv[h][k] = *(const u32x4*)(xr + 64 * k);
    }
    SB();
    STAMP(51);
    params();
    TAGS(16);
#pragma unroll
    for (int h = 0; h < 2; ++h) {
      f32x2 f[32], s2 = {0.f, 0.f}, q2 = {0.f, 0.f};
#pragma unroll
      for (int k = 0; k < 32; ++k) { const u32 d = xv[h][k >> 2][k & 3]; f[k] = f32x2{lo16(d), hi16(d)}; s2 += f[k]; }
      float s = s2[0] + s2[1];
      s += dpp<0xB1>(s); s += dpp<0x4E>(s); s += dpp<0x141>(s);
      const float mean = s * (1.0f / 512.0f);
#pragma unroll
      for (int k = 0; k < 32; ++k) { f[k] -= mean; q2 = __builtin_elementwise_fma(f[k], f[k], q2); }
      float q1 = q2[0] + q2[1];
      q1 += dpp<0xB1>(q1); q1 += dpp<0x4E>(q1); q1 += dpp<0x141>(q1);
      const float rs = __builtin_amdgcn_rsqf(q1 * (1.0f / 512.0f) + 1e-5f);
      const u32 lt = (n & 126) + h, o = (lt < 125 ? 0u : 0x40000000u) + (u32)WS_XA + (b * 125 + lt) * XA_T + (lc >> 1) * 1024 + pos * 32 + (lc & 1) * 16;
#pragma unroll
      for (int k = 0; k < 8; ++k) {
        u32x4 t;
#pragma unroll
        for (int e = 0; e < 4; ++e) { const f32x2 y = f[4 * k + e] * rs; t[e] = pk(y[0], y[1]); }
        __builtin_amdgcn_raw_buffer_store_b128(t, rw, o + k * 4096, 0, SC1);
      }
      SB();
    }
    STAMP(52);
    const u32 swt = lr * 1024 + (lc ^ (lr & 2)) * 16;
    asm volatile(
R"(s_nop 4
s_lshl_b32 s64, %[w], 13
s_add_u32 s64, s64, 0x20000
s_lshl_b32 s65, %[w], 15
s_add_u32 s65, s65, 0x4000
s_add_u32 m0, s64, 0
s_add_u32 s66, %[ao], 0
buffer_load_dwordx4 %[va0], %[ra], s66 offen lds
s_add_u32 m0, s64, 1024
s_add_u32 s66, %[ao], 24576
buffer_load_dwordx4 %[va1], %[ra], s66 offen lds
s_add_u32 m0, s64, 2048
s_add_u32 s66, %[ao], 49152
buffer_load_dwordx4 %[va0], %[ra], s66 offen lds
s_add_u32 m0, s64, 3072
s_add_u32 s66, %[ao], 73728
buffer_load_dwordx4 %[va1], %[ra], s66 offen lds
s_add_u32 m0, s65, 0
s_add_u32 s66, %[bo], 0
buffer_load_dwordx4 %[vb0], %[rb], s66 offen lds
s_add_u32 m0, s65, 1024
s_add_u32 s66, %[bo], 8192
buffer_load_dwordx4 %[vb1], %[rb], s66 offen lds
s_add_u32 m0, s65, 2048
s_add_u32 s66, %[bo], 16384
buffer_load_dwordx4 %[vb0], %[rb], s66 offen lds
s_add_u32 m0, s65, 3072
s_add_u32 s66, %[bo], 24576
buffer_load_dwordx4 %[vb1], %[rb], s66 offen lds
s_add_u32 m0, s65, 4096
s_add_u32 s66, %[bo], 32768
buffer_load_dwordx4 %[vb0], %[rb], s66 offen lds
s_add_u32 m0, s65, 5120
s_add_u32 s66, %[bo], 40960
buffer_load_dwordx4 %[vb1], %[rb], s66 offen lds
s_add_u32 m0, s65, 6144
s_add_u32 s66, %[bo], 49152
buffer_load_dwordx4 %[vb0], %[rb], s66 offen lds
s_add_u32 m0, s65, 7168
s_add_u32 s66, %[bo], 57344
buffer_load_dwordx4 %[vb1], %[rb], s66 offen lds
s_waitcnt vmcnt(12)
s_barrier
buffer_store_dwordx2 %[fv], %[fo], %[rw], 0 offen sc1
s_add_u32 m0, s64, 4096
s_add_u32 s66, %[ao], 128
buffer_load_dwordx4 %[va0], %[ra], s66 offen lds
s_add_u32 m0, s64, 5120
s_add_u32 s66, %[ao], 24704
buffer_load_dwordx4 %[va1], %[ra], s66 offen lds
s_add_u32 m0, s64, 6144
s_add_u32 s66, %[ao], 49280
buffer_load_dwordx4 %[va0], %[ra], s66 offen lds
s_add_u32 m0, s64, 7168
s_add_u32 s66, %[ao], 73856
buffer_load_dwordx4 %[va1], %[ra], s66 offen lds
s_add_u32 m0, s65, 8192
s_add_u32 s66, %[bo], 65536
buffer_load_dwordx4 %[vb0], %[rb], s66 offen lds
s_add_u32 m0, s65, 9216
s_add_u32 s66, %[bo], 73728
buffer_load_dwordx4 %[vb1], %[rb], s66 offen lds
s_add_u32 m0, s65, 10240
s_add_u32 s66, %[bo], 81920
buffer_load_dwordx4 %[vb0], %[rb], s66 offen lds
s_add_u32 m0, s65, 11264
s_add_u32 s66, %[bo], 90112
buffer_load_dwordx4 %[vb1], %[rb], s66 offen lds
s_add_u32 m0, s65, 12288
s_add_u32 s66, %[bo], 98304
buffer_load_dwordx4 %[vb0], %[rb], s66 offen lds
s_add_u32 m0, s65, 13312
s_add_u32 s66, %[bo], 106496
buffer_load_dwordx4 %[vb1], %[rb], s66 offen lds
s_add_u32 m0, s65, 14336
s_add_u32 s66, %[bo], 114688
buffer_load_dwordx4 %[vb0], %[rb], s66 offen lds
s_add_u32 m0, s65, 15360
s_add_u32 s66, %[bo], 122880
buffer_load_dwordx4 %[vb1], %[rb], s66 offen lds
)"
      :: [ra] "s"(RSRC(wqkv, 512 * 3072)), [rb] "s"(RSRC(wo, 512 * 1024)), [rw] "s"(rw), [ao] "s"(rfl(32 * ks * 3072 + 2048 + 256 * w)), [bo] "s"(rfl(128 * w * 1024 + 128 * ng)), [w] "s"(w),
         [va0] "v"(lr * 3072 + sw0), [va1] "v"(lr * 3072 + (sw0 ^ 64)), [vb0] "v"(swt), [vb1] "v"(swt ^ 64), [fv] "v"(u32x2{tagl, tagh}), [fo] "v"(oob + f1 + n * 128), "v"(pfd)
      : "s64", "s65", "s66", "scc", "memory");
  } else {
    const u32 vr = (8 * (lr >> 2) + (lr & 3)) * 3072;
    asm volatile(
R"(s_nop 4
s_lshl_b32 s64, %[w], 12
s_add_u32 s64, s64, 98304
s_add_u32 m0, s64, 0
s_add_u32 s66, %[io], 0
buffer_load_dwordx4 %[vi], %[rb], s66 offen lds
s_add_u32 m0, s64, 1024
s_add_u32 s66, %[io], 16384
buffer_load_dwordx4 %[vi], %[rb], s66 offen lds
s_add_u32 m0, s64, 2048
s_add_u32 s66, %[io], 32768
buffer_load_dwordx4 %[vi], %[rb], s66 offen lds
s_add_u32 m0, s64, 3072
s_add_u32 s66, %[io], 49152
buffer_load_dwordx4 %[vi], %[rb], s66 offen lds
s_lshl_b32 s64, %[w], 13
s_lshl_b32 s65, %[w], 14
s_add_u32 s65, s65, 0x8000
s_add_u32 m0, s64, 0
s_add_u32 s66, %[ao], 0
buffer_load_dwordx4 %[va0], %[ra], s66 offen lds
s_add_u32 m0, s64, 1024
s_add_u32 s66, %[ao], 49152
buffer_load_dwordx4 %[va1], %[ra], s66 offen lds
s_add_u32 m0, s64, 2048
s_add_u32 s66, %[ao], 12288
buffer_load_dwordx4 %[va0], %[ra], s66 offen lds
s_add_u32 m0, s64, 3072
s_add_u32 s66, %[ao], 61440
buffer_load_dwordx4 %[va1], %[ra], s66 offen lds
s_add_u32 m0, s65, 0
s_add_u32 s66, %[bo], 0
buffer_load_dwordx4 %[va0], %[ra], s66 offen lds
s_add_u32 m0, s65, 1024
s_add_u32 s66, %[bo], 49152
buffer_load_dwordx4 %[va1], %[ra], s66 offen lds
s_add_u32 m0, s65, 2048
s_add_u32 s66, %[bo], 12288
buffer_load_dwordx4 %[va0], %[ra], s66 offen lds
s_add_u32 m0, s65, 3072
s_add_u32 s66, %[bo], 61440
buffer_load_dwordx4 %[va1], %[ra], s66 offen lds
s_add_u32 m0, s65, 4096
s_add_u32 s66, %[bo], 98304
buffer_load_dwordx4 %[va0], %[ra], s66 offen lds
s_add_u32 m0, s65, 5120
s_add_u32 s66, %[bo], 147456
buffer_load_dwordx4 %[va1], %[ra], s66 offen lds
s_add_u32 m0, s65, 6144
s_add_u32 s66, %[bo], 110592
buffer_load_dwordx4 %[va0], %[ra], s66 offen lds
s_add_u32 m0, s65, 7168
s_add_u32 s66, %[bo], 159744
buffer_load_dwordx4 %[va1], %[ra], s66 offen lds
s_add_u32 m0, s64, 4096
s_add_u32 s66, %[ao], 128
buffer_load_dwordx4 %[va0], %[ra], s66 offen lds
s_add_u32 m0, s64, 5120
s_add_u32 s66, %[ao], 49280
buffer_load_dwordx4 %[va1], %[ra], s66 offen lds
s_add_u32 m0, s64, 6144
s_add_u32 s66, %[ao], 12416
buffer_load_dwordx4 %[va0], %[ra], s66 offen lds
s_add_u32 m0, s64, 7168
s_add_u32 s66, %[ao], 61568
buffer_load_dwordx4 %[va1], %[ra], s66 offen lds
s_add_u32 m0, s65, 8192
s_add_u32 s66, %[bo], 128
buffer_load_dwordx4 %[va0], %[ra], s66 offen lds
s_add_u32 m0, s65, 9216
s_add_u32 s66, %[bo], 49280
buffer_load_dwordx4 %[va1], %[ra], s66 offen lds
s_add_u32 m0, s65, 10240
s_add_u32 s66, %[bo], 12416
buffer_load_dwordx4 %[va0], %[ra], s66 offen lds
s_add_u32 m0, s65, 11264
s_add_u32 s66, %[bo], 61568
buffer_load_dwordx4 %[va1], %[ra], s66 offen lds
s_add_u32 m0, s65, 12288
s_add_u32 s66, %[bo], 98432
buffer_load_dwordx4 %[va0], %[ra], s66 offen lds
s_add_u32 m0, s65, 13312
s_add_u32 s66, %[bo], 147584
buffer_load_dwordx4 %[va1], %[ra], s66 offen lds
s_add_u32 m0, s65, 14336
s_add_u32 s66, %[bo], 110720
buffer_load_dwordx4 %[va0], %[ra], s66 offen lds
s_add_u32 m0, s65, 15360
s_add_u32 s66, %[bo], 159872
buffer_load_dwordx4 %[va1], %[ra], s66 offen lds
)"
      :: [ra] "s"(RSRC(wqkv, 512 * 3072)), [rb] "s"(RSRC(win, 512 * 2048)), [ao] "s"(rfl(32 * ks * 3072 + 256 * w)), [bo] "s"(rfl(64 * ng * 3072 + 1024 + 256 * w)),
         [io] "s"(rfl(32 * ks * 2048 + (w & 1) * 1024 + 128 * ng)), [w] "s"(w), [va0] "v"(vr + sw0), [va1] "v"(vr + (sw0 ^ 64)), [vi] "v"((w < 2 ? 0u : 0x40000000u) + lr * 2048 + (lc ^ (lr & 6)) * 16)
      : "s64", "s65", "s66", "scc", "memory");
    STAMP(51);
    params();
    TAGS(24);
    {
      const u32 r0_ = 8 * g + (i >> 2), a0_ = 98304 + w * 4096 + r0_ * 128 + (i & 1) * 8;
      u32x2 t[4][2];
#pragma unroll
      for (int c = 0; c < 4; ++c) { const u32 ch = 2 * c + ((i & 3) >> 1); t[c][0] = tr_read(a0_ + (ch ^ (r0_ & 6)) * 16); t[c][1] = tr_read(a0_ + 512 + (ch ^ ((r0_ + 4) & 6)) * 16); }
      LGKM0("+v"(t[0][0]), "+v"(t[0][1]), "+v"(t[1][0]), "+v"(t[1][1]), "+v"(t[2][0]), "+v"(t[2][1]), "+v"(t[3][0]), "+v"(t[3][1]));
      const u32 d0 = (w < 2 ? 0u : 0x40000000u) + (u32)WS_W1 + 2 * ng * W1_S + (ks * 3 + (w ? 49 : 2)) * 1024, d1 = w ? 1024 : 46 * 1024;
#pragma unroll
      for (int c = 0; c < 4; ++c) __builtin_amdgcn_raw_buffer_store_b128(u32x4{t[c][0][0], t[c][0][1], t[c][1][0], t[c][1][1]}, rw, lane * 16 + d0 + (c >> 1) * W1_S + (c & 1) * d1, 0, SC1);
    }
    STAMP(52);
    f32x4 r0, r1;
    asm volatile("s_waitcnt vmcnt(5)\n s_barrier\n"
R"(s_waitcnt lgkmcnt(0)
ds_read_b128 a[128:131], %[a0] offset:0
ds_read_b128 a[132:135], %[a0] offset:2048
ds_read_b128 a[136:139], %[b0] offset:0
ds_read_b128 a[140:143], %[a0x] offset:0
ds_read_b128 a[144:147], %[a0x] offset:2048
ds_read_b128 a[148:151], %[b1] offset:0
ds_read_b128 a[152:155], %[a0] offset:4096
ds_read_b128 a[156:159], %[a0] offset:6144
ds_read_b128 a[160:163], %[b0] offset:8192
ds_read_b128 a[164:167], %[a0x] offset:4096
ds_read_b128 a[168:171], %[a0x] offset:6144
ds_read_b128 a[172:175], %[b1] offset:8192
ds_read_b128 a[176:179], %[a0] offset:8192
ds_read_b128 a[180:183], %[a0] offset:10240
ds_read_b128 a[184:187], %[b0] offset:16384
s_waitcnt lgkmcnt(12)
v_mfma_f32_16x16x32_bf16 %0, a[128:131], a[136:139], 0
v_mfma_f32_16x16x32_bf16 %1, a[132:135], a[136:139], 0
ds_read_b128 a[128:131], %[a0x] offset:8192
ds_read_b128 a[132:135], %[a0x] offset:10240
ds_read_b128 a[136:139], %[b1] offset:16384
s_waitcnt lgkmcnt(12)
v_mfma_f32_16x16x32_bf16 %0, a[140:143], a[148:151], %0
v_mfma_f32_16x16x32_bf16 %1, a[144:147], a[148:151], %1
ds_read_b128 a[140:143], %[a0] offset:12288
ds_read_b128 a[144:147], %[a0] offset:14336
ds_read_b128 a[148:151], %[b0] offset:24576
s_waitcnt lgkmcnt(12)
v_mfma_f32_16x16x32_bf16 %0, a[152:155], a[160:163], %0
v_mfma_f32_16x16x32_bf16 %1, a[156:159], a[160:163], %1
ds_read_b128 a[152:155], %[a0x] offset:12288
ds_read_b128 a[156:159], %[a0x] offset:14336
ds_read_b128 a[160:163], %[b1] offset:24576
s_waitcnt lgkmcnt(12)
v_mfma_f32_16x16x32_bf16 %0, a[164:167], a[172:175], %0
v_mfma_f32_16x16x32_bf16 %1, a[168:171], a[172:175], %1
ds_read_b128 a[164:167], %[a0] offset:16384
ds_read_b128 a[168:171], %[a0] offset:18432
ds_read_b128 a[172:175], %[b0] offset:32768
s_waitcnt lgkmcnt(12)
v_mfma_f32_16x16x32_bf16 %0, a[176:179], a[184:187], %0
v_mfma_f32_16x16x32_bf16 %1, a[180:183], a[184:187], %1
ds_read_b128 a[176:179], %[a0x] offset:16384
ds_read_b128 a[180:183], %[a0x] offset:18432
ds_read_b128 a[184:187], %[b1] offset:32768
s_waitcnt lgkmcnt(12)
v_mfma_f32_16x16x32_bf16 %0, a[128:131], a[136:139], %0
v_mfma_f32_16x16x32_bf16 %1, a[132:135], a[136:139], %1
ds_read_b128 a[128:131], %[a0] offset:20480
ds_read_b128 a[132:135], %[a0] offset:22528
ds_read_b128 a[136:139], %[b0] offset:40960
s_waitcnt lgkmcnt(12)
v_mfma_f32_16x16x32_bf16 %0, a[140:143], a[148:151], %0
v_mfma_f32_16x16x32_bf16 %1, a[144:147], a[148:151], %1
ds_read_b128 a[140:143], %[a0x] offset:20480
ds_read_b128 a[144:147], %[a0x] offset:22528
ds_read_b128 a[148:151], %[b1] offset:40960
s_waitcnt lgkmcnt(12)
v_mfma_f32_16x16x32_bf16 %0, a[152:155], a[160:163], %0
v_mfma_f32_16x16x32_bf16 %1, a[156:159], a[160:163], %1
ds_read_b128 a[152:155], %[a0] offset:24576
ds_read_b128 a[156:159], %[a0] offset:26624
ds_read_b128 a[160:163], %[b0] offset:49152
s_waitcnt lgkmcnt(12)
v_mfma_f32_16x16x32_bf16 %0, a[164:167], a[172:175], %0
v_mfma_f32_16x16x32_bf16 %1, a[168:171], a[172:175], %1
ds_read_b128 a[164:167], %[a0x] offset:24576
ds_read_b128 a[168:171], %[a0x] offset:26624
ds_read_b128 a[172:175], %[b1] offset:49152
s_waitcnt lgkmcnt(12)
v_mfma_f32_16x16x32_bf16 %0, a[176:179], a[184:187], %0
v_mfma_f32_16x16x32_bf16 %1, a[180:183], a[184:187], %1
ds_read_b128 a[176:179], %[a0] offset:28672
ds_read_b128 a[180:183], %[a0] offset:30720
ds_read_b128 a[184:187], %[b0] offset:57344
s_waitcnt lgkmcnt(12)
v_mfma_f32_16x16x32_bf16 %0, a[128:131], a[136:139], %0
v_mfma_f32_16x16x32_bf16 %1, a[132:135], a[136:139], %1
ds_read_b128 a[128:131], %[a0x] offset:28672
ds_read_b128 a[132:135], %[a0x] offset:30720
ds_read_b128 a[136:139], %[b1] offset:57344
s_waitcnt lgkmcnt(12)
v_mfma_f32_16x16x32_bf16 %0, a[140:143], a[148:151], %0
v_mfma_f32_16x16x32_bf16 %1, a[144:147], a[148:151], %1
s_waitcnt lgkmcnt(9)
v_mfma_f32_16x16x32_bf16 %0, a[152:155], a[160:163], %0
v_mfma_f32_16x16x32_bf16 %1, a[156:159], a[160:163], %1
s_waitcnt lgkmcnt(6)
v_mfma_f32_16x16x32_bf16 %0, a[164:167], a[172:175], %0
v_mfma_f32_16x16x32_bf16 %1, a[168:171], a[172:175], %1
s_waitcnt lgkmcnt(3)
v_mfma_f32_16x16x32_bf16 %0, a[176:179], a[184:187], %0
v_mfma_f32_16x16x32_bf16 %1, a[180:183], a[184:187], %1
s_waitcnt lgkmcnt(0)
v_mfma_f32_16x16x32_bf16 %0, a[128:131], a[136:139], %0
v_mfma_f32_16x16x32_bf16 %1, a[132:135], a[136:139], %1
s_nop 7
)"
      : "=&v"(r0), "=&v"(r1) : [a0] "v"(oa), [a0x] "v"(oa ^ 64), [b0] "v"(32768 + w * 2048 + oa), [b1] "v"(32768 + w * 2048 + (oa ^ 64)), "v"(pfd)
      : "a128","a129","a130","a131","a132","a133","a134","a135","a136","a137","a138","a139","a140","a141","a142","a143","a144","a145","a146","a147","a148","a149","a150","a151","a152","a153","a154","a155","a156","a157","a158","a159","a160","a161","a162","a163","a164","a165","a166","a167","a168","a169","a170","a171","a172","a173","a174","a175","a176","a177","a178","a179","a180","a181","a182","a183","a184","a185","a186","a187","scc","memory");
    const float sc = 0.04419417382415922f * 1.4426950408889634f;
    r0 *= sc; r1 *= sc;
    __builtin_amdgcn_raw_buffer_store_b128(u32x4{pk(r0[0], r0[1]), pk(r0[2], r0[3]), pk(r1[0], r1[1]), pk(r1[2], r1[3])}, rw,
                                           lane * 16 + (u32)WS_W1 + (2 * ng + (w >> 1)) * W1_S + (ks * 3 + (w & 1)) * 1024, 0, SC1);
    asm volatile("s_waitcnt vmcnt(0)\n s_barrier" ::: "memory");
    __builtin_amdgcn_raw_buffer_store_b64(u32x2{tagl, tagh}, rw, oob + f1 + n * 128, 0, SC1);
    const int ld1 = (w == 1 && grp >= 32) ? 1024 : 512;
    const u16* s1 = w != 1 ? (w ? wm + (size_t)(w & 1) * 512 * 512 : wout) + (size_t)(32 * ks + lr) * 512 + 64 * ng + 8 * lc
                           : (grp < 32 ? dwc + 16 * grp : dwm + 16 * (min(grp, 95) - 32)) + (size_t)min(lane >> 1, 30u) * ld1 + (lane & 1) * 8;
#pragma unroll
    for (int r = 0; r < 4; ++r) t2[r] = *(const u32x4*)(s1 + (w != 1 ? r * 4096 : 0));
  }
  SB();
  STAMP(53);
  const u32 P_tag = tagl, P_tagh = tagh;
  asm volatile("s_nop 1\nv_writelane_b32 %0, %1, 11\n" : "+v"(pv2) : "s"(rfl(P_tagh)));
  asm volatile("s_nop 1\nv_writelane_b32 %0, %1, 1\n" : "+v"(pv) : "s"(rfl(P_tag)));
  SB();
  STAMP(54);
  if (w == 0) {
    const u32 fa1 = f1 + (lane < 32 ? (idx & 3) * 64 + 2 * lane : (n & ~3u) + (lane & 3)) * 128;
#pragma unroll 1
    for (int it = 0; it < (1 << 18); ++it) {
      const u32x2 fl = __builtin_amdgcn_raw_buffer_load_b64(rw, fa1, 0, SC1);
      if (__builtin_amdgcn_ballot_w64(fl[0] != P_tag || fl[1] != P_tagh) == 0) break;
      __builtin_amdgcn_s_sleep(1);
      asm volatile("" ::: "memory");
    }
  }
  asm volatile("" : "+v"(t2[0]), "+v"(t2[1]), "+v"(t2[2]), "+v"(t2[3]));
  asm volatile(
R"(s_waitcnt lgkmcnt(0)
s_nop 4
v_readlane_b32 s80,%[pv2],0
v_readlane_b32 s82,%[pv2],1
v_readlane_b32 s83,%[pv2],2
s_waitcnt vmcnt(0)
s_barrier
s_add_u32 m0,s82,0
s_add_u32 s81,s83,0
buffer_load_dwordx4 %[vL16],%[srdW],s81 offen offset:0 sc1 lds
buffer_load_dwordx4 %[vL16],%[srdW],s81 offen offset:1024 sc1 lds
buffer_load_dwordx4 %[vL16],%[srdW],s81 offen offset:2048 sc1 lds
buffer_load_dwordx4 %[vL16],%[srdW],s81 offen offset:3072 sc1 lds
buffer_load_dwordx4 a[32:35],%[vL16],%[srdW],s80 offen offset:0 sc1
buffer_load_dwordx4 a[36:39],%[vL16],%[srdW],s80 offen offset:1024 sc1
buffer_load_dwordx4 a[40:43],%[vL16],%[srdW],s80 offen offset:2048 sc1
buffer_load_dwordx4 a[44:47],%[vL16],%[srdW],s80 offen offset:3072 sc1
s_add_u32 s80,s80,0x1000
buffer_load_dwordx4 a[48:51],%[vL16],%[srdW],s80 offen offset:0 sc1
buffer_load_dwordx4 a[52:55],%[vL16],%[srdW],s80 offen offset:1024 sc1
s_add_u32 m0,s82,4096
s_add_u32 s81,s83,4096
buffer_load_dwordx4 %[vL16],%[srdW],s81 offen offset:0 sc1 lds
buffer_load_dwordx4 %[vL16],%[srdW],s81 offen offset:1024 sc1 lds
buffer_load_dwordx4 %[vL16],%[srdW],s81 offen offset:2048 sc1 lds
buffer_load_dwordx4 %[vL16],%[srdW],s81 offen offset:3072 sc1 lds
buffer_load_dwordx4 a[56:59],%[vL16],%[srdW],s80 offen offset:2048 sc1
buffer_load_dwordx4 a[60:63],%[vL16],%[srdW],s80 offen offset:3072 sc1
s_add_u32 s80,s80,0x1000
buffer_load_dwordx4 a[64:67],%[vL16],%[srdW],s80 offen offset:0 sc1
s_add_u32 m0,s82,8192
s_add_u32 s81,s83,8192
buffer_load_dwordx4 %[vL16],%[srdW],s81 offen offset:0 sc1 lds
buffer_load_dwordx4 %[vL16],%[srdW],s81 offen offset:1024 sc1 lds
buffer_load_dwordx4 %[vL16],%[srdW],s81 offen offset:2048 sc1 lds
buffer_load_dwordx4 %[vL16],%[srdW],s81 offen offset:3072 sc1 lds
)"
    :: [srdW] "s"(rw), [pv2] "v"(pv2), [vL16] "v"(lane * 16) : "s80", "s81", "s82", "s83", "a32", "a67", "scc", "memory");
  STAMP(56);
  if (which) {
    const int tr0 = 8 * g + (i >> 2), tch = 2 * w + ((i & 3) >> 1);
    const u32 ot = 16384 + tr0 * 128 + ((tch ^ ((tr0 & 2) | ((tr0 >> 1) & 4))) * 16) + (i & 1) * 8;
    f32x4 r0, r1;
    asm volatile(
R"(s_waitcnt lgkmcnt(0)
ds_read_b128 a[128:131], %[a0] offset:0
ds_read_b128 a[132:135], %[a0] offset:2048
ds_read_b64_tr_b16 a[136:137], %[b0] offset:0
ds_read_b64_tr_b16 a[138:139], %[b0] offset:512
ds_read_b128 a[140:143], %[a0x] offset:0
ds_read_b128 a[144:147], %[a0x] offset:2048
ds_read_b64_tr_b16 a[148:149], %[b0] offset:4096
ds_read_b64_tr_b16 a[150:151], %[b0] offset:4608
ds_read_b128 a[152:155], %[a0] offset:4096
ds_read_b128 a[156:159], %[a0] offset:6144
ds_read_b64_tr_b16 a[160:161], %[b0] offset:8192
ds_read_b64_tr_b16 a[162:163], %[b0] offset:8704
ds_read_b128 a[164:167], %[a0x] offset:4096
ds_read_b128 a[168:171], %[a0x] offset:6144
ds_read_b64_tr_b16 a[172:173], %[b0] offset:12288
ds_read_b64_tr_b16 a[174:175], %[b0] offset:12800
s_waitcnt lgkmcnt(12)
v_mfma_f32_16x16x32_bf16 %0, a[128:131], a[136:139], 0
v_mfma_f32_16x16x32_bf16 %1, a[132:135], a[136:139], 0
ds_read_b128 a[176:179], %[a0] offset:8192
ds_read_b128 a[180:183], %[a0] offset:10240
ds_read_b64_tr_b16 a[184:185], %[b0] offset:32768
ds_read_b64_tr_b16 a[186:187], %[b0] offset:33280
s_waitcnt lgkmcnt(12)
v_mfma_f32_16x16x32_bf16 %0, a[140:143], a[148:151], %0
v_mfma_f32_16x16x32_bf16 %1, a[144:147], a[148:151], %1
ds_read_b128 a[128:131], %[a0x] offset:8192
ds_read_b128 a[132:135], %[a0x] offset:10240
ds_read_b64_tr_b16 a[136:137], %[b0] offset:36864
ds_read_b64_tr_b16 a[138:139], %[b0] offset:37376
s_waitcnt lgkmcnt(12)
v_mfma_f32_16x16x32_bf16 %0, a[152:155], a[160:163], %0
v_mfma_f32_16x16x32_bf16 %1, a[156:159], a[160:163], %1
ds_read_b128 a[140:143], %[a0] offset:12288
ds_read_b128 a[144:147], %[a0] offset:14336
ds_read_b64_tr_b16 a[148:149], %[b0] offset:40960
ds_read_b64_tr_b16 a[150:151], %[b0] offset:41472
s_waitcnt lgkmcnt(12)
v_mfma_f32_16x16x32_bf16 %0, a[164:167], a[172:175], %0
v_mfma_f32_16x16x32_bf16 %1, a[168:171], a[172:175], %1
ds_read_b128 a[152:155], %[a0x] offset:12288
ds_read_b128 a[156:159], %[a0x] offset:14336
ds_read_b64_tr_b16 a[160:161], %[b0] offset:45056
ds_read_b64_tr_b16 a[162:163], %[b0] offset:45568
s_waitcnt lgkmcnt(12)
v_mfma_f32_16x16x32_bf16 %0, a[176:179], a[184:187], %0
v_mfma_f32_16x16x32_bf16 %1, a[180:183], a[184:187], %1
ds_read_b128 a[164:167], %[a0] offset:16384
ds_read_b128 a[168:171], %[a0] offset:18432
ds_read_b64_tr_b16 a[172:173], %[b1] offset:0
ds_read_b64_tr_b16 a[174:175], %[b1] offset:512
s_waitcnt lgkmcnt(12)
v_mfma_f32_16x16x32_bf16 %0, a[128:131], a[136:139], %0
v_mfma_f32_16x16x32_bf16 %1, a[132:135], a[136:139], %1
ds_read_b128 a[176:179], %[a0x] offset:16384
ds_read_b128 a[180:183], %[a0x] offset:18432
ds_read_b64_tr_b16 a[184:185], %[b1] offset:4096
ds_read_b64_tr_b16 a[186:187], %[b1] offset:4608
s_waitcnt lgkmcnt(12)
v_mfma_f32_16x16x32_bf16 %0, a[140:143], a[148:151], %0
v_mfma_f32_16x16x32_bf16 %1, a[144:147], a[148:151], %1
ds_read_b128 a[128:131], %[a0] offset:20480
ds_read_b128 a[132:135], %[a0] offset:22528
ds_read_b64_tr_b16 a[136:137], %[b1] offset:8192
ds_read_b64_tr_b16 a[138:139], %[b1] offset:8704
s_waitcnt lgkmcnt(12)
v_mfma_f32_16x16x32_bf16 %0, a[152:155], a[160:163], %0
v_mfma_f32_16x16x32_bf16 %1, a[156:159], a[160:163], %1
ds_read_b128 a[140:143], %[a0x] offset:20480
ds_read_b128 a[144:147], %[a0x] offset:22528
ds_read_b64_tr_b16 a[148:149], %[b1] offset:12288
ds_read_b64_tr_b16 a[150:151], %[b1] offset:12800
s_waitcnt lgkmcnt(12)
v_mfma_f32_16x16x32_bf16 %0, a[164:167], a[172:175], %0
v_mfma_f32_16x16x32_bf16 %1, a[168:171], a[172:175], %1
ds_read_b128 a[152:155], %[a0] offset:24576
ds_read_b128 a[156:159], %[a0] offset:26624
ds_read_b64_tr_b16 a[160:161], %[b1] offset:32768
ds_read_b64_tr_b16 a[162:163], %[b1] offset:33280
s_waitcnt lgkmcnt(12)
v_mfma_f32_16x16x32_bf16 %0, a[176:179], a[184:187], %0
v_mfma_f32_16x16x32_bf16 %1, a[180:183], a[184:187], %1
ds_read_b128 a[164:167], %[a0x] offset:24576
ds_read_b128 a[168:171], %[a0x] offset:26624
ds_read_b64_tr_b16 a[172:173], %[b1] offset:36864
ds_read_b64_tr_b16 a[174:175], %[b1] offset:37376
s_waitcnt lgkmcnt(12)
v_mfma_f32_16x16x32_bf16 %0, a[128:131], a[136:139], %0
v_mfma_f32_16x16x32_bf16 %1, a[132:135], a[136:139], %1
ds_read_b128 a[176:179], %[a0] offset:28672
ds_read_b128 a[180:183], %[a0] offset:30720
ds_read_b64_tr_b16 a[184:185], %[b1] offset:40960
ds_read_b64_tr_b16 a[186:187], %[b1] offset:41472
s_waitcnt lgkmcnt(12)
v_mfma_f32_16x16x32_bf16 %0, a[140:143], a[148:151], %0
v_mfma_f32_16x16x32_bf16 %1, a[144:147], a[148:151], %1
ds_read_b128 a[128:131], %[a0x] offset:28672
ds_read_b128 a[132:135], %[a0x] offset:30720
ds_read_b64_tr_b16 a[136:137], %[b1] offset:45056
ds_read_b64_tr_b16 a[138:139], %[b1] offset:45568
s_waitcnt lgkmcnt(12)
v_mfma_f32_16x16x32_bf16 %0, a[152:155], a[160:163], %0
v_mfma_f32_16x16x32_bf16 %1, a[156:159], a[160:163], %1
s_waitcnt lgkmcnt(8)
v_mfma_f32_16x16x32_bf16 %0, a[164:167], a[172:175], %0
v_mfma_f32_16x16x32_bf16 %1, a[168:171], a[172:175], %1
s_waitcnt lgkmcnt(4)
v_mfma_f32_16x16x32_bf16 %0, a[176:179], a[184:187], %0
v_mfma_f32_16x16x32_bf16 %1, a[180:183], a[184:187], %1
s_waitcnt lgkmcnt(0)
v_mfma_f32_16x16x32_bf16 %0, a[128:131], a[136:139], %0
v_mfma_f32_16x16x32_bf16 %1, a[132:135], a[136:139], %1
s_nop 7
)"
      : "=&v"(r0), "=&v"(r1) : [a0] "v"(131072 + oa), [a0x] "v"(131072 + (oa ^ 64)), [b0] "v"(ot), [b1] "v"(ot + 65536)
      : "a128","a129","a130","a131","a132","a133","a134","a135","a136","a137","a138","a139","a140","a141","a142","a143","a144","a145","a146","a147","a148","a149","a150","a151","a152","a153","a154","a155","a156","a157","a158","a159","a160","a161","a162","a163","a164","a165","a166","a167","a168","a169","a170","a171","a172","a173","a174","a175","a176","a177","a178","a179","a180","a181","a182","a183","a184","a185","a186","a187","scc","memory");
    __builtin_amdgcn_raw_buffer_store_b128(u32x4{pk(r0[0], r0[1]), pk(r0[2], r0[3]), pk(r1[0], r1[1]), pk(r1[2], r1[3])}, rw, lane * 16 + (u32)WS_W4 + ng * W4_S + (ks * 4 + w) * 1024, 0, SC1);
  } else {
    char* wb = lds + 135168 + w * 4224;
    if (w != 1) {
      char* st = wb + (lc >> 1) * 1056 + lr * 32 + (lc & 1) * 16;
#pragma unroll
      for (int r = 0; r < 4; ++r) *(u32x4*)(st + r * 256) = t2[r];
      const u32 ra = 135168 + w * 4224 + (4 * g + (i >> 2)) * 32 + (w ? ((i & 3) >> 1) * 1056 + (i & 1) * 16 : (i & 3) * 8);
      u32x2 t[4][2];
#pragma unroll
      for (int c = 0; c < 4; ++c) { const u32 a = ra + (w ? (c >> 1) * 2112 + (c & 1) * 8 : c * 1056); t[c][0] = tr_read(a); t[c][1] = tr_read(a + 512); }
      LGKM0("+v"(t[0][0]), "+v"(t[0][1]), "+v"(t[1][0]), "+v"(t[1][1]), "+v"(t[2][0]), "+v"(t[2][1]), "+v"(t[3][0]), "+v"(t[3][1]));
      const u32 d0 = w ? (u32)WS_W5 + 2 * ng * W5_S + (ks + 16 * (w & 1)) * 2048 : (u32)WS_W4 + (8 + ng) * W4_S + ks * 4096, d2 = w ? W5_S : 2048;
#pragma unroll
      for (int c = 0; c < 4; ++c) __builtin_amdgcn_raw_buffer_store_b128(u32x4{t[c][0][0], t[c][0][1], t[c][1][0], t[c][1][1]}, rw, lane * 16 + d0 + (c >> 1) * d2 + (c & 1) * 1024, 0, SC1);
    } else if (ks < 12) {
      *(u32x4*)(wb + lane * 16) = lane >= 62 ? u32x4{0u, 0u, 0u, 0u} : t2[0];
      const u16* tp = (const u16*)wb + (lane >> 2);
#pragma unroll
      for (int pc = 0; pc < 5; ++pc) {
        u32x4 t;
#pragma unroll
        for (int e = 0; e < 4; ++e) {
          const u32 i0 = min((u32)(8 * pc + 2 * e - 1) - (lane & 3), 31u), i1 = min((u32)(8 * pc + 2 * e) - (lane & 3), 31u);
          t[e] = (u32)tp[i0 * 16] | ((u32)tp[i1 * 16] << 16);
        }
        __builtin_amdgcn_raw_buffer_store_b128(t, rw, lane * 16 + (u32)WS_TZ + (grp * 5 + pc) * 1024, 0, SC1);
      }
    }
  }
  SB();
  STAMP(55);
  asm volatile(
R"(s_waitcnt lgkmcnt(0)
s_nop 4
v_readlane_b32 s80,v15,0
v_readlane_b32 s82,v15,1
v_readlane_b32 s83,v15,2
s_add_u32 s80,s80,0x2000
v_mov_b32 v16,v2
v_add_u32 v17,0x10000,v2
s_waitcnt vmcnt(17)
s_barrier
ds_read_b128 a[0:3],v16 offset:0
ds_read_b128 a[4:7],v16 offset:512
ds_read_b128 a[8:11],v16 offset:32768
ds_read_b128 a[12:15],v16 offset:33280
s_waitcnt lgkmcnt(3)
s_waitcnt vmcnt(16)
v_mfma_f32_16x16x32_bf16 v[192:195],a[32:35],a[0:3],0
s_waitcnt vmcnt(15)
ds_read_b128 a[16:19],v17 offset:0
v_mfma_f32_16x16x32_bf16 v[196:199],a[36:39],a[0:3],0
s_waitcnt vmcnt(14)
buffer_load_dwordx4 a[68:71],%[vL16],%[srdW],s80 offen offset:1024 sc1
v_mfma_f32_16x16x32_bf16 v[96:99],a[0:3],a[40:43],0
s_waitcnt lgkmcnt(3)
v_mfma_f32_16x16x32_bf16 v[200:203],a[32:35],a[4:7],0
ds_read_b128 a[20:23],v17 offset:512
v_mfma_f32_16x16x32_bf16 v[204:207],a[36:39],a[4:7],0
v_mfma_f32_16x16x32_bf16 v[100:103],a[4:7],a[40:43],0
s_waitcnt lgkmcnt(3)
buffer_load_dwordx4 a[72:75],%[vL16],%[srdW],s80 offen offset:2048 sc1
v_mfma_f32_16x16x32_bf16 v[208:211],a[32:35],a[8:11],0
ds_read_b128 a[24:27],v17 offset:32768
v_mfma_f32_16x16x32_bf16 v[212:215],a[36:39],a[8:11],0
v_mfma_f32_16x16x32_bf16 v[104:107],a[8:11],a[40:43],0
s_waitcnt lgkmcnt(3)
v_mfma_f32_16x16x32_bf16 v[216:219],a[32:35],a[12:15],0
ds_read_b128 a[28:31],v17 offset:33280
v_mfma_f32_16x16x32_bf16 v[220:223],a[36:39],a[12:15],0
buffer_load_dwordx4 a[76:79],%[vL16],%[srdW],s80 offen offset:3072 sc1
v_mfma_f32_16x16x32_bf16 v[108:111],a[12:15],a[40:43],0
s_waitcnt lgkmcnt(3)
s_add_u32 m0,s82,12288
s_add_u32 s81,s83,12288
v_mfma_f32_16x16x32_bf16 v[224:227],a[32:35],a[16:19],0
ds_read_b128 a[0:3],v16 offset:2048
v_mfma_f32_16x16x32_bf16 v[228:231],a[36:39],a[16:19],0
v_mfma_f32_16x16x32_bf16 v[112:115],a[16:19],a[40:43],0
s_waitcnt lgkmcnt(3)
buffer_load_dwordx4 %[vL16],%[srdW],s81 offen offset:0 sc1 lds
v_mfma_f32_16x16x32_bf16 v[232:235],a[32:35],a[20:23],0
ds_read_b128 a[4:7],v16 offset:2560
v_mfma_f32_16x16x32_bf16 v[236:239],a[36:39],a[20:23],0
v_mfma_f32_16x16x32_bf16 v[116:119],a[20:23],a[40:43],0
s_waitcnt lgkmcnt(3)
v_mfma_f32_16x16x32_bf16 v[240:243],a[32:35],a[24:27],0
ds_read_b128 a[8:11],v16 offset:34816
v_mfma_f32_16x16x32_bf16 v[244:247],a[36:39],a[24:27],0
buffer_load_dwordx4 %[vL16],%[srdW],s81 offen offset:1024 sc1 lds
v_mfma_f32_16x16x32_bf16 v[120:123],a[24:27],a[40:43],0
s_waitcnt lgkmcnt(3)
v_mfma_f32_16x16x32_bf16 v[248:251],a[32:35],a[28:31],0
ds_read_b128 a[12:15],v16 offset:35328
v_mfma_f32_16x16x32_bf16 v[252:255],a[36:39],a[28:31],0
v_mfma_f32_16x16x32_bf16 v[124:127],a[28:31],a[40:43],0
s_waitcnt lgkmcnt(3)
s_waitcnt vmcnt(18)
v_mfma_f32_16x16x32_bf16 v[192:195],a[44:47],a[0:3],v[192:195]
s_waitcnt vmcnt(17)
ds_read_b128 a[16:19],v17 offset:2048
s_add_u32 s80,s80,0x1000
v_mfma_f32_16x16x32_bf16 v[196:199],a[48:51],a[0:3],v[196:199]
s_waitcnt vmcnt(16)
buffer_load_dwordx4 a[80:83],%[vL16],%[srdW],s80 offen offset:0 sc1
v_mfma_f32_16x16x32_bf16 v[96:99],a[0:3],a[52:55],v[96:99]
s_waitcnt lgkmcnt(3)
v_mfma_f32_16x16x32_bf16 v[200:203],a[44:47],a[4:7],v[200:203]
ds_read_b128 a[20:23],v17 offset:2560
v_mfma_f32_16x16x32_bf16 v[204:207],a[48:51],a[4:7],v[204:207]
v_mfma_f32_16x16x32_bf16 v[100:103],a[4:7],a[52:55],v[100:103]
s_waitcnt lgkmcnt(3)
buffer_load_dwordx4 a[84:87],%[vL16],%[srdW],s80 offen offset:1024 sc1
v_mfma_f32_16x16x32_bf16 v[208:211],a[44:47],a[8:11],v[208:211]
ds_read_b128 a[24:27],v17 offset:34816
v_mfma_f32_16x16x32_bf16 v[212:215],a[48:51],a[8:11],v[212:215]
v_mfma_f32_16x16x32_bf16 v[104:107],a[8:11],a[52:55],v[104:107]
s_waitcnt lgkmcnt(3)
v_mfma_f32_16x16x32_bf16 v[216:219],a[44:47],a[12:15],v[216:219]
ds_read_b128 a[28:31],v17 offset:35328
v_mfma_f32_16x16x32_bf16 v[220:223],a[48:51],a[12:15],v[220:223]
buffer_load_dwordx4 a[88:91],%[vL16],%[srdW],s80 offen offset:2048 sc1
v_mfma_f32_16x16x32_bf16 v[108:111],a[12:15],a[52:55],v[108:111]
s_waitcnt lgkmcnt(3)
v_mfma_f32_16x16x32_bf16 v[224:227],a[44:47],a[16:19],v[224:227]
s_waitcnt vmcnt(15)
s_barrier
ds_read_b128 a[0:3],v16 offset:4096
v_mfma_f32_16x16x32_bf16 v[228:231],a[48:51],a[16:19],v[228:231]
v_mfma_f32_16x16x32_bf16 v[112:115],a[16:19],a[52:55],v[112:115]
s_waitcnt lgkmcnt(3)
buffer_load_dwordx4 %[vL16],%[srdW],s81 offen offset:2048 sc1 lds
v_mfma_f32_16x16x32_bf16 v[232:235],a[44:47],a[20:23],v[232:235]
ds_read_b128 a[4:7],v16 offset:4608
v_mfma_f32_16x16x32_bf16 v[236:239],a[48:51],a[20:23],v[236:239]
v_mfma_f32_16x16x32_bf16 v[116:119],a[20:23],a[52:55],v[116:119]
s_waitcnt lgkmcnt(3)
v_mfma_f32_16x16x32_bf16 v[240:243],a[44:47],a[24:27],v[240:243]
ds_read_b128 a[8:11],v16 offset:36864
v_mfma_f32_16x16x32_bf16 v[244:247],a[48:51],a[24:27],v[244:247]
buffer_load_dwordx4 %[vL16],%[srdW],s81 offen offset:3072 sc1 lds
v_mfma_f32_16x16x32_bf16 v[120:123],a[24:27],a[52:55],v[120:123]
s_waitcnt lgkmcnt(3)
v_mfma_f32_16x16x32_bf16 v[248:251],a[44:47],a[28:31],v[248:251]
ds_read_b128 a[12:15],v16 offset:37376
v_mfma_f32_16x16x32_bf16 v[252:255],a[48:51],a[28:31],v[252:255]
v_mfma_f32_16x16x32_bf16 v[124:127],a[28:31],a[52:55],v[124:127]
s_waitcnt lgkmcnt(3)
s_waitcnt vmcnt(16)
v_mfma_f32_16x16x32_bf16 v[192:195],a[56:59],a[0:3],v[192:195]
s_waitcnt vmcnt(15)
ds_read_b128 a[16:19],v17 offset:4096
v_mfma_f32_16x16x32_bf16 v[196:199],a[60:63],a[0:3],v[196:199]
s_waitcnt vmcnt(14)
buffer_load_dwordx4 a[92:95],%[vL16],%[srdW],s80 offen offset:3072 sc1
v_mfma_f32_16x16x32_bf16 v[96:99],a[0:3],a[64:67],v[96:99]
s_waitcnt lgkmcnt(3)
s_add_u32 s80,s80,0x1000
v_mfma_f32_16x16x32_bf16 v[200:203],a[56:59],a[4:7],v[200:203]
ds_read_b128 a[20:23],v17 offset:4608
v_mfma_f32_16x16x32_bf16 v[204:207],a[60:63],a[4:7],v[204:207]
v_mfma_f32_16x16x32_bf16 v[100:103],a[4:7],a[64:67],v[100:103]
s_waitcnt lgkmcnt(3)
buffer_load_dwordx4 a[96:99],%[vL16],%[srdW],s80 offen offset:0 sc1
v_mfma_f32_16x16x32_bf16 v[208:211],a[56:59],a[8:11],v[208:211]
ds_read_b128 a[24:27],v17 offset:36864
v_mfma_f32_16x16x32_bf16 v[212:215],a[60:63],a[8:11],v[212:215]
v_mfma_f32_16x16x32_bf16 v[104:107],a[8:11],a[64:67],v[104:107]
s_waitcnt lgkmcnt(3)
v_mfma_f32_16x16x32_bf16 v[216:219],a[56:59],a[12:15],v[216:219]
ds_read_b128 a[28:31],v17 offset:37376
v_mfma_f32_16x16x32_bf16 v[220:223],a[60:63],a[12:15],v[220:223]
buffer_load_dwordx4 a[100:103],%[vL16],%[srdW],s80 offen offset:1024 sc1
v_mfma_f32_16x16x32_bf16 v[108:111],a[12:15],a[64:67],v[108:111]
s_waitcnt lgkmcnt(3)
s_add_u32 m0,s82,16384
s_add_u32 s81,s83,16384
v_mfma_f32_16x16x32_bf16 v[224:227],a[56:59],a[16:19],v[224:227]
ds_read_b128 a[0:3],v16 offset:6144
v_mfma_f32_16x16x32_bf16 v[228:231],a[60:63],a[16:19],v[228:231]
v_mfma_f32_16x16x32_bf16 v[112:115],a[16:19],a[64:67],v[112:115]
s_waitcnt lgkmcnt(3)
buffer_load_dwordx4 %[vL16],%[srdW],s81 offen offset:0 sc1 lds
v_mfma_f32_16x16x32_bf16 v[232:235],a[56:59],a[20:23],v[232:235]
ds_read_b128 a[4:7],v16 offset:6656
v_mfma_f32_16x16x32_bf16 v[236:239],a[60:63],a[20:23],v[236:239]
v_mfma_f32_16x16x32_bf16 v[116:119],a[20:23],a[64:67],v[116:119]
s_waitcnt lgkmcnt(3)
v_mfma_f32_16x16x32_bf16 v[240:243],a[56:59],a[24:27],v[240:243]
ds_read_b128 a[8:11],v16 offset:38912
v_mfma_f32_16x16x32_bf16 v[244:247],a[60:63],a[24:27],v[244:247]
buffer_load_dwordx4 %[vL16],%[srdW],s81 offen offset:1024 sc1 lds
v_mfma_f32_16x16x32_bf16 v[120:123],a[24:27],a[64:67],v[120:123]
s_waitcnt lgkmcnt(3)
v_mfma_f32_16x16x32_bf16 v[248:251],a[56:59],a[28:31],v[248:251]
ds_read_b128 a[12:15],v16 offset:39424
v_mfma_f32_16x16x32_bf16 v[252:255],a[60:63],a[28:31],v[252:255]
v_mfma_f32_16x16x32_bf16 v[124:127],a[28:31],a[64:67],v[124:127]
s_waitcnt lgkmcnt(3)
s_waitcnt vmcnt(14)
v_mfma_f32_16x16x32_bf16 v[192:195],a[68:71],a[0:3],v[192:195]
s_waitcnt vmcnt(13)
ds_read_b128 a[16:19],v17 offset:6144
v_mfma_f32_16x16x32_bf16 v[196:199],a[72:75],a[0:3],v[196:199]
s_waitcnt vmcnt(12)
buffer_load_dwordx4 a[176:179],%[vL16],%[srdW],s80 offen offset:2048 sc1
v_mfma_f32_16x16x32_bf16 v[96:99],a[0:3],a[76:79],v[96:99]
s_waitcnt lgkmcnt(3)
v_mfma_f32_16x16x32_bf16 v[200:203],a[68:71],a[4:7],v[200:203]
ds_read_b128 a[20:23],v17 offset:6656
v_mfma_f32_16x16x32_bf16 v[204:207],a[72:75],a[4:7],v[204:207]
v_mfma_f32_16x16x32_bf16 v[100:103],a[4:7],a[76:79],v[100:103]
s_waitcnt lgkmcnt(3)
buffer_load_dwordx4 a[180:183],%[vL16],%[srdW],s80 offen offset:3072 sc1
v_mfma_f32_16x16x32_bf16 v[208:211],a[68:71],a[8:11],v[208:211]
s_add_u32 s80,s80,0x1000
ds_read_b128 a[24:27],v17 offset:38912
v_mfma_f32_16x16x32_bf16 v[212:215],a[72:75],a[8:11],v[212:215]
v_mfma_f32_16x16x32_bf16 v[104:107],a[8:11],a[76:79],v[104:107]
s_waitcnt lgkmcnt(3)
v_mfma_f32_16x16x32_bf16 v[216:219],a[68:71],a[12:15],v[216:219]
ds_read_b128 a[28:31],v17 offset:39424
v_mfma_f32_16x16x32_bf16 v[220:223],a[72:75],a[12:15],v[220:223]
buffer_load_dwordx4 a[184:187],%[vL16],%[srdW],s80 offen offset:0 sc1
v_mfma_f32_16x16x32_bf16 v[108:111],a[12:15],a[76:79],v[108:111]
s_waitcnt lgkmcnt(3)
v_mfma_f32_16x16x32_bf16 v[224:227],a[68:71],a[16:19],v[224:227]
s_barrier
ds_read_b128 a[0:3],v16 offset:8192
v_mfma_f32_16x16x32_bf16 v[228:231],a[72:75],a[16:19],v[228:231]
v_mfma_f32_16x16x32_bf16 v[112:115],a[16:19],a[76:79],v[112:115]
s_waitcnt lgkmcnt(3)
buffer_load_dwordx4 %[vL16],%[srdW],s81 offen offset:2048 sc1 lds
v_mfma_f32_16x16x32_bf16 v[232:235],a[68:71],a[20:23],v[232:235]
ds_read_b128 a[4:7],v16 offset:8704
v_mfma_f32_16x16x32_bf16 v[236:239],a[72:75],a[20:23],v[236:239]
v_mfma_f32_16x16x32_bf16 v[116:119],a[20:23],a[76:79],v[116:119]
s_waitcnt lgkmcnt(3)
v_mfma_f32_16x16x32_bf16 v[240:243],a[68:71],a[24:27],v[240:243]
ds_read_b128 a[8:11],v16 offset:40960
v_mfma_f32_16x16x32_bf16 v[244:247],a[72:75],a[24:27],v[244:247]
buffer_load_dwordx4 %[vL16],%[srdW],s81 offen offset:3072 sc1 lds
v_mfma_f32_16x16x32_bf16 v[120:123],a[24:27],a[76:79],v[120:123]
s_waitcnt lgkmcnt(3)
v_mfma_f32_16x16x32_bf16 v[248:251],a[68:71],a[28:31],v[248:251]
ds_read_b128 a[12:15],v16 offset:41472
v_mfma_f32_16x16x32_bf16 v[252:255],a[72:75],a[28:31],v[252:255]
v_mfma_f32_16x16x32_bf16 v[124:127],a[28:31],a[76:79],v[124:127]
s_waitcnt lgkmcnt(3)
s_waitcnt vmcnt(14)
v_mfma_f32_16x16x32_bf16 v[192:195],a[80:83],a[0:3],v[192:195]
s_waitcnt vmcnt(13)
ds_read_b128 a[16:19],v17 offset:8192
v_readlane_b32 s84,v15,3
v_mfma_f32_16x16x32_bf16 v[196:199],a[84:87],a[0:3],v[196:199]
s_waitcnt vmcnt(12)
buffer_load_dwordx4 a[188:191],%[vL16],%[srdW],s80 offen offset:1024 sc1
v_mfma_f32_16x16x32_bf16 v[96:99],a[0:3],a[88:91],v[96:99]
s_waitcnt lgkmcnt(3)
v_readlane_b32 s85,v15,4
v_readlane_b32 s86,v15,5
v_mfma_f32_16x16x32_bf16 v[200:203],a[80:83],a[4:7],v[200:203]
ds_read_b128 a[20:23],v17 offset:8704
v_readlane_b32 s87,v15,6
v_mfma_f32_16x16x32_bf16 v[204:207],a[84:87],a[4:7],v[204:207]
v_readlane_b32 s88,v15,7
v_readlane_b32 s89,v15,8
v_mfma_f32_16x16x32_bf16 v[100:103],a[4:7],a[88:91],v[100:103]
s_waitcnt lgkmcnt(3)
buffer_load_dwordx4 a[192:195],%[vL16],%[srdW],s80 offen offset:2048 sc1
v_mfma_f32_16x16x32_bf16 v[208:211],a[80:83],a[8:11],v[208:211]
ds_read_b128 a[24:27],v17 offset:40960
v_readlane_b32 s90,v15,9
v_mfma_f32_16x16x32_bf16 v[212:215],a[84:87],a[8:11],v[212:215]
v_readlane_b32 s91,%[pv],0
v_readlane_b32 s92,v15,10
v_mfma_f32_16x16x32_bf16 v[104:107],a[8:11],a[88:91],v[104:107]
s_waitcnt lgkmcnt(3)
v_readlane_b32 s78,%[pv],1
v_readlane_b32 s79,v15,11
v_mfma_f32_16x16x32_bf16 v[216:219],a[80:83],a[12:15],v[216:219]
ds_read_b128 a[28:31],v17 offset:41472
v_readlane_b32 s76,v15,12
v_mfma_f32_16x16x32_bf16 v[220:223],a[84:87],a[12:15],v[220:223]
buffer_load_dwordx4 a[196:199],%[vL16],%[srdW],s80 offen offset:3072 sc1
v_mfma_f32_16x16x32_bf16 v[108:111],a[12:15],a[88:91],v[108:111]
s_waitcnt lgkmcnt(3)
s_add_u32 m0,s82,20480
s_add_u32 s81,s83,20480
v_mfma_f32_16x16x32_bf16 v[224:227],a[80:83],a[16:19],v[224:227]
ds_read_b128 a[0:3],v16 offset:10240
v_readlane_b32 s77,v15,13
v_mfma_f32_16x16x32_bf16 v[228:231],a[84:87],a[16:19],v[228:231]
v_readlane_b32 s93,v15,14
v_readlane_b32 s94,v15,15
v_mfma_f32_16x16x32_bf16 v[112:115],a[16:19],a[88:91],v[112:115]
s_waitcnt lgkmcnt(3)
buffer_load_dwordx4 %[vL16],%[srdW],s81 offen offset:0 sc1 lds
v_mfma_f32_16x16x32_bf16 v[232:235],a[80:83],a[20:23],v[232:235]
ds_read_b128 a[4:7],v16 offset:10752
v_readlane_b32 s40,%[pv],2
v_mfma_f32_16x16x32_bf16 v[236:239],a[84:87],a[20:23],v[236:239]
v_readlane_b32 s41,%[pv],3
v_readlane_b32 s42,%[pv],4
v_mfma_f32_16x16x32_bf16 v[116:119],a[20:23],a[88:91],v[116:119]
s_waitcnt lgkmcnt(3)
v_readlane_b32 s43,%[pv],5
v_readlane_b32 s44,%[pv],6
v_mfma_f32_16x16x32_bf16 v[240:243],a[80:83],a[24:27],v[240:243]
ds_read_b128 a[8:11],v16 offset:43008
v_readlane_b32 s45,%[pv],7
v_mfma_f32_16x16x32_bf16 v[244:247],a[84:87],a[24:27],v[244:247]
buffer_load_dwordx4 %[vL16],%[srdW],s81 offen offset:1024 sc1 lds
v_mfma_f32_16x16x32_bf16 v[120:123],a[24:27],a[88:91],v[120:123]
s_waitcnt lgkmcnt(3)
v_readlane_b32 s46,%[pv],8
v_readlane_b32 s47,%[pv],9
v_mfma_f32_16x16x32_bf16 v[248:251],a[80:83],a[28:31],v[248:251]
ds_read_b128 a[12:15],v16 offset:43520
v_readlane_b32 s48,%[pv],10
v_mfma_f32_16x16x32_bf16 v[252:255],a[84:87],a[28:31],v[252:255]
v_readlane_b32 s49,%[pv],11
v_readlane_b32 s50,%[pv],12
v_mfma_f32_16x16x32_bf16 v[124:127],a[28:31],a[88:91],v[124:127]
s_waitcnt lgkmcnt(3)
s_waitcnt vmcnt(14)
v_readlane_b32 s51,%[pv],13
v_mfma_f32_16x16x32_bf16 v[192:195],a[92:95],a[0:3],v[192:195]
s_waitcnt vmcnt(13)
ds_read_b128 a[16:19],v17 offset:10240
s_add_u32 s80,s80,0x1000
v_mfma_f32_16x16x32_bf16 v[196:199],a[96:99],a[0:3],v[196:199]
s_waitcnt vmcnt(12)
buffer_load_dwordx4 a[200:203],%[vL16],%[srdW],s80 offen offset:0 sc1
v_mfma_f32_16x16x32_bf16 v[96:99],a[0:3],a[100:103],v[96:99]
s_waitcnt lgkmcnt(3)
v_readlane_b32 s52,%[pv],14
v_readlane_b32 s53,%[pv],15
v_mfma_f32_16x16x32_bf16 v[200:203],a[92:95],a[4:7],v[200:203]
ds_read_b128 a[20:23],v17 offset:10752
v_readlane_b32 s54,%[pv],16
v_mfma_f32_16x16x32_bf16 v[204:207],a[96:99],a[4:7],v[204:207]
v_readlane_b32 s55,%[pv],1
v_readlane_b32 s56,%[pv],17
v_mfma_f32_16x16x32_bf16 v[100:103],a[4:7],a[100:103],v[100:103]
s_waitcnt lgkmcnt(3)
buffer_load_dwordx4 a[204:207],%[vL16],%[srdW],s80 offen offset:1024 sc1
v_mfma_f32_16x16x32_bf16 v[208:211],a[92:95],a[8:11],v[208:211]
ds_read_b128 a[24:27],v17 offset:43008
v_lshrrev_b32 v40,8,%[vL16]
v_mfma_f32_16x16x32_bf16 v[212:215],a[96:99],a[8:11],v[212:215]
v_bfe_u32 v41,%[vL16],4,4
v_add_u32 v18,s84,v2
v_mfma_f32_16x16x32_bf16 v[104:107],a[8:11],a[100:103],v[104:107]
s_waitcnt lgkmcnt(3)
v_xor_b32 v42,s86,v40
v_lshlrev_b32 v42,4,v42
v_mfma_f32_16x16x32_bf16 v[216:219],a[92:95],a[12:15],v[216:219]
ds_read_b128 a[28:31],v17 offset:43520
v_lshl_add_u32 v42,v41,6,v42
v_mfma_f32_16x16x32_bf16 v[220:223],a[96:99],a[12:15],v[220:223]
buffer_load_dwordx4 a[208:211],%[vL16],%[srdW],s80 offen offset:2048 sc1
v_mfma_f32_16x16x32_bf16 v[108:111],a[12:15],a[100:103],v[108:111]
s_waitcnt lgkmcnt(3)
v_add_u32 v22,s85,v42
v_xor_b32 v23,16,v22
v_mfma_f32_16x16x32_bf16 v[224:227],a[92:95],a[16:19],v[224:227]
s_barrier
ds_read_b128 a[0:3],v16 offset:12288
v_lshlrev_b32 v19,10,v40
v_mfma_f32_16x16x32_bf16 v[228:231],a[96:99],a[16:19],v[228:231]
v_lshl_add_u32 v19,v41,3,v19
v_add_u32 v19,s87,v19
v_mfma_f32_16x16x32_bf16 v[112:115],a[16:19],a[100:103],v[112:115]
s_waitcnt lgkmcnt(3)
buffer_load_dwordx4 %[vL16],%[srdW],s81 offen offset:2048 sc1 lds
v_mfma_f32_16x16x32_bf16 v[232:235],a[92:95],a[20:23],v[232:235]
ds_read_b128 a[4:7],v16 offset:12800
v_mov_b32 v20,0xbdd2d3e7
v_mfma_f32_16x16x32_bf16 v[236:239],a[96:99],a[20:23],v[236:239]
v_mov_b32 v21,0xc0135761
v_and_b32 v42,1,v41
v_mfma_f32_16x16x32_bf16 v[116:119],a[20:23],a[100:103],v[116:119]
s_waitcnt lgkmcnt(3)
v_lshlrev_b32 v42,4,v42
v_mov_b32 v43,0x3f80
v_mfma_f32_16x16x32_bf16 v[240:243],a[92:95],a[24:27],v[240:243]
ds_read_b128 a[8:11],v16 offset:45056
v_lshlrev_b32 v42,v42,v43
v_mfma_f32_16x16x32_bf16 v[244:247],a[96:99],a[24:27],v[244:247]
buffer_load_dwordx4 %[vL16],%[srdW],s81 offen offset:3072 sc1 lds
v_mfma_f32_16x16x32_bf16 v[120:123],a[24:27],a[100:103],v[120:123]
s_waitcnt lgkmcnt(3)
v_lshrrev_b32 v43,3,v41
v_sub_u32 v43,v40,v43
v_mfma_f32_16x16x32_bf16 v[248:251],a[92:95],a[28:31],v[248:251]
ds_read_b128 a[12:15],v16 offset:45568
v_bfe_u32 v44,v41,1,2
v_mfma_f32_16x16x32_bf16 v[252:255],a[96:99],a[28:31],v[252:255]
v_mul_i32_i24 v43,12,v43
v_add_u32 v43,v43,v44
v_mfma_f32_16x16x32_bf16 v[124:127],a[28:31],a[100:103],v[124:127]
s_waitcnt lgkmcnt(3)
s_waitcnt vmcnt(14)
v_cmp_eq_u32 vcc,0,v43
v_mfma_f32_16x16x32_bf16 v[192:195],a[176:179],a[0:3],v[192:195]
s_waitcnt vmcnt(13)
ds_read_b128 a[16:19],v17 offset:12288
s_nop 1
v_mfma_f32_16x16x32_bf16 v[196:199],a[180:183],a[0:3],v[196:199]
s_waitcnt vmcnt(12)
buffer_load_dwordx4 a[212:215],%[vL16],%[srdW],s80 offen offset:3072 sc1
v_mfma_f32_16x16x32_bf16 v[96:99],a[0:3],a[184:187],v[96:99]
s_waitcnt lgkmcnt(3)
s_add_u32 s80,s80,0x1000
v_cndmask_b32 v32,0,v42,vcc
v_mfma_f32_16x16x32_bf16 v[200:203],a[176:179],a[4:7],v[200:203]
ds_read_b128 a[20:23],v17 offset:12800
v_cmp_eq_u32 vcc,1,v43
v_mfma_f32_16x16x32_bf16 v[204:207],a[180:183],a[4:7],v[204:207]
s_nop 1
v_cndmask_b32 v33,0,v42,vcc
v_cmp_eq_u32 vcc,2,v43
v_mfma_f32_16x16x32_bf16 v[100:103],a[4:7],a[184:187],v[100:103]
s_waitcnt lgkmcnt(3)
buffer_load_dwordx4 a[216:219],%[vL16],%[srdW],s80 offen offset:0 sc1
v_mfma_f32_16x16x32_bf16 v[208:211],a[176:179],a[8:11],v[208:211]
s_nop 1
ds_read_b128 a[24:27],v17 offset:45056
v_cndmask_b32 v34,0,v42,vcc
v_mfma_f32_16x16x32_bf16 v[212:215],a[180:183],a[8:11],v[212:215]
v_cmp_eq_u32 vcc,3,v43
)"
R"(s_nop 1
v_mfma_f32_16x16x32_bf16 v[104:107],a[8:11],a[184:187],v[104:107]
s_waitcnt lgkmcnt(3)
v_cndmask_b32 v35,0,v42,vcc
v_cmp_eq_u32 vcc,24,v43
v_mfma_f32_16x16x32_bf16 v[216:219],a[176:179],a[12:15],v[216:219]
s_nop 1
ds_read_b128 a[28:31],v17 offset:45568
v_cndmask_b32 v36,0,v42,vcc
v_mfma_f32_16x16x32_bf16 v[220:223],a[180:183],a[12:15],v[220:223]
buffer_load_dwordx4 a[220:223],%[vL16],%[srdW],s80 offen offset:1024 sc1
v_mfma_f32_16x16x32_bf16 v[108:111],a[12:15],a[184:187],v[108:111]
s_waitcnt lgkmcnt(3)
s_add_u32 m0,s82,24576
s_add_u32 s81,s83,24576
v_mfma_f32_16x16x32_bf16 v[224:227],a[176:179],a[16:19],v[224:227]
ds_read_b128 a[0:3],v16 offset:14336
v_cmp_eq_u32 vcc,25,v43
v_mfma_f32_16x16x32_bf16 v[228:231],a[180:183],a[16:19],v[228:231]
s_nop 1
v_cndmask_b32 v37,0,v42,vcc
v_cmp_eq_u32 vcc,26,v43
v_mfma_f32_16x16x32_bf16 v[112:115],a[16:19],a[184:187],v[112:115]
s_waitcnt lgkmcnt(3)
buffer_load_dwordx4 %[vL16],%[srdW],s81 offen offset:0 sc1 lds
v_mfma_f32_16x16x32_bf16 v[232:235],a[176:179],a[20:23],v[232:235]
s_nop 1
ds_read_b128 a[4:7],v16 offset:14848
v_cndmask_b32 v38,0,v42,vcc
v_mfma_f32_16x16x32_bf16 v[236:239],a[180:183],a[20:23],v[236:239]
v_cmp_eq_u32 vcc,27,v43
s_nop 1
v_mfma_f32_16x16x32_bf16 v[116:119],a[20:23],a[184:187],v[116:119]
s_waitcnt lgkmcnt(3)
v_cndmask_b32 v39,0,v42,vcc
v_mov_b32 v28,0
v_mfma_f32_16x16x32_bf16 v[240:243],a[176:179],a[24:27],v[240:243]
ds_read_b128 a[8:11],v16 offset:47104
v_mov_b32 v29,0
v_mfma_f32_16x16x32_bf16 v[244:247],a[180:183],a[24:27],v[244:247]
buffer_load_dwordx4 %[vL16],%[srdW],s81 offen offset:1024 sc1 lds
v_mfma_f32_16x16x32_bf16 v[120:123],a[24:27],a[184:187],v[120:123]
s_waitcnt lgkmcnt(3)
v_mov_b32 v30,0
v_mov_b32 v31,0
v_mfma_f32_16x16x32_bf16 v[248:251],a[176:179],a[28:31],v[248:251]
ds_read_b128 a[12:15],v16 offset:47616
v_and_b32 v14,0x30,%[vL16]
v_mfma_f32_16x16x32_bf16 v[252:255],a[180:183],a[28:31],v[252:255]
v_lshlrev_b32 v14,3,v14
v_add_u32 v14,s90,v14
v_mfma_f32_16x16x32_bf16 v[124:127],a[28:31],a[184:187],v[124:127]
s_waitcnt lgkmcnt(3)
s_waitcnt vmcnt(14)
v_lshlrev_b32 v48,3,%[vL16]
v_mfma_f32_16x16x32_bf16 v[192:195],a[188:191],a[0:3],v[192:195]
s_waitcnt vmcnt(13)
ds_read_b128 a[16:19],v17 offset:14336
v_add_u32 v48,s76,v48
v_mfma_f32_16x16x32_bf16 v[196:199],a[192:195],a[0:3],v[196:199]
s_waitcnt vmcnt(12)
buffer_load_dwordx4 a[224:227],%[vL16],%[srdW],s80 offen offset:2048 sc1
v_mfma_f32_16x16x32_bf16 v[96:99],a[0:3],a[196:199],v[96:99]
s_waitcnt lgkmcnt(3)
v_mov_b32 v52,s78
v_mov_b32 v53,s79
v_mfma_f32_16x16x32_bf16 v[200:203],a[188:191],a[4:7],v[200:203]
ds_read_b128 a[20:23],v17 offset:14848
v_mov_b32 v49,s77
v_mfma_f32_16x16x32_bf16 v[204:207],a[192:195],a[4:7],v[204:207]
s_mov_b32 s65,s40
s_bfe_u32 s58,s47,0x20010
s_lshl_b32 s58,s58,13
v_mfma_f32_16x16x32_bf16 v[100:103],a[4:7],a[196:199],v[100:103]
s_waitcnt lgkmcnt(3)
buffer_load_dwordx4 a[228:231],%[vL16],%[srdW],s80 offen offset:3072 sc1
v_mfma_f32_16x16x32_bf16 v[208:211],a[188:191],a[8:11],v[208:211]
s_add_u32 s80,s80,0x1000
ds_read_b128 a[24:27],v17 offset:47104
s_mov_b32 s68,0xff800000
v_mfma_f32_16x16x32_bf16 v[212:215],a[192:195],a[8:11],v[212:215]
s_mov_b32 s69,0xff800000
s_mov_b32 s70,s46
s_sub_u32 s71,36864,s46
v_mfma_f32_16x16x32_bf16 v[104:107],a[8:11],a[196:199],v[104:107]
s_waitcnt lgkmcnt(3)
s_mov_b32 s60,0
s_mov_b32 s61,16384
v_mfma_f32_16x16x32_bf16 v[216:219],a[188:191],a[12:15],v[216:219]
s_mov_b32 s62,32768
ds_read_b128 a[28:31],v17 offset:47616
s_mov_b32 s63,49152
v_mfma_f32_16x16x32_bf16 v[220:223],a[192:195],a[12:15],v[220:223]
s_mov_b32 s64,65536
buffer_load_dwordx4 a[232:235],%[vL16],%[srdW],s80 offen offset:0 sc1
v_mfma_f32_16x16x32_bf16 v[108:111],a[12:15],a[196:199],v[108:111]
s_waitcnt lgkmcnt(3)
v_mfma_f32_16x16x32_bf16 v[224:227],a[188:191],a[16:19],v[224:227]
s_barrier
ds_read_b128 a[0:3],v16 offset:16384
v_mfma_f32_16x16x32_bf16 v[228:231],a[192:195],a[16:19],v[228:231]
v_mfma_f32_16x16x32_bf16 v[112:115],a[16:19],a[196:199],v[112:115]
s_waitcnt lgkmcnt(3)
buffer_load_dwordx4 %[vL16],%[srdW],s81 offen offset:2048 sc1 lds
v_mfma_f32_16x16x32_bf16 v[232:235],a[188:191],a[20:23],v[232:235]
ds_read_b128 a[4:7],v16 offset:16896
v_mfma_f32_16x16x32_bf16 v[236:239],a[192:195],a[20:23],v[236:239]
v_mfma_f32_16x16x32_bf16 v[116:119],a[20:23],a[196:199],v[116:119]
s_waitcnt lgkmcnt(3)
v_mfma_f32_16x16x32_bf16 v[240:243],a[188:191],a[24:27],v[240:243]
ds_read_b128 a[8:11],v16 offset:49152
v_mfma_f32_16x16x32_bf16 v[244:247],a[192:195],a[24:27],v[244:247]
buffer_load_dwordx4 %[vL16],%[srdW],s81 offen offset:3072 sc1 lds
v_mfma_f32_16x16x32_bf16 v[120:123],a[24:27],a[196:199],v[120:123]
s_waitcnt lgkmcnt(3)
v_mfma_f32_16x16x32_bf16 v[248:251],a[188:191],a[28:31],v[248:251]
ds_read_b128 a[12:15],v16 offset:49664
v_mfma_f32_16x16x32_bf16 v[252:255],a[192:195],a[28:31],v[252:255]
v_mfma_f32_16x16x32_bf16 v[124:127],a[28:31],a[196:199],v[124:127]
s_waitcnt lgkmcnt(3)
s_waitcnt vmcnt(14)
v_mfma_f32_16x16x32_bf16 v[192:195],a[200:203],a[0:3],v[192:195]
s_waitcnt vmcnt(13)
ds_read_b128 a[16:19],v17 offset:16384
v_mfma_f32_16x16x32_bf16 v[196:199],a[204:207],a[0:3],v[196:199]
s_waitcnt vmcnt(12)
buffer_load_dwordx4 a[236:239],%[vL16],%[srdW],s80 offen offset:1024 sc1
v_mfma_f32_16x16x32_bf16 v[96:99],a[0:3],a[208:211],v[96:99]
s_waitcnt lgkmcnt(3)
v_mfma_f32_16x16x32_bf16 v[200:203],a[200:203],a[4:7],v[200:203]
ds_read_b128 a[20:23],v17 offset:16896
v_mfma_f32_16x16x32_bf16 v[204:207],a[204:207],a[4:7],v[204:207]
v_mfma_f32_16x16x32_bf16 v[100:103],a[4:7],a[208:211],v[100:103]
s_waitcnt lgkmcnt(3)
buffer_load_dwordx4 a[240:243],%[vL16],%[srdW],s80 offen offset:2048 sc1
v_mfma_f32_16x16x32_bf16 v[208:211],a[200:203],a[8:11],v[208:211]
ds_read_b128 a[24:27],v17 offset:49152
v_mfma_f32_16x16x32_bf16 v[212:215],a[204:207],a[8:11],v[212:215]
v_mfma_f32_16x16x32_bf16 v[104:107],a[8:11],a[208:211],v[104:107]
s_waitcnt lgkmcnt(3)
v_mfma_f32_16x16x32_bf16 v[216:219],a[200:203],a[12:15],v[216:219]
ds_read_b128 a[28:31],v17 offset:49664
v_mfma_f32_16x16x32_bf16 v[220:223],a[204:207],a[12:15],v[220:223]
buffer_load_dwordx4 a[244:247],%[vL16],%[srdW],s80 offen offset:3072 sc1
v_mfma_f32_16x16x32_bf16 v[108:111],a[12:15],a[208:211],v[108:111]
s_waitcnt lgkmcnt(3)
s_add_u32 m0,s82,28672
s_add_u32 s81,s83,28672
v_mfma_f32_16x16x32_bf16 v[224:227],a[200:203],a[16:19],v[224:227]
ds_read_b128 a[0:3],v16 offset:18432
v_mfma_f32_16x16x32_bf16 v[228:231],a[204:207],a[16:19],v[228:231]
v_mfma_f32_16x16x32_bf16 v[112:115],a[16:19],a[208:211],v[112:115]
s_waitcnt lgkmcnt(3)
buffer_load_dwordx4 %[vL16],%[srdW],s81 offen offset:0 sc1 lds
v_mfma_f32_16x16x32_bf16 v[232:235],a[200:203],a[20:23],v[232:235]
ds_read_b128 a[4:7],v16 offset:18944
v_mfma_f32_16x16x32_bf16 v[236:239],a[204:207],a[20:23],v[236:239]
v_mfma_f32_16x16x32_bf16 v[116:119],a[20:23],a[208:211],v[116:119]
s_waitcnt lgkmcnt(3)
v_mfma_f32_16x16x32_bf16 v[240:243],a[200:203],a[24:27],v[240:243]
ds_read_b128 a[8:11],v16 offset:51200
v_mfma_f32_16x16x32_bf16 v[244:247],a[204:207],a[24:27],v[244:247]
buffer_load_dwordx4 %[vL16],%[srdW],s81 offen offset:1024 sc1 lds
v_mfma_f32_16x16x32_bf16 v[120:123],a[24:27],a[208:211],v[120:123]
s_waitcnt lgkmcnt(3)
v_mfma_f32_16x16x32_bf16 v[248:251],a[200:203],a[28:31],v[248:251]
ds_read_b128 a[12:15],v16 offset:51712
v_mfma_f32_16x16x32_bf16 v[252:255],a[204:207],a[28:31],v[252:255]
v_mfma_f32_16x16x32_bf16 v[124:127],a[28:31],a[208:211],v[124:127]
s_waitcnt lgkmcnt(3)
s_waitcnt vmcnt(14)
v_mfma_f32_16x16x32_bf16 v[192:195],a[212:215],a[0:3],v[192:195]
s_waitcnt vmcnt(13)
ds_read_b128 a[16:19],v17 offset:18432
s_add_u32 s80,s80,0x1000
v_mfma_f32_16x16x32_bf16 v[196:199],a[216:219],a[0:3],v[196:199]
s_waitcnt vmcnt(12)
buffer_load_dwordx4 a[32:35],%[vL16],%[srdW],s80 offen offset:0 sc1
v_mfma_f32_16x16x32_bf16 v[96:99],a[0:3],a[220:223],v[96:99]
s_waitcnt lgkmcnt(3)
v_mfma_f32_16x16x32_bf16 v[200:203],a[212:215],a[4:7],v[200:203]
ds_read_b128 a[20:23],v17 offset:18944
v_mfma_f32_16x16x32_bf16 v[204:207],a[216:219],a[4:7],v[204:207]
v_mfma_f32_16x16x32_bf16 v[100:103],a[4:7],a[220:223],v[100:103]
s_waitcnt lgkmcnt(3)
buffer_load_dwordx4 a[36:39],%[vL16],%[srdW],s80 offen offset:1024 sc1
v_mfma_f32_16x16x32_bf16 v[208:211],a[212:215],a[8:11],v[208:211]
ds_read_b128 a[24:27],v17 offset:51200
v_mfma_f32_16x16x32_bf16 v[212:215],a[216:219],a[8:11],v[212:215]
v_mfma_f32_16x16x32_bf16 v[104:107],a[8:11],a[220:223],v[104:107]
s_waitcnt lgkmcnt(3)
v_mfma_f32_16x16x32_bf16 v[216:219],a[212:215],a[12:15],v[216:219]
ds_read_b128 a[28:31],v17 offset:51712
v_mfma_f32_16x16x32_bf16 v[220:223],a[216:219],a[12:15],v[220:223]
buffer_load_dwordx4 a[40:43],%[vL16],%[srdW],s80 offen offset:2048 sc1
v_mfma_f32_16x16x32_bf16 v[108:111],a[12:15],a[220:223],v[108:111]
s_waitcnt lgkmcnt(3)
v_mfma_f32_16x16x32_bf16 v[224:227],a[212:215],a[16:19],v[224:227]
s_barrier
ds_read_b128 a[0:3],v16 offset:20480
v_mfma_f32_16x16x32_bf16 v[228:231],a[216:219],a[16:19],v[228:231]
v_mfma_f32_16x16x32_bf16 v[112:115],a[16:19],a[220:223],v[112:115]
s_waitcnt lgkmcnt(3)
buffer_load_dwordx4 %[vL16],%[srdW],s81 offen offset:2048 sc1 lds
v_mfma_f32_16x16x32_bf16 v[232:235],a[212:215],a[20:23],v[232:235]
ds_read_b128 a[4:7],v16 offset:20992
v_mfma_f32_16x16x32_bf16 v[236:239],a[216:219],a[20:23],v[236:239]
v_mfma_f32_16x16x32_bf16 v[116:119],a[20:23],a[220:223],v[116:119]
s_waitcnt lgkmcnt(3)
v_mfma_f32_16x16x32_bf16 v[240:243],a[212:215],a[24:27],v[240:243]
ds_read_b128 a[8:11],v16 offset:53248
v_mfma_f32_16x16x32_bf16 v[244:247],a[216:219],a[24:27],v[244:247]
buffer_load_dwordx4 %[vL16],%[srdW],s81 offen offset:3072 sc1 lds
v_mfma_f32_16x16x32_bf16 v[120:123],a[24:27],a[220:223],v[120:123]
s_waitcnt lgkmcnt(3)
v_mfma_f32_16x16x32_bf16 v[248:251],a[212:215],a[28:31],v[248:251]
ds_read_b128 a[12:15],v16 offset:53760
v_mfma_f32_16x16x32_bf16 v[252:255],a[216:219],a[28:31],v[252:255]
v_mfma_f32_16x16x32_bf16 v[124:127],a[28:31],a[220:223],v[124:127]
s_waitcnt lgkmcnt(3)
s_waitcnt vmcnt(14)
v_mfma_f32_16x16x32_bf16 v[192:195],a[224:227],a[0:3],v[192:195]
s_waitcnt vmcnt(13)
ds_read_b128 a[16:19],v17 offset:20480
v_mfma_f32_16x16x32_bf16 v[196:199],a[228:231],a[0:3],v[196:199]
s_waitcnt vmcnt(12)
buffer_load_dwordx4 a[44:47],%[vL16],%[srdW],s80 offen offset:3072 sc1
v_mfma_f32_16x16x32_bf16 v[96:99],a[0:3],a[232:235],v[96:99]
s_waitcnt lgkmcnt(3)
s_add_u32 s80,s80,0x1000
v_mfma_f32_16x16x32_bf16 v[200:203],a[224:227],a[4:7],v[200:203]
ds_read_b128 a[20:23],v17 offset:20992
v_mfma_f32_16x16x32_bf16 v[204:207],a[228:231],a[4:7],v[204:207]
v_mfma_f32_16x16x32_bf16 v[100:103],a[4:7],a[232:235],v[100:103]
s_waitcnt lgkmcnt(3)
buffer_load_dwordx4 a[48:51],%[vL16],%[srdW],s80 offen offset:0 sc1
v_mfma_f32_16x16x32_bf16 v[208:211],a[224:227],a[8:11],v[208:211]
ds_read_b128 a[24:27],v17 offset:53248
v_mfma_f32_16x16x32_bf16 v[212:215],a[228:231],a[8:11],v[212:215]
v_mfma_f32_16x16x32_bf16 v[104:107],a[8:11],a[232:235],v[104:107]
s_waitcnt lgkmcnt(3)
v_mfma_f32_16x16x32_bf16 v[216:219],a[224:227],a[12:15],v[216:219]
ds_read_b128 a[28:31],v17 offset:53760
v_mfma_f32_16x16x32_bf16 v[220:223],a[228:231],a[12:15],v[220:223]
buffer_load_dwordx4 a[52:55],%[vL16],%[srdW],s80 offen offset:1024 sc1
v_mfma_f32_16x16x32_bf16 v[108:111],a[12:15],a[232:235],v[108:111]
s_waitcnt lgkmcnt(3)
v_mfma_f32_16x16x32_bf16 v[224:227],a[224:227],a[16:19],v[224:227]
ds_read_b128 a[0:3],v16 offset:22528
v_mfma_f32_16x16x32_bf16 v[228:231],a[228:231],a[16:19],v[228:231]
v_mfma_f32_16x16x32_bf16 v[112:115],a[16:19],a[232:235],v[112:115]
s_waitcnt lgkmcnt(3)
buffer_load_dwordx4 a[56:59],%[vL16],%[srdW],s80 offen offset:2048 sc1
v_mfma_f32_16x16x32_bf16 v[232:235],a[224:227],a[20:23],v[232:235]
ds_read_b128 a[4:7],v16 offset:23040
v_mfma_f32_16x16x32_bf16 v[236:239],a[228:231],a[20:23],v[236:239]
v_mfma_f32_16x16x32_bf16 v[116:119],a[20:23],a[232:235],v[116:119]
s_waitcnt lgkmcnt(3)
v_mfma_f32_16x16x32_bf16 v[240:243],a[224:227],a[24:27],v[240:243]
ds_read_b128 a[8:11],v16 offset:55296
v_mfma_f32_16x16x32_bf16 v[244:247],a[228:231],a[24:27],v[244:247]
buffer_load_dwordx4 a[60:63],%[vL16],%[srdW],s80 offen offset:3072 sc1
v_mfma_f32_16x16x32_bf16 v[120:123],a[24:27],a[232:235],v[120:123]
s_waitcnt lgkmcnt(3)
s_add_u32 s80,s80,0x1000
v_mfma_f32_16x16x32_bf16 v[248:251],a[224:227],a[28:31],v[248:251]
ds_read_b128 a[12:15],v16 offset:55808
v_mfma_f32_16x16x32_bf16 v[252:255],a[228:231],a[28:31],v[252:255]
v_mfma_f32_16x16x32_bf16 v[124:127],a[28:31],a[232:235],v[124:127]
s_waitcnt lgkmcnt(3)
s_waitcnt vmcnt(14)
v_mfma_f32_16x16x32_bf16 v[192:195],a[236:239],a[0:3],v[192:195]
s_waitcnt vmcnt(13)
ds_read_b128 a[16:19],v17 offset:22528
v_mfma_f32_16x16x32_bf16 v[196:199],a[240:243],a[0:3],v[196:199]
s_waitcnt vmcnt(12)
buffer_load_dwordx4 a[64:67],%[vL16],%[srdW],s80 offen offset:0 sc1
v_mfma_f32_16x16x32_bf16 v[96:99],a[0:3],a[244:247],v[96:99]
s_waitcnt lgkmcnt(3)
v_mfma_f32_16x16x32_bf16 v[200:203],a[236:239],a[4:7],v[200:203]
ds_read_b128 a[20:23],v17 offset:23040
v_mfma_f32_16x16x32_bf16 v[204:207],a[240:243],a[4:7],v[204:207]
v_mfma_f32_16x16x32_bf16 v[100:103],a[4:7],a[244:247],v[100:103]
s_waitcnt lgkmcnt(3)
buffer_load_dwordx4 a[68:71],%[vL16],%[srdW],s80 offen offset:1024 sc1
v_mfma_f32_16x16x32_bf16 v[208:211],a[236:239],a[8:11],v[208:211]
ds_read_b128 a[24:27],v17 offset:55296
v_mfma_f32_16x16x32_bf16 v[212:215],a[240:243],a[8:11],v[212:215]
v_mfma_f32_16x16x32_bf16 v[104:107],a[8:11],a[244:247],v[104:107]
s_waitcnt lgkmcnt(3)
v_mfma_f32_16x16x32_bf16 v[216:219],a[236:239],a[12:15],v[216:219]
ds_read_b128 a[28:31],v17 offset:55808
v_mfma_f32_16x16x32_bf16 v[220:223],a[240:243],a[12:15],v[220:223]
buffer_load_dwordx4 a[72:75],%[vL16],%[srdW],s80 offen offset:2048 sc1
v_mfma_f32_16x16x32_bf16 v[108:111],a[12:15],a[244:247],v[108:111]
s_waitcnt lgkmcnt(3)
v_mfma_f32_16x16x32_bf16 v[224:227],a[236:239],a[16:19],v[224:227]
s_barrier
ds_read_b128 a[0:3],v16 offset:24576
v_mfma_f32_16x16x32_bf16 v[228:231],a[240:243],a[16:19],v[228:231]
v_mfma_f32_16x16x32_bf16 v[112:115],a[16:19],a[244:247],v[112:115]
s_waitcnt lgkmcnt(3)
buffer_load_dwordx4 a[76:79],%[vL16],%[srdW],s80 offen offset:3072 sc1
v_mfma_f32_16x16x32_bf16 v[232:235],a[236:239],a[20:23],v[232:235]
s_add_u32 s80,s80,0x1000
ds_read_b128 a[4:7],v16 offset:25088
v_mfma_f32_16x16x32_bf16 v[236:239],a[240:243],a[20:23],v[236:239]
v_mfma_f32_16x16x32_bf16 v[116:119],a[20:23],a[244:247],v[116:119]
s_waitcnt lgkmcnt(3)
v_mfma_f32_16x16x32_bf16 v[240:243],a[236:239],a[24:27],v[240:243]
ds_read_b128 a[8:11],v16 offset:57344
v_mfma_f32_16x16x32_bf16 v[244:247],a[240:243],a[24:27],v[244:247]
buffer_load_dwordx4 a[80:83],%[vL16],%[srdW],s80 offen offset:0 sc1
v_mfma_f32_16x16x32_bf16 v[120:123],a[24:27],a[244:247],v[120:123]
s_waitcnt lgkmcnt(3)
v_mfma_f32_16x16x32_bf16 v[248:251],a[236:239],a[28:31],v[248:251]
ds_read_b128 a[12:15],v16 offset:57856
v_mfma_f32_16x16x32_bf16 v[252:255],a[240:243],a[28:31],v[252:255]
v_mfma_f32_16x16x32_bf16 v[124:127],a[28:31],a[244:247],v[124:127]
s_waitcnt lgkmcnt(3)
s_waitcnt vmcnt(14)
v_mfma_f32_16x16x32_bf16 v[192:195],a[32:35],a[0:3],v[192:195]
s_waitcnt vmcnt(13)
ds_read_b128 a[16:19],v17 offset:24576
v_mfma_f32_16x16x32_bf16 v[196:199],a[36:39],a[0:3],v[196:199]
s_waitcnt vmcnt(12)
buffer_load_dwordx4 a[84:87],%[vL16],%[srdW],s80 offen offset:1024 sc1
v_mfma_f32_16x16x32_bf16 v[96:99],a[0:3],a[40:43],v[96:99]
s_waitcnt lgkmcnt(3)
v_mfma_f32_16x16x32_bf16 v[200:203],a[32:35],a[4:7],v[200:203]
ds_read_b128 a[20:23],v17 offset:25088
v_mfma_f32_16x16x32_bf16 v[204:207],a[36:39],a[4:7],v[204:207]
v_mfma_f32_16x16x32_bf16 v[100:103],a[4:7],a[40:43],v[100:103]
s_waitcnt lgkmcnt(3)
buffer_load_dwordx4 a[88:91],%[vL16],%[srdW],s80 offen offset:2048 sc1
v_mfma_f32_16x16x32_bf16 v[208:211],a[32:35],a[8:11],v[208:211]
ds_read_b128 a[24:27],v17 offset:57344
v_mfma_f32_16x16x32_bf16 v[212:215],a[36:39],a[8:11],v[212:215]
v_mfma_f32_16x16x32_bf16 v[104:107],a[8:11],a[40:43],v[104:107]
s_waitcnt lgkmcnt(3)
v_mfma_f32_16x16x32_bf16 v[216:219],a[32:35],a[12:15],v[216:219]
ds_read_b128 a[28:31],v17 offset:57856
v_mfma_f32_16x16x32_bf16 v[220:223],a[36:39],a[12:15],v[220:223]
buffer_load_dwordx4 a[92:95],%[vL16],%[srdW],s80 offen offset:3072 sc1
v_mfma_f32_16x16x32_bf16 v[108:111],a[12:15],a[40:43],v[108:111]
s_waitcnt lgkmcnt(3)
s_add_u32 s80,s80,0x1000
v_mfma_f32_16x16x32_bf16 v[224:227],a[32:35],a[16:19],v[224:227]
ds_read_b128 a[0:3],v16 offset:26624
v_mfma_f32_16x16x32_bf16 v[228:231],a[36:39],a[16:19],v[228:231]
v_mfma_f32_16x16x32_bf16 v[112:115],a[16:19],a[40:43],v[112:115]
s_waitcnt lgkmcnt(3)
buffer_load_dwordx4 a[96:99],%[vL16],%[srdW],s80 offen offset:0 sc1
v_mfma_f32_16x16x32_bf16 v[232:235],a[32:35],a[20:23],v[232:235]
ds_read_b128 a[4:7],v16 offset:27136
v_mfma_f32_16x16x32_bf16 v[236:239],a[36:39],a[20:23],v[236:239]
v_mfma_f32_16x16x32_bf16 v[116:119],a[20:23],a[40:43],v[116:119]
s_waitcnt lgkmcnt(3)
v_mfma_f32_16x16x32_bf16 v[240:243],a[32:35],a[24:27],v[240:243]
ds_read_b128 a[8:11],v16 offset:59392
v_mfma_f32_16x16x32_bf16 v[244:247],a[36:39],a[24:27],v[244:247]
buffer_load_dwordx4 a[100:103],%[vL16],%[srdW],s80 offen offset:1024 sc1
v_mfma_f32_16x16x32_bf16 v[120:123],a[24:27],a[40:43],v[120:123]
s_waitcnt lgkmcnt(3)
v_mfma_f32_16x16x32_bf16 v[248:251],a[32:35],a[28:31],v[248:251]
ds_read_b128 a[12:15],v16 offset:59904
v_mfma_f32_16x16x32_bf16 v[252:255],a[36:39],a[28:31],v[252:255]
v_mfma_f32_16x16x32_bf16 v[124:127],a[28:31],a[40:43],v[124:127]
s_waitcnt lgkmcnt(3)
s_waitcnt vmcnt(14)
v_mfma_f32_16x16x32_bf16 v[192:195],a[44:47],a[0:3],v[192:195]
s_waitcnt vmcnt(13)
ds_read_b128 a[16:19],v17 offset:26624
v_mfma_f32_16x16x32_bf16 v[196:199],a[48:51],a[0:3],v[196:199]
s_waitcnt vmcnt(12)
buffer_load_dwordx4 a[176:179],%[vL16],%[srdW],s80 offen offset:2048 sc1
v_mfma_f32_16x16x32_bf16 v[96:99],a[0:3],a[52:55],v[96:99]
s_waitcnt lgkmcnt(3)
v_mfma_f32_16x16x32_bf16 v[200:203],a[44:47],a[4:7],v[200:203]
ds_read_b128 a[20:23],v17 offset:27136
v_mfma_f32_16x16x32_bf16 v[204:207],a[48:51],a[4:7],v[204:207]
v_mfma_f32_16x16x32_bf16 v[100:103],a[4:7],a[52:55],v[100:103]
s_waitcnt lgkmcnt(3)
buffer_load_dwordx4 a[180:183],%[vL16],%[srdW],s80 offen offset:3072 sc1
v_mfma_f32_16x16x32_bf16 v[208:211],a[44:47],a[8:11],v[208:211]
s_add_u32 s80,s80,0x1000
ds_read_b128 a[24:27],v17 offset:59392
v_mfma_f32_16x16x32_bf16 v[212:215],a[48:51],a[8:11],v[212:215]
v_mfma_f32_16x16x32_bf16 v[104:107],a[8:11],a[52:55],v[104:107]
s_waitcnt lgkmcnt(3)
v_mfma_f32_16x16x32_bf16 v[216:219],a[44:47],a[12:15],v[216:219]
ds_read_b128 a[28:31],v17 offset:59904
v_mfma_f32_16x16x32_bf16 v[220:223],a[48:51],a[12:15],v[220:223]
buffer_load_dwordx4 a[184:187],%[vL16],%[srdW],s80 offen offset:0 sc1
v_mfma_f32_16x16x32_bf16 v[108:111],a[12:15],a[52:55],v[108:111]
s_waitcnt lgkmcnt(3)
v_mfma_f32_16x16x32_bf16 v[224:227],a[44:47],a[16:19],v[224:227]
s_barrier
ds_read_b128 a[0:3],v16 offset:28672
v_mfma_f32_16x16x32_bf16 v[228:231],a[48:51],a[16:19],v[228:231]
)"
R"(v_mfma_f32_16x16x32_bf16 v[112:115],a[16:19],a[52:55],v[112:115]
s_waitcnt lgkmcnt(3)
buffer_load_dwordx4 a[188:191],%[vL16],%[srdW],s80 offen offset:1024 sc1
v_mfma_f32_16x16x32_bf16 v[232:235],a[44:47],a[20:23],v[232:235]
ds_read_b128 a[4:7],v16 offset:29184
v_mfma_f32_16x16x32_bf16 v[236:239],a[48:51],a[20:23],v[236:239]
v_mfma_f32_16x16x32_bf16 v[116:119],a[20:23],a[52:55],v[116:119]
s_waitcnt lgkmcnt(3)
v_mfma_f32_16x16x32_bf16 v[240:243],a[44:47],a[24:27],v[240:243]
ds_read_b128 a[8:11],v16 offset:61440
v_mfma_f32_16x16x32_bf16 v[244:247],a[48:51],a[24:27],v[244:247]
buffer_load_dwordx4 a[192:195],%[vL16],%[srdW],s80 offen offset:2048 sc1
v_mfma_f32_16x16x32_bf16 v[120:123],a[24:27],a[52:55],v[120:123]
s_waitcnt lgkmcnt(3)
v_mfma_f32_16x16x32_bf16 v[248:251],a[44:47],a[28:31],v[248:251]
ds_read_b128 a[12:15],v16 offset:61952
v_mfma_f32_16x16x32_bf16 v[252:255],a[48:51],a[28:31],v[252:255]
v_mfma_f32_16x16x32_bf16 v[124:127],a[28:31],a[52:55],v[124:127]
s_waitcnt lgkmcnt(3)
s_waitcnt vmcnt(16)
v_mfma_f32_16x16x32_bf16 v[192:195],a[56:59],a[0:3],v[192:195]
s_waitcnt vmcnt(15)
ds_read_b128 a[16:19],v17 offset:28672
v_mfma_f32_16x16x32_bf16 v[196:199],a[60:63],a[0:3],v[196:199]
s_waitcnt vmcnt(14)
buffer_load_dwordx4 a[196:199],%[vL16],%[srdW],s80 offen offset:3072 sc1
v_mfma_f32_16x16x32_bf16 v[96:99],a[0:3],a[64:67],v[96:99]
s_waitcnt lgkmcnt(3)
s_add_u32 s80,s80,0x1000
v_mfma_f32_16x16x32_bf16 v[200:203],a[56:59],a[4:7],v[200:203]
ds_read_b128 a[20:23],v17 offset:29184
v_mfma_f32_16x16x32_bf16 v[204:207],a[60:63],a[4:7],v[204:207]
v_mfma_f32_16x16x32_bf16 v[100:103],a[4:7],a[64:67],v[100:103]
s_waitcnt lgkmcnt(3)
buffer_load_dwordx4 a[200:203],%[vL16],%[srdW],s80 offen offset:0 sc1
v_mfma_f32_16x16x32_bf16 v[208:211],a[56:59],a[8:11],v[208:211]
ds_read_b128 a[24:27],v17 offset:61440
v_mfma_f32_16x16x32_bf16 v[212:215],a[60:63],a[8:11],v[212:215]
v_mfma_f32_16x16x32_bf16 v[104:107],a[8:11],a[64:67],v[104:107]
s_waitcnt lgkmcnt(3)
v_mfma_f32_16x16x32_bf16 v[216:219],a[56:59],a[12:15],v[216:219]
ds_read_b128 a[28:31],v17 offset:61952
v_mfma_f32_16x16x32_bf16 v[220:223],a[60:63],a[12:15],v[220:223]
buffer_load_dwordx4 a[204:207],%[vL16],%[srdW],s80 offen offset:1024 sc1
v_mfma_f32_16x16x32_bf16 v[108:111],a[12:15],a[64:67],v[108:111]
s_waitcnt lgkmcnt(3)
v_mfma_f32_16x16x32_bf16 v[224:227],a[56:59],a[16:19],v[224:227]
ds_read_b128 a[0:3],v16 offset:30720
v_mfma_f32_16x16x32_bf16 v[228:231],a[60:63],a[16:19],v[228:231]
v_mfma_f32_16x16x32_bf16 v[112:115],a[16:19],a[64:67],v[112:115]
s_waitcnt lgkmcnt(3)
buffer_load_dwordx4 a[208:211],%[vL16],%[srdW],s80 offen offset:2048 sc1
v_mfma_f32_16x16x32_bf16 v[232:235],a[56:59],a[20:23],v[232:235]
ds_read_b128 a[4:7],v16 offset:31232
v_mfma_f32_16x16x32_bf16 v[236:239],a[60:63],a[20:23],v[236:239]
v_mfma_f32_16x16x32_bf16 v[116:119],a[20:23],a[64:67],v[116:119]
s_waitcnt lgkmcnt(3)
v_mfma_f32_16x16x32_bf16 v[240:243],a[56:59],a[24:27],v[240:243]
ds_read_b128 a[8:11],v16 offset:63488
v_mfma_f32_16x16x32_bf16 v[244:247],a[60:63],a[24:27],v[244:247]
buffer_load_dwordx4 a[212:215],%[vL16],%[srdW],s80 offen offset:3072 sc1
v_mfma_f32_16x16x32_bf16 v[120:123],a[24:27],a[64:67],v[120:123]
s_waitcnt lgkmcnt(3)
s_add_u32 s80,s80,0x1000
v_mfma_f32_16x16x32_bf16 v[248:251],a[56:59],a[28:31],v[248:251]
ds_read_b128 a[12:15],v16 offset:64000
v_mfma_f32_16x16x32_bf16 v[252:255],a[60:63],a[28:31],v[252:255]
v_mfma_f32_16x16x32_bf16 v[124:127],a[28:31],a[64:67],v[124:127]
s_waitcnt lgkmcnt(3)
s_waitcnt vmcnt(18)
v_mfma_f32_16x16x32_bf16 v[192:195],a[68:71],a[0:3],v[192:195]
s_waitcnt vmcnt(17)
ds_read_b128 a[16:19],v17 offset:30720
v_mfma_f32_16x16x32_bf16 v[196:199],a[72:75],a[0:3],v[196:199]
s_waitcnt vmcnt(16)
buffer_load_dwordx4 a[216:219],%[vL16],%[srdW],s80 offen offset:0 sc1
v_mfma_f32_16x16x32_bf16 v[96:99],a[0:3],a[76:79],v[96:99]
s_waitcnt lgkmcnt(3)
v_mfma_f32_16x16x32_bf16 v[200:203],a[68:71],a[4:7],v[200:203]
ds_read_b128 a[20:23],v17 offset:31232
v_mfma_f32_16x16x32_bf16 v[204:207],a[72:75],a[4:7],v[204:207]
v_mfma_f32_16x16x32_bf16 v[100:103],a[4:7],a[76:79],v[100:103]
s_waitcnt lgkmcnt(3)
buffer_load_dwordx4 a[220:223],%[vL16],%[srdW],s80 offen offset:1024 sc1
v_mfma_f32_16x16x32_bf16 v[208:211],a[68:71],a[8:11],v[208:211]
ds_read_b128 a[24:27],v17 offset:63488
v_mfma_f32_16x16x32_bf16 v[212:215],a[72:75],a[8:11],v[212:215]
v_mfma_f32_16x16x32_bf16 v[104:107],a[8:11],a[76:79],v[104:107]
s_waitcnt lgkmcnt(3)
v_mfma_f32_16x16x32_bf16 v[216:219],a[68:71],a[12:15],v[216:219]
ds_read_b128 a[28:31],v17 offset:64000
v_mfma_f32_16x16x32_bf16 v[220:223],a[72:75],a[12:15],v[220:223]
buffer_load_dwordx4 a[224:227],%[vL16],%[srdW],s80 offen offset:2048 sc1
v_mfma_f32_16x16x32_bf16 v[108:111],a[12:15],a[76:79],v[108:111]
s_waitcnt lgkmcnt(3)
v_mfma_f32_16x16x32_bf16 v[224:227],a[68:71],a[16:19],v[224:227]
ds_read_b128 a[0:3],v16 offset:0
v_mfma_f32_16x16x32_bf16 v[228:231],a[72:75],a[16:19],v[228:231]
v_mfma_f32_16x16x32_bf16 v[112:115],a[16:19],a[76:79],v[112:115]
s_waitcnt lgkmcnt(3)
buffer_load_dwordx4 a[228:231],%[vL16],%[srdW],s80 offen offset:3072 sc1
v_mfma_f32_16x16x32_bf16 v[232:235],a[68:71],a[20:23],v[232:235]
s_add_u32 s80,s80,0x1000
ds_read_b128 a[4:7],v16 offset:512
v_mfma_f32_16x16x32_bf16 v[236:239],a[72:75],a[20:23],v[236:239]
v_mfma_f32_16x16x32_bf16 v[116:119],a[20:23],a[76:79],v[116:119]
s_waitcnt lgkmcnt(3)
v_mfma_f32_16x16x32_bf16 v[240:243],a[68:71],a[24:27],v[240:243]
ds_read_b128 a[8:11],v16 offset:32768
v_mfma_f32_16x16x32_bf16 v[244:247],a[72:75],a[24:27],v[244:247]
buffer_load_dwordx4 a[232:235],%[vL16],%[srdW],s80 offen offset:0 sc1
v_mfma_f32_16x16x32_bf16 v[120:123],a[24:27],a[76:79],v[120:123]
s_waitcnt lgkmcnt(3)
v_mfma_f32_16x16x32_bf16 v[248:251],a[68:71],a[28:31],v[248:251]
ds_read_b128 a[12:15],v16 offset:33280
v_mfma_f32_16x16x32_bf16 v[252:255],a[72:75],a[28:31],v[252:255]
v_mfma_f32_16x16x32_bf16 v[124:127],a[28:31],a[76:79],v[124:127]
s_waitcnt lgkmcnt(3)
s_waitcnt vmcnt(20)
v_mfma_f32_16x16x32_bf16 v[64:67],a[0:3],a[80:83],0
s_waitcnt vmcnt(19)
ds_read_b128 a[16:19],v17 offset:0
v_mfma_f32_16x16x32_bf16 a[112:115],a[0:3],a[84:87],0
s_waitcnt vmcnt(18)
buffer_load_dwordx4 a[236:239],%[vL16],%[srdW],s80 offen offset:1024 sc1
v_mfma_f32_16x16x32_bf16 a[116:119],a[0:3],a[88:91],0
s_waitcnt lgkmcnt(3)
v_mfma_f32_16x16x32_bf16 v[68:71],a[4:7],a[80:83],0
ds_read_b128 a[20:23],v17 offset:512
v_cvt_pk_bf16_f32 v192,v192,v193
v_mfma_f32_16x16x32_bf16 a[120:123],a[4:7],a[84:87],0
v_cvt_pk_bf16_f32 v193,v194,v195
v_cvt_pk_bf16_f32 v194,v196,v197
v_mfma_f32_16x16x32_bf16 a[124:127],a[4:7],a[88:91],0
s_waitcnt lgkmcnt(3)
buffer_load_dwordx4 a[240:243],%[vL16],%[srdW],s80 offen offset:2048 sc1
v_mfma_f32_16x16x32_bf16 v[72:75],a[8:11],a[80:83],0
ds_read_b128 a[24:27],v17 offset:32768
v_cvt_pk_bf16_f32 v195,v198,v199
v_mfma_f32_16x16x32_bf16 a[128:131],a[8:11],a[84:87],0
v_cvt_pk_bf16_f32 v200,v200,v201
v_cvt_pk_bf16_f32 v201,v202,v203
v_mfma_f32_16x16x32_bf16 a[132:135],a[8:11],a[88:91],0
s_waitcnt lgkmcnt(3)
v_cvt_pk_bf16_f32 v202,v204,v205
v_cvt_pk_bf16_f32 v203,v206,v207
v_mfma_f32_16x16x32_bf16 v[76:79],a[12:15],a[80:83],0
ds_read_b128 a[28:31],v17 offset:33280
v_cvt_pk_bf16_f32 v208,v208,v209
v_mfma_f32_16x16x32_bf16 a[136:139],a[12:15],a[84:87],0
buffer_load_dwordx4 a[244:247],%[vL16],%[srdW],s80 offen offset:3072 sc1
v_mfma_f32_16x16x32_bf16 a[140:143],a[12:15],a[88:91],0
s_waitcnt lgkmcnt(3)
v_add_u32 v24,0,v18
v_cvt_pk_bf16_f32 v209,v210,v211
v_mfma_f32_16x16x32_bf16 v[80:83],a[16:19],a[80:83],0
ds_read_b128 a[0:3],v16 offset:2048
v_cvt_pk_bf16_f32 v210,v212,v213
v_mfma_f32_16x16x32_bf16 a[144:147],a[16:19],a[84:87],0
v_cvt_pk_bf16_f32 v211,v214,v215
v_cvt_pk_bf16_f32 v216,v216,v217
v_mfma_f32_16x16x32_bf16 a[148:151],a[16:19],a[88:91],0
s_waitcnt lgkmcnt(3)
buffer_store_dwordx4 v[192:195],v24,%[srdW],0 offen offset:0 sc1
v_mfma_f32_16x16x32_bf16 v[84:87],a[20:23],a[80:83],0
ds_read_b128 a[4:7],v16 offset:2560
v_cvt_pk_bf16_f32 v217,v218,v219
v_mfma_f32_16x16x32_bf16 a[152:155],a[20:23],a[84:87],0
v_cvt_pk_bf16_f32 v218,v220,v221
v_cvt_pk_bf16_f32 v219,v222,v223
v_mfma_f32_16x16x32_bf16 a[156:159],a[20:23],a[88:91],0
s_waitcnt lgkmcnt(3)
v_cvt_pk_bf16_f32 v224,v224,v225
v_cvt_pk_bf16_f32 v225,v226,v227
v_mfma_f32_16x16x32_bf16 v[88:91],a[24:27],a[80:83],0
ds_read_b128 a[8:11],v16 offset:34816
v_cvt_pk_bf16_f32 v226,v228,v229
v_mfma_f32_16x16x32_bf16 a[160:163],a[24:27],a[84:87],0
buffer_store_dwordx4 v[200:203],v24,%[srdW],0 offen offset:512 sc1
v_mfma_f32_16x16x32_bf16 a[164:167],a[24:27],a[88:91],0
s_waitcnt lgkmcnt(3)
v_add_u32 v24,32768,v18
v_add_u32 v24,s92,v24
v_mfma_f32_16x16x32_bf16 v[92:95],a[28:31],a[80:83],0
ds_read_b128 a[12:15],v16 offset:35328
v_cvt_pk_bf16_f32 v227,v230,v231
v_mfma_f32_16x16x32_bf16 a[168:171],a[28:31],a[84:87],0
v_cvt_pk_bf16_f32 v232,v232,v233
v_cvt_pk_bf16_f32 v233,v234,v235
v_mfma_f32_16x16x32_bf16 a[172:175],a[28:31],a[88:91],0
s_waitcnt lgkmcnt(3)
s_waitcnt vmcnt(22)
v_cvt_pk_bf16_f32 v234,v236,v237
v_mfma_f32_16x16x32_bf16 v[64:67],a[0:3],a[92:95],v[64:67]
s_waitcnt vmcnt(21)
ds_read_b128 a[16:19],v17 offset:2048
s_add_u32 s80,s80,0x1000
v_mfma_f32_16x16x32_bf16 a[112:115],a[0:3],a[96:99],a[112:115]
s_waitcnt vmcnt(20)
buffer_load_dwordx4 a[32:35],%[vL16],%[srdW],s80 offen offset:0 sc1
v_mfma_f32_16x16x32_bf16 a[116:119],a[0:3],a[100:103],a[116:119]
s_waitcnt lgkmcnt(3)
v_cvt_pk_bf16_f32 v235,v238,v239
v_cvt_pk_bf16_f32 v240,v240,v241
v_mfma_f32_16x16x32_bf16 v[68:71],a[4:7],a[92:95],v[68:71]
ds_read_b128 a[20:23],v17 offset:2560
v_cvt_pk_bf16_f32 v241,v242,v243
v_mfma_f32_16x16x32_bf16 a[120:123],a[4:7],a[96:99],a[120:123]
v_cvt_pk_bf16_f32 v242,v244,v245
v_cvt_pk_bf16_f32 v243,v246,v247
v_mfma_f32_16x16x32_bf16 a[124:127],a[4:7],a[100:103],a[124:127]
s_waitcnt lgkmcnt(3)
buffer_load_dwordx4 a[36:39],%[vL16],%[srdW],s80 offen offset:1024 sc1
v_mfma_f32_16x16x32_bf16 v[72:75],a[8:11],a[92:95],v[72:75]
ds_read_b128 a[24:27],v17 offset:34816
v_cvt_pk_bf16_f32 v248,v248,v249
v_mfma_f32_16x16x32_bf16 a[128:131],a[8:11],a[96:99],a[128:131]
v_cvt_pk_bf16_f32 v249,v250,v251
v_cvt_pk_bf16_f32 v250,v252,v253
v_mfma_f32_16x16x32_bf16 a[132:135],a[8:11],a[100:103],a[132:135]
s_waitcnt lgkmcnt(3)
v_cvt_pk_bf16_f32 v251,v254,v255
v_mul_f32 v40,v96,v96
v_mfma_f32_16x16x32_bf16 v[76:79],a[12:15],a[92:95],v[76:79]
ds_read_b128 a[28:31],v17 offset:35328
v_fma_f32 v40,v40,v20,v21
v_mfma_f32_16x16x32_bf16 a[136:139],a[12:15],a[96:99],a[136:139]
buffer_load_dwordx4 a[40:43],%[vL16],%[srdW],s80 offen offset:2048 sc1
v_mfma_f32_16x16x32_bf16 a[140:143],a[12:15],a[100:103],a[140:143]
s_waitcnt lgkmcnt(3)
v_mul_f32 v41,v97,v97
v_mul_f32 v40,v40,v96
v_mfma_f32_16x16x32_bf16 v[80:83],a[16:19],a[92:95],v[80:83]
ds_read_b128 a[0:3],v16 offset:4096
v_fma_f32 v41,v41,v20,v21
v_mfma_f32_16x16x32_bf16 a[144:147],a[16:19],a[96:99],a[144:147]
v_mul_f32 v42,v98,v98
v_mfma_f32_16x16x32_bf16 a[148:151],a[16:19],a[100:103],a[148:151]
s_waitcnt lgkmcnt(3)
buffer_store_dwordx4 v[208:211],v24,%[srdW],0 offen offset:0 sc1
v_mfma_f32_16x16x32_bf16 v[84:87],a[20:23],a[92:95],v[84:87]
ds_read_b128 a[4:7],v16 offset:4608
v_mfma_f32_16x16x32_bf16 a[152:155],a[20:23],a[96:99],a[152:155]
v_exp_f32 v40,v40
v_mfma_f32_16x16x32_bf16 a[156:159],a[20:23],a[100:103],a[156:159]
s_waitcnt lgkmcnt(3)
v_mul_f32 v41,v41,v97
v_fma_f32 v42,v42,v20,v21
v_mfma_f32_16x16x32_bf16 v[88:91],a[24:27],a[92:95],v[88:91]
ds_read_b128 a[8:11],v16 offset:36864
v_mul_f32 v43,v99,v99
v_mfma_f32_16x16x32_bf16 a[160:163],a[24:27],a[96:99],a[160:163]
buffer_store_dwordx4 v[216:219],v24,%[srdW],0 offen offset:512 sc1
v_mfma_f32_16x16x32_bf16 a[164:167],a[24:27],a[100:103],a[164:167]
s_waitcnt lgkmcnt(3)
v_add_u32 v24,65536,v18
v_add_u32 v24,s92,v24
v_mfma_f32_16x16x32_bf16 v[92:95],a[28:31],a[92:95],v[92:95]
ds_read_b128 a[12:15],v16 offset:37376
v_add_f32 v40,1.0,v40
v_mfma_f32_16x16x32_bf16 a[168:171],a[28:31],a[96:99],a[168:171]
v_exp_f32 v41,v41
v_mfma_f32_16x16x32_bf16 a[172:175],a[28:31],a[100:103],a[172:175]
s_waitcnt lgkmcnt(3)
s_waitcnt vmcnt(24)
v_mul_f32 v42,v42,v98
v_mfma_f32_16x16x32_bf16 v[64:67],a[0:3],a[176:179],v[64:67]
s_waitcnt vmcnt(23)
ds_read_b128 a[16:19],v17 offset:4096
v_fma_f32 v43,v43,v20,v21
v_mfma_f32_16x16x32_bf16 a[112:115],a[0:3],a[180:183],a[112:115]
s_waitcnt vmcnt(22)
buffer_load_dwordx4 a[44:47],%[vL16],%[srdW],s80 offen offset:3072 sc1
v_mfma_f32_16x16x32_bf16 a[116:119],a[0:3],a[184:187],a[116:119]
s_waitcnt lgkmcnt(3)
s_add_u32 s80,s80,0x1000
v_mul_f32 v44,v100,v100
v_mfma_f32_16x16x32_bf16 v[68:71],a[4:7],a[176:179],v[68:71]
ds_read_b128 a[20:23],v17 offset:4608
v_mfma_f32_16x16x32_bf16 a[120:123],a[4:7],a[180:183],a[120:123]
v_rcp_f32 v40,v40
v_mfma_f32_16x16x32_bf16 a[124:127],a[4:7],a[184:187],a[124:127]
s_waitcnt lgkmcnt(3)
buffer_load_dwordx4 a[48:51],%[vL16],%[srdW],s80 offen offset:0 sc1
v_mfma_f32_16x16x32_bf16 v[72:75],a[8:11],a[176:179],v[72:75]
ds_read_b128 a[24:27],v17 offset:36864
v_add_f32 v41,1.0,v41
v_mfma_f32_16x16x32_bf16 a[128:131],a[8:11],a[180:183],a[128:131]
v_exp_f32 v42,v42
v_mfma_f32_16x16x32_bf16 a[132:135],a[8:11],a[184:187],a[132:135]
s_waitcnt lgkmcnt(3)
v_mul_f32 v43,v43,v99
v_fma_f32 v44,v44,v20,v21
v_mfma_f32_16x16x32_bf16 v[76:79],a[12:15],a[176:179],v[76:79]
ds_read_b128 a[28:31],v17 offset:37376
v_mul_f32 v45,v101,v101
v_mfma_f32_16x16x32_bf16 a[136:139],a[12:15],a[180:183],a[136:139]
buffer_load_dwordx4 a[52:55],%[vL16],%[srdW],s80 offen offset:1024 sc1
v_mfma_f32_16x16x32_bf16 a[140:143],a[12:15],a[184:187],a[140:143]
s_waitcnt lgkmcnt(3)
v_mul_f32 v40,v40,v96
v_mfma_f32_16x16x32_bf16 v[80:83],a[16:19],a[176:179],v[80:83]
ds_read_b128 a[0:3],v16 offset:6144
v_mfma_f32_16x16x32_bf16 a[144:147],a[16:19],a[180:183],a[144:147]
v_rcp_f32 v41,v41
v_mfma_f32_16x16x32_bf16 a[148:151],a[16:19],a[184:187],a[148:151]
s_waitcnt lgkmcnt(3)
buffer_store_dwordx4 v[224:227],v24,%[srdW],0 offen offset:0 sc1
v_mfma_f32_16x16x32_bf16 v[84:87],a[20:23],a[176:179],v[84:87]
ds_read_b128 a[4:7],v16 offset:6656
v_add_f32 v42,1.0,v42
v_mfma_f32_16x16x32_bf16 a[152:155],a[20:23],a[180:183],a[152:155]
v_exp_f32 v43,v43
v_mfma_f32_16x16x32_bf16 a[156:159],a[20:23],a[184:187],a[156:159]
s_waitcnt lgkmcnt(3)
v_mul_f32 v44,v44,v100
v_fma_f32 v45,v45,v20,v21
v_mfma_f32_16x16x32_bf16 v[88:91],a[24:27],a[176:179],v[88:91]
ds_read_b128 a[8:11],v16 offset:38912
v_mul_f32 v46,v102,v102
v_mfma_f32_16x16x32_bf16 a[160:163],a[24:27],a[180:183],a[160:163]
buffer_store_dwordx4 v[232:235],v24,%[srdW],0 offen offset:512 sc1
v_mfma_f32_16x16x32_bf16 a[164:167],a[24:27],a[184:187],a[164:167]
s_waitcnt lgkmcnt(3)
v_add_u32 v24,98304,v18
v_add_u32 v24,s92,v24
v_mfma_f32_16x16x32_bf16 v[92:95],a[28:31],a[176:179],v[92:95]
ds_read_b128 a[12:15],v16 offset:39424
v_mul_f32 v41,v41,v97
v_mfma_f32_16x16x32_bf16 a[168:171],a[28:31],a[180:183],a[168:171]
v_rcp_f32 v42,v42
v_mfma_f32_16x16x32_bf16 a[172:175],a[28:31],a[184:187],a[172:175]
s_waitcnt lgkmcnt(3)
s_waitcnt vmcnt(26)
v_add_f32 v43,1.0,v43
v_mfma_f32_16x16x32_bf16 v[64:67],a[0:3],a[188:191],v[64:67]
s_waitcnt vmcnt(25)
ds_read_b128 a[16:19],v17 offset:6144
v_mfma_f32_16x16x32_bf16 a[112:115],a[0:3],a[192:195],a[112:115]
s_waitcnt vmcnt(24)
buffer_load_dwordx4 a[56:59],%[vL16],%[srdW],s80 offen offset:2048 sc1
v_mfma_f32_16x16x32_bf16 a[116:119],a[0:3],a[196:199],a[116:119]
s_waitcnt lgkmcnt(3)
v_exp_f32 v44,v44
v_mfma_f32_16x16x32_bf16 v[68:71],a[4:7],a[188:191],v[68:71]
ds_read_b128 a[20:23],v17 offset:6656
v_mul_f32 v45,v45,v101
v_mfma_f32_16x16x32_bf16 a[120:123],a[4:7],a[192:195],a[120:123]
v_fma_f32 v46,v46,v20,v21
v_mul_f32 v47,v103,v103
v_mfma_f32_16x16x32_bf16 a[124:127],a[4:7],a[196:199],a[124:127]
s_waitcnt lgkmcnt(3)
buffer_load_dwordx4 a[60:63],%[vL16],%[srdW],s80 offen offset:3072 sc1
v_mfma_f32_16x16x32_bf16 v[72:75],a[8:11],a[188:191],v[72:75]
s_add_u32 s80,s80,0x1000
ds_read_b128 a[24:27],v17 offset:38912
v_mul_f32 v42,v42,v98
v_mfma_f32_16x16x32_bf16 a[128:131],a[8:11],a[192:195],a[128:131]
v_rcp_f32 v43,v43
v_mfma_f32_16x16x32_bf16 a[132:135],a[8:11],a[196:199],a[132:135]
s_waitcnt lgkmcnt(3)
v_add_f32 v44,1.0,v44
v_mfma_f32_16x16x32_bf16 v[76:79],a[12:15],a[188:191],v[76:79]
ds_read_b128 a[28:31],v17 offset:39424
v_mfma_f32_16x16x32_bf16 a[136:139],a[12:15],a[192:195],a[136:139]
buffer_load_dwordx4 a[64:67],%[vL16],%[srdW],s80 offen offset:0 sc1
v_mfma_f32_16x16x32_bf16 a[140:143],a[12:15],a[196:199],a[140:143]
s_waitcnt lgkmcnt(3)
v_exp_f32 v45,v45
v_mfma_f32_16x16x32_bf16 v[80:83],a[16:19],a[188:191],v[80:83]
ds_read_b128 a[0:3],v16 offset:8192
v_mul_f32 v46,v46,v102
v_mfma_f32_16x16x32_bf16 a[144:147],a[16:19],a[192:195],a[144:147]
v_fma_f32 v47,v47,v20,v21
v_mul_f32 v43,v43,v99
v_mfma_f32_16x16x32_bf16 a[148:151],a[16:19],a[196:199],a[148:151]
s_waitcnt lgkmcnt(3)
buffer_store_dwordx4 v[240:243],v24,%[srdW],0 offen offset:0 sc1
v_mfma_f32_16x16x32_bf16 v[84:87],a[20:23],a[188:191],v[84:87]
ds_read_b128 a[4:7],v16 offset:8704
v_mfma_f32_16x16x32_bf16 a[152:155],a[20:23],a[192:195],a[152:155]
v_rcp_f32 v44,v44
v_mfma_f32_16x16x32_bf16 a[156:159],a[20:23],a[196:199],a[156:159]
s_waitcnt lgkmcnt(3)
v_add_f32 v45,1.0,v45
v_mfma_f32_16x16x32_bf16 v[88:91],a[24:27],a[188:191],v[88:91]
ds_read_b128 a[8:11],v16 offset:40960
v_mfma_f32_16x16x32_bf16 a[160:163],a[24:27],a[192:195],a[160:163]
buffer_store_dwordx4 v[248:251],v24,%[srdW],0 offen offset:512 sc1
v_mfma_f32_16x16x32_bf16 a[164:167],a[24:27],a[196:199],a[164:167]
s_waitcnt lgkmcnt(3)
v_exp_f32 v46,v46
v_mfma_f32_16x16x32_bf16 v[92:95],a[28:31],a[188:191],v[92:95]
ds_read_b128 a[12:15],v16 offset:41472
v_mul_f32 v47,v47,v103
v_mfma_f32_16x16x32_bf16 a[168:171],a[28:31],a[192:195],a[168:171]
v_mul_f32 v44,v44,v100
v_mfma_f32_16x16x32_bf16 a[172:175],a[28:31],a[196:199],a[172:175]
s_waitcnt lgkmcnt(3)
s_waitcnt vmcnt(28)
)"
R"(v_mfma_f32_16x16x32_bf16 v[64:67],a[0:3],a[200:203],v[64:67]
s_waitcnt vmcnt(27)
ds_read_b128 a[16:19],v17 offset:8192
v_mfma_f32_16x16x32_bf16 a[112:115],a[0:3],a[204:207],a[112:115]
s_waitcnt vmcnt(26)
buffer_load_dwordx4 a[68:71],%[vL16],%[srdW],s80 offen offset:1024 sc1
v_mfma_f32_16x16x32_bf16 a[116:119],a[0:3],a[208:211],a[116:119]
s_waitcnt lgkmcnt(3)
v_rcp_f32 v45,v45
v_mfma_f32_16x16x32_bf16 v[68:71],a[4:7],a[200:203],v[68:71]
ds_read_b128 a[20:23],v17 offset:8704
v_add_f32 v46,1.0,v46
v_mfma_f32_16x16x32_bf16 a[120:123],a[4:7],a[204:207],a[120:123]
v_exp_f32 v47,v47
v_mfma_f32_16x16x32_bf16 a[124:127],a[4:7],a[208:211],a[124:127]
s_waitcnt lgkmcnt(3)
buffer_load_dwordx4 a[72:75],%[vL16],%[srdW],s80 offen offset:2048 sc1
v_mfma_f32_16x16x32_bf16 v[72:75],a[8:11],a[200:203],v[72:75]
ds_read_b128 a[24:27],v17 offset:40960
v_mul_f32 v45,v45,v101
v_mfma_f32_16x16x32_bf16 a[128:131],a[8:11],a[204:207],a[128:131]
v_rcp_f32 v46,v46
v_mfma_f32_16x16x32_bf16 a[132:135],a[8:11],a[208:211],a[132:135]
s_waitcnt lgkmcnt(3)
v_add_f32 v47,1.0,v47
v_mul_f32 v46,v46,v102
v_mfma_f32_16x16x32_bf16 v[76:79],a[12:15],a[200:203],v[76:79]
ds_read_b128 a[28:31],v17 offset:41472
v_mfma_f32_16x16x32_bf16 a[136:139],a[12:15],a[204:207],a[136:139]
buffer_load_dwordx4 a[76:79],%[vL16],%[srdW],s80 offen offset:3072 sc1
v_mfma_f32_16x16x32_bf16 a[140:143],a[12:15],a[208:211],a[140:143]
s_waitcnt lgkmcnt(3)
v_rcp_f32 v47,v47
v_mfma_f32_16x16x32_bf16 v[80:83],a[16:19],a[200:203],v[80:83]
ds_read_b128 a[0:3],v16 offset:10240
v_mul_f32 v47,v47,v103
v_mfma_f32_16x16x32_bf16 a[144:147],a[16:19],a[204:207],a[144:147]
v_cvt_pk_bf16_f32 v96,v40,v41
v_cvt_pk_bf16_f32 v97,v42,v43
v_mfma_f32_16x16x32_bf16 a[148:151],a[16:19],a[208:211],a[148:151]
s_waitcnt lgkmcnt(3)
v_cvt_pk_bf16_f32 v98,v44,v45
v_cvt_pk_bf16_f32 v99,v46,v47
v_mfma_f32_16x16x32_bf16 v[84:87],a[20:23],a[200:203],v[84:87]
ds_read_b128 a[4:7],v16 offset:10752
v_add_u32 v25,0,v22
v_mfma_f32_16x16x32_bf16 a[152:155],a[20:23],a[204:207],a[152:155]
buffer_store_dwordx4 v[96:99],v25,%[srdW],0 offen offset:1024 sc1
v_mfma_f32_16x16x32_bf16 a[156:159],a[20:23],a[208:211],a[156:159]
s_waitcnt lgkmcnt(3)
v_mul_f32 v40,v104,v104
v_fma_f32 v40,v40,v20,v21
v_mfma_f32_16x16x32_bf16 v[88:91],a[24:27],a[200:203],v[88:91]
ds_read_b128 a[8:11],v16 offset:43008
v_mul_f32 v41,v105,v105
v_mfma_f32_16x16x32_bf16 a[160:163],a[24:27],a[204:207],a[160:163]
v_mul_f32 v40,v40,v104
v_fma_f32 v41,v41,v20,v21
v_mfma_f32_16x16x32_bf16 a[164:167],a[24:27],a[208:211],a[164:167]
s_waitcnt lgkmcnt(3)
v_mul_f32 v42,v106,v106
v_mfma_f32_16x16x32_bf16 v[92:95],a[28:31],a[200:203],v[92:95]
ds_read_b128 a[12:15],v16 offset:43520
v_mfma_f32_16x16x32_bf16 a[168:171],a[28:31],a[204:207],a[168:171]
v_exp_f32 v40,v40
v_mfma_f32_16x16x32_bf16 a[172:175],a[28:31],a[208:211],a[172:175]
s_waitcnt lgkmcnt(3)
s_waitcnt vmcnt(29)
v_mul_f32 v41,v41,v105
v_mfma_f32_16x16x32_bf16 v[64:67],a[0:3],a[212:215],v[64:67]
s_waitcnt vmcnt(28)
ds_read_b128 a[16:19],v17 offset:10240
s_add_u32 s80,s80,0x1000
v_mfma_f32_16x16x32_bf16 a[112:115],a[0:3],a[216:219],a[112:115]
s_waitcnt vmcnt(27)
buffer_load_dwordx4 a[80:83],%[vL16],%[srdW],s80 offen offset:0 sc1
v_mfma_f32_16x16x32_bf16 a[116:119],a[0:3],a[220:223],a[116:119]
s_waitcnt lgkmcnt(3)
v_fma_f32 v42,v42,v20,v21
v_mul_f32 v43,v107,v107
v_mfma_f32_16x16x32_bf16 v[68:71],a[4:7],a[212:215],v[68:71]
ds_read_b128 a[20:23],v17 offset:10752
v_add_f32 v40,1.0,v40
v_mfma_f32_16x16x32_bf16 a[120:123],a[4:7],a[216:219],a[120:123]
v_exp_f32 v41,v41
v_mfma_f32_16x16x32_bf16 a[124:127],a[4:7],a[220:223],a[124:127]
s_waitcnt lgkmcnt(3)
buffer_load_dwordx4 a[84:87],%[vL16],%[srdW],s80 offen offset:1024 sc1
v_mfma_f32_16x16x32_bf16 v[72:75],a[8:11],a[212:215],v[72:75]
ds_read_b128 a[24:27],v17 offset:43008
v_mul_f32 v42,v42,v106
v_mfma_f32_16x16x32_bf16 a[128:131],a[8:11],a[216:219],a[128:131]
v_fma_f32 v43,v43,v20,v21
v_mul_f32 v44,v108,v108
v_mfma_f32_16x16x32_bf16 a[132:135],a[8:11],a[220:223],a[132:135]
s_waitcnt lgkmcnt(3)
v_rcp_f32 v40,v40
v_mfma_f32_16x16x32_bf16 v[76:79],a[12:15],a[212:215],v[76:79]
ds_read_b128 a[28:31],v17 offset:43520
v_add_f32 v41,1.0,v41
v_mfma_f32_16x16x32_bf16 a[136:139],a[12:15],a[216:219],a[136:139]
buffer_load_dwordx4 a[88:91],%[vL16],%[srdW],s80 offen offset:2048 sc1
v_mfma_f32_16x16x32_bf16 a[140:143],a[12:15],a[220:223],a[140:143]
s_waitcnt lgkmcnt(3)
v_exp_f32 v42,v42
v_mfma_f32_16x16x32_bf16 v[80:83],a[16:19],a[212:215],v[80:83]
ds_read_b128 a[0:3],v16 offset:12288
v_mul_f32 v43,v43,v107
v_mfma_f32_16x16x32_bf16 a[144:147],a[16:19],a[216:219],a[144:147]
v_fma_f32 v44,v44,v20,v21
v_mul_f32 v45,v109,v109
v_mfma_f32_16x16x32_bf16 a[148:151],a[16:19],a[220:223],a[148:151]
s_waitcnt lgkmcnt(3)
v_mul_f32 v40,v40,v104
v_mfma_f32_16x16x32_bf16 v[84:87],a[20:23],a[212:215],v[84:87]
ds_read_b128 a[4:7],v16 offset:12800
v_mfma_f32_16x16x32_bf16 a[152:155],a[20:23],a[216:219],a[152:155]
v_rcp_f32 v41,v41
v_mfma_f32_16x16x32_bf16 a[156:159],a[20:23],a[220:223],a[156:159]
s_waitcnt lgkmcnt(3)
v_add_f32 v42,1.0,v42
v_mfma_f32_16x16x32_bf16 v[88:91],a[24:27],a[212:215],v[88:91]
ds_read_b128 a[8:11],v16 offset:45056
v_mfma_f32_16x16x32_bf16 a[160:163],a[24:27],a[216:219],a[160:163]
v_exp_f32 v43,v43
v_mfma_f32_16x16x32_bf16 a[164:167],a[24:27],a[220:223],a[164:167]
s_waitcnt lgkmcnt(3)
v_mul_f32 v44,v44,v108
v_fma_f32 v45,v45,v20,v21
v_mfma_f32_16x16x32_bf16 v[92:95],a[28:31],a[212:215],v[92:95]
ds_read_b128 a[12:15],v16 offset:45568
v_mul_f32 v46,v110,v110
v_mfma_f32_16x16x32_bf16 a[168:171],a[28:31],a[216:219],a[168:171]
v_mul_f32 v41,v41,v105
v_mfma_f32_16x16x32_bf16 a[172:175],a[28:31],a[220:223],a[172:175]
s_waitcnt lgkmcnt(3)
s_waitcnt vmcnt(29)
s_waitcnt vmcnt(7)
s_barrier
v_mov_b32 v26,s55
v_mov_b32 v27,s89
buffer_store_dword v26,v27,%[srdW],0 offen sc1
v_mfma_f32_16x16x32_bf16 v[64:67],a[0:3],a[224:227],v[64:67]
ds_read_b128 a[16:19],v17 offset:12288
v_mfma_f32_16x16x32_bf16 a[112:115],a[0:3],a[228:231],a[112:115]
v_rcp_f32 v42,v42
v_mfma_f32_16x16x32_bf16 a[116:119],a[0:3],a[232:235],a[116:119]
s_waitcnt lgkmcnt(3)
v_add_f32 v43,1.0,v43
v_mfma_f32_16x16x32_bf16 v[68:71],a[4:7],a[224:227],v[68:71]
ds_read_b128 a[20:23],v17 offset:12800
v_mfma_f32_16x16x32_bf16 a[120:123],a[4:7],a[228:231],a[120:123]
buffer_load_dwordx4 a[92:95],%[vL16],%[srdW],s80 offen offset:3072 sc1
v_mfma_f32_16x16x32_bf16 a[124:127],a[4:7],a[232:235],a[124:127]
s_waitcnt lgkmcnt(3)
s_add_u32 s80,s80,0x1000
v_mfma_f32_16x16x32_bf16 v[72:75],a[8:11],a[224:227],v[72:75]
ds_read_b128 a[24:27],v17 offset:45056
v_mfma_f32_16x16x32_bf16 a[128:131],a[8:11],a[228:231],a[128:131]
v_exp_f32 v44,v44
v_mfma_f32_16x16x32_bf16 a[132:135],a[8:11],a[232:235],a[132:135]
s_waitcnt lgkmcnt(3)
buffer_load_dwordx4 a[96:99],%[vL16],%[srdW],s80 offen offset:0 sc1
v_mfma_f32_16x16x32_bf16 v[76:79],a[12:15],a[224:227],v[76:79]
ds_read_b128 a[28:31],v17 offset:45568
v_mul_f32 v45,v45,v109
v_mfma_f32_16x16x32_bf16 a[136:139],a[12:15],a[228:231],a[136:139]
v_fma_f32 v46,v46,v20,v21
v_mul_f32 v47,v111,v111
v_mfma_f32_16x16x32_bf16 a[140:143],a[12:15],a[232:235],a[140:143]
s_waitcnt lgkmcnt(3)
v_mul_f32 v42,v42,v106
v_mfma_f32_16x16x32_bf16 v[80:83],a[16:19],a[224:227],v[80:83]
ds_read_b128 a[0:3],v16 offset:14336
v_mfma_f32_16x16x32_bf16 a[144:147],a[16:19],a[228:231],a[144:147]
buffer_load_dwordx4 a[100:103],%[vL16],%[srdW],s80 offen offset:1024 sc1
v_mfma_f32_16x16x32_bf16 a[148:151],a[16:19],a[232:235],a[148:151]
s_waitcnt lgkmcnt(3)
v_rcp_f32 v43,v43
v_mfma_f32_16x16x32_bf16 v[84:87],a[20:23],a[224:227],v[84:87]
ds_read_b128 a[4:7],v16 offset:14848
v_add_f32 v44,1.0,v44
v_mfma_f32_16x16x32_bf16 a[152:155],a[20:23],a[228:231],a[152:155]
v_exp_f32 v45,v45
v_mfma_f32_16x16x32_bf16 a[156:159],a[20:23],a[232:235],a[156:159]
s_waitcnt lgkmcnt(3)
v_mul_f32 v46,v46,v110
v_fma_f32 v47,v47,v20,v21
v_mfma_f32_16x16x32_bf16 v[88:91],a[24:27],a[224:227],v[88:91]
ds_read_b128 a[8:11],v16 offset:47104
v_mul_f32 v43,v43,v107
v_mfma_f32_16x16x32_bf16 a[160:163],a[24:27],a[228:231],a[160:163]
v_rcp_f32 v44,v44
v_mfma_f32_16x16x32_bf16 a[164:167],a[24:27],a[232:235],a[164:167]
s_waitcnt lgkmcnt(3)
v_add_f32 v45,1.0,v45
v_mfma_f32_16x16x32_bf16 v[92:95],a[28:31],a[224:227],v[92:95]
ds_read_b128 a[12:15],v16 offset:47616
v_mfma_f32_16x16x32_bf16 a[168:171],a[28:31],a[228:231],a[168:171]
v_exp_f32 v46,v46
v_mfma_f32_16x16x32_bf16 a[172:175],a[28:31],a[232:235],a[172:175]
s_waitcnt lgkmcnt(3)
v_mul_f32 v47,v47,v111
v_mul_f32 v44,v44,v108
v_mfma_f32_16x16x32_bf16 v[64:67],a[0:3],a[236:239],v[64:67]
ds_read_b128 a[16:19],v17 offset:14336
v_mfma_f32_16x16x32_bf16 a[112:115],a[0:3],a[240:243],a[112:115]
buffer_load_dwordx4 a[176:179],%[vL16],%[srdW],s80 offen offset:2048 sc1
v_mfma_f32_16x16x32_bf16 a[116:119],a[0:3],a[244:247],a[116:119]
s_waitcnt lgkmcnt(3)
v_rcp_f32 v45,v45
v_mfma_f32_16x16x32_bf16 v[68:71],a[4:7],a[236:239],v[68:71]
ds_read_b128 a[20:23],v17 offset:14848
v_add_f32 v46,1.0,v46
v_mfma_f32_16x16x32_bf16 a[120:123],a[4:7],a[240:243],a[120:123]
v_exp_f32 v47,v47
v_mfma_f32_16x16x32_bf16 a[124:127],a[4:7],a[244:247],a[124:127]
s_waitcnt lgkmcnt(3)
buffer_load_dwordx4 a[180:183],%[vL16],%[srdW],s80 offen offset:3072 sc1
v_mfma_f32_16x16x32_bf16 v[72:75],a[8:11],a[236:239],v[72:75]
s_add_u32 s80,s80,0x1000
ds_read_b128 a[24:27],v17 offset:47104
v_mul_f32 v45,v45,v109
v_mfma_f32_16x16x32_bf16 a[128:131],a[8:11],a[240:243],a[128:131]
v_rcp_f32 v46,v46
v_mfma_f32_16x16x32_bf16 a[132:135],a[8:11],a[244:247],a[132:135]
s_waitcnt lgkmcnt(3)
v_add_f32 v47,1.0,v47
v_mul_f32 v46,v46,v110
v_mfma_f32_16x16x32_bf16 v[76:79],a[12:15],a[236:239],v[76:79]
ds_read_b128 a[28:31],v17 offset:47616
v_mfma_f32_16x16x32_bf16 a[136:139],a[12:15],a[240:243],a[136:139]
buffer_load_dwordx4 a[184:187],%[vL16],%[srdW],s80 offen offset:0 sc1
v_mfma_f32_16x16x32_bf16 a[140:143],a[12:15],a[244:247],a[140:143]
s_waitcnt lgkmcnt(3)
v_rcp_f32 v47,v47
v_mfma_f32_16x16x32_bf16 v[80:83],a[16:19],a[236:239],v[80:83]
ds_read_b128 a[0:3],v16 offset:16384
v_mul_f32 v47,v47,v111
v_mfma_f32_16x16x32_bf16 a[144:147],a[16:19],a[240:243],a[144:147]
v_cvt_pk_bf16_f32 v104,v40,v41
v_cvt_pk_bf16_f32 v105,v42,v43
v_mfma_f32_16x16x32_bf16 a[148:151],a[16:19],a[244:247],a[148:151]
s_waitcnt lgkmcnt(3)
v_cvt_pk_bf16_f32 v106,v44,v45
v_cvt_pk_bf16_f32 v107,v46,v47
v_mfma_f32_16x16x32_bf16 v[84:87],a[20:23],a[236:239],v[84:87]
ds_read_b128 a[4:7],v16 offset:16896
v_add_u32 v25,2048,v22
v_mfma_f32_16x16x32_bf16 a[152:155],a[20:23],a[240:243],a[152:155]
v_add_u32 v25,s92,v25
v_mul_f32 v40,v112,v112
v_mfma_f32_16x16x32_bf16 a[156:159],a[20:23],a[244:247],a[156:159]
s_waitcnt lgkmcnt(3)
buffer_store_dwordx4 v[104:107],v25,%[srdW],0 offen offset:1024 sc1
v_mfma_f32_16x16x32_bf16 v[88:91],a[24:27],a[236:239],v[88:91]
ds_read_b128 a[8:11],v16 offset:49152
v_fma_f32 v40,v40,v20,v21
v_mfma_f32_16x16x32_bf16 a[160:163],a[24:27],a[240:243],a[160:163]
v_mul_f32 v41,v113,v113
v_mul_f32 v40,v40,v112
v_mfma_f32_16x16x32_bf16 a[164:167],a[24:27],a[244:247],a[164:167]
s_waitcnt lgkmcnt(3)
v_fma_f32 v41,v41,v20,v21
v_mul_f32 v42,v114,v114
v_mfma_f32_16x16x32_bf16 v[92:95],a[28:31],a[236:239],v[92:95]
ds_read_b128 a[12:15],v16 offset:49664
v_mfma_f32_16x16x32_bf16 a[168:171],a[28:31],a[240:243],a[168:171]
v_exp_f32 v40,v40
v_mfma_f32_16x16x32_bf16 a[172:175],a[28:31],a[244:247],a[172:175]
s_waitcnt lgkmcnt(3)
v_mul_f32 v41,v41,v113
v_fma_f32 v42,v42,v20,v21
v_mfma_f32_16x16x32_bf16 v[64:67],a[0:3],a[32:35],v[64:67]
ds_read_b128 a[16:19],v17 offset:16384
v_mul_f32 v43,v115,v115
v_mfma_f32_16x16x32_bf16 a[112:115],a[0:3],a[36:39],a[112:115]
buffer_load_dwordx4 a[188:191],%[vL16],%[srdW],s80 offen offset:1024 sc1
v_mfma_f32_16x16x32_bf16 a[116:119],a[0:3],a[40:43],a[116:119]
s_waitcnt lgkmcnt(3)
v_add_f32 v40,1.0,v40
v_mfma_f32_16x16x32_bf16 v[68:71],a[4:7],a[32:35],v[68:71]
ds_read_b128 a[20:23],v17 offset:16896
v_mfma_f32_16x16x32_bf16 a[120:123],a[4:7],a[36:39],a[120:123]
v_exp_f32 v41,v41
v_mfma_f32_16x16x32_bf16 a[124:127],a[4:7],a[40:43],a[124:127]
s_waitcnt lgkmcnt(3)
buffer_load_dwordx4 a[192:195],%[vL16],%[srdW],s80 offen offset:2048 sc1
v_mfma_f32_16x16x32_bf16 v[72:75],a[8:11],a[32:35],v[72:75]
ds_read_b128 a[24:27],v17 offset:49152
v_mul_f32 v42,v42,v114
v_mfma_f32_16x16x32_bf16 a[128:131],a[8:11],a[36:39],a[128:131]
v_fma_f32 v43,v43,v20,v21
v_mul_f32 v44,v116,v116
v_mfma_f32_16x16x32_bf16 a[132:135],a[8:11],a[40:43],a[132:135]
s_waitcnt lgkmcnt(3)
v_rcp_f32 v40,v40
v_mfma_f32_16x16x32_bf16 v[76:79],a[12:15],a[32:35],v[76:79]
ds_read_b128 a[28:31],v17 offset:49664
v_add_f32 v41,1.0,v41
v_mfma_f32_16x16x32_bf16 a[136:139],a[12:15],a[36:39],a[136:139]
buffer_load_dwordx4 a[196:199],%[vL16],%[srdW],s80 offen offset:3072 sc1
v_mfma_f32_16x16x32_bf16 a[140:143],a[12:15],a[40:43],a[140:143]
s_waitcnt lgkmcnt(3)
v_exp_f32 v42,v42
v_mfma_f32_16x16x32_bf16 v[80:83],a[16:19],a[32:35],v[80:83]
ds_read_b128 a[0:3],v16 offset:18432
v_mul_f32 v43,v43,v115
v_mfma_f32_16x16x32_bf16 a[144:147],a[16:19],a[36:39],a[144:147]
v_fma_f32 v44,v44,v20,v21
v_mul_f32 v45,v117,v117
v_mfma_f32_16x16x32_bf16 a[148:151],a[16:19],a[40:43],a[148:151]
s_waitcnt lgkmcnt(3)
v_mul_f32 v40,v40,v112
v_mfma_f32_16x16x32_bf16 v[84:87],a[20:23],a[32:35],v[84:87]
ds_read_b128 a[4:7],v16 offset:18944
v_mfma_f32_16x16x32_bf16 a[152:155],a[20:23],a[36:39],a[152:155]
v_rcp_f32 v41,v41
v_mfma_f32_16x16x32_bf16 a[156:159],a[20:23],a[40:43],a[156:159]
s_waitcnt lgkmcnt(3)
v_add_f32 v42,1.0,v42
v_mfma_f32_16x16x32_bf16 v[88:91],a[24:27],a[32:35],v[88:91]
ds_read_b128 a[8:11],v16 offset:51200
v_mfma_f32_16x16x32_bf16 a[160:163],a[24:27],a[36:39],a[160:163]
v_exp_f32 v43,v43
v_mfma_f32_16x16x32_bf16 a[164:167],a[24:27],a[40:43],a[164:167]
s_waitcnt lgkmcnt(3)
v_mul_f32 v44,v44,v116
v_fma_f32 v45,v45,v20,v21
v_mfma_f32_16x16x32_bf16 v[92:95],a[28:31],a[32:35],v[92:95]
ds_read_b128 a[12:15],v16 offset:51712
v_mul_f32 v46,v118,v118
v_mfma_f32_16x16x32_bf16 a[168:171],a[28:31],a[36:39],a[168:171]
v_mul_f32 v41,v41,v113
v_mfma_f32_16x16x32_bf16 a[172:175],a[28:31],a[40:43],a[172:175]
s_waitcnt lgkmcnt(3)
v_rcp_f32 v42,v42
v_mfma_f32_16x16x32_bf16 v[64:67],a[0:3],a[44:47],v[64:67]
ds_read_b128 a[16:19],v17 offset:18432
v_add_f32 v43,1.0,v43
v_mfma_f32_16x16x32_bf16 a[112:115],a[0:3],a[48:51],a[112:115]
buffer_load_dwordx2 v[50:51],v48,%[srdW],0 offen sc1
v_mfma_f32_16x16x32_bf16 a[116:119],a[0:3],a[52:55],a[116:119]
s_waitcnt lgkmcnt(3)
v_exp_f32 v44,v44
v_mfma_f32_16x16x32_bf16 v[68:71],a[4:7],a[44:47],v[68:71]
ds_read_b128 a[20:23],v17 offset:18944
v_mul_f32 v45,v45,v117
v_mfma_f32_16x16x32_bf16 a[120:123],a[4:7],a[48:51],a[120:123]
v_fma_f32 v46,v46,v20,v21
v_mul_f32 v47,v119,v119
v_mfma_f32_16x16x32_bf16 a[124:127],a[4:7],a[52:55],a[124:127]
s_waitcnt lgkmcnt(3)
buffer_load_dword v15,v14,%[srdW],0 offen sc1
v_mfma_f32_16x16x32_bf16 v[72:75],a[8:11],a[44:47],v[72:75]
ds_read_b128 a[24:27],v17 offset:51200
v_mul_f32 v42,v42,v114
v_mfma_f32_16x16x32_bf16 a[128:131],a[8:11],a[48:51],a[128:131]
v_rcp_f32 v43,v43
v_mfma_f32_16x16x32_bf16 a[132:135],a[8:11],a[52:55],a[132:135]
s_waitcnt lgkmcnt(3)
v_add_f32 v44,1.0,v44
v_mfma_f32_16x16x32_bf16 v[76:79],a[12:15],a[44:47],v[76:79]
ds_read_b128 a[28:31],v17 offset:51712
v_mfma_f32_16x16x32_bf16 a[136:139],a[12:15],a[48:51],a[136:139]
v_exp_f32 v45,v45
v_mfma_f32_16x16x32_bf16 a[140:143],a[12:15],a[52:55],a[140:143]
s_waitcnt lgkmcnt(3)
v_mul_f32 v46,v46,v118
v_fma_f32 v47,v47,v20,v21
v_mfma_f32_16x16x32_bf16 v[80:83],a[16:19],a[44:47],v[80:83]
ds_read_b128 a[0:3],v16 offset:20480
v_mul_f32 v43,v43,v115
v_mfma_f32_16x16x32_bf16 a[144:147],a[16:19],a[48:51],a[144:147]
v_rcp_f32 v44,v44
v_mfma_f32_16x16x32_bf16 a[148:151],a[16:19],a[52:55],a[148:151]
s_waitcnt lgkmcnt(3)
v_add_f32 v45,1.0,v45
v_mfma_f32_16x16x32_bf16 v[84:87],a[20:23],a[44:47],v[84:87]
ds_read_b128 a[4:7],v16 offset:20992
v_mfma_f32_16x16x32_bf16 a[152:155],a[20:23],a[48:51],a[152:155]
v_exp_f32 v46,v46
v_mfma_f32_16x16x32_bf16 a[156:159],a[20:23],a[52:55],a[156:159]
s_waitcnt lgkmcnt(3)
v_mul_f32 v47,v47,v119
v_mul_f32 v44,v44,v116
v_mfma_f32_16x16x32_bf16 v[88:91],a[24:27],a[44:47],v[88:91]
ds_read_b128 a[8:11],v16 offset:53248
v_mfma_f32_16x16x32_bf16 a[160:163],a[24:27],a[48:51],a[160:163]
v_rcp_f32 v45,v45
v_mfma_f32_16x16x32_bf16 a[164:167],a[24:27],a[52:55],a[164:167]
s_waitcnt lgkmcnt(3)
v_add_f32 v46,1.0,v46
v_mfma_f32_16x16x32_bf16 v[92:95],a[28:31],a[44:47],v[92:95]
ds_read_b128 a[12:15],v16 offset:53760
v_mfma_f32_16x16x32_bf16 a[168:171],a[28:31],a[48:51],a[168:171]
v_exp_f32 v47,v47
v_mfma_f32_16x16x32_bf16 a[172:175],a[28:31],a[52:55],a[172:175]
s_waitcnt lgkmcnt(3)
v_mul_f32 v45,v45,v117
)"
R"(v_mfma_f32_16x16x32_bf16 v[64:67],a[0:3],a[56:59],v[64:67]
ds_read_b128 a[16:19],v17 offset:20480
v_mfma_f32_16x16x32_bf16 a[112:115],a[0:3],a[60:63],a[112:115]
v_rcp_f32 v46,v46
v_mfma_f32_16x16x32_bf16 a[116:119],a[0:3],a[64:67],a[116:119]
s_waitcnt lgkmcnt(3)
v_add_f32 v47,1.0,v47
v_mul_f32 v46,v46,v118
v_mfma_f32_16x16x32_bf16 v[68:71],a[4:7],a[56:59],v[68:71]
ds_read_b128 a[20:23],v17 offset:20992
v_mfma_f32_16x16x32_bf16 a[120:123],a[4:7],a[60:63],a[120:123]
v_rcp_f32 v47,v47
v_mfma_f32_16x16x32_bf16 a[124:127],a[4:7],a[64:67],a[124:127]
s_waitcnt lgkmcnt(3)
v_mul_f32 v47,v47,v119
v_cvt_pk_bf16_f32 v112,v40,v41
v_mfma_f32_16x16x32_bf16 v[72:75],a[8:11],a[56:59],v[72:75]
ds_read_b128 a[24:27],v17 offset:53248
v_cvt_pk_bf16_f32 v113,v42,v43
v_mfma_f32_16x16x32_bf16 a[128:131],a[8:11],a[60:63],a[128:131]
v_cvt_pk_bf16_f32 v114,v44,v45
v_cvt_pk_bf16_f32 v115,v46,v47
v_mfma_f32_16x16x32_bf16 a[132:135],a[8:11],a[64:67],a[132:135]
s_waitcnt lgkmcnt(3)
v_add_u32 v25,4096,v23
v_add_u32 v25,s92,v25
v_mfma_f32_16x16x32_bf16 v[76:79],a[12:15],a[56:59],v[76:79]
ds_read_b128 a[28:31],v17 offset:53760
v_mul_f32 v40,v120,v120
v_mfma_f32_16x16x32_bf16 a[136:139],a[12:15],a[60:63],a[136:139]
buffer_store_dwordx4 v[112:115],v25,%[srdW],0 offen offset:1024 sc1
v_mfma_f32_16x16x32_bf16 a[140:143],a[12:15],a[64:67],a[140:143]
s_waitcnt lgkmcnt(3)
v_fma_f32 v40,v40,v20,v21
v_mul_f32 v41,v121,v121
v_mfma_f32_16x16x32_bf16 v[80:83],a[16:19],a[56:59],v[80:83]
ds_read_b128 a[0:3],v16 offset:22528
v_mul_f32 v40,v40,v120
v_mfma_f32_16x16x32_bf16 a[144:147],a[16:19],a[60:63],a[144:147]
v_fma_f32 v41,v41,v20,v21
v_mul_f32 v42,v122,v122
v_mfma_f32_16x16x32_bf16 a[148:151],a[16:19],a[64:67],a[148:151]
s_waitcnt lgkmcnt(3)
v_exp_f32 v40,v40
v_mfma_f32_16x16x32_bf16 v[84:87],a[20:23],a[56:59],v[84:87]
ds_read_b128 a[4:7],v16 offset:23040
v_mul_f32 v41,v41,v121
v_mfma_f32_16x16x32_bf16 a[152:155],a[20:23],a[60:63],a[152:155]
v_fma_f32 v42,v42,v20,v21
v_mul_f32 v43,v123,v123
v_mfma_f32_16x16x32_bf16 a[156:159],a[20:23],a[64:67],a[156:159]
s_waitcnt lgkmcnt(3)
v_add_f32 v40,1.0,v40
v_mfma_f32_16x16x32_bf16 v[88:91],a[24:27],a[56:59],v[88:91]
ds_read_b128 a[8:11],v16 offset:55296
v_mfma_f32_16x16x32_bf16 a[160:163],a[24:27],a[60:63],a[160:163]
v_exp_f32 v41,v41
v_mfma_f32_16x16x32_bf16 a[164:167],a[24:27],a[64:67],a[164:167]
s_waitcnt lgkmcnt(3)
v_mul_f32 v42,v42,v122
v_fma_f32 v43,v43,v20,v21
v_mfma_f32_16x16x32_bf16 v[92:95],a[28:31],a[56:59],v[92:95]
ds_read_b128 a[12:15],v16 offset:55808
v_mul_f32 v44,v124,v124
v_mfma_f32_16x16x32_bf16 a[168:171],a[28:31],a[60:63],a[168:171]
v_rcp_f32 v40,v40
v_mfma_f32_16x16x32_bf16 a[172:175],a[28:31],a[64:67],a[172:175]
s_waitcnt lgkmcnt(3)
s_waitcnt vmcnt(20)
v_add_f32 v41,1.0,v41
s_waitcnt vmcnt(1)
v_cmp_ne_u32 vcc,s55,v15
s_nop 0
s_and_b32 vcc_lo,vcc_lo,s91
s_cbranch_scc0 SPB2_%=
s_mov_b32 s97,0x40000
POLL2_%=:
buffer_load_dword v15,v14,%[srdW],0 offen sc1
s_waitcnt vmcnt(0)
v_cmp_ne_u32 vcc,s55,v15
s_cbranch_vccz GO2_%=
s_sub_u32 s97,s97,1
s_cbranch_scc1 GO2_%=
s_sleep 4
s_branch POLL2_%=
GO2_%=:
SPB2_%=:
s_mov_b32 s97,0x40000
PL1_%=:
v_cmp_ne_u64 vcc,s[78:79],v[50:51]
s_cbranch_vccz GO1_%=
s_sub_u32 s97,s97,1
s_cbranch_scc1 GO1_%=
s_sleep 2
buffer_load_dwordx2 v[50:51],v48,%[srdW],0 offen sc1
s_waitcnt vmcnt(0)
s_branch PL1_%=
GO1_%=:
s_barrier
buffer_store_dwordx2 v[52:53],v49,%[srdW],0 offen sc1
v_mfma_f32_16x16x32_bf16 v[64:67],a[0:3],a[68:71],v[64:67]
s_mov_b32 s67,s42
ds_read_b128 a[16:19],v17 offset:22528
v_mfma_f32_16x16x32_bf16 a[112:115],a[0:3],a[72:75],a[112:115]
v_exp_f32 v42,v42
v_mfma_f32_16x16x32_bf16 a[116:119],a[0:3],a[76:79],a[116:119]
s_waitcnt lgkmcnt(3)
v_mul_f32 v43,v43,v123
v_fma_f32 v44,v44,v20,v21
v_mfma_f32_16x16x32_bf16 v[68:71],a[4:7],a[68:71],v[68:71]
ds_read_b128 a[20:23],v17 offset:23040
v_mul_f32 v45,v125,v125
v_mfma_f32_16x16x32_bf16 a[120:123],a[4:7],a[72:75],a[120:123]
buffer_load_dwordx4 v[128:131],v2,%[srdW],s67 offen offset:0 sc1
v_mfma_f32_16x16x32_bf16 a[124:127],a[4:7],a[76:79],a[124:127]
s_waitcnt lgkmcnt(3)
v_mul_f32 v40,v40,v120
v_mfma_f32_16x16x32_bf16 v[72:75],a[8:11],a[68:71],v[72:75]
ds_read_b128 a[24:27],v17 offset:55296
v_mfma_f32_16x16x32_bf16 a[128:131],a[8:11],a[72:75],a[128:131]
v_rcp_f32 v41,v41
v_mfma_f32_16x16x32_bf16 a[132:135],a[8:11],a[76:79],a[132:135]
s_waitcnt lgkmcnt(3)
buffer_load_dwordx4 v[132:135],v2,%[srdW],s67 offen offset:512 sc1
v_mfma_f32_16x16x32_bf16 v[76:79],a[12:15],a[68:71],v[76:79]
ds_read_b128 a[28:31],v17 offset:55808
v_add_f32 v42,1.0,v42
v_mfma_f32_16x16x32_bf16 a[136:139],a[12:15],a[72:75],a[136:139]
v_exp_f32 v43,v43
v_mfma_f32_16x16x32_bf16 a[140:143],a[12:15],a[76:79],a[140:143]
s_waitcnt lgkmcnt(3)
v_mul_f32 v44,v44,v124
v_fma_f32 v45,v45,v20,v21
v_mfma_f32_16x16x32_bf16 v[80:83],a[16:19],a[68:71],v[80:83]
ds_read_b128 a[0:3],v16 offset:24576
v_mul_f32 v46,v126,v126
v_mfma_f32_16x16x32_bf16 a[144:147],a[16:19],a[72:75],a[144:147]
buffer_load_dwordx4 v[136:139],v2,%[srdW],s67 offen offset:2048 sc1
v_mfma_f32_16x16x32_bf16 a[148:151],a[16:19],a[76:79],a[148:151]
s_waitcnt lgkmcnt(3)
v_mul_f32 v41,v41,v121
v_mfma_f32_16x16x32_bf16 v[84:87],a[20:23],a[68:71],v[84:87]
ds_read_b128 a[4:7],v16 offset:25088
v_mfma_f32_16x16x32_bf16 a[152:155],a[20:23],a[72:75],a[152:155]
v_rcp_f32 v42,v42
v_mfma_f32_16x16x32_bf16 a[156:159],a[20:23],a[76:79],a[156:159]
s_waitcnt lgkmcnt(3)
buffer_load_dwordx4 v[140:143],v2,%[srdW],s67 offen offset:2560 sc1
v_mfma_f32_16x16x32_bf16 v[88:91],a[24:27],a[68:71],v[88:91]
s_add_u32 s67,s67,0x1000
ds_read_b128 a[8:11],v16 offset:57344
v_add_f32 v43,1.0,v43
v_mfma_f32_16x16x32_bf16 a[160:163],a[24:27],a[72:75],a[160:163]
v_exp_f32 v44,v44
v_mfma_f32_16x16x32_bf16 a[164:167],a[24:27],a[76:79],a[164:167]
s_waitcnt lgkmcnt(3)
v_mul_f32 v45,v45,v125
v_fma_f32 v46,v46,v20,v21
v_mfma_f32_16x16x32_bf16 v[92:95],a[28:31],a[68:71],v[92:95]
ds_read_b128 a[12:15],v16 offset:57856
v_mul_f32 v47,v127,v127
v_mfma_f32_16x16x32_bf16 a[168:171],a[28:31],a[72:75],a[168:171]
buffer_load_dwordx4 v[144:147],v2,%[srdW],s67 offen offset:0 sc1
v_mfma_f32_16x16x32_bf16 a[172:175],a[28:31],a[76:79],a[172:175]
s_waitcnt lgkmcnt(3)
v_mul_f32 v42,v42,v122
v_mfma_f32_16x16x32_bf16 v[64:67],a[0:3],a[80:83],v[64:67]
ds_read_b128 a[16:19],v17 offset:24576
v_mfma_f32_16x16x32_bf16 a[112:115],a[0:3],a[84:87],a[112:115]
v_rcp_f32 v43,v43
v_mfma_f32_16x16x32_bf16 a[116:119],a[0:3],a[88:91],a[116:119]
s_waitcnt lgkmcnt(3)
buffer_load_dwordx4 v[148:151],v2,%[srdW],s67 offen offset:512 sc1
v_mfma_f32_16x16x32_bf16 v[68:71],a[4:7],a[80:83],v[68:71]
ds_read_b128 a[20:23],v17 offset:25088
v_add_f32 v44,1.0,v44
v_mfma_f32_16x16x32_bf16 a[120:123],a[4:7],a[84:87],a[120:123]
v_exp_f32 v45,v45
v_mfma_f32_16x16x32_bf16 a[124:127],a[4:7],a[88:91],a[124:127]
s_waitcnt lgkmcnt(3)
v_mul_f32 v46,v46,v126
v_fma_f32 v47,v47,v20,v21
v_mfma_f32_16x16x32_bf16 v[72:75],a[8:11],a[80:83],v[72:75]
ds_read_b128 a[24:27],v17 offset:57344
v_mul_f32 v43,v43,v123
v_mfma_f32_16x16x32_bf16 a[128:131],a[8:11],a[84:87],a[128:131]
buffer_load_dwordx4 v[152:155],v2,%[srdW],s67 offen offset:2048 sc1
v_mfma_f32_16x16x32_bf16 a[132:135],a[8:11],a[88:91],a[132:135]
s_waitcnt lgkmcnt(3)
v_rcp_f32 v44,v44
v_mfma_f32_16x16x32_bf16 v[76:79],a[12:15],a[80:83],v[76:79]
ds_read_b128 a[28:31],v17 offset:57856
v_add_f32 v45,1.0,v45
v_mfma_f32_16x16x32_bf16 a[136:139],a[12:15],a[84:87],a[136:139]
v_exp_f32 v46,v46
v_mfma_f32_16x16x32_bf16 a[140:143],a[12:15],a[88:91],a[140:143]
s_waitcnt lgkmcnt(3)
buffer_load_dwordx4 v[156:159],v2,%[srdW],s67 offen offset:2560 sc1
v_mfma_f32_16x16x32_bf16 v[80:83],a[16:19],a[80:83],v[80:83]
s_add_u32 s67,s67,0x1000
ds_read_b128 a[0:3],v16 offset:26624
v_mul_f32 v47,v47,v127
v_mfma_f32_16x16x32_bf16 a[144:147],a[16:19],a[84:87],a[144:147]
v_mul_f32 v44,v44,v124
v_mfma_f32_16x16x32_bf16 a[148:151],a[16:19],a[88:91],a[148:151]
s_waitcnt lgkmcnt(3)
v_rcp_f32 v45,v45
v_mfma_f32_16x16x32_bf16 v[84:87],a[20:23],a[80:83],v[84:87]
ds_read_b128 a[4:7],v16 offset:27136
v_add_f32 v46,1.0,v46
v_mfma_f32_16x16x32_bf16 a[152:155],a[20:23],a[84:87],a[152:155]
buffer_load_dwordx4 v[160:163],v2,%[srdW],s67 offen offset:0 sc1
v_mfma_f32_16x16x32_bf16 a[156:159],a[20:23],a[88:91],a[156:159]
s_waitcnt lgkmcnt(3)
v_exp_f32 v47,v47
v_mfma_f32_16x16x32_bf16 v[88:91],a[24:27],a[80:83],v[88:91]
ds_read_b128 a[8:11],v16 offset:59392
v_mul_f32 v45,v45,v125
v_mfma_f32_16x16x32_bf16 a[160:163],a[24:27],a[84:87],a[160:163]
v_rcp_f32 v46,v46
v_mfma_f32_16x16x32_bf16 a[164:167],a[24:27],a[88:91],a[164:167]
s_waitcnt lgkmcnt(3)
buffer_load_dwordx4 v[164:167],v2,%[srdW],s67 offen offset:512 sc1
v_mfma_f32_16x16x32_bf16 v[92:95],a[28:31],a[80:83],v[92:95]
ds_read_b128 a[12:15],v16 offset:59904
v_add_f32 v47,1.0,v47
v_mfma_f32_16x16x32_bf16 a[168:171],a[28:31],a[84:87],a[168:171]
v_mul_f32 v46,v46,v126
v_mfma_f32_16x16x32_bf16 a[172:175],a[28:31],a[88:91],a[172:175]
s_waitcnt lgkmcnt(3)
v_rcp_f32 v47,v47
v_mfma_f32_16x16x32_bf16 v[64:67],a[0:3],a[92:95],v[64:67]
ds_read_b128 a[16:19],v17 offset:26624
v_mul_f32 v47,v47,v127
v_mfma_f32_16x16x32_bf16 a[112:115],a[0:3],a[96:99],a[112:115]
buffer_load_dwordx4 v[168:171],v2,%[srdW],s67 offen offset:2048 sc1
v_mfma_f32_16x16x32_bf16 a[116:119],a[0:3],a[100:103],a[116:119]
s_waitcnt lgkmcnt(3)
v_cvt_pk_bf16_f32 v120,v40,v41
v_cvt_pk_bf16_f32 v121,v42,v43
v_mfma_f32_16x16x32_bf16 v[68:71],a[4:7],a[92:95],v[68:71]
ds_read_b128 a[20:23],v17 offset:27136
v_cvt_pk_bf16_f32 v122,v44,v45
v_mfma_f32_16x16x32_bf16 a[120:123],a[4:7],a[96:99],a[120:123]
v_cvt_pk_bf16_f32 v123,v46,v47
v_mfma_f32_16x16x32_bf16 a[124:127],a[4:7],a[100:103],a[124:127]
s_waitcnt lgkmcnt(3)
buffer_load_dwordx4 v[172:175],v2,%[srdW],s67 offen offset:2560 sc1
v_mfma_f32_16x16x32_bf16 v[72:75],a[8:11],a[92:95],v[72:75]
s_add_u32 s67,s67,0x1000
ds_read_b128 a[24:27],v17 offset:59392
v_mfma_f32_16x16x32_bf16 a[128:131],a[8:11],a[96:99],a[128:131]
v_mfma_f32_16x16x32_bf16 a[132:135],a[8:11],a[100:103],a[132:135]
s_waitcnt lgkmcnt(3)
v_mfma_f32_16x16x32_bf16 v[76:79],a[12:15],a[92:95],v[76:79]
ds_read_b128 a[28:31],v17 offset:59904
v_mfma_f32_16x16x32_bf16 a[136:139],a[12:15],a[96:99],a[136:139]
buffer_load_dwordx4 v[176:179],v2,%[srdW],s67 offen offset:0 sc1
v_mfma_f32_16x16x32_bf16 a[140:143],a[12:15],a[100:103],a[140:143]
s_waitcnt lgkmcnt(3)
v_mfma_f32_16x16x32_bf16 v[80:83],a[16:19],a[92:95],v[80:83]
ds_read_b128 a[0:3],v16 offset:28672
v_mfma_f32_16x16x32_bf16 a[144:147],a[16:19],a[96:99],a[144:147]
v_mfma_f32_16x16x32_bf16 a[148:151],a[16:19],a[100:103],a[148:151]
s_waitcnt lgkmcnt(3)
buffer_load_dwordx4 v[180:183],v2,%[srdW],s67 offen offset:512 sc1
v_mfma_f32_16x16x32_bf16 v[84:87],a[20:23],a[92:95],v[84:87]
ds_read_b128 a[4:7],v16 offset:29184
v_mfma_f32_16x16x32_bf16 a[152:155],a[20:23],a[96:99],a[152:155]
v_mfma_f32_16x16x32_bf16 a[156:159],a[20:23],a[100:103],a[156:159]
s_waitcnt lgkmcnt(3)
v_mfma_f32_16x16x32_bf16 v[88:91],a[24:27],a[92:95],v[88:91]
ds_read_b128 a[8:11],v16 offset:61440
v_mfma_f32_16x16x32_bf16 a[160:163],a[24:27],a[96:99],a[160:163]
buffer_load_dwordx4 v[184:187],v2,%[srdW],s67 offen offset:2048 sc1
v_mfma_f32_16x16x32_bf16 a[164:167],a[24:27],a[100:103],a[164:167]
s_waitcnt lgkmcnt(3)
v_mfma_f32_16x16x32_bf16 v[92:95],a[28:31],a[92:95],v[92:95]
ds_read_b128 a[12:15],v16 offset:61952
v_mfma_f32_16x16x32_bf16 a[168:171],a[28:31],a[96:99],a[168:171]
v_mfma_f32_16x16x32_bf16 a[172:175],a[28:31],a[100:103],a[172:175]
s_waitcnt lgkmcnt(3)
buffer_load_dwordx4 v[188:191],v2,%[srdW],s67 offen offset:2560 sc1
v_mfma_f32_16x16x32_bf16 v[64:67],a[0:3],a[176:179],v[64:67]
s_add_u32 s67,s67,0x1000
ds_read_b128 a[16:19],v17 offset:28672
v_add_u32 v25,6144,v23
v_mfma_f32_16x16x32_bf16 a[112:115],a[0:3],a[180:183],a[112:115]
v_add_u32 v25,s92,v25
v_mfma_f32_16x16x32_bf16 a[116:119],a[0:3],a[184:187],a[116:119]
s_waitcnt lgkmcnt(3)
v_mfma_f32_16x16x32_bf16 v[68:71],a[4:7],a[176:179],v[68:71]
ds_read_b128 a[20:23],v17 offset:29184
v_mfma_f32_16x16x32_bf16 a[120:123],a[4:7],a[180:183],a[120:123]
buffer_store_dwordx4 v[120:123],v25,%[srdW],0 offen offset:1024 sc1
v_mfma_f32_16x16x32_bf16 a[124:127],a[4:7],a[184:187],a[124:127]
s_waitcnt lgkmcnt(3)
v_mfma_f32_16x16x32_bf16 v[72:75],a[8:11],a[176:179],v[72:75]
ds_read_b128 a[24:27],v17 offset:61440
v_mfma_f32_16x16x32_bf16 a[128:131],a[8:11],a[180:183],a[128:131]
v_mfma_f32_16x16x32_bf16 a[132:135],a[8:11],a[184:187],a[132:135]
s_waitcnt lgkmcnt(3)
v_mfma_f32_16x16x32_bf16 v[76:79],a[12:15],a[176:179],v[76:79]
ds_read_b128 a[28:31],v17 offset:61952
v_mfma_f32_16x16x32_bf16 a[136:139],a[12:15],a[180:183],a[136:139]
v_mfma_f32_16x16x32_bf16 a[140:143],a[12:15],a[184:187],a[140:143]
s_waitcnt lgkmcnt(3)
v_mfma_f32_16x16x32_bf16 v[80:83],a[16:19],a[176:179],v[80:83]
ds_read_b128 a[0:3],v16 offset:30720
v_mfma_f32_16x16x32_bf16 a[144:147],a[16:19],a[180:183],a[144:147]
v_mfma_f32_16x16x32_bf16 a[148:151],a[16:19],a[184:187],a[148:151]
s_waitcnt lgkmcnt(3)
v_mfma_f32_16x16x32_bf16 v[84:87],a[20:23],a[176:179],v[84:87]
ds_read_b128 a[4:7],v16 offset:31232
v_mfma_f32_16x16x32_bf16 a[152:155],a[20:23],a[180:183],a[152:155]
v_mfma_f32_16x16x32_bf16 a[156:159],a[20:23],a[184:187],a[156:159]
s_waitcnt lgkmcnt(3)
v_mfma_f32_16x16x32_bf16 v[88:91],a[24:27],a[176:179],v[88:91]
ds_read_b128 a[8:11],v16 offset:63488
v_mfma_f32_16x16x32_bf16 a[160:163],a[24:27],a[180:183],a[160:163]
v_mfma_f32_16x16x32_bf16 a[164:167],a[24:27],a[184:187],a[164:167]
s_waitcnt lgkmcnt(3)
v_mfma_f32_16x16x32_bf16 v[92:95],a[28:31],a[176:179],v[92:95]
ds_read_b128 a[12:15],v16 offset:64000
v_mfma_f32_16x16x32_bf16 a[168:171],a[28:31],a[180:183],a[168:171]
v_mfma_f32_16x16x32_bf16 a[172:175],a[28:31],a[184:187],a[172:175]
s_waitcnt lgkmcnt(3)
v_mfma_f32_16x16x32_bf16 v[64:67],a[0:3],a[188:191],v[64:67]
ds_read_b128 a[16:19],v17 offset:30720
v_mfma_f32_16x16x32_bf16 a[112:115],a[0:3],a[192:195],a[112:115]
v_mfma_f32_16x16x32_bf16 a[116:119],a[0:3],a[196:199],a[116:119]
s_waitcnt lgkmcnt(3)
v_mfma_f32_16x16x32_bf16 v[68:71],a[4:7],a[188:191],v[68:71]
ds_read_b128 a[20:23],v17 offset:31232
v_mfma_f32_16x16x32_bf16 a[120:123],a[4:7],a[192:195],a[120:123]
v_mfma_f32_16x16x32_bf16 a[124:127],a[4:7],a[196:199],a[124:127]
s_waitcnt lgkmcnt(3)
v_mfma_f32_16x16x32_bf16 v[72:75],a[8:11],a[188:191],v[72:75]
ds_read_b128 a[24:27],v17 offset:63488
v_mfma_f32_16x16x32_bf16 a[128:131],a[8:11],a[192:195],a[128:131]
v_mfma_f32_16x16x32_bf16 a[132:135],a[8:11],a[196:199],a[132:135]
s_waitcnt lgkmcnt(3)
v_mfma_f32_16x16x32_bf16 v[76:79],a[12:15],a[188:191],v[76:79]
ds_read_b128 a[28:31],v17 offset:64000
v_mfma_f32_16x16x32_bf16 a[136:139],a[12:15],a[192:195],a[136:139]
v_mfma_f32_16x16x32_bf16 a[140:143],a[12:15],a[196:199],a[140:143]
s_waitcnt lgkmcnt(3)
v_mfma_f32_16x16x32_bf16 v[80:83],a[16:19],a[188:191],v[80:83]
v_mfma_f32_16x16x32_bf16 a[144:147],a[16:19],a[192:195],a[144:147]
v_mfma_f32_16x16x32_bf16 a[148:151],a[16:19],a[196:199],a[148:151]
s_waitcnt lgkmcnt(2)
v_mfma_f32_16x16x32_bf16 v[84:87],a[20:23],a[188:191],v[84:87]
v_mfma_f32_16x16x32_bf16 a[152:155],a[20:23],a[192:195],a[152:155]
v_mfma_f32_16x16x32_bf16 a[156:159],a[20:23],a[196:199],a[156:159]
s_waitcnt lgkmcnt(1)
v_mfma_f32_16x16x32_bf16 v[88:91],a[24:27],a[188:191],v[88:91]
v_mfma_f32_16x16x32_bf16 a[160:163],a[24:27],a[192:195],a[160:163]
v_mfma_f32_16x16x32_bf16 a[164:167],a[24:27],a[196:199],a[164:167]
s_waitcnt lgkmcnt(0)
v_mfma_f32_16x16x32_bf16 v[92:95],a[28:31],a[188:191],v[92:95]
v_mfma_f32_16x16x32_bf16 a[168:171],a[28:31],a[192:195],a[168:171]
v_mfma_f32_16x16x32_bf16 a[172:175],a[28:31],a[196:199],a[172:175]
s_barrier
v_accvgpr_read_b32 v96,a112
v_accvgpr_read_b32 v97,a113
v_accvgpr_read_b32 v98,a114
v_accvgpr_read_b32 v99,a115
v_accvgpr_read_b32 v100,a120
v_accvgpr_read_b32 v101,a121
v_accvgpr_read_b32 v102,a122
v_accvgpr_read_b32 v103,a123
v_pk_mul_f32 v[40:41],v[96:97],v[96:97]
v_pk_mul_f32 v[42:43],v[98:99],v[98:99]
s_add_u32 m0,s41,0
s_add_u32 s67,s65,0
buffer_load_dwordx4 %[vL16],%[srdW],s67 offen offset:0 sc1 lds
v_pk_mul_f32 v[44:45],v[100:101],v[100:101]
v_pk_mul_f32 v[46:47],v[102:103],v[102:103]
v_pk_fma_f32 v[40:41],v[40:41],v[20:21],v[20:21] op_sel:[0,0,1] op_sel_hi:[1,0,1]
v_pk_fma_f32 v[42:43],v[42:43],v[20:21],v[20:21] op_sel:[0,0,1] op_sel_hi:[1,0,1]
v_pk_fma_f32 v[44:45],v[44:45],v[20:21],v[20:21] op_sel:[0,0,1] op_sel_hi:[1,0,1]
v_pk_fma_f32 v[46:47],v[46:47],v[20:21],v[20:21] op_sel:[0,0,1] op_sel_hi:[1,0,1]
v_pk_mul_f32 v[40:41],v[40:41],v[96:97]
v_pk_mul_f32 v[42:43],v[42:43],v[98:99]
v_pk_mul_f32 v[44:45],v[44:45],v[100:101]
v_pk_mul_f32 v[46:47],v[46:47],v[102:103]
buffer_load_dwordx4 %[vL16],%[srdW],s67 offen offset:1024 sc1 lds
v_mfma_f32_32x32x16_bf16 a[0:15],v[28:31],v[28:31],0
v_mfma_f32_32x32x16_bf16 a[16:31],v[28:31],v[28:31],0
v_exp_f32 v40,v40
v_exp_f32 v41,v41
v_exp_f32 v42,v42
v_exp_f32 v43,v43
v_exp_f32 v44,v44
v_exp_f32 v45,v45
v_exp_f32 v46,v46
v_exp_f32 v47,v47
buffer_load_dwordx4 %[vL16],%[srdW],s67 offen offset:2048 sc1 lds
v_pk_add_f32 v[40:41],v[40:41],1.0 op_sel_hi:[1,0]
v_pk_add_f32 v[42:43],v[42:43],1.0 op_sel_hi:[1,0]
)"
R"(v_pk_add_f32 v[44:45],v[44:45],1.0 op_sel_hi:[1,0]
v_pk_add_f32 v[46:47],v[46:47],1.0 op_sel_hi:[1,0]
v_rcp_f32 v40,v40
v_rcp_f32 v41,v41
v_rcp_f32 v42,v42
v_rcp_f32 v43,v43
v_rcp_f32 v44,v44
v_rcp_f32 v45,v45
buffer_load_dwordx4 %[vL16],%[srdW],s67 offen offset:3072 sc1 lds
v_rcp_f32 v46,v46
v_rcp_f32 v47,v47
v_pk_mul_f32 v[40:41],v[40:41],v[96:97]
v_pk_mul_f32 v[42:43],v[42:43],v[98:99]
v_pk_mul_f32 v[44:45],v[44:45],v[100:101]
v_pk_mul_f32 v[46:47],v[46:47],v[102:103]
v_cvt_pk_bf16_f32 v112,v40,v41
v_cvt_pk_bf16_f32 v113,v42,v43
v_cvt_pk_bf16_f32 v114,v44,v45
v_cvt_pk_bf16_f32 v115,v46,v47
s_add_u32 m0,s41,4096
s_add_u32 s67,s65,4096
buffer_load_dwordx4 %[vL16],%[srdW],s67 offen offset:0 sc1 lds
v_add_u32 v24,0,v22
buffer_store_dwordx4 v[112:115],v24,%[srdW],0 offen offset:0 sc1
v_accvgpr_read_b32 v104,a116
v_accvgpr_read_b32 v105,a117
v_accvgpr_read_b32 v106,a118
v_accvgpr_read_b32 v107,a119
v_accvgpr_read_b32 v108,a124
v_accvgpr_read_b32 v109,a125
v_accvgpr_read_b32 v110,a126
v_accvgpr_read_b32 v111,a127
buffer_load_dwordx4 %[vL16],%[srdW],s67 offen offset:1024 sc1 lds
v_pk_mul_f32 v[40:41],v[104:105],v[104:105]
v_pk_mul_f32 v[42:43],v[106:107],v[106:107]
v_pk_mul_f32 v[44:45],v[108:109],v[108:109]
v_pk_mul_f32 v[46:47],v[110:111],v[110:111]
v_pk_fma_f32 v[40:41],v[40:41],v[20:21],v[20:21] op_sel:[0,0,1] op_sel_hi:[1,0,1]
v_pk_fma_f32 v[42:43],v[42:43],v[20:21],v[20:21] op_sel:[0,0,1] op_sel_hi:[1,0,1]
v_pk_fma_f32 v[44:45],v[44:45],v[20:21],v[20:21] op_sel:[0,0,1] op_sel_hi:[1,0,1]
v_pk_fma_f32 v[46:47],v[46:47],v[20:21],v[20:21] op_sel:[0,0,1] op_sel_hi:[1,0,1]
v_pk_mul_f32 v[40:41],v[40:41],v[104:105]
v_pk_mul_f32 v[42:43],v[42:43],v[106:107]
buffer_load_dwordx4 %[vL16],%[srdW],s67 offen offset:2048 sc1 lds
v_pk_mul_f32 v[44:45],v[44:45],v[108:109]
v_pk_mul_f32 v[46:47],v[46:47],v[110:111]
v_mfma_f32_32x32x16_bf16 a[112:127],v[28:31],v[28:31],0
v_mfma_f32_32x32x16_bf16 a[32:47],v[28:31],v[28:31],0
v_exp_f32 v40,v40
v_exp_f32 v41,v41
v_exp_f32 v42,v42
v_exp_f32 v43,v43
v_exp_f32 v44,v44
v_exp_f32 v45,v45
buffer_load_dwordx4 %[vL16],%[srdW],s67 offen offset:3072 sc1 lds
v_exp_f32 v46,v46
v_exp_f32 v47,v47
v_pk_add_f32 v[40:41],v[40:41],1.0 op_sel_hi:[1,0]
v_pk_add_f32 v[42:43],v[42:43],1.0 op_sel_hi:[1,0]
v_pk_add_f32 v[44:45],v[44:45],1.0 op_sel_hi:[1,0]
v_pk_add_f32 v[46:47],v[46:47],1.0 op_sel_hi:[1,0]
v_rcp_f32 v40,v40
v_rcp_f32 v41,v41
v_rcp_f32 v42,v42
v_rcp_f32 v43,v43
s_add_u32 s65,s65,s70
s_add_u32 s67,s42,s46
buffer_load_dwordx4 v[192:195],v2,%[srdW],s67 offen offset:0 sc1
v_rcp_f32 v44,v44
v_rcp_f32 v45,v45
v_rcp_f32 v46,v46
v_rcp_f32 v47,v47
v_pk_mul_f32 v[40:41],v[40:41],v[104:105]
v_pk_mul_f32 v[42:43],v[42:43],v[106:107]
v_pk_mul_f32 v[44:45],v[44:45],v[108:109]
v_pk_mul_f32 v[46:47],v[46:47],v[110:111]
v_cvt_pk_bf16_f32 v116,v40,v41
v_cvt_pk_bf16_f32 v117,v42,v43
buffer_load_dwordx4 v[196:199],v2,%[srdW],s67 offen offset:512 sc1
v_cvt_pk_bf16_f32 v118,v44,v45
v_cvt_pk_bf16_f32 v119,v46,v47
v_add_u32 v24,512000,v22
buffer_store_dwordx4 v[116:119],v24,%[srdW],0 offen offset:0 sc1
v_accvgpr_read_b32 v96,a128
v_accvgpr_read_b32 v97,a129
v_accvgpr_read_b32 v98,a130
v_accvgpr_read_b32 v99,a131
v_accvgpr_read_b32 v100,a136
v_accvgpr_read_b32 v101,a137
buffer_load_dwordx4 v[200:203],v2,%[srdW],s67 offen offset:2048 sc1
v_accvgpr_read_b32 v102,a138
v_accvgpr_read_b32 v103,a139
v_pk_mul_f32 v[40:41],v[96:97],v[96:97]
v_pk_mul_f32 v[42:43],v[98:99],v[98:99]
v_pk_mul_f32 v[44:45],v[100:101],v[100:101]
v_pk_mul_f32 v[46:47],v[102:103],v[102:103]
v_pk_fma_f32 v[40:41],v[40:41],v[20:21],v[20:21] op_sel:[0,0,1] op_sel_hi:[1,0,1]
v_pk_fma_f32 v[42:43],v[42:43],v[20:21],v[20:21] op_sel:[0,0,1] op_sel_hi:[1,0,1]
v_pk_fma_f32 v[44:45],v[44:45],v[20:21],v[20:21] op_sel:[0,0,1] op_sel_hi:[1,0,1]
v_pk_fma_f32 v[46:47],v[46:47],v[20:21],v[20:21] op_sel:[0,0,1] op_sel_hi:[1,0,1]
buffer_load_dwordx4 v[204:207],v2,%[srdW],s67 offen offset:2560 sc1
v_pk_mul_f32 v[40:41],v[40:41],v[96:97]
v_pk_mul_f32 v[42:43],v[42:43],v[98:99]
v_pk_mul_f32 v[44:45],v[44:45],v[100:101]
v_pk_mul_f32 v[46:47],v[46:47],v[102:103]
v_mfma_f32_16x16x32_bf16 v[56:59],v[112:115],v[32:35],0
v_mfma_f32_16x16x32_bf16 v[60:63],v[116:119],v[32:35],0
v_mfma_f32_16x16x32_bf16 v[48:51],v[112:115],v[36:39],0
v_mfma_f32_16x16x32_bf16 v[52:55],v[116:119],v[36:39],0
v_exp_f32 v40,v40
v_exp_f32 v41,v41
s_add_u32 s67,s67,0x1000
buffer_load_dwordx4 v[208:211],v2,%[srdW],s67 offen offset:0 sc1
v_exp_f32 v42,v42
v_exp_f32 v43,v43
v_exp_f32 v44,v44
v_exp_f32 v45,v45
v_exp_f32 v46,v46
v_exp_f32 v47,v47
v_pk_add_f32 v[40:41],v[40:41],1.0 op_sel_hi:[1,0]
v_pk_add_f32 v[42:43],v[42:43],1.0 op_sel_hi:[1,0]
v_pk_add_f32 v[44:45],v[44:45],1.0 op_sel_hi:[1,0]
v_pk_add_f32 v[46:47],v[46:47],1.0 op_sel_hi:[1,0]
buffer_load_dwordx4 v[212:215],v2,%[srdW],s67 offen offset:512 sc1
v_rcp_f32 v40,v40
v_rcp_f32 v41,v41
v_rcp_f32 v42,v42
v_rcp_f32 v43,v43
v_rcp_f32 v44,v44
v_rcp_f32 v45,v45
v_rcp_f32 v46,v46
v_rcp_f32 v47,v47
v_pk_mul_f32 v[40:41],v[40:41],v[96:97]
v_pk_mul_f32 v[42:43],v[42:43],v[98:99]
buffer_load_dwordx4 v[216:219],v2,%[srdW],s67 offen offset:2048 sc1
v_pk_mul_f32 v[44:45],v[44:45],v[100:101]
v_pk_mul_f32 v[46:47],v[46:47],v[102:103]
v_cvt_pk_bf16_f32 v120,v40,v41
v_cvt_pk_bf16_f32 v121,v42,v43
v_cvt_pk_bf16_f32 v122,v44,v45
v_cvt_pk_bf16_f32 v123,v46,v47
v_add_u32 v24,2048,v22
v_add_u32 v24,s92,v24
buffer_store_dwordx4 v[120:123],v24,%[srdW],0 offen offset:0 sc1
v_pk_mul_f32 v[26:27],v[56:57],v[56:57]
buffer_load_dwordx4 v[220:223],v2,%[srdW],s67 offen offset:2560 sc1
v_pk_fma_f32 v[26:27],v[58:59],v[58:59],v[26:27]
v_pk_fma_f32 v[26:27],v[60:61],v[60:61],v[26:27]
v_pk_fma_f32 v[26:27],v[62:63],v[62:63],v[26:27]
v_pk_add_f32 v[56:57],v[56:57],v[58:59]
v_pk_add_f32 v[60:61],v[60:61],v[62:63]
v_pk_add_f32 v[56:57],v[56:57],v[60:61]
v_add_f32 v56,v56,v57
v_add_f32 v57,v26,v27
ds_write_b64 v19,v[56:57] offset:0
v_pk_mul_f32 v[26:27],v[48:49],v[48:49]
s_add_u32 s67,s67,0x1000
buffer_load_dwordx4 v[224:227],v2,%[srdW],s67 offen offset:0 sc1
v_pk_fma_f32 v[26:27],v[50:51],v[50:51],v[26:27]
v_pk_fma_f32 v[26:27],v[52:53],v[52:53],v[26:27]
v_pk_fma_f32 v[26:27],v[54:55],v[54:55],v[26:27]
v_pk_add_f32 v[48:49],v[48:49],v[50:51]
v_pk_add_f32 v[52:53],v[52:53],v[54:55]
v_pk_add_f32 v[48:49],v[48:49],v[52:53]
v_add_f32 v48,v48,v49
v_add_f32 v49,v26,v27
ds_write_b64 v19,v[48:49] offset:128
v_accvgpr_read_b32 v104,a132
buffer_load_dwordx4 v[228:231],v2,%[srdW],s67 offen offset:512 sc1
v_accvgpr_read_b32 v105,a133
v_accvgpr_read_b32 v106,a134
v_accvgpr_read_b32 v107,a135
v_accvgpr_read_b32 v108,a140
v_accvgpr_read_b32 v109,a141
v_accvgpr_read_b32 v110,a142
v_accvgpr_read_b32 v111,a143
v_pk_mul_f32 v[40:41],v[104:105],v[104:105]
v_pk_mul_f32 v[42:43],v[106:107],v[106:107]
v_pk_mul_f32 v[44:45],v[108:109],v[108:109]
buffer_load_dwordx4 v[232:235],v2,%[srdW],s67 offen offset:2048 sc1
v_pk_mul_f32 v[46:47],v[110:111],v[110:111]
v_pk_fma_f32 v[40:41],v[40:41],v[20:21],v[20:21] op_sel:[0,0,1] op_sel_hi:[1,0,1]
v_pk_fma_f32 v[42:43],v[42:43],v[20:21],v[20:21] op_sel:[0,0,1] op_sel_hi:[1,0,1]
v_pk_fma_f32 v[44:45],v[44:45],v[20:21],v[20:21] op_sel:[0,0,1] op_sel_hi:[1,0,1]
v_pk_fma_f32 v[46:47],v[46:47],v[20:21],v[20:21] op_sel:[0,0,1] op_sel_hi:[1,0,1]
v_pk_mul_f32 v[40:41],v[40:41],v[104:105]
v_pk_mul_f32 v[42:43],v[42:43],v[106:107]
v_pk_mul_f32 v[44:45],v[44:45],v[108:109]
v_pk_mul_f32 v[46:47],v[46:47],v[110:111]
v_mfma_f32_32x32x16_bf16 a[128:143],v[28:31],v[28:31],0
buffer_load_dwordx4 v[236:239],v2,%[srdW],s67 offen offset:2560 sc1
v_mfma_f32_32x32x16_bf16 a[48:63],v[28:31],v[28:31],0
v_exp_f32 v40,v40
v_exp_f32 v41,v41
v_exp_f32 v42,v42
v_exp_f32 v43,v43
v_exp_f32 v44,v44
v_exp_f32 v45,v45
v_exp_f32 v46,v46
v_exp_f32 v47,v47
v_pk_add_f32 v[40:41],v[40:41],1.0 op_sel_hi:[1,0]
s_add_u32 s67,s67,0x1000
buffer_load_dwordx4 v[240:243],v2,%[srdW],s67 offen offset:0 sc1
v_pk_add_f32 v[42:43],v[42:43],1.0 op_sel_hi:[1,0]
v_pk_add_f32 v[44:45],v[44:45],1.0 op_sel_hi:[1,0]
v_pk_add_f32 v[46:47],v[46:47],1.0 op_sel_hi:[1,0]
v_rcp_f32 v40,v40
v_rcp_f32 v41,v41
v_rcp_f32 v42,v42
v_rcp_f32 v43,v43
v_rcp_f32 v44,v44
v_rcp_f32 v45,v45
v_rcp_f32 v46,v46
buffer_load_dwordx4 v[244:247],v2,%[srdW],s67 offen offset:512 sc1
v_rcp_f32 v47,v47
v_pk_mul_f32 v[40:41],v[40:41],v[104:105]
v_pk_mul_f32 v[42:43],v[42:43],v[106:107]
v_pk_mul_f32 v[44:45],v[44:45],v[108:109]
v_pk_mul_f32 v[46:47],v[46:47],v[110:111]
v_cvt_pk_bf16_f32 v124,v40,v41
v_cvt_pk_bf16_f32 v125,v42,v43
v_cvt_pk_bf16_f32 v126,v44,v45
v_cvt_pk_bf16_f32 v127,v46,v47
v_add_u32 v24,514048,v22
buffer_load_dwordx4 v[248:251],v2,%[srdW],s67 offen offset:2048 sc1
v_add_u32 v24,s92,v24
buffer_store_dwordx4 v[124:127],v24,%[srdW],0 offen offset:0 sc1
v_accvgpr_read_b32 v96,a144
v_accvgpr_read_b32 v97,a145
v_accvgpr_read_b32 v98,a146
v_accvgpr_read_b32 v99,a147
v_accvgpr_read_b32 v100,a152
v_accvgpr_read_b32 v101,a153
v_accvgpr_read_b32 v102,a154
v_accvgpr_read_b32 v103,a155
buffer_load_dwordx4 v[252:255],v2,%[srdW],s67 offen offset:2560 sc1
v_pk_mul_f32 v[40:41],v[96:97],v[96:97]
v_pk_mul_f32 v[42:43],v[98:99],v[98:99]
v_pk_mul_f32 v[44:45],v[100:101],v[100:101]
v_pk_mul_f32 v[46:47],v[102:103],v[102:103]
v_pk_fma_f32 v[40:41],v[40:41],v[20:21],v[20:21] op_sel:[0,0,1] op_sel_hi:[1,0,1]
v_pk_fma_f32 v[42:43],v[42:43],v[20:21],v[20:21] op_sel:[0,0,1] op_sel_hi:[1,0,1]
v_pk_fma_f32 v[44:45],v[44:45],v[20:21],v[20:21] op_sel:[0,0,1] op_sel_hi:[1,0,1]
v_pk_fma_f32 v[46:47],v[46:47],v[20:21],v[20:21] op_sel:[0,0,1] op_sel_hi:[1,0,1]
v_pk_mul_f32 v[40:41],v[40:41],v[96:97]
v_pk_mul_f32 v[42:43],v[42:43],v[98:99]
s_add_u32 s67,s67,0x1000
s_add_u32 m0,s41,16384
s_add_u32 s67,s65,0
buffer_load_dwordx4 %[vL16],%[srdW],s67 offen offset:0 sc1 lds
v_pk_mul_f32 v[44:45],v[44:45],v[100:101]
v_pk_mul_f32 v[46:47],v[46:47],v[102:103]
v_mfma_f32_16x16x32_bf16 v[56:59],v[120:123],v[32:35],0
v_mfma_f32_16x16x32_bf16 v[60:63],v[124:127],v[32:35],0
v_mfma_f32_16x16x32_bf16 v[48:51],v[120:123],v[36:39],0
v_mfma_f32_16x16x32_bf16 v[52:55],v[124:127],v[36:39],0
v_exp_f32 v40,v40
v_exp_f32 v41,v41
v_exp_f32 v42,v42
v_exp_f32 v43,v43
buffer_load_dwordx4 %[vL16],%[srdW],s67 offen offset:1024 sc1 lds
v_exp_f32 v44,v44
v_exp_f32 v45,v45
v_exp_f32 v46,v46
v_exp_f32 v47,v47
v_pk_add_f32 v[40:41],v[40:41],1.0 op_sel_hi:[1,0]
v_pk_add_f32 v[42:43],v[42:43],1.0 op_sel_hi:[1,0]
v_pk_add_f32 v[44:45],v[44:45],1.0 op_sel_hi:[1,0]
v_pk_add_f32 v[46:47],v[46:47],1.0 op_sel_hi:[1,0]
v_rcp_f32 v40,v40
v_rcp_f32 v41,v41
buffer_load_dwordx4 %[vL16],%[srdW],s67 offen offset:2048 sc1 lds
v_rcp_f32 v42,v42
v_rcp_f32 v43,v43
v_rcp_f32 v44,v44
v_rcp_f32 v45,v45
v_rcp_f32 v46,v46
v_rcp_f32 v47,v47
v_pk_mul_f32 v[40:41],v[40:41],v[96:97]
v_pk_mul_f32 v[42:43],v[42:43],v[98:99]
v_pk_mul_f32 v[44:45],v[44:45],v[100:101]
v_pk_mul_f32 v[46:47],v[46:47],v[102:103]
buffer_load_dwordx4 %[vL16],%[srdW],s67 offen offset:3072 sc1 lds
v_cvt_pk_bf16_f32 v112,v40,v41
v_cvt_pk_bf16_f32 v113,v42,v43
v_cvt_pk_bf16_f32 v114,v44,v45
v_cvt_pk_bf16_f32 v115,v46,v47
v_add_u32 v24,4096,v23
v_add_u32 v24,s92,v24
buffer_store_dwordx4 v[112:115],v24,%[srdW],0 offen offset:0 sc1
v_pk_mul_f32 v[26:27],v[56:57],v[56:57]
v_pk_fma_f32 v[26:27],v[58:59],v[58:59],v[26:27]
v_pk_fma_f32 v[26:27],v[60:61],v[60:61],v[26:27]
s_add_u32 m0,s41,20480
s_add_u32 s67,s65,4096
buffer_load_dwordx4 %[vL16],%[srdW],s67 offen offset:0 sc1 lds
v_pk_fma_f32 v[26:27],v[62:63],v[62:63],v[26:27]
v_pk_add_f32 v[56:57],v[56:57],v[58:59]
v_pk_add_f32 v[60:61],v[60:61],v[62:63]
v_pk_add_f32 v[56:57],v[56:57],v[60:61]
v_add_f32 v56,v56,v57
v_add_f32 v57,v26,v27
ds_write_b64 v19,v[56:57] offset:256
v_pk_mul_f32 v[26:27],v[48:49],v[48:49]
v_pk_fma_f32 v[26:27],v[50:51],v[50:51],v[26:27]
v_pk_fma_f32 v[26:27],v[52:53],v[52:53],v[26:27]
buffer_load_dwordx4 %[vL16],%[srdW],s67 offen offset:1024 sc1 lds
v_pk_fma_f32 v[26:27],v[54:55],v[54:55],v[26:27]
v_pk_add_f32 v[48:49],v[48:49],v[50:51]
v_pk_add_f32 v[52:53],v[52:53],v[54:55]
v_pk_add_f32 v[48:49],v[48:49],v[52:53]
v_add_f32 v48,v48,v49
v_add_f32 v49,v26,v27
ds_write_b64 v19,v[48:49] offset:384
v_accvgpr_read_b32 v104,a148
v_accvgpr_read_b32 v105,a149
v_accvgpr_read_b32 v106,a150
buffer_load_dwordx4 %[vL16],%[srdW],s67 offen offset:2048 sc1 lds
v_accvgpr_read_b32 v107,a151
v_accvgpr_read_b32 v108,a156
v_accvgpr_read_b32 v109,a157
v_accvgpr_read_b32 v110,a158
v_accvgpr_read_b32 v111,a159
v_pk_mul_f32 v[40:41],v[104:105],v[104:105]
v_pk_mul_f32 v[42:43],v[106:107],v[106:107]
v_pk_mul_f32 v[44:45],v[108:109],v[108:109]
v_pk_mul_f32 v[46:47],v[110:111],v[110:111]
v_pk_fma_f32 v[40:41],v[40:41],v[20:21],v[20:21] op_sel:[0,0,1] op_sel_hi:[1,0,1]
buffer_load_dwordx4 %[vL16],%[srdW],s67 offen offset:3072 sc1 lds
v_pk_fma_f32 v[42:43],v[42:43],v[20:21],v[20:21] op_sel:[0,0,1] op_sel_hi:[1,0,1]
v_pk_fma_f32 v[44:45],v[44:45],v[20:21],v[20:21] op_sel:[0,0,1] op_sel_hi:[1,0,1]
v_pk_fma_f32 v[46:47],v[46:47],v[20:21],v[20:21] op_sel:[0,0,1] op_sel_hi:[1,0,1]
v_pk_mul_f32 v[40:41],v[40:41],v[104:105]
v_pk_mul_f32 v[42:43],v[42:43],v[106:107]
v_pk_mul_f32 v[44:45],v[44:45],v[108:109]
v_pk_mul_f32 v[46:47],v[46:47],v[110:111]
v_mfma_f32_32x32x16_bf16 a[144:159],v[28:31],v[28:31],0
v_mfma_f32_32x32x16_bf16 a[64:79],v[28:31],v[28:31],0
v_exp_f32 v40,v40
s_add_u32 s65,s65,s71
s_add_u32 m0,s41,32768
s_add_u32 s67,s65,0
buffer_load_dwordx4 %[vL16],%[srdW],s67 offen offset:0 sc1 lds
v_exp_f32 v41,v41
v_exp_f32 v42,v42
v_exp_f32 v43,v43
v_exp_f32 v44,v44
v_exp_f32 v45,v45
v_exp_f32 v46,v46
v_exp_f32 v47,v47
v_pk_add_f32 v[40:41],v[40:41],1.0 op_sel_hi:[1,0]
v_pk_add_f32 v[42:43],v[42:43],1.0 op_sel_hi:[1,0]
v_pk_add_f32 v[44:45],v[44:45],1.0 op_sel_hi:[1,0]
buffer_load_dwordx4 %[vL16],%[srdW],s67 offen offset:1024 sc1 lds
v_pk_add_f32 v[46:47],v[46:47],1.0 op_sel_hi:[1,0]
v_rcp_f32 v40,v40
v_rcp_f32 v41,v41
v_rcp_f32 v42,v42
v_rcp_f32 v43,v43
v_rcp_f32 v44,v44
v_rcp_f32 v45,v45
v_rcp_f32 v46,v46
v_rcp_f32 v47,v47
v_pk_mul_f32 v[40:41],v[40:41],v[104:105]
buffer_load_dwordx4 %[vL16],%[srdW],s67 offen offset:2048 sc1 lds
v_pk_mul_f32 v[42:43],v[42:43],v[106:107]
v_pk_mul_f32 v[44:45],v[44:45],v[108:109]
v_pk_mul_f32 v[46:47],v[46:47],v[110:111]
v_cvt_pk_bf16_f32 v116,v40,v41
v_cvt_pk_bf16_f32 v117,v42,v43
v_cvt_pk_bf16_f32 v118,v44,v45
v_cvt_pk_bf16_f32 v119,v46,v47
v_add_u32 v24,516096,v23
v_add_u32 v24,s92,v24
buffer_store_dwordx4 v[116:119],v24,%[srdW],0 offen offset:0 sc1
buffer_load_dwordx4 %[vL16],%[srdW],s67 offen offset:3072 sc1 lds
v_accvgpr_read_b32 v96,a160
v_accvgpr_read_b32 v97,a161
v_accvgpr_read_b32 v98,a162
v_accvgpr_read_b32 v99,a163
v_accvgpr_read_b32 v100,a168
v_accvgpr_read_b32 v101,a169
v_accvgpr_read_b32 v102,a170
v_accvgpr_read_b32 v103,a171
v_pk_mul_f32 v[40:41],v[96:97],v[96:97]
v_pk_mul_f32 v[42:43],v[98:99],v[98:99]
s_add_u32 m0,s41,36864
s_add_u32 s67,s65,4096
buffer_load_dwordx4 %[vL16],%[srdW],s67 offen offset:0 sc1 lds
v_pk_mul_f32 v[44:45],v[100:101],v[100:101]
v_pk_mul_f32 v[46:47],v[102:103],v[102:103]
v_pk_fma_f32 v[40:41],v[40:41],v[20:21],v[20:21] op_sel:[0,0,1] op_sel_hi:[1,0,1]
v_pk_fma_f32 v[42:43],v[42:43],v[20:21],v[20:21] op_sel:[0,0,1] op_sel_hi:[1,0,1]
v_pk_fma_f32 v[44:45],v[44:45],v[20:21],v[20:21] op_sel:[0,0,1] op_sel_hi:[1,0,1]
v_pk_fma_f32 v[46:47],v[46:47],v[20:21],v[20:21] op_sel:[0,0,1] op_sel_hi:[1,0,1]
v_pk_mul_f32 v[40:41],v[40:41],v[96:97]
v_pk_mul_f32 v[42:43],v[42:43],v[98:99]
v_pk_mul_f32 v[44:45],v[44:45],v[100:101]
v_pk_mul_f32 v[46:47],v[46:47],v[102:103]
buffer_load_dwordx4 %[vL16],%[srdW],s67 offen offset:1024 sc1 lds
)"
R"(v_mfma_f32_16x16x32_bf16 v[56:59],v[112:115],v[32:35],0
v_mfma_f32_16x16x32_bf16 v[60:63],v[116:119],v[32:35],0
v_mfma_f32_16x16x32_bf16 v[48:51],v[112:115],v[36:39],0
v_mfma_f32_16x16x32_bf16 v[52:55],v[116:119],v[36:39],0
v_exp_f32 v40,v40
v_exp_f32 v41,v41
v_exp_f32 v42,v42
v_exp_f32 v43,v43
v_exp_f32 v44,v44
v_exp_f32 v45,v45
buffer_load_dwordx4 %[vL16],%[srdW],s67 offen offset:2048 sc1 lds
v_exp_f32 v46,v46
v_exp_f32 v47,v47
v_pk_add_f32 v[40:41],v[40:41],1.0 op_sel_hi:[1,0]
v_pk_add_f32 v[42:43],v[42:43],1.0 op_sel_hi:[1,0]
v_pk_add_f32 v[44:45],v[44:45],1.0 op_sel_hi:[1,0]
v_pk_add_f32 v[46:47],v[46:47],1.0 op_sel_hi:[1,0]
v_rcp_f32 v40,v40
v_rcp_f32 v41,v41
v_rcp_f32 v42,v42
v_rcp_f32 v43,v43
buffer_load_dwordx4 %[vL16],%[srdW],s67 offen offset:3072 sc1 lds
v_rcp_f32 v44,v44
v_rcp_f32 v45,v45
v_rcp_f32 v46,v46
v_rcp_f32 v47,v47
v_pk_mul_f32 v[40:41],v[40:41],v[96:97]
v_pk_mul_f32 v[42:43],v[42:43],v[98:99]
v_pk_mul_f32 v[44:45],v[44:45],v[100:101]
v_pk_mul_f32 v[46:47],v[46:47],v[102:103]
v_cvt_pk_bf16_f32 v120,v40,v41
v_cvt_pk_bf16_f32 v121,v42,v43
s_add_u32 s65,s65,s70
s_mov_b32 s59,s65
s_add_u32 m0,s41,49152
s_add_u32 s67,s65,0
buffer_load_dwordx4 %[vL16],%[srdW],s67 offen offset:0 sc1 lds
v_cvt_pk_bf16_f32 v122,v44,v45
v_cvt_pk_bf16_f32 v123,v46,v47
v_add_u32 v24,6144,v23
v_add_u32 v24,s92,v24
buffer_store_dwordx4 v[120:123],v24,%[srdW],0 offen offset:0 sc1
v_pk_mul_f32 v[26:27],v[56:57],v[56:57]
v_pk_fma_f32 v[26:27],v[58:59],v[58:59],v[26:27]
v_pk_fma_f32 v[26:27],v[60:61],v[60:61],v[26:27]
v_pk_fma_f32 v[26:27],v[62:63],v[62:63],v[26:27]
v_pk_add_f32 v[56:57],v[56:57],v[58:59]
buffer_load_dwordx4 %[vL16],%[srdW],s67 offen offset:1024 sc1 lds
v_pk_add_f32 v[60:61],v[60:61],v[62:63]
v_pk_add_f32 v[56:57],v[56:57],v[60:61]
v_add_f32 v56,v56,v57
v_add_f32 v57,v26,v27
ds_write_b64 v19,v[56:57] offset:512
v_pk_mul_f32 v[26:27],v[48:49],v[48:49]
v_pk_fma_f32 v[26:27],v[50:51],v[50:51],v[26:27]
v_pk_fma_f32 v[26:27],v[52:53],v[52:53],v[26:27]
v_pk_fma_f32 v[26:27],v[54:55],v[54:55],v[26:27]
v_pk_add_f32 v[48:49],v[48:49],v[50:51]
buffer_load_dwordx4 %[vL16],%[srdW],s67 offen offset:2048 sc1 lds
v_pk_add_f32 v[52:53],v[52:53],v[54:55]
v_pk_add_f32 v[48:49],v[48:49],v[52:53]
v_add_f32 v48,v48,v49
v_add_f32 v49,v26,v27
ds_write_b64 v19,v[48:49] offset:640
v_accvgpr_read_b32 v104,a164
v_accvgpr_read_b32 v105,a165
v_accvgpr_read_b32 v106,a166
v_accvgpr_read_b32 v107,a167
v_accvgpr_read_b32 v108,a172
buffer_load_dwordx4 %[vL16],%[srdW],s67 offen offset:3072 sc1 lds
v_accvgpr_read_b32 v109,a173
v_accvgpr_read_b32 v110,a174
v_accvgpr_read_b32 v111,a175
v_pk_mul_f32 v[40:41],v[104:105],v[104:105]
v_pk_mul_f32 v[42:43],v[106:107],v[106:107]
v_pk_mul_f32 v[44:45],v[108:109],v[108:109]
v_pk_mul_f32 v[46:47],v[110:111],v[110:111]
v_pk_fma_f32 v[40:41],v[40:41],v[20:21],v[20:21] op_sel:[0,0,1] op_sel_hi:[1,0,1]
v_pk_fma_f32 v[42:43],v[42:43],v[20:21],v[20:21] op_sel:[0,0,1] op_sel_hi:[1,0,1]
v_pk_fma_f32 v[44:45],v[44:45],v[20:21],v[20:21] op_sel:[0,0,1] op_sel_hi:[1,0,1]
s_add_u32 m0,s41,53248
s_add_u32 s67,s65,4096
buffer_load_dwordx4 %[vL16],%[srdW],s67 offen offset:0 sc1 lds
v_pk_fma_f32 v[46:47],v[46:47],v[20:21],v[20:21] op_sel:[0,0,1] op_sel_hi:[1,0,1]
v_pk_mul_f32 v[40:41],v[40:41],v[104:105]
v_pk_mul_f32 v[42:43],v[42:43],v[106:107]
v_pk_mul_f32 v[44:45],v[44:45],v[108:109]
v_pk_mul_f32 v[46:47],v[46:47],v[110:111]
v_mfma_f32_32x32x16_bf16 a[160:175],v[28:31],v[28:31],0
v_mfma_f32_32x32x16_bf16 a[80:95],v[28:31],v[28:31],0
v_exp_f32 v40,v40
v_exp_f32 v41,v41
v_exp_f32 v42,v42
buffer_load_dwordx4 %[vL16],%[srdW],s67 offen offset:1024 sc1 lds
v_exp_f32 v43,v43
v_exp_f32 v44,v44
v_exp_f32 v45,v45
v_exp_f32 v46,v46
v_exp_f32 v47,v47
v_pk_add_f32 v[40:41],v[40:41],1.0 op_sel_hi:[1,0]
v_pk_add_f32 v[42:43],v[42:43],1.0 op_sel_hi:[1,0]
v_pk_add_f32 v[44:45],v[44:45],1.0 op_sel_hi:[1,0]
v_pk_add_f32 v[46:47],v[46:47],1.0 op_sel_hi:[1,0]
v_rcp_f32 v40,v40
s_add_u32 s65,s65,s71
v_rcp_f32 v41,v41
v_rcp_f32 v42,v42
v_rcp_f32 v43,v43
v_rcp_f32 v44,v44
v_rcp_f32 v45,v45
v_rcp_f32 v46,v46
v_rcp_f32 v47,v47
v_pk_mul_f32 v[40:41],v[40:41],v[104:105]
v_pk_mul_f32 v[42:43],v[42:43],v[106:107]
v_pk_mul_f32 v[44:45],v[44:45],v[108:109]
v_pk_mul_f32 v[46:47],v[46:47],v[110:111]
v_cvt_pk_bf16_f32 v124,v40,v41
v_cvt_pk_bf16_f32 v125,v42,v43
v_cvt_pk_bf16_f32 v126,v44,v45
v_cvt_pk_bf16_f32 v127,v46,v47
v_add_u32 v24,518144,v23
v_add_u32 v24,s92,v24
buffer_store_dwordx4 v[124:127],v24,%[srdW],0 offen offset:0 sc1
v_pk_mul_f32 v[40:41],v[64:65],v[64:65]
v_pk_mul_f32 v[42:43],v[66:67],v[66:67]
v_pk_mul_f32 v[44:45],v[68:69],v[68:69]
v_pk_mul_f32 v[46:47],v[70:71],v[70:71]
v_pk_fma_f32 v[40:41],v[40:41],v[20:21],v[20:21] op_sel:[0,0,1] op_sel_hi:[1,0,1]
v_pk_fma_f32 v[42:43],v[42:43],v[20:21],v[20:21] op_sel:[0,0,1] op_sel_hi:[1,0,1]
v_pk_fma_f32 v[44:45],v[44:45],v[20:21],v[20:21] op_sel:[0,0,1] op_sel_hi:[1,0,1]
v_pk_fma_f32 v[46:47],v[46:47],v[20:21],v[20:21] op_sel:[0,0,1] op_sel_hi:[1,0,1]
v_pk_mul_f32 v[40:41],v[40:41],v[64:65]
v_pk_mul_f32 v[42:43],v[42:43],v[66:67]
v_pk_mul_f32 v[44:45],v[44:45],v[68:69]
v_pk_mul_f32 v[46:47],v[46:47],v[70:71]
v_mfma_f32_16x16x32_bf16 v[56:59],v[120:123],v[32:35],0
v_mfma_f32_16x16x32_bf16 v[60:63],v[124:127],v[32:35],0
v_mfma_f32_16x16x32_bf16 v[48:51],v[120:123],v[36:39],0
v_mfma_f32_16x16x32_bf16 v[52:55],v[124:127],v[36:39],0
v_exp_f32 v40,v40
v_exp_f32 v41,v41
v_exp_f32 v42,v42
v_exp_f32 v43,v43
v_exp_f32 v44,v44
v_exp_f32 v45,v45
v_exp_f32 v46,v46
v_exp_f32 v47,v47
v_pk_add_f32 v[40:41],v[40:41],1.0 op_sel_hi:[1,0]
v_pk_add_f32 v[42:43],v[42:43],1.0 op_sel_hi:[1,0]
v_pk_add_f32 v[44:45],v[44:45],1.0 op_sel_hi:[1,0]
v_pk_add_f32 v[46:47],v[46:47],1.0 op_sel_hi:[1,0]
v_rcp_f32 v40,v40
v_rcp_f32 v41,v41
v_rcp_f32 v42,v42
v_rcp_f32 v43,v43
v_rcp_f32 v44,v44
v_rcp_f32 v45,v45
v_rcp_f32 v46,v46
v_rcp_f32 v47,v47
v_pk_mul_f32 v[40:41],v[40:41],v[64:65]
v_pk_mul_f32 v[42:43],v[42:43],v[66:67]
v_pk_mul_f32 v[44:45],v[44:45],v[68:69]
v_pk_mul_f32 v[46:47],v[46:47],v[70:71]
v_cvt_pk_bf16_f32 v64,v40,v41
v_cvt_pk_bf16_f32 v65,v42,v43
v_cvt_pk_bf16_f32 v66,v44,v45
v_cvt_pk_bf16_f32 v67,v46,v47
v_add_u32 v24,512000,v22
buffer_store_dwordx4 v[64:67],v24,%[srdW],0 offen offset:1024 sc1
v_pk_mul_f32 v[26:27],v[56:57],v[56:57]
v_pk_fma_f32 v[26:27],v[58:59],v[58:59],v[26:27]
v_pk_fma_f32 v[26:27],v[60:61],v[60:61],v[26:27]
v_pk_fma_f32 v[26:27],v[62:63],v[62:63],v[26:27]
v_pk_add_f32 v[56:57],v[56:57],v[58:59]
v_pk_add_f32 v[60:61],v[60:61],v[62:63]
v_pk_add_f32 v[56:57],v[56:57],v[60:61]
v_add_f32 v56,v56,v57
v_add_f32 v57,v26,v27
ds_write_b64 v19,v[56:57] offset:768
v_pk_mul_f32 v[26:27],v[48:49],v[48:49]
v_pk_fma_f32 v[26:27],v[50:51],v[50:51],v[26:27]
v_pk_fma_f32 v[26:27],v[52:53],v[52:53],v[26:27]
v_pk_fma_f32 v[26:27],v[54:55],v[54:55],v[26:27]
v_pk_add_f32 v[48:49],v[48:49],v[50:51]
v_pk_add_f32 v[52:53],v[52:53],v[54:55]
v_pk_add_f32 v[48:49],v[48:49],v[52:53]
v_add_f32 v48,v48,v49
v_add_f32 v49,v26,v27
ds_write_b64 v19,v[48:49] offset:896
s_waitcnt vmcnt(18)
s_waitcnt lgkmcnt(0)
s_barrier
v_lshrrev_b32 v25,2,%[vL16]
s_lshr_b32 s81,s87,4
s_and_b32 s81,s81,0x300
v_add_u32 v25,s81,v25
v_add_u32 v26,0x10000,v25
ds_read2st64_b32 v[48:49],v26 offset0:0 offset1:4
ds_read2st64_b32 v[50:51],v26 offset0:8 offset1:12
ds_read2st64_b32 v[52:53],v26 offset0:16 offset1:20
ds_read2st64_b32 v[54:55],v26 offset0:24 offset1:28
ds_read2st64_b32 v[56:57],v26 offset0:32 offset1:36
ds_read2st64_b32 v[58:59],v26 offset0:40 offset1:44
ds_read2st64_b32 v[60:61],v26 offset0:48 offset1:52
ds_read2st64_b32 v[62:63],v26 offset0:56 offset1:60
v_pk_mul_f32 v[40:41],v[72:73],v[72:73]
v_pk_mul_f32 v[42:43],v[74:75],v[74:75]
v_pk_mul_f32 v[44:45],v[76:77],v[76:77]
v_pk_mul_f32 v[46:47],v[78:79],v[78:79]
v_pk_fma_f32 v[40:41],v[40:41],v[20:21],v[20:21] op_sel:[0,0,1] op_sel_hi:[1,0,1]
v_pk_fma_f32 v[42:43],v[42:43],v[20:21],v[20:21] op_sel:[0,0,1] op_sel_hi:[1,0,1]
v_pk_fma_f32 v[44:45],v[44:45],v[20:21],v[20:21] op_sel:[0,0,1] op_sel_hi:[1,0,1]
v_pk_fma_f32 v[46:47],v[46:47],v[20:21],v[20:21] op_sel:[0,0,1] op_sel_hi:[1,0,1]
v_pk_mul_f32 v[40:41],v[40:41],v[72:73]
v_pk_mul_f32 v[42:43],v[42:43],v[74:75]
v_pk_mul_f32 v[44:45],v[44:45],v[76:77]
v_pk_mul_f32 v[46:47],v[46:47],v[78:79]
v_mfma_f32_32x32x16_bf16 a[96:111],v[28:31],v[28:31],0
v_mfma_f32_32x32x16_bf16 a[176:191],v[28:31],v[28:31],0
v_exp_f32 v40,v40
v_exp_f32 v41,v41
v_exp_f32 v42,v42
v_exp_f32 v43,v43
v_exp_f32 v44,v44
v_exp_f32 v45,v45
v_exp_f32 v46,v46
v_exp_f32 v47,v47
v_pk_add_f32 v[40:41],v[40:41],1.0 op_sel_hi:[1,0]
v_pk_add_f32 v[42:43],v[42:43],1.0 op_sel_hi:[1,0]
v_pk_add_f32 v[44:45],v[44:45],1.0 op_sel_hi:[1,0]
v_pk_add_f32 v[46:47],v[46:47],1.0 op_sel_hi:[1,0]
v_rcp_f32 v40,v40
v_rcp_f32 v41,v41
v_rcp_f32 v42,v42
v_rcp_f32 v43,v43
v_rcp_f32 v44,v44
v_rcp_f32 v45,v45
v_rcp_f32 v46,v46
v_rcp_f32 v47,v47
v_pk_mul_f32 v[40:41],v[40:41],v[72:73]
v_pk_mul_f32 v[42:43],v[42:43],v[74:75]
v_pk_mul_f32 v[44:45],v[44:45],v[76:77]
v_pk_mul_f32 v[46:47],v[46:47],v[78:79]
v_cvt_pk_bf16_f32 v72,v40,v41
v_cvt_pk_bf16_f32 v73,v42,v43
v_cvt_pk_bf16_f32 v74,v44,v45
v_cvt_pk_bf16_f32 v75,v46,v47
v_add_u32 v24,514048,v22
v_add_u32 v24,s92,v24
buffer_store_dwordx4 v[72:75],v24,%[srdW],0 offen offset:1024 sc1
s_waitcnt lgkmcnt(0)
v_pk_add_f32 v[48:49],v[48:49],v[56:57]
v_pk_add_f32 v[50:51],v[50:51],v[58:59]
v_pk_add_f32 v[52:53],v[52:53],v[60:61]
v_pk_add_f32 v[54:55],v[54:55],v[62:63]
v_pk_add_f32 v[48:49],v[48:49],v[52:53]
v_pk_add_f32 v[50:51],v[50:51],v[54:55]
v_pk_add_f32 v[48:49],v[48:49],v[50:51]
v_add_f32 v48,v48,v49
v_and_b32 v26,4,v25
v_lshrrev_b32 v25,3,v25
v_lshl_add_u32 v26,v25,5,v26
v_add_u32 v26,s88,v26
buffer_store_dword v48,v26,%[srdW],0 offen sc1
v_pk_mul_f32 v[40:41],v[80:81],v[80:81]
v_pk_mul_f32 v[42:43],v[82:83],v[82:83]
v_pk_mul_f32 v[44:45],v[84:85],v[84:85]
v_pk_mul_f32 v[46:47],v[86:87],v[86:87]
v_pk_fma_f32 v[40:41],v[40:41],v[20:21],v[20:21] op_sel:[0,0,1] op_sel_hi:[1,0,1]
v_pk_fma_f32 v[42:43],v[42:43],v[20:21],v[20:21] op_sel:[0,0,1] op_sel_hi:[1,0,1]
v_pk_fma_f32 v[44:45],v[44:45],v[20:21],v[20:21] op_sel:[0,0,1] op_sel_hi:[1,0,1]
v_pk_fma_f32 v[46:47],v[46:47],v[20:21],v[20:21] op_sel:[0,0,1] op_sel_hi:[1,0,1]
v_pk_mul_f32 v[40:41],v[40:41],v[80:81]
v_pk_mul_f32 v[42:43],v[42:43],v[82:83]
v_pk_mul_f32 v[44:45],v[44:45],v[84:85]
v_pk_mul_f32 v[46:47],v[46:47],v[86:87]
v_mfma_f32_32x32x16_bf16 a[192:207],v[28:31],v[28:31],0
v_mfma_f32_32x32x16_bf16 a[208:223],v[28:31],v[28:31],0
v_exp_f32 v40,v40
v_exp_f32 v41,v41
v_exp_f32 v42,v42
v_exp_f32 v43,v43
v_exp_f32 v44,v44
v_exp_f32 v45,v45
v_exp_f32 v46,v46
v_exp_f32 v47,v47
v_pk_add_f32 v[40:41],v[40:41],1.0 op_sel_hi:[1,0]
v_pk_add_f32 v[42:43],v[42:43],1.0 op_sel_hi:[1,0]
v_pk_add_f32 v[44:45],v[44:45],1.0 op_sel_hi:[1,0]
v_pk_add_f32 v[46:47],v[46:47],1.0 op_sel_hi:[1,0]
v_rcp_f32 v40,v40
v_rcp_f32 v41,v41
v_rcp_f32 v42,v42
v_rcp_f32 v43,v43
v_rcp_f32 v44,v44
v_rcp_f32 v45,v45
v_rcp_f32 v46,v46
v_rcp_f32 v47,v47
v_pk_mul_f32 v[40:41],v[40:41],v[80:81]
v_pk_mul_f32 v[42:43],v[42:43],v[82:83]
v_pk_mul_f32 v[44:45],v[44:45],v[84:85]
v_pk_mul_f32 v[46:47],v[46:47],v[86:87]
v_cvt_pk_bf16_f32 v80,v40,v41
v_cvt_pk_bf16_f32 v81,v42,v43
v_cvt_pk_bf16_f32 v82,v44,v45
v_cvt_pk_bf16_f32 v83,v46,v47
v_add_u32 v24,516096,v23
v_add_u32 v24,s92,v24
buffer_store_dwordx4 v[80:83],v24,%[srdW],0 offen offset:1024 sc1
v_pk_mul_f32 v[40:41],v[88:89],v[88:89]
v_pk_mul_f32 v[42:43],v[90:91],v[90:91]
v_pk_mul_f32 v[44:45],v[92:93],v[92:93]
v_pk_mul_f32 v[46:47],v[94:95],v[94:95]
v_pk_fma_f32 v[40:41],v[40:41],v[20:21],v[20:21] op_sel:[0,0,1] op_sel_hi:[1,0,1]
v_pk_fma_f32 v[42:43],v[42:43],v[20:21],v[20:21] op_sel:[0,0,1] op_sel_hi:[1,0,1]
v_pk_fma_f32 v[44:45],v[44:45],v[20:21],v[20:21] op_sel:[0,0,1] op_sel_hi:[1,0,1]
v_pk_fma_f32 v[46:47],v[46:47],v[20:21],v[20:21] op_sel:[0,0,1] op_sel_hi:[1,0,1]
v_pk_mul_f32 v[40:41],v[40:41],v[88:89]
v_pk_mul_f32 v[42:43],v[42:43],v[90:91]
v_pk_mul_f32 v[44:45],v[44:45],v[92:93]
v_pk_mul_f32 v[46:47],v[46:47],v[94:95]
v_mfma_f32_32x32x16_bf16 a[224:239],v[28:31],v[28:31],0
v_mfma_f32_32x32x16_bf16 a[240:255],v[28:31],v[28:31],0
v_exp_f32 v40,v40
v_exp_f32 v41,v41
v_exp_f32 v42,v42
v_exp_f32 v43,v43
v_exp_f32 v44,v44
v_exp_f32 v45,v45
v_exp_f32 v46,v46
v_exp_f32 v47,v47
v_pk_add_f32 v[40:41],v[40:41],1.0 op_sel_hi:[1,0]
v_pk_add_f32 v[42:43],v[42:43],1.0 op_sel_hi:[1,0]
v_pk_add_f32 v[44:45],v[44:45],1.0 op_sel_hi:[1,0]
v_pk_add_f32 v[46:47],v[46:47],1.0 op_sel_hi:[1,0]
v_rcp_f32 v40,v40
v_rcp_f32 v41,v41
v_rcp_f32 v42,v42
v_rcp_f32 v43,v43
v_rcp_f32 v44,v44
v_rcp_f32 v45,v45
v_rcp_f32 v46,v46
v_rcp_f32 v47,v47
v_pk_mul_f32 v[40:41],v[40:41],v[88:89]
v_pk_mul_f32 v[42:43],v[42:43],v[90:91]
v_pk_mul_f32 v[44:45],v[44:45],v[92:93]
v_pk_mul_f32 v[46:47],v[46:47],v[94:95]
v_cvt_pk_bf16_f32 v88,v40,v41
v_cvt_pk_bf16_f32 v89,v42,v43
v_cvt_pk_bf16_f32 v90,v44,v45
v_cvt_pk_bf16_f32 v91,v46,v47
v_add_u32 v24,518144,v23
v_add_u32 v24,s92,v24
buffer_store_dwordx4 v[88:91],v24,%[srdW],0 offen offset:1024 sc1
v_add_u32 v22,0x1000,%[vL16]
v_mov_b32 v48,0xc1300000
v_mov_b32 v49,0xc1300000
v_mov_b32 v50,0xc1300000
v_mov_b32 v51,0xc1300000
v_mov_b32 v52,0xc1300000
v_mov_b32 v53,0xc1300000
v_mov_b32 v54,0xc1300000
v_mov_b32 v55,0xc1300000
v_mov_b32 v28,0x41300000
v_mov_b32 v29,0x41300000
v_mov_b32 v30,0
v_mov_b32 v31,0
v_mov_b32 v15,s55
v_mov_b32 v24,v3
v_add_u32 v23,0x4000,v3
ds_read_b128 v[64:67],v24 offset:0
ds_read_b128 v[68:71],v24 offset:512
ds_read_b128 v[72:75],v24 offset:2048
ds_read_b128 v[76:79],v24 offset:2560
ds_read_b128 v[80:83],v24 offset:4096
ds_read_b128 v[84:87],v24 offset:4608
ds_read_b128 v[88:91],v24 offset:6144
s_waitcnt lgkmcnt(6)
v_mfma_f32_16x16x32_bf16 v[96:99],v[64:67],v[128:131],v[48:51]
ds_read_b128 v[92:95],v24 offset:6656
v_mfma_f32_16x16x32_bf16 v[100:103],v[64:67],v[132:135],v[52:55]
s_waitcnt lgkmcnt(6)
v_mfma_f32_16x16x32_bf16 v[104:107],v[68:71],v[128:131],v[48:51]
ds_read_b128 v[64:67],v24 offset:8192
v_mfma_f32_16x16x32_bf16 v[108:111],v[68:71],v[132:135],v[52:55]
s_waitcnt lgkmcnt(6)
v_mfma_f32_16x16x32_bf16 v[96:99],v[72:75],v[136:139],v[96:99]
ds_read_b128 v[68:71],v24 offset:8704
v_mfma_f32_16x16x32_bf16 v[100:103],v[72:75],v[140:143],v[100:103]
s_waitcnt lgkmcnt(6)
v_mfma_f32_16x16x32_bf16 v[104:107],v[76:79],v[136:139],v[104:107]
ds_read_b128 v[72:75],v24 offset:10240
v_mfma_f32_16x16x32_bf16 v[108:111],v[76:79],v[140:143],v[108:111]
s_waitcnt lgkmcnt(6)
v_mfma_f32_16x16x32_bf16 v[96:99],v[80:83],v[144:147],v[96:99]
ds_read_b128 v[76:79],v24 offset:10752
v_mfma_f32_16x16x32_bf16 v[100:103],v[80:83],v[148:151],v[100:103]
s_waitcnt lgkmcnt(6)
v_mfma_f32_16x16x32_bf16 v[104:107],v[84:87],v[144:147],v[104:107]
ds_read_b128 v[80:83],v24 offset:12288
)"
R"(v_mfma_f32_16x16x32_bf16 v[108:111],v[84:87],v[148:151],v[108:111]
s_waitcnt lgkmcnt(6)
v_mfma_f32_16x16x32_bf16 v[96:99],v[88:91],v[152:155],v[96:99]
ds_read_b128 v[84:87],v24 offset:12800
v_mfma_f32_16x16x32_bf16 v[100:103],v[88:91],v[156:159],v[100:103]
s_waitcnt lgkmcnt(6)
v_mfma_f32_16x16x32_bf16 v[104:107],v[92:95],v[152:155],v[104:107]
ds_read_b128 v[88:91],v24 offset:14336
v_mfma_f32_16x16x32_bf16 v[108:111],v[92:95],v[156:159],v[108:111]
s_waitcnt lgkmcnt(6)
v_mfma_f32_16x16x32_bf16 v[96:99],v[64:67],v[160:163],v[96:99]
ds_read_b128 v[92:95],v24 offset:14848
v_mfma_f32_16x16x32_bf16 v[100:103],v[64:67],v[164:167],v[100:103]
s_waitcnt lgkmcnt(6)
v_mfma_f32_16x16x32_bf16 v[104:107],v[68:71],v[160:163],v[104:107]
ds_read_b128 v[64:67],v23 offset:0
v_mfma_f32_16x16x32_bf16 v[108:111],v[68:71],v[164:167],v[108:111]
s_waitcnt lgkmcnt(6)
v_mfma_f32_16x16x32_bf16 v[96:99],v[72:75],v[168:171],v[96:99]
ds_read_b128 v[68:71],v23 offset:2048
v_mfma_f32_16x16x32_bf16 v[100:103],v[72:75],v[172:175],v[100:103]
s_waitcnt lgkmcnt(6)
v_mfma_f32_16x16x32_bf16 v[104:107],v[76:79],v[168:171],v[104:107]
ds_read_b128 v[72:75],v23 offset:4096
v_mfma_f32_16x16x32_bf16 v[108:111],v[76:79],v[172:175],v[108:111]
s_waitcnt lgkmcnt(6)
v_mfma_f32_16x16x32_bf16 v[96:99],v[80:83],v[176:179],v[96:99]
ds_read_b128 v[76:79],v23 offset:6144
v_mfma_f32_16x16x32_bf16 v[100:103],v[80:83],v[180:183],v[100:103]
s_waitcnt lgkmcnt(6)
v_mfma_f32_16x16x32_bf16 v[104:107],v[84:87],v[176:179],v[104:107]
ds_read_b128 v[80:83],v23 offset:8192
v_mfma_f32_16x16x32_bf16 v[108:111],v[84:87],v[180:183],v[108:111]
s_waitcnt lgkmcnt(6)
v_mfma_f32_16x16x32_bf16 v[96:99],v[88:91],v[184:187],v[96:99]
ds_read_b128 v[84:87],v23 offset:10240
v_mfma_f32_16x16x32_bf16 v[100:103],v[88:91],v[188:191],v[100:103]
s_waitcnt lgkmcnt(6)
v_mfma_f32_16x16x32_bf16 v[104:107],v[92:95],v[184:187],v[104:107]
ds_read_b128 v[88:91],v23 offset:12288
v_mfma_f32_16x16x32_bf16 v[108:111],v[92:95],v[188:191],v[108:111]
s_and_b32 s66,s47,0xff
s_sub_u32 s66,s66,1
s_sub_u32 s57,s66,1
s_waitcnt lgkmcnt(6)
.p2align 4
LOOP_%=:
v_mfma_f32_16x16x32_bf16 v[96:99],v[64:67],v[192:195],v[96:99]
ds_read_b128 v[92:95],v23 offset:14336
v_cmp_ne_u32 vcc,s55,v15
v_mfma_f32_16x16x32_bf16 v[100:103],v[64:67],v[196:199],v[100:103]
s_waitcnt lgkmcnt(6)
v_mfma_f32_16x16x32_bf16 v[96:99],v[68:71],v[200:203],v[96:99]
s_cbranch_vccnz SPWA_%=
SPWBA_%=:
ds_read_b128 v[64:67],v23 offset:512
v_mfma_f32_16x16x32_bf16 v[100:103],v[68:71],v[204:207],v[100:103]
s_waitcnt lgkmcnt(6)
v_mfma_f32_16x16x32_bf16 v[96:99],v[72:75],v[208:211],v[96:99]
ds_read_b128 v[68:71],v23 offset:2560
v_add_u32 v24,s62,v3
v_mfma_f32_16x16x32_bf16 v[100:103],v[72:75],v[212:215],v[100:103]
s_waitcnt lgkmcnt(6)
buffer_load_dwordx4 v22,%[srdW],s59 offen offset:2048 sc1 lds
v_mfma_f32_16x16x32_bf16 v[96:99],v[76:79],v[216:219],v[96:99]
ds_read_b128 v[72:75],v23 offset:4608
v_mfma_f32_16x16x32_bf16 v[100:103],v[76:79],v[220:223],v[100:103]
s_waitcnt lgkmcnt(6)
v_mfma_f32_16x16x32_bf16 v[96:99],v[80:83],v[224:227],v[96:99]
ds_read_b128 v[76:79],v23 offset:6656
v_add_u32 v25,s60,v4
v_mfma_f32_16x16x32_bf16 v[100:103],v[80:83],v[228:231],v[100:103]
s_waitcnt lgkmcnt(6)
v_mfma_f32_16x16x32_bf16 v[96:99],v[84:87],v[232:235],v[96:99]
ds_read_b128 v[80:83],v23 offset:8704
v_mfma_f32_16x16x32_bf16 v[100:103],v[84:87],v[236:239],v[100:103]
s_waitcnt lgkmcnt(6)
buffer_load_dwordx4 v22,%[srdW],s59 offen offset:3072 sc1 lds
v_mfma_f32_16x16x32_bf16 v[96:99],v[88:91],v[240:243],v[96:99]
s_add_u32 m0,s64,s41
ds_read_b128 v[84:87],v23 offset:10752
v_add_u32 v26,s61,v4
v_mfma_f32_16x16x32_bf16 v[100:103],v[88:91],v[244:247],v[100:103]
s_waitcnt lgkmcnt(6)
v_mfma_f32_16x16x32_bf16 v[96:99],v[92:95],v[248:251],v[96:99]
s_waitcnt vmcnt(8)
ds_read_b128 v[88:91],v23 offset:12800
v_mfma_f32_16x16x32_bf16 v[100:103],v[92:95],v[252:255],v[100:103]
s_waitcnt lgkmcnt(6)
v_mfma_f32_16x16x32_bf16 v[104:107],v[64:67],v[192:195],v[104:107]
s_barrier
ds_read_b128 v[92:95],v23 offset:14848
v_mfma_f32_16x16x32_bf16 v[108:111],v[64:67],v[196:199],v[108:111]
s_waitcnt lgkmcnt(6)
buffer_load_dwordx4 %[vL16],%[srdW],s65 offen offset:0 sc1 lds
v_mfma_f32_16x16x32_bf16 v[104:107],v[68:71],v[200:203],v[104:107]
s_cmp_eq_u32 s66,s57
ds_read_b128 v[64:67],v24 offset:0
v_mfma_f32_16x16x32_bf16 v[108:111],v[68:71],v[204:207],v[108:111]
s_waitcnt lgkmcnt(6)
v_exp_f32 v32,v96
v_mfma_f32_16x16x32_bf16 v[104:107],v[72:75],v[208:211],v[104:107]
s_cbranch_scc1 SIDEA_%=
SBACKA_%=:
ds_read_b128 v[68:71],v24 offset:512
v_max3_f32 v27,v96,v97,v98
v_mfma_f32_16x16x32_bf16 v[108:111],v[72:75],v[212:215],v[108:111]
s_waitcnt lgkmcnt(6)
v_exp_f32 v33,v97
v_mfma_f32_16x16x32_bf16 v[104:107],v[76:79],v[216:219],v[104:107]
ds_read_b128 v[72:75],v24 offset:2048
v_cvt_pk_bf16_f32 v56,v32,v33
v_mfma_f32_16x16x32_bf16 v[108:111],v[76:79],v[220:223],v[108:111]
s_waitcnt lgkmcnt(6)
v_exp_f32 v34,v98
v_mfma_f32_16x16x32_bf16 v[104:107],v[80:83],v[224:227],v[104:107]
ds_read_b128 v[76:79],v24 offset:2560
v_max3_f32 v27,v27,v99,v100
v_mfma_f32_16x16x32_bf16 v[108:111],v[80:83],v[228:231],v[108:111]
s_waitcnt lgkmcnt(6)
buffer_load_dwordx4 %[vL16],%[srdW],s65 offen offset:1024 sc1 lds
v_mfma_f32_16x16x32_bf16 v[104:107],v[84:87],v[232:235],v[104:107]
ds_read_b128 v[80:83],v24 offset:4096
v_max3_f32 v27,v27,v101,v102
v_mfma_f32_16x16x32_bf16 v[108:111],v[84:87],v[236:239],v[108:111]
s_waitcnt lgkmcnt(6)
v_exp_f32 v35,v99
v_mfma_f32_16x16x32_bf16 v[104:107],v[88:91],v[240:243],v[104:107]
ds_read_b128 v[84:87],v24 offset:4608
v_cvt_pk_bf16_f32 v57,v34,v35
v_mfma_f32_16x16x32_bf16 v[108:111],v[88:91],v[244:247],v[108:111]
s_waitcnt lgkmcnt(6)
v_exp_f32 v36,v100
v_mfma_f32_16x16x32_bf16 v[104:107],v[92:95],v[248:251],v[104:107]
ds_read_b128 v[88:91],v24 offset:6144
v_max_f32 v27,v27,v103
v_mfma_f32_16x16x32_bf16 v[108:111],v[92:95],v[252:255],v[108:111]
s_waitcnt lgkmcnt(6)
v_exp_f32 v37,v101
v_mfma_f32_16x16x32_bf16 v[112:115],v[64:67],v[128:131],v[48:51]
ds_read_b128 v[92:95],v24 offset:6656
v_cvt_pk_bf16_f32 v60,v36,v37
v_mfma_f32_16x16x32_bf16 v[116:119],v[64:67],v[132:135],v[52:55]
s_waitcnt lgkmcnt(6)
buffer_load_dwordx4 %[vL16],%[srdW],s65 offen offset:2048 sc1 lds
v_mfma_f32_16x16x32_bf16 v[124:127],v[68:71],v[132:135],v[52:55]
ds_read_b128 v[64:67],v24 offset:8192
v_mfma_f32_16x16x32_bf16 v[120:123],v[68:71],v[128:131],v[48:51]
s_waitcnt lgkmcnt(6)
v_exp_f32 v38,v102
v_mfma_f32_16x16x32_bf16 v[112:115],v[72:75],v[136:139],v[112:115]
ds_read_b128 v[68:71],v24 offset:8704
v_max3_f32 v27,v27,v104,v105
v_mfma_f32_16x16x32_bf16 v[116:119],v[72:75],v[140:143],v[116:119]
s_waitcnt lgkmcnt(6)
v_exp_f32 v39,v103
v_mfma_f32_16x16x32_bf16 v[124:127],v[76:79],v[140:143],v[124:127]
ds_read_b128 v[72:75],v24 offset:10240
v_cvt_pk_bf16_f32 v61,v38,v39
v_mfma_f32_16x16x32_bf16 v[120:123],v[76:79],v[136:139],v[120:123]
s_waitcnt lgkmcnt(6)
v_exp_f32 v40,v104
v_mfma_f32_16x16x32_bf16 v[112:115],v[80:83],v[144:147],v[112:115]
ds_read_b128 v[76:79],v24 offset:10752
v_max3_f32 v27,v27,v106,v107
v_mfma_f32_16x16x32_bf16 v[116:119],v[80:83],v[148:151],v[116:119]
s_waitcnt lgkmcnt(6)
buffer_load_dwordx4 %[vL16],%[srdW],s65 offen offset:3072 sc1 lds
v_mfma_f32_16x16x32_bf16 v[124:127],v[84:87],v[148:151],v[124:127]
ds_read_b128 v[80:83],v24 offset:12288
v_max3_f32 v27,v27,v108,v109
v_mfma_f32_16x16x32_bf16 v[120:123],v[84:87],v[144:147],v[120:123]
s_waitcnt lgkmcnt(6)
v_exp_f32 v41,v105
v_mfma_f32_16x16x32_bf16 v[112:115],v[88:91],v[152:155],v[112:115]
s_add_u32 m0,m0,0x1000
ds_read_b128 v[84:87],v24 offset:12800
v_cvt_pk_bf16_f32 v58,v40,v41
v_mfma_f32_16x16x32_bf16 v[116:119],v[88:91],v[156:159],v[116:119]
s_waitcnt lgkmcnt(6)
v_exp_f32 v42,v106
v_mfma_f32_16x16x32_bf16 v[124:127],v[92:95],v[156:159],v[124:127]
ds_read_b128 v[88:91],v24 offset:14336
v_max3_f32 v27,v27,v110,v111
v_mfma_f32_16x16x32_bf16 v[120:123],v[92:95],v[152:155],v[120:123]
s_waitcnt lgkmcnt(6)
v_exp_f32 v43,v107
v_mfma_f32_16x16x32_bf16 v[112:115],v[64:67],v[160:163],v[112:115]
ds_read_b128 v[92:95],v24 offset:14848
v_cvt_pk_bf16_f32 v59,v42,v43
v_mfma_f32_16x16x32_bf16 v[116:119],v[64:67],v[164:167],v[116:119]
s_waitcnt lgkmcnt(6)
buffer_load_dwordx4 v22,%[srdW],s65 offen offset:0 sc1 lds
v_mfma_f32_16x16x32_bf16 v[124:127],v[68:71],v[164:167],v[124:127]
ds_read_b64_tr_b16 v[64:65],v25 offset:0
ds_read_b64_tr_b16 v[66:67],v25 offset:512
v_mfma_f32_16x16x32_bf16 v[120:123],v[68:71],v[160:163],v[120:123]
s_waitcnt lgkmcnt(7)
v_exp_f32 v44,v108
v_mfma_f32_16x16x32_bf16 v[112:115],v[72:75],v[168:171],v[112:115]
ds_read_b64_tr_b16 v[68:69],v25 offset:1024
ds_read_b64_tr_b16 v[70:71],v25 offset:1536
v_mfma_f32_16x16x32_bf16 v[116:119],v[72:75],v[172:175],v[116:119]
s_waitcnt lgkmcnt(8)
v_exp_f32 v45,v109
v_mfma_f32_16x16x32_bf16 v[124:127],v[76:79],v[172:175],v[124:127]
ds_read_b64_tr_b16 v[72:73],v25 offset:2048
ds_read_b64_tr_b16 v[74:75],v25 offset:2560
v_mfma_f32_16x16x32_bf16 v[120:123],v[76:79],v[168:171],v[120:123]
s_waitcnt lgkmcnt(9)
v_exp_f32 v46,v110
v_mfma_f32_16x16x32_bf16 v[112:115],v[80:83],v[176:179],v[112:115]
ds_read_b64_tr_b16 v[76:77],v25 offset:3072
ds_read_b64_tr_b16 v[78:79],v25 offset:3584
v_mfma_f32_16x16x32_bf16 v[116:119],v[80:83],v[180:183],v[116:119]
s_waitcnt lgkmcnt(10)
v_exp_f32 v47,v111
v_mfma_f32_16x16x32_bf16 v[124:127],v[84:87],v[180:183],v[124:127]
ds_read_b64_tr_b16 v[80:81],v25 offset:4096
ds_read_b64_tr_b16 v[82:83],v25 offset:4608
v_mfma_f32_16x16x32_bf16 v[120:123],v[84:87],v[176:179],v[120:123]
s_waitcnt lgkmcnt(11)
buffer_load_dwordx4 v22,%[srdW],s65 offen offset:1024 sc1 lds
v_mfma_f32_16x16x32_bf16 v[112:115],v[88:91],v[184:187],v[112:115]
ds_read_b64_tr_b16 v[84:85],v25 offset:5120
v_cvt_pk_bf16_f32 v62,v44,v45
v_mfma_f32_16x16x32_bf16 v[116:119],v[88:91],v[188:191],v[116:119]
s_waitcnt lgkmcnt(11)
ds_read_b64_tr_b16 v[86:87],v25 offset:5632
v_cvt_pk_bf16_f32 v63,v46,v47
v_mfma_f32_16x16x32_bf16 v[124:127],v[92:95],v[188:191],v[124:127]
ds_read_b64_tr_b16 v[88:89],v25 offset:6144
v_cmp_lt_f32 vcc,s68,v27
v_mfma_f32_16x16x32_bf16 v[120:123],v[92:95],v[184:187],v[120:123]
s_waitcnt lgkmcnt(11)
ds_read_b64_tr_b16 v[90:91],v25 offset:6656
s_cbranch_vccnz RAREA_%=
BACKA_%=:
v_mfma_f32_16x16x32_bf16 a[0:3],v[64:67],v[56:59],a[0:3]
ds_read_b64_tr_b16 v[92:93],v25 offset:7168
v_add_f32 v30,v30,v32
v_mfma_f32_16x16x32_bf16 a[4:7],v[64:67],v[60:63],a[4:7]
s_waitcnt lgkmcnt(11)
ds_read_b64_tr_b16 v[94:95],v25 offset:7680
v_add_f32 v31,v31,v36
v_mfma_f32_16x16x32_bf16 a[12:15],v[68:71],v[60:63],a[12:15]
ds_read_b64_tr_b16 v[64:65],v25 offset:8192
ds_read_b64_tr_b16 v[66:67],v25 offset:8704
v_mfma_f32_16x16x32_bf16 a[8:11],v[68:71],v[56:59],a[8:11]
s_waitcnt lgkmcnt(12)
buffer_load_dwordx4 v22,%[srdW],s65 offen offset:2048 sc1 lds
v_mfma_f32_16x16x32_bf16 a[16:19],v[72:75],v[56:59],a[16:19]
ds_read_b64_tr_b16 v[68:69],v25 offset:9216
v_add_f32 v30,v30,v40
v_mfma_f32_16x16x32_bf16 a[20:23],v[72:75],v[60:63],a[20:23]
s_waitcnt lgkmcnt(11)
ds_read_b64_tr_b16 v[70:71],v25 offset:9728
v_add_f32 v31,v31,v44
v_mfma_f32_16x16x32_bf16 a[28:31],v[76:79],v[60:63],a[28:31]
ds_read_b64_tr_b16 v[72:73],v25 offset:10240
v_add_f32 v30,v30,v33
v_mfma_f32_16x16x32_bf16 a[24:27],v[76:79],v[56:59],a[24:27]
s_waitcnt lgkmcnt(11)
ds_read_b64_tr_b16 v[74:75],v25 offset:10752
v_add_f32 v31,v31,v37
v_mfma_f32_16x16x32_bf16 a[32:35],v[80:83],v[56:59],a[32:35]
ds_read_b64_tr_b16 v[76:77],v25 offset:11264
v_add_f32 v30,v30,v41
v_mfma_f32_16x16x32_bf16 a[36:39],v[80:83],v[60:63],a[36:39]
s_waitcnt lgkmcnt(11)
ds_read_b64_tr_b16 v[78:79],v25 offset:11776
v_add_f32 v31,v31,v45
v_mfma_f32_16x16x32_bf16 a[44:47],v[84:87],v[60:63],a[44:47]
ds_read_b64_tr_b16 v[80:81],v25 offset:12288
ds_read_b64_tr_b16 v[82:83],v25 offset:12800
v_mfma_f32_16x16x32_bf16 a[40:43],v[84:87],v[56:59],a[40:43]
s_waitcnt lgkmcnt(12)
buffer_load_dwordx4 v22,%[srdW],s65 offen offset:3072 sc1 lds
v_mfma_f32_16x16x32_bf16 a[48:51],v[88:91],v[56:59],a[48:51]
s_add_u32 s59,s65,s70
ds_read_b64_tr_b16 v[84:85],v25 offset:13312
v_add_f32 v30,v30,v34
v_mfma_f32_16x16x32_bf16 a[52:55],v[88:91],v[60:63],a[52:55]
s_waitcnt lgkmcnt(11)
ds_read_b64_tr_b16 v[86:87],v25 offset:13824
v_add_f32 v31,v31,v38
v_mfma_f32_16x16x32_bf16 a[60:63],v[92:95],v[60:63],a[60:63]
s_add_u32 s65,s59,s71
ds_read_b64_tr_b16 v[88:89],v25 offset:14336
v_add_f32 v30,v30,v42
v_mfma_f32_16x16x32_bf16 a[56:59],v[92:95],v[56:59],a[56:59]
s_waitcnt lgkmcnt(11)
ds_read_b64_tr_b16 v[90:91],v25 offset:14848
v_add_f32 v31,v31,v46
v_mfma_f32_16x16x32_bf16 a[64:67],v[64:67],v[56:59],a[64:67]
s_cmp_eq_u32 s66,2
ds_read_b64_tr_b16 v[92:93],v25 offset:15360
v_add_f32 v30,v30,v35
v_mfma_f32_16x16x32_bf16 a[68:71],v[64:67],v[60:63],a[68:71]
s_waitcnt lgkmcnt(11)
ds_read_b64_tr_b16 v[94:95],v25 offset:15872
v_add_f32 v31,v31,v39
v_mfma_f32_16x16x32_bf16 a[76:79],v[68:71],v[60:63],a[76:79]
s_cselect_b32 s65,s43,s65
ds_read_b64_tr_b16 v[64:65],v26 offset:0
v_add_f32 v30,v30,v43
v_mfma_f32_16x16x32_bf16 a[72:75],v[68:71],v[56:59],a[72:75]
s_waitcnt lgkmcnt(11)
ds_read_b64_tr_b16 v[66:67],v26 offset:512
v_add_f32 v31,v31,v47
v_mfma_f32_16x16x32_bf16 a[80:83],v[72:75],v[56:59],a[80:83]
s_cselect_b32 s70,0x2000,s70
ds_read_b64_tr_b16 v[68:69],v26 offset:1024
v_mfma_f32_16x16x32_bf16 a[84:87],v[72:75],v[60:63],a[84:87]
s_waitcnt lgkmcnt(11)
ds_read_b64_tr_b16 v[70:71],v26 offset:1536
v_mfma_f32_16x16x32_bf16 a[92:95],v[76:79],v[60:63],a[92:95]
s_cselect_b32 s71,0x2000,s71
ds_read_b64_tr_b16 v[72:73],v26 offset:2048
v_mfma_f32_16x16x32_bf16 a[88:91],v[76:79],v[56:59],a[88:91]
s_waitcnt lgkmcnt(11)
ds_read_b64_tr_b16 v[74:75],v26 offset:2560
v_mfma_f32_16x16x32_bf16 a[96:99],v[80:83],v[56:59],a[96:99]
ds_read_b64_tr_b16 v[76:77],v26 offset:3072
v_mfma_f32_16x16x32_bf16 a[100:103],v[80:83],v[60:63],a[100:103]
s_waitcnt lgkmcnt(11)
ds_read_b64_tr_b16 v[78:79],v26 offset:3584
v_mfma_f32_16x16x32_bf16 a[108:111],v[84:87],v[60:63],a[108:111]
ds_read_b64_tr_b16 v[80:81],v26 offset:4096
v_mfma_f32_16x16x32_bf16 a[104:107],v[84:87],v[56:59],a[104:107]
s_waitcnt lgkmcnt(11)
ds_read_b64_tr_b16 v[82:83],v26 offset:4608
v_mfma_f32_16x16x32_bf16 a[112:115],v[88:91],v[56:59],a[112:115]
s_add_u32 m0,s60,s41
ds_read_b64_tr_b16 v[84:85],v26 offset:5120
v_mfma_f32_16x16x32_bf16 a[116:119],v[88:91],v[60:63],a[116:119]
s_waitcnt lgkmcnt(11)
ds_read_b64_tr_b16 v[86:87],v26 offset:5632
v_mfma_f32_16x16x32_bf16 a[124:127],v[92:95],v[60:63],a[124:127]
s_waitcnt vmcnt(8)
ds_read_b64_tr_b16 v[88:89],v26 offset:6144
v_mfma_f32_16x16x32_bf16 a[120:123],v[92:95],v[56:59],a[120:123]
s_waitcnt lgkmcnt(11)
ds_read_b64_tr_b16 v[90:91],v26 offset:6656
v_mfma_f32_16x16x32_bf16 a[128:131],v[64:67],v[56:59],a[128:131]
s_barrier
ds_read_b64_tr_b16 v[92:93],v26 offset:7168
ds_read_b64_tr_b16 v[94:95],v26 offset:7680
v_mfma_f32_16x16x32_bf16 a[132:135],v[64:67],v[60:63],a[132:135]
s_waitcnt lgkmcnt(12)
buffer_load_dwordx4 %[vL16],%[srdW],s59 offen offset:0 sc1 lds
v_mfma_f32_16x16x32_bf16 a[140:143],v[68:71],v[60:63],a[140:143]
ds_read_b64_tr_b16 v[64:65],v26 offset:8192
v_mfma_f32_16x16x32_bf16 a[136:139],v[68:71],v[56:59],a[136:139]
s_waitcnt lgkmcnt(11)
ds_read_b64_tr_b16 v[66:67],v26 offset:8704
v_mfma_f32_16x16x32_bf16 a[144:147],v[72:75],v[56:59],a[144:147]
ds_read_b64_tr_b16 v[68:69],v26 offset:9216
v_add_u32 v23,s63,v3
v_mfma_f32_16x16x32_bf16 a[148:151],v[72:75],v[60:63],a[148:151]
s_waitcnt lgkmcnt(11)
ds_read_b64_tr_b16 v[70:71],v26 offset:9728
v_mfma_f32_16x16x32_bf16 a[156:159],v[76:79],v[60:63],a[156:159]
ds_read_b64_tr_b16 v[72:73],v26 offset:10240
ds_read_b64_tr_b16 v[74:75],v26 offset:10752
v_mfma_f32_16x16x32_bf16 a[152:155],v[76:79],v[56:59],a[152:155]
s_waitcnt lgkmcnt(12)
buffer_load_dwordx4 %[vL16],%[srdW],s59 offen offset:1024 sc1 lds
v_mfma_f32_16x16x32_bf16 a[160:163],v[80:83],v[56:59],a[160:163]
s_mov_b32 s67,s60
ds_read_b64_tr_b16 v[76:77],v26 offset:11264
v_mfma_f32_16x16x32_bf16 a[164:167],v[80:83],v[60:63],a[164:167]
s_waitcnt lgkmcnt(11)
ds_read_b64_tr_b16 v[78:79],v26 offset:11776
v_mfma_f32_16x16x32_bf16 a[172:175],v[84:87],v[60:63],a[172:175]
s_mov_b32 s60,s62
ds_read_b64_tr_b16 v[80:81],v26 offset:12288
v_mfma_f32_16x16x32_bf16 a[168:171],v[84:87],v[56:59],a[168:171]
s_waitcnt lgkmcnt(11)
ds_read_b64_tr_b16 v[82:83],v26 offset:12800
v_mfma_f32_16x16x32_bf16 a[176:179],v[88:91],v[56:59],a[176:179]
s_mov_b32 s62,s64
ds_read_b64_tr_b16 v[84:85],v26 offset:13312
ds_read_b64_tr_b16 v[86:87],v26 offset:13824
v_mfma_f32_16x16x32_bf16 a[180:183],v[88:91],v[60:63],a[180:183]
s_waitcnt lgkmcnt(12)
buffer_load_dwordx4 %[vL16],%[srdW],s59 offen offset:2048 sc1 lds
v_mfma_f32_16x16x32_bf16 a[188:191],v[92:95],v[60:63],a[188:191]
ds_read_b64_tr_b16 v[88:89],v26 offset:14336
v_mfma_f32_16x16x32_bf16 a[184:187],v[92:95],v[56:59],a[184:187]
s_waitcnt lgkmcnt(11)
ds_read_b64_tr_b16 v[90:91],v26 offset:14848
v_mfma_f32_16x16x32_bf16 a[192:195],v[64:67],v[56:59],a[192:195]
s_mov_b32 s64,s61
ds_read_b64_tr_b16 v[92:93],v26 offset:15360
v_mfma_f32_16x16x32_bf16 a[196:199],v[64:67],v[60:63],a[196:199]
s_waitcnt lgkmcnt(11)
ds_read_b64_tr_b16 v[94:95],v26 offset:15872
v_mfma_f32_16x16x32_bf16 a[204:207],v[68:71],v[60:63],a[204:207]
s_mov_b32 s61,s63
)"
R"(ds_read_b128 v[64:67],v23 offset:0
v_mfma_f32_16x16x32_bf16 a[200:203],v[68:71],v[56:59],a[200:203]
s_waitcnt lgkmcnt(11)
buffer_load_dwordx4 %[vL16],%[srdW],s59 offen offset:3072 sc1 lds
v_mfma_f32_16x16x32_bf16 a[208:211],v[72:75],v[56:59],a[208:211]
s_mov_b32 s63,s67
ds_read_b128 v[68:71],v23 offset:2048
v_mfma_f32_16x16x32_bf16 a[212:215],v[72:75],v[60:63],a[212:215]
s_waitcnt lgkmcnt(10)
v_mfma_f32_16x16x32_bf16 a[220:223],v[76:79],v[60:63],a[220:223]
s_add_u32 m0,m0,0x1000
ds_read_b128 v[72:75],v23 offset:4096
v_mfma_f32_16x16x32_bf16 a[216:219],v[76:79],v[56:59],a[216:219]
s_waitcnt lgkmcnt(9)
v_mfma_f32_16x16x32_bf16 a[224:227],v[80:83],v[56:59],a[224:227]
ds_read_b128 v[76:79],v23 offset:6144
v_mfma_f32_16x16x32_bf16 a[228:231],v[80:83],v[60:63],a[228:231]
s_waitcnt lgkmcnt(8)
buffer_load_dwordx4 v22,%[srdW],s59 offen offset:0 sc1 lds
v_mfma_f32_16x16x32_bf16 a[236:239],v[84:87],v[60:63],a[236:239]
s_sub_u32 s66,s66,1
ds_read_b128 v[80:83],v23 offset:8192
v_mfma_f32_16x16x32_bf16 a[232:235],v[84:87],v[56:59],a[232:235]
s_waitcnt lgkmcnt(7)
v_mfma_f32_16x16x32_bf16 a[240:243],v[88:91],v[56:59],a[240:243]
ds_read_b128 v[84:87],v23 offset:10240
v_mfma_f32_16x16x32_bf16 a[244:247],v[88:91],v[60:63],a[244:247]
s_waitcnt lgkmcnt(6)
buffer_load_dwordx4 v22,%[srdW],s59 offen offset:1024 sc1 lds
v_mfma_f32_16x16x32_bf16 a[252:255],v[92:95],v[60:63],a[252:255]
s_cmp_eq_u32 s66,0
ds_read_b128 v[88:91],v23 offset:12288
v_mfma_f32_16x16x32_bf16 a[248:251],v[92:95],v[56:59],a[248:251]
s_waitcnt lgkmcnt(6)
s_cbranch_scc1 TAILB_%=
v_mfma_f32_16x16x32_bf16 v[112:115],v[64:67],v[192:195],v[112:115]
ds_read_b128 v[92:95],v23 offset:14336
v_cmp_ne_u32 vcc,s55,v15
v_mfma_f32_16x16x32_bf16 v[116:119],v[64:67],v[196:199],v[116:119]
s_waitcnt lgkmcnt(6)
v_mfma_f32_16x16x32_bf16 v[112:115],v[68:71],v[200:203],v[112:115]
s_cbranch_vccnz SPWB_%=
SPWBB_%=:
ds_read_b128 v[64:67],v23 offset:512
v_mfma_f32_16x16x32_bf16 v[116:119],v[68:71],v[204:207],v[116:119]
s_waitcnt lgkmcnt(6)
v_mfma_f32_16x16x32_bf16 v[112:115],v[72:75],v[208:211],v[112:115]
ds_read_b128 v[68:71],v23 offset:2560
v_add_u32 v24,s62,v3
v_mfma_f32_16x16x32_bf16 v[116:119],v[72:75],v[212:215],v[116:119]
s_waitcnt lgkmcnt(6)
buffer_load_dwordx4 v22,%[srdW],s59 offen offset:2048 sc1 lds
v_mfma_f32_16x16x32_bf16 v[112:115],v[76:79],v[216:219],v[112:115]
ds_read_b128 v[72:75],v23 offset:4608
v_mfma_f32_16x16x32_bf16 v[116:119],v[76:79],v[220:223],v[116:119]
s_waitcnt lgkmcnt(6)
v_mfma_f32_16x16x32_bf16 v[112:115],v[80:83],v[224:227],v[112:115]
ds_read_b128 v[76:79],v23 offset:6656
v_add_u32 v25,s60,v4
v_mfma_f32_16x16x32_bf16 v[116:119],v[80:83],v[228:231],v[116:119]
s_waitcnt lgkmcnt(6)
v_mfma_f32_16x16x32_bf16 v[112:115],v[84:87],v[232:235],v[112:115]
ds_read_b128 v[80:83],v23 offset:8704
v_mfma_f32_16x16x32_bf16 v[116:119],v[84:87],v[236:239],v[116:119]
s_waitcnt lgkmcnt(6)
buffer_load_dwordx4 v22,%[srdW],s59 offen offset:3072 sc1 lds
v_mfma_f32_16x16x32_bf16 v[112:115],v[88:91],v[240:243],v[112:115]
s_add_u32 m0,s64,s41
ds_read_b128 v[84:87],v23 offset:10752
v_add_u32 v26,s61,v4
v_mfma_f32_16x16x32_bf16 v[116:119],v[88:91],v[244:247],v[116:119]
s_waitcnt lgkmcnt(6)
v_mfma_f32_16x16x32_bf16 v[112:115],v[92:95],v[248:251],v[112:115]
s_waitcnt vmcnt(8)
ds_read_b128 v[88:91],v23 offset:12800
v_mfma_f32_16x16x32_bf16 v[116:119],v[92:95],v[252:255],v[116:119]
s_waitcnt lgkmcnt(6)
v_mfma_f32_16x16x32_bf16 v[120:123],v[64:67],v[192:195],v[120:123]
s_barrier
ds_read_b128 v[92:95],v23 offset:14848
v_mfma_f32_16x16x32_bf16 v[124:127],v[64:67],v[196:199],v[124:127]
s_waitcnt lgkmcnt(6)
buffer_load_dwordx4 %[vL16],%[srdW],s65 offen offset:0 sc1 lds
v_mfma_f32_16x16x32_bf16 v[120:123],v[68:71],v[200:203],v[120:123]
s_cmp_eq_u32 s66,s57
ds_read_b128 v[64:67],v24 offset:0
v_mfma_f32_16x16x32_bf16 v[124:127],v[68:71],v[204:207],v[124:127]
s_waitcnt lgkmcnt(6)
v_exp_f32 v32,v112
v_mfma_f32_16x16x32_bf16 v[120:123],v[72:75],v[208:211],v[120:123]
s_cbranch_scc1 SIDEB_%=
SBACKB_%=:
ds_read_b128 v[68:71],v24 offset:512
v_max3_f32 v27,v112,v113,v114
v_mfma_f32_16x16x32_bf16 v[124:127],v[72:75],v[212:215],v[124:127]
s_waitcnt lgkmcnt(6)
v_exp_f32 v33,v113
v_mfma_f32_16x16x32_bf16 v[120:123],v[76:79],v[216:219],v[120:123]
ds_read_b128 v[72:75],v24 offset:2048
v_cvt_pk_bf16_f32 v56,v32,v33
v_mfma_f32_16x16x32_bf16 v[124:127],v[76:79],v[220:223],v[124:127]
s_waitcnt lgkmcnt(6)
v_exp_f32 v34,v114
v_mfma_f32_16x16x32_bf16 v[120:123],v[80:83],v[224:227],v[120:123]
ds_read_b128 v[76:79],v24 offset:2560
v_max3_f32 v27,v27,v115,v116
v_mfma_f32_16x16x32_bf16 v[124:127],v[80:83],v[228:231],v[124:127]
s_waitcnt lgkmcnt(6)
buffer_load_dwordx4 %[vL16],%[srdW],s65 offen offset:1024 sc1 lds
v_mfma_f32_16x16x32_bf16 v[120:123],v[84:87],v[232:235],v[120:123]
ds_read_b128 v[80:83],v24 offset:4096
v_max3_f32 v27,v27,v117,v118
v_mfma_f32_16x16x32_bf16 v[124:127],v[84:87],v[236:239],v[124:127]
s_waitcnt lgkmcnt(6)
v_exp_f32 v35,v115
v_mfma_f32_16x16x32_bf16 v[120:123],v[88:91],v[240:243],v[120:123]
ds_read_b128 v[84:87],v24 offset:4608
v_cvt_pk_bf16_f32 v57,v34,v35
v_mfma_f32_16x16x32_bf16 v[124:127],v[88:91],v[244:247],v[124:127]
s_waitcnt lgkmcnt(6)
v_exp_f32 v36,v116
v_mfma_f32_16x16x32_bf16 v[120:123],v[92:95],v[248:251],v[120:123]
ds_read_b128 v[88:91],v24 offset:6144
v_max_f32 v27,v27,v119
v_mfma_f32_16x16x32_bf16 v[124:127],v[92:95],v[252:255],v[124:127]
s_waitcnt lgkmcnt(6)
v_exp_f32 v37,v117
v_mfma_f32_16x16x32_bf16 v[96:99],v[64:67],v[128:131],v[48:51]
ds_read_b128 v[92:95],v24 offset:6656
v_cvt_pk_bf16_f32 v60,v36,v37
v_mfma_f32_16x16x32_bf16 v[100:103],v[64:67],v[132:135],v[52:55]
s_waitcnt lgkmcnt(6)
buffer_load_dwordx4 %[vL16],%[srdW],s65 offen offset:2048 sc1 lds
v_mfma_f32_16x16x32_bf16 v[108:111],v[68:71],v[132:135],v[52:55]
ds_read_b128 v[64:67],v24 offset:8192
v_mfma_f32_16x16x32_bf16 v[104:107],v[68:71],v[128:131],v[48:51]
s_waitcnt lgkmcnt(6)
v_exp_f32 v38,v118
v_mfma_f32_16x16x32_bf16 v[96:99],v[72:75],v[136:139],v[96:99]
ds_read_b128 v[68:71],v24 offset:8704
v_max3_f32 v27,v27,v120,v121
v_mfma_f32_16x16x32_bf16 v[100:103],v[72:75],v[140:143],v[100:103]
s_waitcnt lgkmcnt(6)
v_exp_f32 v39,v119
v_mfma_f32_16x16x32_bf16 v[108:111],v[76:79],v[140:143],v[108:111]
ds_read_b128 v[72:75],v24 offset:10240
v_cvt_pk_bf16_f32 v61,v38,v39
v_mfma_f32_16x16x32_bf16 v[104:107],v[76:79],v[136:139],v[104:107]
s_waitcnt lgkmcnt(6)
v_exp_f32 v40,v120
v_mfma_f32_16x16x32_bf16 v[96:99],v[80:83],v[144:147],v[96:99]
ds_read_b128 v[76:79],v24 offset:10752
v_max3_f32 v27,v27,v122,v123
v_mfma_f32_16x16x32_bf16 v[100:103],v[80:83],v[148:151],v[100:103]
s_waitcnt lgkmcnt(6)
buffer_load_dwordx4 %[vL16],%[srdW],s65 offen offset:3072 sc1 lds
v_mfma_f32_16x16x32_bf16 v[108:111],v[84:87],v[148:151],v[108:111]
ds_read_b128 v[80:83],v24 offset:12288
v_max3_f32 v27,v27,v124,v125
v_mfma_f32_16x16x32_bf16 v[104:107],v[84:87],v[144:147],v[104:107]
s_waitcnt lgkmcnt(6)
v_exp_f32 v41,v121
v_mfma_f32_16x16x32_bf16 v[96:99],v[88:91],v[152:155],v[96:99]
s_add_u32 m0,m0,0x1000
ds_read_b128 v[84:87],v24 offset:12800
v_cvt_pk_bf16_f32 v58,v40,v41
v_mfma_f32_16x16x32_bf16 v[100:103],v[88:91],v[156:159],v[100:103]
s_waitcnt lgkmcnt(6)
v_exp_f32 v42,v122
v_mfma_f32_16x16x32_bf16 v[108:111],v[92:95],v[156:159],v[108:111]
ds_read_b128 v[88:91],v24 offset:14336
v_max3_f32 v27,v27,v126,v127
v_mfma_f32_16x16x32_bf16 v[104:107],v[92:95],v[152:155],v[104:107]
s_waitcnt lgkmcnt(6)
v_exp_f32 v43,v123
v_mfma_f32_16x16x32_bf16 v[96:99],v[64:67],v[160:163],v[96:99]
ds_read_b128 v[92:95],v24 offset:14848
v_cvt_pk_bf16_f32 v59,v42,v43
v_mfma_f32_16x16x32_bf16 v[100:103],v[64:67],v[164:167],v[100:103]
s_waitcnt lgkmcnt(6)
buffer_load_dwordx4 v22,%[srdW],s65 offen offset:0 sc1 lds
v_mfma_f32_16x16x32_bf16 v[108:111],v[68:71],v[164:167],v[108:111]
ds_read_b64_tr_b16 v[64:65],v25 offset:0
ds_read_b64_tr_b16 v[66:67],v25 offset:512
v_mfma_f32_16x16x32_bf16 v[104:107],v[68:71],v[160:163],v[104:107]
s_waitcnt lgkmcnt(7)
v_exp_f32 v44,v124
v_mfma_f32_16x16x32_bf16 v[96:99],v[72:75],v[168:171],v[96:99]
ds_read_b64_tr_b16 v[68:69],v25 offset:1024
ds_read_b64_tr_b16 v[70:71],v25 offset:1536
v_mfma_f32_16x16x32_bf16 v[100:103],v[72:75],v[172:175],v[100:103]
s_waitcnt lgkmcnt(8)
v_exp_f32 v45,v125
v_mfma_f32_16x16x32_bf16 v[108:111],v[76:79],v[172:175],v[108:111]
ds_read_b64_tr_b16 v[72:73],v25 offset:2048
ds_read_b64_tr_b16 v[74:75],v25 offset:2560
v_mfma_f32_16x16x32_bf16 v[104:107],v[76:79],v[168:171],v[104:107]
s_waitcnt lgkmcnt(9)
v_exp_f32 v46,v126
v_mfma_f32_16x16x32_bf16 v[96:99],v[80:83],v[176:179],v[96:99]
ds_read_b64_tr_b16 v[76:77],v25 offset:3072
ds_read_b64_tr_b16 v[78:79],v25 offset:3584
v_mfma_f32_16x16x32_bf16 v[100:103],v[80:83],v[180:183],v[100:103]
s_waitcnt lgkmcnt(10)
v_exp_f32 v47,v127
v_mfma_f32_16x16x32_bf16 v[108:111],v[84:87],v[180:183],v[108:111]
ds_read_b64_tr_b16 v[80:81],v25 offset:4096
ds_read_b64_tr_b16 v[82:83],v25 offset:4608
v_mfma_f32_16x16x32_bf16 v[104:107],v[84:87],v[176:179],v[104:107]
s_waitcnt lgkmcnt(11)
buffer_load_dwordx4 v22,%[srdW],s65 offen offset:1024 sc1 lds
v_mfma_f32_16x16x32_bf16 v[96:99],v[88:91],v[184:187],v[96:99]
ds_read_b64_tr_b16 v[84:85],v25 offset:5120
v_cvt_pk_bf16_f32 v62,v44,v45
v_mfma_f32_16x16x32_bf16 v[100:103],v[88:91],v[188:191],v[100:103]
s_waitcnt lgkmcnt(11)
ds_read_b64_tr_b16 v[86:87],v25 offset:5632
v_cvt_pk_bf16_f32 v63,v46,v47
v_mfma_f32_16x16x32_bf16 v[108:111],v[92:95],v[188:191],v[108:111]
ds_read_b64_tr_b16 v[88:89],v25 offset:6144
v_cmp_lt_f32 vcc,s68,v27
v_mfma_f32_16x16x32_bf16 v[104:107],v[92:95],v[184:187],v[104:107]
s_waitcnt lgkmcnt(11)
ds_read_b64_tr_b16 v[90:91],v25 offset:6656
s_cbranch_vccnz RAREB_%=
BACKB_%=:
v_mfma_f32_16x16x32_bf16 a[0:3],v[64:67],v[56:59],a[0:3]
ds_read_b64_tr_b16 v[92:93],v25 offset:7168
v_add_f32 v30,v30,v32
v_mfma_f32_16x16x32_bf16 a[4:7],v[64:67],v[60:63],a[4:7]
s_waitcnt lgkmcnt(11)
ds_read_b64_tr_b16 v[94:95],v25 offset:7680
v_add_f32 v31,v31,v36
v_mfma_f32_16x16x32_bf16 a[12:15],v[68:71],v[60:63],a[12:15]
ds_read_b64_tr_b16 v[64:65],v25 offset:8192
ds_read_b64_tr_b16 v[66:67],v25 offset:8704
v_mfma_f32_16x16x32_bf16 a[8:11],v[68:71],v[56:59],a[8:11]
s_waitcnt lgkmcnt(12)
buffer_load_dwordx4 v22,%[srdW],s65 offen offset:2048 sc1 lds
v_mfma_f32_16x16x32_bf16 a[16:19],v[72:75],v[56:59],a[16:19]
ds_read_b64_tr_b16 v[68:69],v25 offset:9216
v_add_f32 v30,v30,v40
v_mfma_f32_16x16x32_bf16 a[20:23],v[72:75],v[60:63],a[20:23]
s_waitcnt lgkmcnt(11)
ds_read_b64_tr_b16 v[70:71],v25 offset:9728
v_add_f32 v31,v31,v44
v_mfma_f32_16x16x32_bf16 a[28:31],v[76:79],v[60:63],a[28:31]
ds_read_b64_tr_b16 v[72:73],v25 offset:10240
v_add_f32 v30,v30,v33
v_mfma_f32_16x16x32_bf16 a[24:27],v[76:79],v[56:59],a[24:27]
s_waitcnt lgkmcnt(11)
ds_read_b64_tr_b16 v[74:75],v25 offset:10752
v_add_f32 v31,v31,v37
v_mfma_f32_16x16x32_bf16 a[32:35],v[80:83],v[56:59],a[32:35]
ds_read_b64_tr_b16 v[76:77],v25 offset:11264
v_add_f32 v30,v30,v41
v_mfma_f32_16x16x32_bf16 a[36:39],v[80:83],v[60:63],a[36:39]
s_waitcnt lgkmcnt(11)
ds_read_b64_tr_b16 v[78:79],v25 offset:11776
v_add_f32 v31,v31,v45
v_mfma_f32_16x16x32_bf16 a[44:47],v[84:87],v[60:63],a[44:47]
ds_read_b64_tr_b16 v[80:81],v25 offset:12288
ds_read_b64_tr_b16 v[82:83],v25 offset:12800
v_mfma_f32_16x16x32_bf16 a[40:43],v[84:87],v[56:59],a[40:43]
s_waitcnt lgkmcnt(12)
buffer_load_dwordx4 v22,%[srdW],s65 offen offset:3072 sc1 lds
v_mfma_f32_16x16x32_bf16 a[48:51],v[88:91],v[56:59],a[48:51]
s_add_u32 s59,s65,s70
ds_read_b64_tr_b16 v[84:85],v25 offset:13312
v_add_f32 v30,v30,v34
v_mfma_f32_16x16x32_bf16 a[52:55],v[88:91],v[60:63],a[52:55]
s_waitcnt lgkmcnt(11)
ds_read_b64_tr_b16 v[86:87],v25 offset:13824
v_add_f32 v31,v31,v38
v_mfma_f32_16x16x32_bf16 a[60:63],v[92:95],v[60:63],a[60:63]
s_add_u32 s65,s59,s71
ds_read_b64_tr_b16 v[88:89],v25 offset:14336
v_add_f32 v30,v30,v42
v_mfma_f32_16x16x32_bf16 a[56:59],v[92:95],v[56:59],a[56:59]
s_waitcnt lgkmcnt(11)
ds_read_b64_tr_b16 v[90:91],v25 offset:14848
v_add_f32 v31,v31,v46
v_mfma_f32_16x16x32_bf16 a[64:67],v[64:67],v[56:59],a[64:67]
s_cmp_eq_u32 s66,2
ds_read_b64_tr_b16 v[92:93],v25 offset:15360
v_add_f32 v30,v30,v35
v_mfma_f32_16x16x32_bf16 a[68:71],v[64:67],v[60:63],a[68:71]
s_waitcnt lgkmcnt(11)
ds_read_b64_tr_b16 v[94:95],v25 offset:15872
v_add_f32 v31,v31,v39
v_mfma_f32_16x16x32_bf16 a[76:79],v[68:71],v[60:63],a[76:79]
s_cselect_b32 s65,s43,s65
ds_read_b64_tr_b16 v[64:65],v26 offset:0
v_add_f32 v30,v30,v43
v_mfma_f32_16x16x32_bf16 a[72:75],v[68:71],v[56:59],a[72:75]
s_waitcnt lgkmcnt(11)
ds_read_b64_tr_b16 v[66:67],v26 offset:512
v_add_f32 v31,v31,v47
v_mfma_f32_16x16x32_bf16 a[80:83],v[72:75],v[56:59],a[80:83]
s_cselect_b32 s70,0x2000,s70
ds_read_b64_tr_b16 v[68:69],v26 offset:1024
v_mfma_f32_16x16x32_bf16 a[84:87],v[72:75],v[60:63],a[84:87]
s_waitcnt lgkmcnt(11)
ds_read_b64_tr_b16 v[70:71],v26 offset:1536
v_mfma_f32_16x16x32_bf16 a[92:95],v[76:79],v[60:63],a[92:95]
s_cselect_b32 s71,0x2000,s71
ds_read_b64_tr_b16 v[72:73],v26 offset:2048
v_mfma_f32_16x16x32_bf16 a[88:91],v[76:79],v[56:59],a[88:91]
s_waitcnt lgkmcnt(11)
ds_read_b64_tr_b16 v[74:75],v26 offset:2560
v_mfma_f32_16x16x32_bf16 a[96:99],v[80:83],v[56:59],a[96:99]
ds_read_b64_tr_b16 v[76:77],v26 offset:3072
v_mfma_f32_16x16x32_bf16 a[100:103],v[80:83],v[60:63],a[100:103]
s_waitcnt lgkmcnt(11)
ds_read_b64_tr_b16 v[78:79],v26 offset:3584
v_mfma_f32_16x16x32_bf16 a[108:111],v[84:87],v[60:63],a[108:111]
ds_read_b64_tr_b16 v[80:81],v26 offset:4096
v_mfma_f32_16x16x32_bf16 a[104:107],v[84:87],v[56:59],a[104:107]
s_waitcnt lgkmcnt(11)
ds_read_b64_tr_b16 v[82:83],v26 offset:4608
v_mfma_f32_16x16x32_bf16 a[112:115],v[88:91],v[56:59],a[112:115]
s_add_u32 m0,s60,s41
ds_read_b64_tr_b16 v[84:85],v26 offset:5120
v_mfma_f32_16x16x32_bf16 a[116:119],v[88:91],v[60:63],a[116:119]
s_waitcnt lgkmcnt(11)
ds_read_b64_tr_b16 v[86:87],v26 offset:5632
v_mfma_f32_16x16x32_bf16 a[124:127],v[92:95],v[60:63],a[124:127]
s_waitcnt vmcnt(8)
ds_read_b64_tr_b16 v[88:89],v26 offset:6144
v_mfma_f32_16x16x32_bf16 a[120:123],v[92:95],v[56:59],a[120:123]
s_waitcnt lgkmcnt(11)
ds_read_b64_tr_b16 v[90:91],v26 offset:6656
v_mfma_f32_16x16x32_bf16 a[128:131],v[64:67],v[56:59],a[128:131]
s_barrier
ds_read_b64_tr_b16 v[92:93],v26 offset:7168
ds_read_b64_tr_b16 v[94:95],v26 offset:7680
v_mfma_f32_16x16x32_bf16 a[132:135],v[64:67],v[60:63],a[132:135]
s_waitcnt lgkmcnt(12)
buffer_load_dwordx4 %[vL16],%[srdW],s59 offen offset:0 sc1 lds
v_mfma_f32_16x16x32_bf16 a[140:143],v[68:71],v[60:63],a[140:143]
ds_read_b64_tr_b16 v[64:65],v26 offset:8192
v_mfma_f32_16x16x32_bf16 a[136:139],v[68:71],v[56:59],a[136:139]
s_waitcnt lgkmcnt(11)
ds_read_b64_tr_b16 v[66:67],v26 offset:8704
v_mfma_f32_16x16x32_bf16 a[144:147],v[72:75],v[56:59],a[144:147]
ds_read_b64_tr_b16 v[68:69],v26 offset:9216
v_add_u32 v23,s63,v3
v_mfma_f32_16x16x32_bf16 a[148:151],v[72:75],v[60:63],a[148:151]
s_waitcnt lgkmcnt(11)
ds_read_b64_tr_b16 v[70:71],v26 offset:9728
v_mfma_f32_16x16x32_bf16 a[156:159],v[76:79],v[60:63],a[156:159]
ds_read_b64_tr_b16 v[72:73],v26 offset:10240
ds_read_b64_tr_b16 v[74:75],v26 offset:10752
v_mfma_f32_16x16x32_bf16 a[152:155],v[76:79],v[56:59],a[152:155]
s_waitcnt lgkmcnt(12)
buffer_load_dwordx4 %[vL16],%[srdW],s59 offen offset:1024 sc1 lds
v_mfma_f32_16x16x32_bf16 a[160:163],v[80:83],v[56:59],a[160:163]
s_mov_b32 s67,s60
ds_read_b64_tr_b16 v[76:77],v26 offset:11264
v_mfma_f32_16x16x32_bf16 a[164:167],v[80:83],v[60:63],a[164:167]
s_waitcnt lgkmcnt(11)
ds_read_b64_tr_b16 v[78:79],v26 offset:11776
v_mfma_f32_16x16x32_bf16 a[172:175],v[84:87],v[60:63],a[172:175]
s_mov_b32 s60,s62
ds_read_b64_tr_b16 v[80:81],v26 offset:12288
v_mfma_f32_16x16x32_bf16 a[168:171],v[84:87],v[56:59],a[168:171]
s_waitcnt lgkmcnt(11)
ds_read_b64_tr_b16 v[82:83],v26 offset:12800
v_mfma_f32_16x16x32_bf16 a[176:179],v[88:91],v[56:59],a[176:179]
s_mov_b32 s62,s64
ds_read_b64_tr_b16 v[84:85],v26 offset:13312
ds_read_b64_tr_b16 v[86:87],v26 offset:13824
v_mfma_f32_16x16x32_bf16 a[180:183],v[88:91],v[60:63],a[180:183]
s_waitcnt lgkmcnt(12)
buffer_load_dwordx4 %[vL16],%[srdW],s59 offen offset:2048 sc1 lds
v_mfma_f32_16x16x32_bf16 a[188:191],v[92:95],v[60:63],a[188:191]
ds_read_b64_tr_b16 v[88:89],v26 offset:14336
v_mfma_f32_16x16x32_bf16 a[184:187],v[92:95],v[56:59],a[184:187]
s_waitcnt lgkmcnt(11)
ds_read_b64_tr_b16 v[90:91],v26 offset:14848
v_mfma_f32_16x16x32_bf16 a[192:195],v[64:67],v[56:59],a[192:195]
s_mov_b32 s64,s61
ds_read_b64_tr_b16 v[92:93],v26 offset:15360
v_mfma_f32_16x16x32_bf16 a[196:199],v[64:67],v[60:63],a[196:199]
s_waitcnt lgkmcnt(11)
ds_read_b64_tr_b16 v[94:95],v26 offset:15872
v_mfma_f32_16x16x32_bf16 a[204:207],v[68:71],v[60:63],a[204:207]
s_mov_b32 s61,s63
ds_read_b128 v[64:67],v23 offset:0
v_mfma_f32_16x16x32_bf16 a[200:203],v[68:71],v[56:59],a[200:203]
s_waitcnt lgkmcnt(11)
buffer_load_dwordx4 %[vL16],%[srdW],s59 offen offset:3072 sc1 lds
v_mfma_f32_16x16x32_bf16 a[208:211],v[72:75],v[56:59],a[208:211]
s_mov_b32 s63,s67
ds_read_b128 v[68:71],v23 offset:2048
v_mfma_f32_16x16x32_bf16 a[212:215],v[72:75],v[60:63],a[212:215]
s_waitcnt lgkmcnt(10)
v_mfma_f32_16x16x32_bf16 a[220:223],v[76:79],v[60:63],a[220:223]
s_add_u32 m0,m0,0x1000
ds_read_b128 v[72:75],v23 offset:4096
)"
R"(v_mfma_f32_16x16x32_bf16 a[216:219],v[76:79],v[56:59],a[216:219]
s_waitcnt lgkmcnt(9)
v_mfma_f32_16x16x32_bf16 a[224:227],v[80:83],v[56:59],a[224:227]
ds_read_b128 v[76:79],v23 offset:6144
v_mfma_f32_16x16x32_bf16 a[228:231],v[80:83],v[60:63],a[228:231]
s_waitcnt lgkmcnt(8)
buffer_load_dwordx4 v22,%[srdW],s59 offen offset:0 sc1 lds
v_mfma_f32_16x16x32_bf16 a[236:239],v[84:87],v[60:63],a[236:239]
s_sub_u32 s66,s66,1
ds_read_b128 v[80:83],v23 offset:8192
v_mfma_f32_16x16x32_bf16 a[232:235],v[84:87],v[56:59],a[232:235]
s_waitcnt lgkmcnt(7)
v_mfma_f32_16x16x32_bf16 a[240:243],v[88:91],v[56:59],a[240:243]
ds_read_b128 v[84:87],v23 offset:10240
v_mfma_f32_16x16x32_bf16 a[244:247],v[88:91],v[60:63],a[244:247]
s_waitcnt lgkmcnt(6)
buffer_load_dwordx4 v22,%[srdW],s59 offen offset:1024 sc1 lds
v_mfma_f32_16x16x32_bf16 a[252:255],v[92:95],v[60:63],a[252:255]
s_cmp_eq_u32 s66,0
ds_read_b128 v[88:91],v23 offset:12288
v_mfma_f32_16x16x32_bf16 a[248:251],v[92:95],v[56:59],a[248:251]
s_waitcnt lgkmcnt(6)
s_cbranch_scc0 LOOP_%=
s_branch TAILA_%=
SIDEA_%=:
s_cmp_eq_u32 s57,4
s_cbranch_scc1 SID4A_%=
v_readlane_b32 s72,%[pv],18
v_readlane_b32 s73,%[pv],19
s_mov_b32 s57,4
v_mov_b32 v14,s72
buffer_store_dword v15,v14,%[srdW],0 offen sc1
v_lshlrev_b32 v14,3,%[vL16]
v_add_u32 v14,s73,v14
s_branch SBACKA_%=
SID4A_%=:
buffer_load_dword v15,v14,%[srdW],0 offen sc1
v_lshlrev_b32 v16,3,%[vL16]
s_getpc_b64 s[72:73]
PCA_%=:
s_add_u32 s72,s72,TAILB_%=-PCA_%=
s_addc_u32 s73,s73,0
s_and_b32 s73,s73,0xffff
s_mov_b32 s74,EBF_K5_END-TAILB_%=
s_mov_b32 s75,0x20000
s_lshr_b32 s67,s47,16
s_mul_i32 s67,s67,640
v_min_u32 v16,0x200,v16
v_add_u32 v16,s67,v16
buffer_load_dword v21,v16,s[72:75],0 offen
s_branch SBACKA_%=
SPWA_%=:
s_mov_b32 s97,0x40000
POLLWA_%=:
buffer_load_dword v15,v14,%[srdW],0 offen sc1
s_waitcnt vmcnt(0)
v_cmp_ne_u32 vcc,s55,v15
s_cbranch_vccz GOWA_%=
s_sub_u32 s97,s97,1
s_cbranch_scc1 GOWA_%=
s_sleep 4
s_branch POLLWA_%=
GOWA_%=:
v_mov_b32 v15,s55
s_branch SPWBA_%=
SIDEB_%=:
s_cmp_eq_u32 s57,4
s_cbranch_scc1 SID4B_%=
v_readlane_b32 s72,%[pv],18
v_readlane_b32 s73,%[pv],19
s_mov_b32 s57,4
v_mov_b32 v14,s72
buffer_store_dword v15,v14,%[srdW],0 offen sc1
v_lshlrev_b32 v14,3,%[vL16]
v_add_u32 v14,s73,v14
s_branch SBACKB_%=
SID4B_%=:
buffer_load_dword v15,v14,%[srdW],0 offen sc1
v_lshlrev_b32 v16,3,%[vL16]
s_getpc_b64 s[72:73]
PCB_%=:
s_add_u32 s72,s72,TAILB_%=-PCB_%=
s_addc_u32 s73,s73,0
s_and_b32 s73,s73,0xffff
s_mov_b32 s74,EBF_K5_END-TAILB_%=
s_mov_b32 s75,0x20000
s_lshr_b32 s67,s47,16
s_mul_i32 s67,s67,640
v_min_u32 v16,0x200,v16
v_add_u32 v16,s67,v16
buffer_load_dword v21,v16,s[72:75],0 offen
s_branch SBACKB_%=
SPWB_%=:
s_mov_b32 s97,0x40000
POLLWB_%=:
buffer_load_dword v15,v14,%[srdW],0 offen sc1
s_waitcnt vmcnt(0)
v_cmp_ne_u32 vcc,s55,v15
s_cbranch_vccz GOWB_%=
s_sub_u32 s97,s97,1
s_cbranch_scc1 GOWB_%=
s_sleep 4
s_branch POLLWB_%=
GOWB_%=:
v_mov_b32 v15,s55
s_branch SPWBB_%=
RAREA_%=:
s_cmp_eq_u32 s69,0
s_cbranch_scc1 FULLA_%=
v_max3_f32 v16,v96,v97,v98
v_max3_f32 v16,v16,v99,v104
v_max3_f32 v16,v16,v105,v106
v_max_f32 v16,v16,v107
v_max3_f32 v17,v100,v101,v102
v_max3_f32 v17,v17,v103,v108
v_max3_f32 v17,v17,v109,v110
v_max_f32 v17,v17,v111
v_min_f32 v16,v16,v17
v_cmp_nle_f32 vcc,0xc2200000,v16
s_cbranch_vccnz FULLA_%=
v_cmp_nge_f32 vcc,0x41c00000,v27
s_cbranch_vccnz FULLA_%=
s_mov_b32 s69,0
s_mov_b32 s68,0x41c00000
s_branch BACKA_%=
FULLA_%=:
s_nop 15
v_max3_f32 v16,v96,v97,v98
v_max3_f32 v16,v16,v99,v104
v_max3_f32 v16,v16,v105,v106
v_max_f32 v16,v16,v107
v_mov_b32 v18,v16
s_nop 1
v_permlane16_swap_b32 v16,v18
s_nop 1
v_max_f32 v16,v16,v18
v_mov_b32 v18,v16
s_nop 1
v_permlane32_swap_b32 v16,v18
s_nop 1
v_max_f32 v16,v16,v18
v_max_f32 v16,s69,v16
v_add_f32 v28,v28,v16
v_sub_f32 v96,v96,v16
v_sub_f32 v97,v97,v16
v_sub_f32 v98,v98,v16
v_sub_f32 v99,v99,v16
v_sub_f32 v104,v104,v16
v_sub_f32 v105,v105,v16
v_sub_f32 v106,v106,v16
v_sub_f32 v107,v107,v16
v_sub_f32 v112,v112,v16
v_sub_f32 v113,v113,v16
v_sub_f32 v114,v114,v16
v_sub_f32 v115,v115,v16
v_sub_f32 v120,v120,v16
v_sub_f32 v121,v121,v16
v_sub_f32 v122,v122,v16
v_sub_f32 v123,v123,v16
v_sub_f32 v48,0,v28
v_sub_f32 v49,0,v28
v_sub_f32 v50,0,v28
v_sub_f32 v51,0,v28
v_max3_f32 v17,v100,v101,v102
v_max3_f32 v17,v17,v103,v108
v_max3_f32 v17,v17,v109,v110
v_max_f32 v17,v17,v111
v_mov_b32 v18,v17
s_nop 1
v_permlane16_swap_b32 v17,v18
s_nop 1
v_max_f32 v17,v17,v18
v_mov_b32 v18,v17
s_nop 1
v_permlane32_swap_b32 v17,v18
s_nop 1
v_max_f32 v17,v17,v18
v_max_f32 v17,s69,v17
v_add_f32 v29,v29,v17
v_sub_f32 v100,v100,v17
v_sub_f32 v101,v101,v17
v_sub_f32 v102,v102,v17
v_sub_f32 v103,v103,v17
v_sub_f32 v108,v108,v17
v_sub_f32 v109,v109,v17
v_sub_f32 v110,v110,v17
v_sub_f32 v111,v111,v17
v_sub_f32 v116,v116,v17
v_sub_f32 v117,v117,v17
v_sub_f32 v118,v118,v17
v_sub_f32 v119,v119,v17
v_sub_f32 v124,v124,v17
v_sub_f32 v125,v125,v17
v_sub_f32 v126,v126,v17
v_sub_f32 v127,v127,v17
v_sub_f32 v52,0,v29
v_sub_f32 v53,0,v29
v_sub_f32 v54,0,v29
v_sub_f32 v55,0,v29
v_exp_f32 v32,v96
v_exp_f32 v33,v97
v_exp_f32 v34,v98
v_exp_f32 v35,v99
v_exp_f32 v36,v100
v_exp_f32 v37,v101
v_exp_f32 v38,v102
v_exp_f32 v39,v103
v_exp_f32 v40,v104
v_exp_f32 v41,v105
v_exp_f32 v42,v106
v_exp_f32 v43,v107
v_exp_f32 v44,v108
v_exp_f32 v45,v109
v_exp_f32 v46,v110
v_exp_f32 v47,v111
s_nop 0
v_cvt_pk_bf16_f32 v56,v32,v33
v_cvt_pk_bf16_f32 v57,v34,v35
v_cvt_pk_bf16_f32 v60,v36,v37
v_cvt_pk_bf16_f32 v61,v38,v39
v_cvt_pk_bf16_f32 v58,v40,v41
v_cvt_pk_bf16_f32 v59,v42,v43
v_cvt_pk_bf16_f32 v62,v44,v45
v_cvt_pk_bf16_f32 v63,v46,v47
s_getpc_b64 s[72:73]
RETA_%=:
s_add_u32 s72,s72,RBA_%=-RETA_%=
s_addc_u32 s73,s73,0
s_branch RESC_%=
RBA_%=:
s_branch BACKA_%=
EBF_K3_LOOPEND:
RAREB_%=:
s_nop 15
v_max3_f32 v16,v112,v113,v114
v_max3_f32 v16,v16,v115,v120
v_max3_f32 v16,v16,v121,v122
v_max_f32 v16,v16,v123
v_mov_b32 v18,v16
s_nop 1
v_permlane16_swap_b32 v16,v18
s_nop 1
v_max_f32 v16,v16,v18
v_mov_b32 v18,v16
s_nop 1
v_permlane32_swap_b32 v16,v18
s_nop 1
v_max_f32 v16,v16,v18
v_max_f32 v16,s69,v16
v_add_f32 v28,v28,v16
v_sub_f32 v112,v112,v16
v_sub_f32 v113,v113,v16
v_sub_f32 v114,v114,v16
v_sub_f32 v115,v115,v16
v_sub_f32 v120,v120,v16
v_sub_f32 v121,v121,v16
v_sub_f32 v122,v122,v16
v_sub_f32 v123,v123,v16
v_sub_f32 v96,v96,v16
v_sub_f32 v97,v97,v16
v_sub_f32 v98,v98,v16
v_sub_f32 v99,v99,v16
v_sub_f32 v104,v104,v16
v_sub_f32 v105,v105,v16
v_sub_f32 v106,v106,v16
v_sub_f32 v107,v107,v16
v_sub_f32 v48,0,v28
v_sub_f32 v49,0,v28
v_sub_f32 v50,0,v28
v_sub_f32 v51,0,v28
v_max3_f32 v17,v116,v117,v118
v_max3_f32 v17,v17,v119,v124
v_max3_f32 v17,v17,v125,v126
v_max_f32 v17,v17,v127
v_mov_b32 v18,v17
s_nop 1
v_permlane16_swap_b32 v17,v18
s_nop 1
v_max_f32 v17,v17,v18
v_mov_b32 v18,v17
s_nop 1
v_permlane32_swap_b32 v17,v18
s_nop 1
v_max_f32 v17,v17,v18
v_max_f32 v17,s69,v17
v_add_f32 v29,v29,v17
v_sub_f32 v116,v116,v17
v_sub_f32 v117,v117,v17
v_sub_f32 v118,v118,v17
v_sub_f32 v119,v119,v17
v_sub_f32 v124,v124,v17
v_sub_f32 v125,v125,v17
v_sub_f32 v126,v126,v17
v_sub_f32 v127,v127,v17
v_sub_f32 v100,v100,v17
v_sub_f32 v101,v101,v17
v_sub_f32 v102,v102,v17
v_sub_f32 v103,v103,v17
v_sub_f32 v108,v108,v17
v_sub_f32 v109,v109,v17
v_sub_f32 v110,v110,v17
v_sub_f32 v111,v111,v17
v_sub_f32 v52,0,v29
v_sub_f32 v53,0,v29
v_sub_f32 v54,0,v29
v_sub_f32 v55,0,v29
v_exp_f32 v32,v112
v_exp_f32 v33,v113
v_exp_f32 v34,v114
v_exp_f32 v35,v115
v_exp_f32 v36,v116
v_exp_f32 v37,v117
v_exp_f32 v38,v118
v_exp_f32 v39,v119
v_exp_f32 v40,v120
v_exp_f32 v41,v121
v_exp_f32 v42,v122
v_exp_f32 v43,v123
v_exp_f32 v44,v124
v_exp_f32 v45,v125
v_exp_f32 v46,v126
v_exp_f32 v47,v127
s_nop 0
v_cvt_pk_bf16_f32 v56,v32,v33
v_cvt_pk_bf16_f32 v57,v34,v35
v_cvt_pk_bf16_f32 v60,v36,v37
v_cvt_pk_bf16_f32 v61,v38,v39
v_cvt_pk_bf16_f32 v58,v40,v41
v_cvt_pk_bf16_f32 v59,v42,v43
v_cvt_pk_bf16_f32 v62,v44,v45
v_cvt_pk_bf16_f32 v63,v46,v47
s_getpc_b64 s[72:73]
RETB_%=:
s_add_u32 s72,s72,RBB_%=-RETB_%=
s_addc_u32 s73,s73,0
s_branch RESC_%=
RBB_%=:
s_branch BACKB_%=
RARETA_%=:
s_nop 15
v_max3_f32 v16,v96,v97,v98
v_max3_f32 v16,v16,v99,v104
v_max3_f32 v16,v16,v105,v106
v_max_f32 v16,v16,v107
v_mov_b32 v18,v16
s_nop 1
v_permlane16_swap_b32 v16,v18
s_nop 1
v_max_f32 v16,v16,v18
v_mov_b32 v18,v16
s_nop 1
v_permlane32_swap_b32 v16,v18
s_nop 1
v_max_f32 v16,v16,v18
v_max_f32 v16,s69,v16
v_add_f32 v28,v28,v16
v_sub_f32 v96,v96,v16
v_sub_f32 v97,v97,v16
v_sub_f32 v98,v98,v16
v_sub_f32 v99,v99,v16
v_sub_f32 v104,v104,v16
v_sub_f32 v105,v105,v16
v_sub_f32 v106,v106,v16
v_sub_f32 v107,v107,v16
v_sub_f32 v48,0,v28
v_sub_f32 v49,0,v28
v_sub_f32 v50,0,v28
v_sub_f32 v51,0,v28
v_max3_f32 v17,v100,v101,v102
v_max3_f32 v17,v17,v103,v108
v_max3_f32 v17,v17,v109,v110
v_max_f32 v17,v17,v111
v_mov_b32 v18,v17
s_nop 1
v_permlane16_swap_b32 v17,v18
s_nop 1
v_max_f32 v17,v17,v18
v_mov_b32 v18,v17
s_nop 1
v_permlane32_swap_b32 v17,v18
s_nop 1
v_max_f32 v17,v17,v18
v_max_f32 v17,s69,v17
v_add_f32 v29,v29,v17
v_sub_f32 v100,v100,v17
v_sub_f32 v101,v101,v17
v_sub_f32 v102,v102,v17
v_sub_f32 v103,v103,v17
v_sub_f32 v108,v108,v17
v_sub_f32 v109,v109,v17
v_sub_f32 v110,v110,v17
v_sub_f32 v111,v111,v17
v_sub_f32 v52,0,v29
v_sub_f32 v53,0,v29
v_sub_f32 v54,0,v29
v_sub_f32 v55,0,v29
v_exp_f32 v32,v96
v_exp_f32 v33,v97
v_exp_f32 v34,v98
v_exp_f32 v35,v99
)"
R"(v_exp_f32 v36,v100
v_exp_f32 v37,v101
v_exp_f32 v38,v102
v_exp_f32 v39,v103
v_exp_f32 v40,v104
v_exp_f32 v41,v105
v_exp_f32 v42,v106
v_exp_f32 v43,v107
v_exp_f32 v44,v108
v_exp_f32 v45,v109
v_exp_f32 v46,v110
v_exp_f32 v47,v111
s_nop 0
v_cvt_pk_bf16_f32 v56,v32,v33
v_cvt_pk_bf16_f32 v57,v34,v35
v_cvt_pk_bf16_f32 v60,v36,v37
v_cvt_pk_bf16_f32 v61,v38,v39
v_cvt_pk_bf16_f32 v58,v40,v41
v_cvt_pk_bf16_f32 v59,v42,v43
v_cvt_pk_bf16_f32 v62,v44,v45
v_cvt_pk_bf16_f32 v63,v46,v47
s_getpc_b64 s[72:73]
RETTA_%=:
s_add_u32 s72,s72,RBTA_%=-RETTA_%=
s_addc_u32 s73,s73,0
s_branch RESC_%=
RBTA_%=:
s_branch BACKTA_%=
RARETB_%=:
s_nop 15
v_max3_f32 v16,v112,v113,v114
v_max3_f32 v16,v16,v115,v120
v_max3_f32 v16,v16,v121,v122
v_max_f32 v16,v16,v123
v_mov_b32 v18,v16
s_nop 1
v_permlane16_swap_b32 v16,v18
s_nop 1
v_max_f32 v16,v16,v18
v_mov_b32 v18,v16
s_nop 1
v_permlane32_swap_b32 v16,v18
s_nop 1
v_max_f32 v16,v16,v18
v_max_f32 v16,s69,v16
v_add_f32 v28,v28,v16
v_sub_f32 v112,v112,v16
v_sub_f32 v113,v113,v16
v_sub_f32 v114,v114,v16
v_sub_f32 v115,v115,v16
v_sub_f32 v120,v120,v16
v_sub_f32 v121,v121,v16
v_sub_f32 v122,v122,v16
v_sub_f32 v123,v123,v16
v_sub_f32 v48,0,v28
v_sub_f32 v49,0,v28
v_sub_f32 v50,0,v28
v_sub_f32 v51,0,v28
v_max3_f32 v17,v116,v117,v118
v_max3_f32 v17,v17,v119,v124
v_max3_f32 v17,v17,v125,v126
v_max_f32 v17,v17,v127
v_mov_b32 v18,v17
s_nop 1
v_permlane16_swap_b32 v17,v18
s_nop 1
v_max_f32 v17,v17,v18
v_mov_b32 v18,v17
s_nop 1
v_permlane32_swap_b32 v17,v18
s_nop 1
v_max_f32 v17,v17,v18
v_max_f32 v17,s69,v17
v_add_f32 v29,v29,v17
v_sub_f32 v116,v116,v17
v_sub_f32 v117,v117,v17
v_sub_f32 v118,v118,v17
v_sub_f32 v119,v119,v17
v_sub_f32 v124,v124,v17
v_sub_f32 v125,v125,v17
v_sub_f32 v126,v126,v17
v_sub_f32 v127,v127,v17
v_sub_f32 v52,0,v29
v_sub_f32 v53,0,v29
v_sub_f32 v54,0,v29
v_sub_f32 v55,0,v29
v_exp_f32 v32,v112
v_exp_f32 v33,v113
v_exp_f32 v34,v114
v_exp_f32 v35,v115
v_exp_f32 v36,v116
v_exp_f32 v37,v117
v_exp_f32 v38,v118
v_exp_f32 v39,v119
v_exp_f32 v40,v120
v_exp_f32 v41,v121
v_exp_f32 v42,v122
v_exp_f32 v43,v123
v_exp_f32 v44,v124
v_exp_f32 v45,v125
v_exp_f32 v46,v126
v_exp_f32 v47,v127
s_nop 0
v_cvt_pk_bf16_f32 v56,v32,v33
v_cvt_pk_bf16_f32 v57,v34,v35
v_cvt_pk_bf16_f32 v60,v36,v37
v_cvt_pk_bf16_f32 v61,v38,v39
v_cvt_pk_bf16_f32 v58,v40,v41
v_cvt_pk_bf16_f32 v59,v42,v43
v_cvt_pk_bf16_f32 v62,v44,v45
v_cvt_pk_bf16_f32 v63,v46,v47
s_getpc_b64 s[72:73]
RETTB_%=:
s_add_u32 s72,s72,RBTB_%=-RETTB_%=
s_addc_u32 s73,s73,0
s_branch RESC_%=
RBTB_%=:
s_branch BACKTB_%=
RESC_%=:
s_cmp_eq_u32 s69,0
s_mov_b32 s69,0
s_mov_b32 s68,0x41c00000
s_cbranch_scc0 RESCX_%=
v_sub_f32 v16,0,v16
v_exp_f32 v16,v16
s_nop 0
v_mul_f32 v30,v30,v16
v_sub_f32 v17,0,v17
v_exp_f32 v17,v17
s_nop 0
v_mul_f32 v31,v31,v17
v_accvgpr_read_b32 v18,a0
v_accvgpr_read_b32 v19,a1
v_mul_f32 v18,v18,v16
v_mul_f32 v19,v19,v16
v_accvgpr_write_b32 a0,v18
v_accvgpr_write_b32 a1,v19
v_accvgpr_read_b32 v18,a2
v_accvgpr_read_b32 v19,a3
v_mul_f32 v18,v18,v16
v_mul_f32 v19,v19,v16
v_accvgpr_write_b32 a2,v18
v_accvgpr_write_b32 a3,v19
v_accvgpr_read_b32 v18,a4
v_accvgpr_read_b32 v19,a5
v_mul_f32 v18,v18,v17
v_mul_f32 v19,v19,v17
v_accvgpr_write_b32 a4,v18
v_accvgpr_write_b32 a5,v19
v_accvgpr_read_b32 v18,a6
v_accvgpr_read_b32 v19,a7
v_mul_f32 v18,v18,v17
v_mul_f32 v19,v19,v17
v_accvgpr_write_b32 a6,v18
v_accvgpr_write_b32 a7,v19
v_accvgpr_read_b32 v18,a8
v_accvgpr_read_b32 v19,a9
v_mul_f32 v18,v18,v16
v_mul_f32 v19,v19,v16
v_accvgpr_write_b32 a8,v18
v_accvgpr_write_b32 a9,v19
v_accvgpr_read_b32 v18,a10
v_accvgpr_read_b32 v19,a11
v_mul_f32 v18,v18,v16
v_mul_f32 v19,v19,v16
v_accvgpr_write_b32 a10,v18
v_accvgpr_write_b32 a11,v19
v_accvgpr_read_b32 v18,a12
v_accvgpr_read_b32 v19,a13
v_mul_f32 v18,v18,v17
v_mul_f32 v19,v19,v17
v_accvgpr_write_b32 a12,v18
v_accvgpr_write_b32 a13,v19
v_accvgpr_read_b32 v18,a14
v_accvgpr_read_b32 v19,a15
v_mul_f32 v18,v18,v17
v_mul_f32 v19,v19,v17
v_accvgpr_write_b32 a14,v18
v_accvgpr_write_b32 a15,v19
v_accvgpr_read_b32 v18,a16
v_accvgpr_read_b32 v19,a17
v_mul_f32 v18,v18,v16
v_mul_f32 v19,v19,v16
v_accvgpr_write_b32 a16,v18
v_accvgpr_write_b32 a17,v19
v_accvgpr_read_b32 v18,a18
v_accvgpr_read_b32 v19,a19
v_mul_f32 v18,v18,v16
v_mul_f32 v19,v19,v16
v_accvgpr_write_b32 a18,v18
v_accvgpr_write_b32 a19,v19
v_accvgpr_read_b32 v18,a20
v_accvgpr_read_b32 v19,a21
v_mul_f32 v18,v18,v17
v_mul_f32 v19,v19,v17
v_accvgpr_write_b32 a20,v18
v_accvgpr_write_b32 a21,v19
v_accvgpr_read_b32 v18,a22
v_accvgpr_read_b32 v19,a23
v_mul_f32 v18,v18,v17
v_mul_f32 v19,v19,v17
v_accvgpr_write_b32 a22,v18
v_accvgpr_write_b32 a23,v19
v_accvgpr_read_b32 v18,a24
v_accvgpr_read_b32 v19,a25
v_mul_f32 v18,v18,v16
v_mul_f32 v19,v19,v16
v_accvgpr_write_b32 a24,v18
v_accvgpr_write_b32 a25,v19
v_accvgpr_read_b32 v18,a26
v_accvgpr_read_b32 v19,a27
v_mul_f32 v18,v18,v16
v_mul_f32 v19,v19,v16
v_accvgpr_write_b32 a26,v18
v_accvgpr_write_b32 a27,v19
v_accvgpr_read_b32 v18,a28
v_accvgpr_read_b32 v19,a29
v_mul_f32 v18,v18,v17
v_mul_f32 v19,v19,v17
v_accvgpr_write_b32 a28,v18
v_accvgpr_write_b32 a29,v19
v_accvgpr_read_b32 v18,a30
v_accvgpr_read_b32 v19,a31
v_mul_f32 v18,v18,v17
v_mul_f32 v19,v19,v17
v_accvgpr_write_b32 a30,v18
v_accvgpr_write_b32 a31,v19
v_accvgpr_read_b32 v18,a32
v_accvgpr_read_b32 v19,a33
v_mul_f32 v18,v18,v16
v_mul_f32 v19,v19,v16
v_accvgpr_write_b32 a32,v18
v_accvgpr_write_b32 a33,v19
v_accvgpr_read_b32 v18,a34
v_accvgpr_read_b32 v19,a35
v_mul_f32 v18,v18,v16
v_mul_f32 v19,v19,v16
v_accvgpr_write_b32 a34,v18
v_accvgpr_write_b32 a35,v19
v_accvgpr_read_b32 v18,a36
v_accvgpr_read_b32 v19,a37
v_mul_f32 v18,v18,v17
v_mul_f32 v19,v19,v17
v_accvgpr_write_b32 a36,v18
v_accvgpr_write_b32 a37,v19
v_accvgpr_read_b32 v18,a38
v_accvgpr_read_b32 v19,a39
v_mul_f32 v18,v18,v17
v_mul_f32 v19,v19,v17
v_accvgpr_write_b32 a38,v18
v_accvgpr_write_b32 a39,v19
v_accvgpr_read_b32 v18,a40
v_accvgpr_read_b32 v19,a41
v_mul_f32 v18,v18,v16
v_mul_f32 v19,v19,v16
v_accvgpr_write_b32 a40,v18
v_accvgpr_write_b32 a41,v19
v_accvgpr_read_b32 v18,a42
v_accvgpr_read_b32 v19,a43
v_mul_f32 v18,v18,v16
v_mul_f32 v19,v19,v16
v_accvgpr_write_b32 a42,v18
v_accvgpr_write_b32 a43,v19
v_accvgpr_read_b32 v18,a44
v_accvgpr_read_b32 v19,a45
v_mul_f32 v18,v18,v17
v_mul_f32 v19,v19,v17
v_accvgpr_write_b32 a44,v18
v_accvgpr_write_b32 a45,v19
v_accvgpr_read_b32 v18,a46
v_accvgpr_read_b32 v19,a47
v_mul_f32 v18,v18,v17
v_mul_f32 v19,v19,v17
v_accvgpr_write_b32 a46,v18
v_accvgpr_write_b32 a47,v19
v_accvgpr_read_b32 v18,a48
v_accvgpr_read_b32 v19,a49
v_mul_f32 v18,v18,v16
v_mul_f32 v19,v19,v16
v_accvgpr_write_b32 a48,v18
v_accvgpr_write_b32 a49,v19
v_accvgpr_read_b32 v18,a50
v_accvgpr_read_b32 v19,a51
v_mul_f32 v18,v18,v16
v_mul_f32 v19,v19,v16
v_accvgpr_write_b32 a50,v18
v_accvgpr_write_b32 a51,v19
v_accvgpr_read_b32 v18,a52
v_accvgpr_read_b32 v19,a53
v_mul_f32 v18,v18,v17
v_mul_f32 v19,v19,v17
v_accvgpr_write_b32 a52,v18
v_accvgpr_write_b32 a53,v19
v_accvgpr_read_b32 v18,a54
v_accvgpr_read_b32 v19,a55
v_mul_f32 v18,v18,v17
v_mul_f32 v19,v19,v17
v_accvgpr_write_b32 a54,v18
v_accvgpr_write_b32 a55,v19
v_accvgpr_read_b32 v18,a56
v_accvgpr_read_b32 v19,a57
v_mul_f32 v18,v18,v16
v_mul_f32 v19,v19,v16
v_accvgpr_write_b32 a56,v18
v_accvgpr_write_b32 a57,v19
v_accvgpr_read_b32 v18,a58
v_accvgpr_read_b32 v19,a59
v_mul_f32 v18,v18,v16
v_mul_f32 v19,v19,v16
v_accvgpr_write_b32 a58,v18
v_accvgpr_write_b32 a59,v19
v_accvgpr_read_b32 v18,a60
v_accvgpr_read_b32 v19,a61
v_mul_f32 v18,v18,v17
v_mul_f32 v19,v19,v17
v_accvgpr_write_b32 a60,v18
v_accvgpr_write_b32 a61,v19
v_accvgpr_read_b32 v18,a62
v_accvgpr_read_b32 v19,a63
v_mul_f32 v18,v18,v17
v_mul_f32 v19,v19,v17
v_accvgpr_write_b32 a62,v18
v_accvgpr_write_b32 a63,v19
v_accvgpr_read_b32 v18,a64
v_accvgpr_read_b32 v19,a65
v_mul_f32 v18,v18,v16
v_mul_f32 v19,v19,v16
v_accvgpr_write_b32 a64,v18
v_accvgpr_write_b32 a65,v19
v_accvgpr_read_b32 v18,a66
v_accvgpr_read_b32 v19,a67
v_mul_f32 v18,v18,v16
v_mul_f32 v19,v19,v16
v_accvgpr_write_b32 a66,v18
v_accvgpr_write_b32 a67,v19
v_accvgpr_read_b32 v18,a68
v_accvgpr_read_b32 v19,a69
v_mul_f32 v18,v18,v17
v_mul_f32 v19,v19,v17
v_accvgpr_write_b32 a68,v18
v_accvgpr_write_b32 a69,v19
v_accvgpr_read_b32 v18,a70
v_accvgpr_read_b32 v19,a71
v_mul_f32 v18,v18,v17
v_mul_f32 v19,v19,v17
v_accvgpr_write_b32 a70,v18
v_accvgpr_write_b32 a71,v19
v_accvgpr_read_b32 v18,a72
v_accvgpr_read_b32 v19,a73
v_mul_f32 v18,v18,v16
v_mul_f32 v19,v19,v16
v_accvgpr_write_b32 a72,v18
v_accvgpr_write_b32 a73,v19
v_accvgpr_read_b32 v18,a74
v_accvgpr_read_b32 v19,a75
v_mul_f32 v18,v18,v16
v_mul_f32 v19,v19,v16
v_accvgpr_write_b32 a74,v18
v_accvgpr_write_b32 a75,v19
v_accvgpr_read_b32 v18,a76
v_accvgpr_read_b32 v19,a77
v_mul_f32 v18,v18,v17
v_mul_f32 v19,v19,v17
v_accvgpr_write_b32 a76,v18
v_accvgpr_write_b32 a77,v19
v_accvgpr_read_b32 v18,a78
v_accvgpr_read_b32 v19,a79
v_mul_f32 v18,v18,v17
v_mul_f32 v19,v19,v17
v_accvgpr_write_b32 a78,v18
v_accvgpr_write_b32 a79,v19
v_accvgpr_read_b32 v18,a80
v_accvgpr_read_b32 v19,a81
v_mul_f32 v18,v18,v16
v_mul_f32 v19,v19,v16
v_accvgpr_write_b32 a80,v18
v_accvgpr_write_b32 a81,v19
v_accvgpr_read_b32 v18,a82
v_accvgpr_read_b32 v19,a83
v_mul_f32 v18,v18,v16
v_mul_f32 v19,v19,v16
v_accvgpr_write_b32 a82,v18
v_accvgpr_write_b32 a83,v19
v_accvgpr_read_b32 v18,a84
v_accvgpr_read_b32 v19,a85
v_mul_f32 v18,v18,v17
v_mul_f32 v19,v19,v17
v_accvgpr_write_b32 a84,v18
v_accvgpr_write_b32 a85,v19
v_accvgpr_read_b32 v18,a86
v_accvgpr_read_b32 v19,a87
v_mul_f32 v18,v18,v17
v_mul_f32 v19,v19,v17
v_accvgpr_write_b32 a86,v18
v_accvgpr_write_b32 a87,v19
v_accvgpr_read_b32 v18,a88
v_accvgpr_read_b32 v19,a89
v_mul_f32 v18,v18,v16
v_mul_f32 v19,v19,v16
v_accvgpr_write_b32 a88,v18
)"
R"(v_accvgpr_write_b32 a89,v19
v_accvgpr_read_b32 v18,a90
v_accvgpr_read_b32 v19,a91
v_mul_f32 v18,v18,v16
v_mul_f32 v19,v19,v16
v_accvgpr_write_b32 a90,v18
v_accvgpr_write_b32 a91,v19
v_accvgpr_read_b32 v18,a92
v_accvgpr_read_b32 v19,a93
v_mul_f32 v18,v18,v17
v_mul_f32 v19,v19,v17
v_accvgpr_write_b32 a92,v18
v_accvgpr_write_b32 a93,v19
v_accvgpr_read_b32 v18,a94
v_accvgpr_read_b32 v19,a95
v_mul_f32 v18,v18,v17
v_mul_f32 v19,v19,v17
v_accvgpr_write_b32 a94,v18
v_accvgpr_write_b32 a95,v19
v_accvgpr_read_b32 v18,a96
v_accvgpr_read_b32 v19,a97
v_mul_f32 v18,v18,v16
v_mul_f32 v19,v19,v16
v_accvgpr_write_b32 a96,v18
v_accvgpr_write_b32 a97,v19
v_accvgpr_read_b32 v18,a98
v_accvgpr_read_b32 v19,a99
v_mul_f32 v18,v18,v16
v_mul_f32 v19,v19,v16
v_accvgpr_write_b32 a98,v18
v_accvgpr_write_b32 a99,v19
v_accvgpr_read_b32 v18,a100
v_accvgpr_read_b32 v19,a101
v_mul_f32 v18,v18,v17
v_mul_f32 v19,v19,v17
v_accvgpr_write_b32 a100,v18
v_accvgpr_write_b32 a101,v19
v_accvgpr_read_b32 v18,a102
v_accvgpr_read_b32 v19,a103
v_mul_f32 v18,v18,v17
v_mul_f32 v19,v19,v17
v_accvgpr_write_b32 a102,v18
v_accvgpr_write_b32 a103,v19
v_accvgpr_read_b32 v18,a104
v_accvgpr_read_b32 v19,a105
v_mul_f32 v18,v18,v16
v_mul_f32 v19,v19,v16
v_accvgpr_write_b32 a104,v18
v_accvgpr_write_b32 a105,v19
v_accvgpr_read_b32 v18,a106
v_accvgpr_read_b32 v19,a107
v_mul_f32 v18,v18,v16
v_mul_f32 v19,v19,v16
v_accvgpr_write_b32 a106,v18
v_accvgpr_write_b32 a107,v19
v_accvgpr_read_b32 v18,a108
v_accvgpr_read_b32 v19,a109
v_mul_f32 v18,v18,v17
v_mul_f32 v19,v19,v17
v_accvgpr_write_b32 a108,v18
v_accvgpr_write_b32 a109,v19
v_accvgpr_read_b32 v18,a110
v_accvgpr_read_b32 v19,a111
v_mul_f32 v18,v18,v17
v_mul_f32 v19,v19,v17
v_accvgpr_write_b32 a110,v18
v_accvgpr_write_b32 a111,v19
v_accvgpr_read_b32 v18,a112
v_accvgpr_read_b32 v19,a113
v_mul_f32 v18,v18,v16
v_mul_f32 v19,v19,v16
v_accvgpr_write_b32 a112,v18
v_accvgpr_write_b32 a113,v19
v_accvgpr_read_b32 v18,a114
v_accvgpr_read_b32 v19,a115
v_mul_f32 v18,v18,v16
v_mul_f32 v19,v19,v16
v_accvgpr_write_b32 a114,v18
v_accvgpr_write_b32 a115,v19
v_accvgpr_read_b32 v18,a116
v_accvgpr_read_b32 v19,a117
v_mul_f32 v18,v18,v17
v_mul_f32 v19,v19,v17
v_accvgpr_write_b32 a116,v18
v_accvgpr_write_b32 a117,v19
v_accvgpr_read_b32 v18,a118
v_accvgpr_read_b32 v19,a119
v_mul_f32 v18,v18,v17
v_mul_f32 v19,v19,v17
v_accvgpr_write_b32 a118,v18
v_accvgpr_write_b32 a119,v19
v_accvgpr_read_b32 v18,a120
v_accvgpr_read_b32 v19,a121
v_mul_f32 v18,v18,v16
v_mul_f32 v19,v19,v16
v_accvgpr_write_b32 a120,v18
v_accvgpr_write_b32 a121,v19
v_accvgpr_read_b32 v18,a122
v_accvgpr_read_b32 v19,a123
v_mul_f32 v18,v18,v16
v_mul_f32 v19,v19,v16
v_accvgpr_write_b32 a122,v18
v_accvgpr_write_b32 a123,v19
v_accvgpr_read_b32 v18,a124
v_accvgpr_read_b32 v19,a125
v_mul_f32 v18,v18,v17
v_mul_f32 v19,v19,v17
v_accvgpr_write_b32 a124,v18
v_accvgpr_write_b32 a125,v19
v_accvgpr_read_b32 v18,a126
v_accvgpr_read_b32 v19,a127
v_mul_f32 v18,v18,v17
v_mul_f32 v19,v19,v17
v_accvgpr_write_b32 a126,v18
v_accvgpr_write_b32 a127,v19
v_accvgpr_read_b32 v18,a128
v_accvgpr_read_b32 v19,a129
v_mul_f32 v18,v18,v16
v_mul_f32 v19,v19,v16
v_accvgpr_write_b32 a128,v18
v_accvgpr_write_b32 a129,v19
v_accvgpr_read_b32 v18,a130
v_accvgpr_read_b32 v19,a131
v_mul_f32 v18,v18,v16
v_mul_f32 v19,v19,v16
v_accvgpr_write_b32 a130,v18
v_accvgpr_write_b32 a131,v19
v_accvgpr_read_b32 v18,a132
v_accvgpr_read_b32 v19,a133
v_mul_f32 v18,v18,v17
v_mul_f32 v19,v19,v17
v_accvgpr_write_b32 a132,v18
v_accvgpr_write_b32 a133,v19
v_accvgpr_read_b32 v18,a134
v_accvgpr_read_b32 v19,a135
v_mul_f32 v18,v18,v17
v_mul_f32 v19,v19,v17
v_accvgpr_write_b32 a134,v18
v_accvgpr_write_b32 a135,v19
v_accvgpr_read_b32 v18,a136
v_accvgpr_read_b32 v19,a137
v_mul_f32 v18,v18,v16
v_mul_f32 v19,v19,v16
v_accvgpr_write_b32 a136,v18
v_accvgpr_write_b32 a137,v19
v_accvgpr_read_b32 v18,a138
v_accvgpr_read_b32 v19,a139
v_mul_f32 v18,v18,v16
v_mul_f32 v19,v19,v16
v_accvgpr_write_b32 a138,v18
v_accvgpr_write_b32 a139,v19
v_accvgpr_read_b32 v18,a140
v_accvgpr_read_b32 v19,a141
v_mul_f32 v18,v18,v17
v_mul_f32 v19,v19,v17
v_accvgpr_write_b32 a140,v18
v_accvgpr_write_b32 a141,v19
v_accvgpr_read_b32 v18,a142
v_accvgpr_read_b32 v19,a143
v_mul_f32 v18,v18,v17
v_mul_f32 v19,v19,v17
v_accvgpr_write_b32 a142,v18
v_accvgpr_write_b32 a143,v19
v_accvgpr_read_b32 v18,a144
v_accvgpr_read_b32 v19,a145
v_mul_f32 v18,v18,v16
v_mul_f32 v19,v19,v16
v_accvgpr_write_b32 a144,v18
v_accvgpr_write_b32 a145,v19
v_accvgpr_read_b32 v18,a146
v_accvgpr_read_b32 v19,a147
v_mul_f32 v18,v18,v16
v_mul_f32 v19,v19,v16
v_accvgpr_write_b32 a146,v18
v_accvgpr_write_b32 a147,v19
v_accvgpr_read_b32 v18,a148
v_accvgpr_read_b32 v19,a149
v_mul_f32 v18,v18,v17
v_mul_f32 v19,v19,v17
v_accvgpr_write_b32 a148,v18
v_accvgpr_write_b32 a149,v19
v_accvgpr_read_b32 v18,a150
v_accvgpr_read_b32 v19,a151
v_mul_f32 v18,v18,v17
v_mul_f32 v19,v19,v17
v_accvgpr_write_b32 a150,v18
v_accvgpr_write_b32 a151,v19
v_accvgpr_read_b32 v18,a152
v_accvgpr_read_b32 v19,a153
v_mul_f32 v18,v18,v16
v_mul_f32 v19,v19,v16
v_accvgpr_write_b32 a152,v18
v_accvgpr_write_b32 a153,v19
v_accvgpr_read_b32 v18,a154
v_accvgpr_read_b32 v19,a155
v_mul_f32 v18,v18,v16
v_mul_f32 v19,v19,v16
v_accvgpr_write_b32 a154,v18
v_accvgpr_write_b32 a155,v19
v_accvgpr_read_b32 v18,a156
v_accvgpr_read_b32 v19,a157
v_mul_f32 v18,v18,v17
v_mul_f32 v19,v19,v17
v_accvgpr_write_b32 a156,v18
v_accvgpr_write_b32 a157,v19
v_accvgpr_read_b32 v18,a158
v_accvgpr_read_b32 v19,a159
v_mul_f32 v18,v18,v17
v_mul_f32 v19,v19,v17
v_accvgpr_write_b32 a158,v18
v_accvgpr_write_b32 a159,v19
v_accvgpr_read_b32 v18,a160
v_accvgpr_read_b32 v19,a161
v_mul_f32 v18,v18,v16
v_mul_f32 v19,v19,v16
v_accvgpr_write_b32 a160,v18
v_accvgpr_write_b32 a161,v19
v_accvgpr_read_b32 v18,a162
v_accvgpr_read_b32 v19,a163
v_mul_f32 v18,v18,v16
v_mul_f32 v19,v19,v16
v_accvgpr_write_b32 a162,v18
v_accvgpr_write_b32 a163,v19
v_accvgpr_read_b32 v18,a164
v_accvgpr_read_b32 v19,a165
v_mul_f32 v18,v18,v17
v_mul_f32 v19,v19,v17
v_accvgpr_write_b32 a164,v18
v_accvgpr_write_b32 a165,v19
v_accvgpr_read_b32 v18,a166
v_accvgpr_read_b32 v19,a167
v_mul_f32 v18,v18,v17
v_mul_f32 v19,v19,v17
v_accvgpr_write_b32 a166,v18
v_accvgpr_write_b32 a167,v19
v_accvgpr_read_b32 v18,a168
v_accvgpr_read_b32 v19,a169
v_mul_f32 v18,v18,v16
v_mul_f32 v19,v19,v16
v_accvgpr_write_b32 a168,v18
v_accvgpr_write_b32 a169,v19
v_accvgpr_read_b32 v18,a170
v_accvgpr_read_b32 v19,a171
v_mul_f32 v18,v18,v16
v_mul_f32 v19,v19,v16
v_accvgpr_write_b32 a170,v18
v_accvgpr_write_b32 a171,v19
v_accvgpr_read_b32 v18,a172
v_accvgpr_read_b32 v19,a173
v_mul_f32 v18,v18,v17
v_mul_f32 v19,v19,v17
v_accvgpr_write_b32 a172,v18
v_accvgpr_write_b32 a173,v19
v_accvgpr_read_b32 v18,a174
v_accvgpr_read_b32 v19,a175
v_mul_f32 v18,v18,v17
v_mul_f32 v19,v19,v17
v_accvgpr_write_b32 a174,v18
v_accvgpr_write_b32 a175,v19
v_accvgpr_read_b32 v18,a176
v_accvgpr_read_b32 v19,a177
v_mul_f32 v18,v18,v16
v_mul_f32 v19,v19,v16
v_accvgpr_write_b32 a176,v18
v_accvgpr_write_b32 a177,v19
v_accvgpr_read_b32 v18,a178
v_accvgpr_read_b32 v19,a179
v_mul_f32 v18,v18,v16
v_mul_f32 v19,v19,v16
v_accvgpr_write_b32 a178,v18
v_accvgpr_write_b32 a179,v19
v_accvgpr_read_b32 v18,a180
v_accvgpr_read_b32 v19,a181
v_mul_f32 v18,v18,v17
v_mul_f32 v19,v19,v17
v_accvgpr_write_b32 a180,v18
v_accvgpr_write_b32 a181,v19
v_accvgpr_read_b32 v18,a182
v_accvgpr_read_b32 v19,a183
v_mul_f32 v18,v18,v17
v_mul_f32 v19,v19,v17
v_accvgpr_write_b32 a182,v18
v_accvgpr_write_b32 a183,v19
v_accvgpr_read_b32 v18,a184
v_accvgpr_read_b32 v19,a185
v_mul_f32 v18,v18,v16
v_mul_f32 v19,v19,v16
v_accvgpr_write_b32 a184,v18
v_accvgpr_write_b32 a185,v19
v_accvgpr_read_b32 v18,a186
v_accvgpr_read_b32 v19,a187
v_mul_f32 v18,v18,v16
v_mul_f32 v19,v19,v16
v_accvgpr_write_b32 a186,v18
v_accvgpr_write_b32 a187,v19
v_accvgpr_read_b32 v18,a188
v_accvgpr_read_b32 v19,a189
v_mul_f32 v18,v18,v17
v_mul_f32 v19,v19,v17
v_accvgpr_write_b32 a188,v18
v_accvgpr_write_b32 a189,v19
v_accvgpr_read_b32 v18,a190
v_accvgpr_read_b32 v19,a191
v_mul_f32 v18,v18,v17
v_mul_f32 v19,v19,v17
v_accvgpr_write_b32 a190,v18
v_accvgpr_write_b32 a191,v19
v_accvgpr_read_b32 v18,a192
v_accvgpr_read_b32 v19,a193
v_mul_f32 v18,v18,v16
v_mul_f32 v19,v19,v16
v_accvgpr_write_b32 a192,v18
v_accvgpr_write_b32 a193,v19
v_accvgpr_read_b32 v18,a194
v_accvgpr_read_b32 v19,a195
v_mul_f32 v18,v18,v16
v_mul_f32 v19,v19,v16
v_accvgpr_write_b32 a194,v18
v_accvgpr_write_b32 a195,v19
v_accvgpr_read_b32 v18,a196
v_accvgpr_read_b32 v19,a197
v_mul_f32 v18,v18,v17
v_mul_f32 v19,v19,v17
v_accvgpr_write_b32 a196,v18
v_accvgpr_write_b32 a197,v19
v_accvgpr_read_b32 v18,a198
v_accvgpr_read_b32 v19,a199
v_mul_f32 v18,v18,v17
v_mul_f32 v19,v19,v17
v_accvgpr_write_b32 a198,v18
v_accvgpr_write_b32 a199,v19
v_accvgpr_read_b32 v18,a200
v_accvgpr_read_b32 v19,a201
v_mul_f32 v18,v18,v16
v_mul_f32 v19,v19,v16
v_accvgpr_write_b32 a200,v18
v_accvgpr_write_b32 a201,v19
v_accvgpr_read_b32 v18,a202
v_accvgpr_read_b32 v19,a203
v_mul_f32 v18,v18,v16
v_mul_f32 v19,v19,v16
v_accvgpr_write_b32 a202,v18
v_accvgpr_write_b32 a203,v19
v_accvgpr_read_b32 v18,a204
v_accvgpr_read_b32 v19,a205
v_mul_f32 v18,v18,v17
v_mul_f32 v19,v19,v17
v_accvgpr_write_b32 a204,v18
v_accvgpr_write_b32 a205,v19
v_accvgpr_read_b32 v18,a206
v_accvgpr_read_b32 v19,a207
v_mul_f32 v18,v18,v17
v_mul_f32 v19,v19,v17
v_accvgpr_write_b32 a206,v18
v_accvgpr_write_b32 a207,v19
v_accvgpr_read_b32 v18,a208
v_accvgpr_read_b32 v19,a209
v_mul_f32 v18,v18,v16
v_mul_f32 v19,v19,v16
v_accvgpr_write_b32 a208,v18
v_accvgpr_write_b32 a209,v19
v_accvgpr_read_b32 v18,a210
v_accvgpr_read_b32 v19,a211
v_mul_f32 v18,v18,v16
v_mul_f32 v19,v19,v16
v_accvgpr_write_b32 a210,v18
v_accvgpr_write_b32 a211,v19
v_accvgpr_read_b32 v18,a212
v_accvgpr_read_b32 v19,a213
v_mul_f32 v18,v18,v17
v_mul_f32 v19,v19,v17
v_accvgpr_write_b32 a212,v18
v_accvgpr_write_b32 a213,v19
v_accvgpr_read_b32 v18,a214
v_accvgpr_read_b32 v19,a215
v_mul_f32 v18,v18,v17
v_mul_f32 v19,v19,v17
v_accvgpr_write_b32 a214,v18
v_accvgpr_write_b32 a215,v19
v_accvgpr_read_b32 v18,a216
v_accvgpr_read_b32 v19,a217
v_mul_f32 v18,v18,v16
v_mul_f32 v19,v19,v16
v_accvgpr_write_b32 a216,v18
v_accvgpr_write_b32 a217,v19
v_accvgpr_read_b32 v18,a218
v_accvgpr_read_b32 v19,a219
v_mul_f32 v18,v18,v16
v_mul_f32 v19,v19,v16
v_accvgpr_write_b32 a218,v18
v_accvgpr_write_b32 a219,v19
v_accvgpr_read_b32 v18,a220
v_accvgpr_read_b32 v19,a221
v_mul_f32 v18,v18,v17
v_mul_f32 v19,v19,v17
v_accvgpr_write_b32 a220,v18
v_accvgpr_write_b32 a221,v19
v_accvgpr_read_b32 v18,a222
v_accvgpr_read_b32 v19,a223
v_mul_f32 v18,v18,v17
)"
R"(v_mul_f32 v19,v19,v17
v_accvgpr_write_b32 a222,v18
v_accvgpr_write_b32 a223,v19
v_accvgpr_read_b32 v18,a224
v_accvgpr_read_b32 v19,a225
v_mul_f32 v18,v18,v16
v_mul_f32 v19,v19,v16
v_accvgpr_write_b32 a224,v18
v_accvgpr_write_b32 a225,v19
v_accvgpr_read_b32 v18,a226
v_accvgpr_read_b32 v19,a227
v_mul_f32 v18,v18,v16
v_mul_f32 v19,v19,v16
v_accvgpr_write_b32 a226,v18
v_accvgpr_write_b32 a227,v19
v_accvgpr_read_b32 v18,a228
v_accvgpr_read_b32 v19,a229
v_mul_f32 v18,v18,v17
v_mul_f32 v19,v19,v17
v_accvgpr_write_b32 a228,v18
v_accvgpr_write_b32 a229,v19
v_accvgpr_read_b32 v18,a230
v_accvgpr_read_b32 v19,a231
v_mul_f32 v18,v18,v17
v_mul_f32 v19,v19,v17
v_accvgpr_write_b32 a230,v18
v_accvgpr_write_b32 a231,v19
v_accvgpr_read_b32 v18,a232
v_accvgpr_read_b32 v19,a233
v_mul_f32 v18,v18,v16
v_mul_f32 v19,v19,v16
v_accvgpr_write_b32 a232,v18
v_accvgpr_write_b32 a233,v19
v_accvgpr_read_b32 v18,a234
v_accvgpr_read_b32 v19,a235
v_mul_f32 v18,v18,v16
v_mul_f32 v19,v19,v16
v_accvgpr_write_b32 a234,v18
v_accvgpr_write_b32 a235,v19
v_accvgpr_read_b32 v18,a236
v_accvgpr_read_b32 v19,a237
v_mul_f32 v18,v18,v17
v_mul_f32 v19,v19,v17
v_accvgpr_write_b32 a236,v18
v_accvgpr_write_b32 a237,v19
v_accvgpr_read_b32 v18,a238
v_accvgpr_read_b32 v19,a239
v_mul_f32 v18,v18,v17
v_mul_f32 v19,v19,v17
v_accvgpr_write_b32 a238,v18
v_accvgpr_write_b32 a239,v19
v_accvgpr_read_b32 v18,a240
v_accvgpr_read_b32 v19,a241
v_mul_f32 v18,v18,v16
v_mul_f32 v19,v19,v16
v_accvgpr_write_b32 a240,v18
v_accvgpr_write_b32 a241,v19
v_accvgpr_read_b32 v18,a242
v_accvgpr_read_b32 v19,a243
v_mul_f32 v18,v18,v16
v_mul_f32 v19,v19,v16
v_accvgpr_write_b32 a242,v18
v_accvgpr_write_b32 a243,v19
v_accvgpr_read_b32 v18,a244
v_accvgpr_read_b32 v19,a245
v_mul_f32 v18,v18,v17
v_mul_f32 v19,v19,v17
v_accvgpr_write_b32 a244,v18
v_accvgpr_write_b32 a245,v19
v_accvgpr_read_b32 v18,a246
v_accvgpr_read_b32 v19,a247
v_mul_f32 v18,v18,v17
v_mul_f32 v19,v19,v17
v_accvgpr_write_b32 a246,v18
v_accvgpr_write_b32 a247,v19
v_accvgpr_read_b32 v18,a248
v_accvgpr_read_b32 v19,a249
v_mul_f32 v18,v18,v16
v_mul_f32 v19,v19,v16
v_accvgpr_write_b32 a248,v18
v_accvgpr_write_b32 a249,v19
v_accvgpr_read_b32 v18,a250
v_accvgpr_read_b32 v19,a251
v_mul_f32 v18,v18,v16
v_mul_f32 v19,v19,v16
v_accvgpr_write_b32 a250,v18
v_accvgpr_write_b32 a251,v19
v_accvgpr_read_b32 v18,a252
v_accvgpr_read_b32 v19,a253
v_mul_f32 v18,v18,v17
v_mul_f32 v19,v19,v17
v_accvgpr_write_b32 a252,v18
v_accvgpr_write_b32 a253,v19
v_accvgpr_read_b32 v18,a254
v_accvgpr_read_b32 v19,a255
v_mul_f32 v18,v18,v17
v_mul_f32 v19,v19,v17
v_accvgpr_write_b32 a254,v18
v_accvgpr_write_b32 a255,v19
RESCX_%=:
s_nop 3
s_setpc_b64 s[72:73]
TAILB_%=:
v_mfma_f32_16x16x32_bf16 v[112:115],v[64:67],v[192:195],v[112:115]
ds_read_b128 v[92:95],v23 offset:14336
v_mfma_f32_16x16x32_bf16 v[116:119],v[64:67],v[196:199],v[116:119]
s_waitcnt lgkmcnt(6)
buffer_load_dwordx4 v22,%[srdW],s59 offen offset:2048 sc1 lds
v_mfma_f32_16x16x32_bf16 v[112:115],v[68:71],v[200:203],v[112:115]
ds_read_b128 v[64:67],v23 offset:512
v_mfma_f32_16x16x32_bf16 v[116:119],v[68:71],v[204:207],v[116:119]
s_waitcnt lgkmcnt(6)
buffer_load_dwordx4 v[180:183],%[vL16],%[srdW],s45 offen sc1
v_mfma_f32_16x16x32_bf16 v[112:115],v[72:75],v[208:211],v[112:115]
ds_read_b128 v[68:71],v23 offset:2560
v_mfma_f32_16x16x32_bf16 v[116:119],v[72:75],v[212:215],v[116:119]
s_waitcnt lgkmcnt(6)
buffer_load_dwordx4 v22,%[srdW],s59 offen offset:3072 sc1 lds
v_mfma_f32_16x16x32_bf16 v[112:115],v[76:79],v[216:219],v[112:115]
s_add_u32 m0,s64,s41
ds_read_b128 v[72:75],v23 offset:4608
v_mfma_f32_16x16x32_bf16 v[116:119],v[76:79],v[220:223],v[116:119]
s_waitcnt lgkmcnt(6)
s_add_u32 s67,s45,0x1000
buffer_load_dwordx4 v[184:187],%[vL16],%[srdW],s67 offen sc1
v_mfma_f32_16x16x32_bf16 v[112:115],v[80:83],v[224:227],v[112:115]
s_barrier
ds_read_b128 v[76:79],v23 offset:6656
v_add_u32 v25,s60,v4
v_mfma_f32_16x16x32_bf16 v[116:119],v[80:83],v[228:231],v[116:119]
s_waitcnt lgkmcnt(6)
buffer_load_dwordx4 %[vL16],%[srdW],s65 offen offset:0 sc1 lds
v_mfma_f32_16x16x32_bf16 v[112:115],v[84:87],v[232:235],v[112:115]
ds_read_b128 v[80:83],v23 offset:8704
v_mfma_f32_16x16x32_bf16 v[116:119],v[84:87],v[236:239],v[116:119]
s_waitcnt lgkmcnt(6)
s_add_u32 s67,s45,0x2000
buffer_load_dwordx4 v[188:191],%[vL16],%[srdW],s67 offen sc1
v_mfma_f32_16x16x32_bf16 v[112:115],v[88:91],v[240:243],v[112:115]
ds_read_b128 v[84:87],v23 offset:10752
v_add_u32 v26,s61,v4
v_mfma_f32_16x16x32_bf16 v[116:119],v[88:91],v[244:247],v[116:119]
s_waitcnt lgkmcnt(6)
buffer_load_dwordx4 %[vL16],%[srdW],s65 offen offset:1024 sc1 lds
v_mfma_f32_16x16x32_bf16 v[112:115],v[92:95],v[248:251],v[112:115]
ds_read_b128 v[88:91],v23 offset:12800
v_mfma_f32_16x16x32_bf16 v[116:119],v[92:95],v[252:255],v[116:119]
s_waitcnt lgkmcnt(6)
v_mfma_f32_16x16x32_bf16 v[120:123],v[64:67],v[192:195],v[120:123]
ds_read_b128 v[92:95],v23 offset:14848
v_mfma_f32_16x16x32_bf16 v[124:127],v[64:67],v[196:199],v[124:127]
s_waitcnt lgkmcnt(6)
v_mfma_f32_16x16x32_bf16 v[120:123],v[68:71],v[200:203],v[120:123]
ds_read_b64_tr_b16 v[64:65],v25 offset:0
ds_read_b64_tr_b16 v[66:67],v25 offset:512
v_mfma_f32_16x16x32_bf16 v[124:127],v[68:71],v[204:207],v[124:127]
s_waitcnt lgkmcnt(7)
v_exp_f32 v32,v112
v_mfma_f32_16x16x32_bf16 v[120:123],v[72:75],v[208:211],v[120:123]
ds_read_b64_tr_b16 v[68:69],v25 offset:1024
v_mfma_f32_16x16x32_bf16 v[124:127],v[72:75],v[212:215],v[124:127]
s_waitcnt lgkmcnt(7)
ds_read_b64_tr_b16 v[70:71],v25 offset:1536
v_mfma_f32_16x16x32_bf16 v[120:123],v[76:79],v[216:219],v[120:123]
ds_read_b64_tr_b16 v[72:73],v25 offset:2048
ds_read_b64_tr_b16 v[74:75],v25 offset:2560
v_mfma_f32_16x16x32_bf16 v[124:127],v[76:79],v[220:223],v[124:127]
s_waitcnt lgkmcnt(9)
v_exp_f32 v33,v113
v_mfma_f32_16x16x32_bf16 v[120:123],v[80:83],v[224:227],v[120:123]
ds_read_b64_tr_b16 v[76:77],v25 offset:3072
v_mfma_f32_16x16x32_bf16 v[124:127],v[80:83],v[228:231],v[124:127]
s_waitcnt lgkmcnt(9)
ds_read_b64_tr_b16 v[78:79],v25 offset:3584
v_mfma_f32_16x16x32_bf16 v[120:123],v[84:87],v[232:235],v[120:123]
ds_read_b64_tr_b16 v[80:81],v25 offset:4096
ds_read_b64_tr_b16 v[82:83],v25 offset:4608
v_mfma_f32_16x16x32_bf16 v[124:127],v[84:87],v[236:239],v[124:127]
s_waitcnt lgkmcnt(11)
v_exp_f32 v34,v114
v_mfma_f32_16x16x32_bf16 v[120:123],v[88:91],v[240:243],v[120:123]
ds_read_b64_tr_b16 v[84:85],v25 offset:5120
v_mfma_f32_16x16x32_bf16 v[124:127],v[88:91],v[244:247],v[124:127]
s_waitcnt lgkmcnt(11)
ds_read_b64_tr_b16 v[86:87],v25 offset:5632
v_mfma_f32_16x16x32_bf16 v[120:123],v[92:95],v[248:251],v[120:123]
ds_read_b64_tr_b16 v[88:89],v25 offset:6144
ds_read_b64_tr_b16 v[90:91],v25 offset:6656
v_mfma_f32_16x16x32_bf16 v[124:127],v[92:95],v[252:255],v[124:127]
s_waitcnt lgkmcnt(12)
v_exp_f32 v35,v115
v_exp_f32 v36,v116
v_exp_f32 v37,v117
buffer_load_dwordx4 v[208:211],%[vL16],%[srdW],s44 offen offset:0 sc1
v_exp_f32 v38,v118
v_exp_f32 v39,v119
v_max3_f32 v27,v112,v113,v114
buffer_load_dwordx4 v[212:215],%[vL16],%[srdW],s44 offen offset:1024 sc1
v_max3_f32 v27,v27,v115,v116
v_max3_f32 v27,v27,v117,v118
v_max_f32 v27,v27,v119
buffer_load_dwordx4 v[216:219],%[vL16],%[srdW],s44 offen offset:2048 sc1
v_exp_f32 v40,v120
v_exp_f32 v41,v121
v_exp_f32 v42,v122
buffer_load_dwordx4 v[220:223],%[vL16],%[srdW],s44 offen offset:3072 sc1
v_exp_f32 v43,v123
v_exp_f32 v44,v124
v_exp_f32 v45,v125
s_add_u32 s67,s44,0x1000
buffer_load_dwordx4 v[224:227],%[vL16],%[srdW],s67 offen sc1
v_exp_f32 v46,v126
v_exp_f32 v47,v127
v_max3_f32 v27,v27,v120,v121
v_max3_f32 v27,v27,v122,v123
v_max3_f32 v27,v27,v124,v125
v_max3_f32 v27,v27,v126,v127
v_cmp_lt_f32 vcc,s68,v27
v_cvt_pk_bf16_f32 v56,v32,v33
v_cvt_pk_bf16_f32 v57,v34,v35
v_cvt_pk_bf16_f32 v60,v36,v37
v_cvt_pk_bf16_f32 v61,v38,v39
v_cvt_pk_bf16_f32 v58,v40,v41
v_cvt_pk_bf16_f32 v59,v42,v43
v_cvt_pk_bf16_f32 v62,v44,v45
v_cvt_pk_bf16_f32 v63,v46,v47
s_cbranch_vccnz RARETB_%=
BACKTB_%=:
s_nop 1
v_mfma_f32_16x16x32_bf16 a[0:3],v[64:67],v[56:59],a[0:3]
ds_read_b64_tr_b16 v[92:93],v25 offset:7168
v_add_f32 v30,v30,v32
v_mfma_f32_16x16x32_bf16 a[4:7],v[64:67],v[60:63],a[4:7]
s_waitcnt lgkmcnt(11)
ds_read_b64_tr_b16 v[94:95],v25 offset:7680
v_add_f32 v31,v31,v36
v_mfma_f32_16x16x32_bf16 a[12:15],v[68:71],v[60:63],a[12:15]
ds_read_b64_tr_b16 v[64:65],v25 offset:8192
v_add_f32 v30,v30,v40
v_mfma_f32_16x16x32_bf16 a[8:11],v[68:71],v[56:59],a[8:11]
s_waitcnt lgkmcnt(11)
ds_read_b64_tr_b16 v[66:67],v25 offset:8704
v_add_f32 v31,v31,v44
v_mfma_f32_16x16x32_bf16 a[16:19],v[72:75],v[56:59],a[16:19]
ds_read_b64_tr_b16 v[68:69],v25 offset:9216
v_add_f32 v30,v30,v33
v_mfma_f32_16x16x32_bf16 a[20:23],v[72:75],v[60:63],a[20:23]
s_waitcnt lgkmcnt(11)
ds_read_b64_tr_b16 v[70:71],v25 offset:9728
v_add_f32 v31,v31,v37
v_mfma_f32_16x16x32_bf16 a[28:31],v[76:79],v[60:63],a[28:31]
ds_read_b64_tr_b16 v[72:73],v25 offset:10240
v_add_f32 v30,v30,v41
v_mfma_f32_16x16x32_bf16 a[24:27],v[76:79],v[56:59],a[24:27]
s_waitcnt lgkmcnt(11)
ds_read_b64_tr_b16 v[74:75],v25 offset:10752
v_add_f32 v31,v31,v45
v_mfma_f32_16x16x32_bf16 a[32:35],v[80:83],v[56:59],a[32:35]
ds_read_b64_tr_b16 v[76:77],v25 offset:11264
v_add_f32 v30,v30,v34
v_mfma_f32_16x16x32_bf16 a[36:39],v[80:83],v[60:63],a[36:39]
s_waitcnt lgkmcnt(11)
ds_read_b64_tr_b16 v[78:79],v25 offset:11776
v_add_f32 v31,v31,v38
v_mfma_f32_16x16x32_bf16 a[44:47],v[84:87],v[60:63],a[44:47]
ds_read_b64_tr_b16 v[80:81],v25 offset:12288
v_add_f32 v30,v30,v42
v_mfma_f32_16x16x32_bf16 a[40:43],v[84:87],v[56:59],a[40:43]
s_waitcnt lgkmcnt(11)
ds_read_b64_tr_b16 v[82:83],v25 offset:12800
v_add_f32 v31,v31,v46
v_mfma_f32_16x16x32_bf16 a[48:51],v[88:91],v[56:59],a[48:51]
ds_read_b64_tr_b16 v[84:85],v25 offset:13312
v_add_f32 v30,v30,v35
v_mfma_f32_16x16x32_bf16 a[52:55],v[88:91],v[60:63],a[52:55]
s_waitcnt lgkmcnt(11)
ds_read_b64_tr_b16 v[86:87],v25 offset:13824
v_add_f32 v31,v31,v39
v_mfma_f32_16x16x32_bf16 a[60:63],v[92:95],v[60:63],a[60:63]
ds_read_b64_tr_b16 v[88:89],v25 offset:14336
v_add_f32 v30,v30,v43
v_mfma_f32_16x16x32_bf16 a[56:59],v[92:95],v[56:59],a[56:59]
s_waitcnt lgkmcnt(11)
ds_read_b64_tr_b16 v[90:91],v25 offset:14848
v_add_f32 v31,v31,v47
v_mfma_f32_16x16x32_bf16 a[64:67],v[64:67],v[56:59],a[64:67]
ds_read_b64_tr_b16 v[92:93],v25 offset:15360
v_mov_b32 v16,v30
v_mfma_f32_16x16x32_bf16 a[68:71],v[64:67],v[60:63],a[68:71]
s_waitcnt lgkmcnt(11)
ds_read_b64_tr_b16 v[94:95],v25 offset:15872
v_mov_b32 v17,v31
v_mfma_f32_16x16x32_bf16 a[76:79],v[68:71],v[60:63],a[76:79]
ds_read_b64_tr_b16 v[64:65],v26 offset:0
v_permlane16_swap_b32 v30,v16
v_mfma_f32_16x16x32_bf16 a[72:75],v[68:71],v[56:59],a[72:75]
s_waitcnt lgkmcnt(11)
ds_read_b64_tr_b16 v[66:67],v26 offset:512
v_permlane16_swap_b32 v31,v17
v_mfma_f32_16x16x32_bf16 a[80:83],v[72:75],v[56:59],a[80:83]
ds_read_b64_tr_b16 v[68:69],v26 offset:1024
v_add_f32 v30,v30,v16
v_mfma_f32_16x16x32_bf16 a[84:87],v[72:75],v[60:63],a[84:87]
s_waitcnt lgkmcnt(11)
ds_read_b64_tr_b16 v[70:71],v26 offset:1536
v_add_f32 v31,v31,v17
v_mfma_f32_16x16x32_bf16 a[92:95],v[76:79],v[60:63],a[92:95]
ds_read_b64_tr_b16 v[72:73],v26 offset:2048
v_mov_b32 v16,v30
v_mfma_f32_16x16x32_bf16 a[88:91],v[76:79],v[56:59],a[88:91]
s_waitcnt lgkmcnt(11)
ds_read_b64_tr_b16 v[74:75],v26 offset:2560
v_mov_b32 v17,v31
v_mfma_f32_16x16x32_bf16 a[96:99],v[80:83],v[56:59],a[96:99]
ds_read_b64_tr_b16 v[76:77],v26 offset:3072
v_permlane32_swap_b32 v30,v16
v_mfma_f32_16x16x32_bf16 a[100:103],v[80:83],v[60:63],a[100:103]
s_waitcnt lgkmcnt(11)
ds_read_b64_tr_b16 v[78:79],v26 offset:3584
v_permlane32_swap_b32 v31,v17
v_mfma_f32_16x16x32_bf16 a[108:111],v[84:87],v[60:63],a[108:111]
ds_read_b64_tr_b16 v[80:81],v26 offset:4096
v_add_f32 v30,v30,v16
v_mfma_f32_16x16x32_bf16 a[104:107],v[84:87],v[56:59],a[104:107]
s_waitcnt lgkmcnt(11)
ds_read_b64_tr_b16 v[82:83],v26 offset:4608
v_add_f32 v31,v31,v17
v_mfma_f32_16x16x32_bf16 a[112:115],v[88:91],v[56:59],a[112:115]
ds_read_b64_tr_b16 v[84:85],v26 offset:5120
v_add_u32 v18,s52,%[vL16]
v_mfma_f32_16x16x32_bf16 a[116:119],v[88:91],v[60:63],a[116:119]
s_waitcnt lgkmcnt(11)
ds_read_b64_tr_b16 v[86:87],v26 offset:5632
v_mfma_f32_16x16x32_bf16 a[124:127],v[92:95],v[60:63],a[124:127]
ds_read_b64_tr_b16 v[88:89],v26 offset:6144
ds_read_b64_tr_b16 v[90:91],v26 offset:6656
v_mfma_f32_16x16x32_bf16 a[120:123],v[92:95],v[56:59],a[120:123]
s_waitcnt lgkmcnt(12)
s_waitcnt vmcnt(5)
ds_write_b128 v18,v[28:31]
s_waitcnt lgkmcnt(0)
s_barrier
v_mfma_f32_16x16x32_bf16 a[128:131],v[64:67],v[56:59],a[128:131]
ds_read_b64_tr_b16 v[92:93],v26 offset:7168
v_add_f32 v180,v180,v182
v_mfma_f32_16x16x32_bf16 a[132:135],v[64:67],v[60:63],a[132:135]
ds_read_b64_tr_b16 v[94:95],v26 offset:7680
v_add_f32 v184,v184,v186
v_mfma_f32_16x16x32_bf16 a[140:143],v[68:71],v[60:63],a[140:143]
ds_read_b64_tr_b16 v[64:65],v26 offset:8192
v_add_f32 v188,v188,v190
v_mfma_f32_16x16x32_bf16 a[136:139],v[68:71],v[56:59],a[136:139]
ds_read_b64_tr_b16 v[66:67],v26 offset:8704
v_add_f32 v181,v181,v183
v_mfma_f32_16x16x32_bf16 a[144:147],v[72:75],v[56:59],a[144:147]
ds_read_b64_tr_b16 v[68:69],v26 offset:9216
v_add_f32 v185,v185,v187
v_mfma_f32_16x16x32_bf16 a[148:151],v[72:75],v[60:63],a[148:151]
ds_read_b64_tr_b16 v[70:71],v26 offset:9728
v_add_f32 v189,v189,v191
v_mfma_f32_16x16x32_bf16 a[156:159],v[76:79],v[60:63],a[156:159]
ds_read_b64_tr_b16 v[72:73],v26 offset:10240
v_add_f32_dpp v180,v180,v180 quad_perm:[1,0,3,2] row_mask:0xf bank_mask:0xf bound_ctrl:1
v_mfma_f32_16x16x32_bf16 a[152:155],v[76:79],v[56:59],a[152:155]
ds_read_b64_tr_b16 v[74:75],v26 offset:10752
v_add_f32_dpp v184,v184,v184 quad_perm:[1,0,3,2] row_mask:0xf bank_mask:0xf bound_ctrl:1
v_mfma_f32_16x16x32_bf16 a[160:163],v[80:83],v[56:59],a[160:163]
ds_read_b64_tr_b16 v[76:77],v26 offset:11264
v_add_f32_dpp v188,v188,v188 quad_perm:[1,0,3,2] row_mask:0xf bank_mask:0xf bound_ctrl:1
v_mfma_f32_16x16x32_bf16 a[164:167],v[80:83],v[60:63],a[164:167]
ds_read_b64_tr_b16 v[78:79],v26 offset:11776
v_add_f32_dpp v181,v181,v181 quad_perm:[1,0,3,2] row_mask:0xf bank_mask:0xf bound_ctrl:1
v_mfma_f32_16x16x32_bf16 a[172:175],v[84:87],v[60:63],a[172:175]
ds_read_b64_tr_b16 v[80:81],v26 offset:12288
v_add_f32_dpp v185,v185,v185 quad_perm:[1,0,3,2] row_mask:0xf bank_mask:0xf bound_ctrl:1
v_mfma_f32_16x16x32_bf16 a[168:171],v[84:87],v[56:59],a[168:171]
ds_read_b64_tr_b16 v[82:83],v26 offset:12800
v_add_f32_dpp v189,v189,v189 quad_perm:[1,0,3,2] row_mask:0xf bank_mask:0xf bound_ctrl:1
v_mfma_f32_16x16x32_bf16 a[176:179],v[88:91],v[56:59],a[176:179]
ds_read_b64_tr_b16 v[84:85],v26 offset:13312
v_mul_f32 v180,0x3b000000,v180
v_mfma_f32_16x16x32_bf16 a[180:183],v[88:91],v[60:63],a[180:183]
s_waitcnt lgkmcnt(11)
ds_read_b64_tr_b16 v[86:87],v26 offset:13824
v_mul_f32 v184,0x3b000000,v184
v_mfma_f32_16x16x32_bf16 a[188:191],v[92:95],v[60:63],a[188:191]
ds_read_b64_tr_b16 v[88:89],v26 offset:14336
v_mul_f32 v188,0x3b000000,v188
v_mfma_f32_16x16x32_bf16 a[184:187],v[92:95],v[56:59],a[184:187]
s_waitcnt lgkmcnt(11)
ds_read_b64_tr_b16 v[90:91],v26 offset:14848
v_mul_f32_e64 v182,v180,-v180
v_mfma_f32_16x16x32_bf16 a[192:195],v[64:67],v[56:59],a[192:195]
ds_read_b64_tr_b16 v[92:93],v26 offset:15360
v_mul_f32_e64 v186,v184,-v184
v_mfma_f32_16x16x32_bf16 a[196:199],v[64:67],v[60:63],a[196:199]
s_waitcnt lgkmcnt(11)
ds_read_b64_tr_b16 v[94:95],v26 offset:15872
v_mul_f32_e64 v190,v188,-v188
v_mfma_f32_16x16x32_bf16 a[204:207],v[68:71],v[60:63],a[204:207]
)"
R"(v_fmac_f32 v182,0x3b000000,v181
v_mfma_f32_16x16x32_bf16 a[200:203],v[68:71],v[56:59],a[200:203]
s_waitcnt lgkmcnt(10)
v_fmac_f32 v186,0x3b000000,v185
v_mfma_f32_16x16x32_bf16 a[208:211],v[72:75],v[56:59],a[208:211]
v_fmac_f32 v190,0x3b000000,v189
v_mfma_f32_16x16x32_bf16 a[212:215],v[72:75],v[60:63],a[212:215]
s_waitcnt lgkmcnt(8)
v_max_f32 v182,0,v182
v_mfma_f32_16x16x32_bf16 a[220:223],v[76:79],v[60:63],a[220:223]
v_max_f32 v186,0,v186
v_mfma_f32_16x16x32_bf16 a[216:219],v[76:79],v[56:59],a[216:219]
s_waitcnt lgkmcnt(6)
v_max_f32 v190,0,v190
v_mfma_f32_16x16x32_bf16 a[224:227],v[80:83],v[56:59],a[224:227]
v_add_f32 v182,0x3727c5ac,v182
v_mfma_f32_16x16x32_bf16 a[228:231],v[80:83],v[60:63],a[228:231]
s_waitcnt lgkmcnt(4)
v_add_f32 v186,0x3727c5ac,v186
v_mfma_f32_16x16x32_bf16 a[236:239],v[84:87],v[60:63],a[236:239]
v_add_f32 v190,0x3727c5ac,v190
v_mfma_f32_16x16x32_bf16 a[232:235],v[84:87],v[56:59],a[232:235]
s_waitcnt lgkmcnt(2)
v_rsq_f32 v240,v182
v_mfma_f32_16x16x32_bf16 a[240:243],v[88:91],v[56:59],a[240:243]
v_rsq_f32 v241,v186
v_mfma_f32_16x16x32_bf16 a[244:247],v[88:91],v[60:63],a[244:247]
s_waitcnt lgkmcnt(0)
v_rsq_f32 v242,v190
v_mfma_f32_16x16x32_bf16 a[252:255],v[92:95],v[60:63],a[252:255]
v_mul_f32_e64 v181,v240,-v180
v_mfma_f32_16x16x32_bf16 a[248:251],v[92:95],v[56:59],a[248:251]
v_mul_f32_e64 v185,v241,-v184
v_mul_f32_e64 v189,v242,-v188
v_xor_b32 v97,16,v5
v_xor_b32 v98,32,v5
v_xor_b32 v99,48,v5
v_xor_b32 v101,16,v6
v_xor_b32 v102,32,v6
v_xor_b32 v103,48,v6
v_xor_b32 v105,16,v7
v_xor_b32 v106,32,v7
v_xor_b32 v107,48,v7
s_branch EPI_%=
TAILA_%=:
v_mfma_f32_16x16x32_bf16 v[96:99],v[64:67],v[192:195],v[96:99]
ds_read_b128 v[92:95],v23 offset:14336
v_mfma_f32_16x16x32_bf16 v[100:103],v[64:67],v[196:199],v[100:103]
s_waitcnt lgkmcnt(6)
buffer_load_dwordx4 v22,%[srdW],s59 offen offset:2048 sc1 lds
v_mfma_f32_16x16x32_bf16 v[96:99],v[68:71],v[200:203],v[96:99]
ds_read_b128 v[64:67],v23 offset:512
v_mfma_f32_16x16x32_bf16 v[100:103],v[68:71],v[204:207],v[100:103]
s_waitcnt lgkmcnt(6)
buffer_load_dwordx4 v[180:183],%[vL16],%[srdW],s45 offen sc1
v_mfma_f32_16x16x32_bf16 v[96:99],v[72:75],v[208:211],v[96:99]
ds_read_b128 v[68:71],v23 offset:2560
v_mfma_f32_16x16x32_bf16 v[100:103],v[72:75],v[212:215],v[100:103]
s_waitcnt lgkmcnt(6)
buffer_load_dwordx4 v22,%[srdW],s59 offen offset:3072 sc1 lds
v_mfma_f32_16x16x32_bf16 v[96:99],v[76:79],v[216:219],v[96:99]
s_add_u32 m0,s64,s41
ds_read_b128 v[72:75],v23 offset:4608
v_mfma_f32_16x16x32_bf16 v[100:103],v[76:79],v[220:223],v[100:103]
s_waitcnt lgkmcnt(6)
s_add_u32 s67,s45,0x1000
buffer_load_dwordx4 v[184:187],%[vL16],%[srdW],s67 offen sc1
v_mfma_f32_16x16x32_bf16 v[96:99],v[80:83],v[224:227],v[96:99]
s_barrier
ds_read_b128 v[76:79],v23 offset:6656
v_add_u32 v25,s60,v4
v_mfma_f32_16x16x32_bf16 v[100:103],v[80:83],v[228:231],v[100:103]
s_waitcnt lgkmcnt(6)
buffer_load_dwordx4 %[vL16],%[srdW],s65 offen offset:0 sc1 lds
v_mfma_f32_16x16x32_bf16 v[96:99],v[84:87],v[232:235],v[96:99]
ds_read_b128 v[80:83],v23 offset:8704
v_mfma_f32_16x16x32_bf16 v[100:103],v[84:87],v[236:239],v[100:103]
s_waitcnt lgkmcnt(6)
s_add_u32 s67,s45,0x2000
buffer_load_dwordx4 v[188:191],%[vL16],%[srdW],s67 offen sc1
v_mfma_f32_16x16x32_bf16 v[96:99],v[88:91],v[240:243],v[96:99]
ds_read_b128 v[84:87],v23 offset:10752
v_add_u32 v26,s61,v4
v_mfma_f32_16x16x32_bf16 v[100:103],v[88:91],v[244:247],v[100:103]
s_waitcnt lgkmcnt(6)
buffer_load_dwordx4 %[vL16],%[srdW],s65 offen offset:1024 sc1 lds
v_mfma_f32_16x16x32_bf16 v[96:99],v[92:95],v[248:251],v[96:99]
ds_read_b128 v[88:91],v23 offset:12800
v_mfma_f32_16x16x32_bf16 v[100:103],v[92:95],v[252:255],v[100:103]
s_waitcnt lgkmcnt(6)
v_mfma_f32_16x16x32_bf16 v[104:107],v[64:67],v[192:195],v[104:107]
ds_read_b128 v[92:95],v23 offset:14848
v_mfma_f32_16x16x32_bf16 v[108:111],v[64:67],v[196:199],v[108:111]
s_waitcnt lgkmcnt(6)
v_mfma_f32_16x16x32_bf16 v[104:107],v[68:71],v[200:203],v[104:107]
ds_read_b64_tr_b16 v[64:65],v25 offset:0
ds_read_b64_tr_b16 v[66:67],v25 offset:512
v_mfma_f32_16x16x32_bf16 v[108:111],v[68:71],v[204:207],v[108:111]
s_waitcnt lgkmcnt(7)
v_exp_f32 v32,v96
v_mfma_f32_16x16x32_bf16 v[104:107],v[72:75],v[208:211],v[104:107]
ds_read_b64_tr_b16 v[68:69],v25 offset:1024
v_mfma_f32_16x16x32_bf16 v[108:111],v[72:75],v[212:215],v[108:111]
s_waitcnt lgkmcnt(7)
ds_read_b64_tr_b16 v[70:71],v25 offset:1536
v_mfma_f32_16x16x32_bf16 v[104:107],v[76:79],v[216:219],v[104:107]
ds_read_b64_tr_b16 v[72:73],v25 offset:2048
ds_read_b64_tr_b16 v[74:75],v25 offset:2560
v_mfma_f32_16x16x32_bf16 v[108:111],v[76:79],v[220:223],v[108:111]
s_waitcnt lgkmcnt(9)
v_exp_f32 v33,v97
v_mfma_f32_16x16x32_bf16 v[104:107],v[80:83],v[224:227],v[104:107]
ds_read_b64_tr_b16 v[76:77],v25 offset:3072
v_mfma_f32_16x16x32_bf16 v[108:111],v[80:83],v[228:231],v[108:111]
s_waitcnt lgkmcnt(9)
ds_read_b64_tr_b16 v[78:79],v25 offset:3584
v_mfma_f32_16x16x32_bf16 v[104:107],v[84:87],v[232:235],v[104:107]
ds_read_b64_tr_b16 v[80:81],v25 offset:4096
ds_read_b64_tr_b16 v[82:83],v25 offset:4608
v_mfma_f32_16x16x32_bf16 v[108:111],v[84:87],v[236:239],v[108:111]
s_waitcnt lgkmcnt(11)
v_exp_f32 v34,v98
v_mfma_f32_16x16x32_bf16 v[104:107],v[88:91],v[240:243],v[104:107]
ds_read_b64_tr_b16 v[84:85],v25 offset:5120
v_mfma_f32_16x16x32_bf16 v[108:111],v[88:91],v[244:247],v[108:111]
s_waitcnt lgkmcnt(11)
ds_read_b64_tr_b16 v[86:87],v25 offset:5632
v_mfma_f32_16x16x32_bf16 v[104:107],v[92:95],v[248:251],v[104:107]
ds_read_b64_tr_b16 v[88:89],v25 offset:6144
ds_read_b64_tr_b16 v[90:91],v25 offset:6656
v_mfma_f32_16x16x32_bf16 v[108:111],v[92:95],v[252:255],v[108:111]
s_waitcnt lgkmcnt(12)
v_exp_f32 v35,v99
v_exp_f32 v36,v100
v_exp_f32 v37,v101
buffer_load_dwordx4 v[208:211],%[vL16],%[srdW],s44 offen offset:0 sc1
v_exp_f32 v38,v102
v_exp_f32 v39,v103
v_max3_f32 v27,v96,v97,v98
buffer_load_dwordx4 v[212:215],%[vL16],%[srdW],s44 offen offset:1024 sc1
v_max3_f32 v27,v27,v99,v100
v_max3_f32 v27,v27,v101,v102
v_max_f32 v27,v27,v103
buffer_load_dwordx4 v[216:219],%[vL16],%[srdW],s44 offen offset:2048 sc1
v_exp_f32 v40,v104
v_exp_f32 v41,v105
v_exp_f32 v42,v106
buffer_load_dwordx4 v[220:223],%[vL16],%[srdW],s44 offen offset:3072 sc1
v_exp_f32 v43,v107
v_exp_f32 v44,v108
v_exp_f32 v45,v109
s_add_u32 s67,s44,0x1000
buffer_load_dwordx4 v[224:227],%[vL16],%[srdW],s67 offen sc1
v_exp_f32 v46,v110
v_exp_f32 v47,v111
v_max3_f32 v27,v27,v104,v105
v_max3_f32 v27,v27,v106,v107
v_max3_f32 v27,v27,v108,v109
v_max3_f32 v27,v27,v110,v111
v_cmp_lt_f32 vcc,s68,v27
v_cvt_pk_bf16_f32 v56,v32,v33
v_cvt_pk_bf16_f32 v57,v34,v35
v_cvt_pk_bf16_f32 v60,v36,v37
v_cvt_pk_bf16_f32 v61,v38,v39
v_cvt_pk_bf16_f32 v58,v40,v41
v_cvt_pk_bf16_f32 v59,v42,v43
v_cvt_pk_bf16_f32 v62,v44,v45
v_cvt_pk_bf16_f32 v63,v46,v47
s_cbranch_vccnz RARETA_%=
BACKTA_%=:
s_nop 1
v_mfma_f32_16x16x32_bf16 a[0:3],v[64:67],v[56:59],a[0:3]
ds_read_b64_tr_b16 v[92:93],v25 offset:7168
v_add_f32 v30,v30,v32
v_mfma_f32_16x16x32_bf16 a[4:7],v[64:67],v[60:63],a[4:7]
s_waitcnt lgkmcnt(11)
ds_read_b64_tr_b16 v[94:95],v25 offset:7680
v_add_f32 v31,v31,v36
v_mfma_f32_16x16x32_bf16 a[12:15],v[68:71],v[60:63],a[12:15]
ds_read_b64_tr_b16 v[64:65],v25 offset:8192
v_add_f32 v30,v30,v40
v_mfma_f32_16x16x32_bf16 a[8:11],v[68:71],v[56:59],a[8:11]
s_waitcnt lgkmcnt(11)
ds_read_b64_tr_b16 v[66:67],v25 offset:8704
v_add_f32 v31,v31,v44
v_mfma_f32_16x16x32_bf16 a[16:19],v[72:75],v[56:59],a[16:19]
ds_read_b64_tr_b16 v[68:69],v25 offset:9216
v_add_f32 v30,v30,v33
v_mfma_f32_16x16x32_bf16 a[20:23],v[72:75],v[60:63],a[20:23]
s_waitcnt lgkmcnt(11)
ds_read_b64_tr_b16 v[70:71],v25 offset:9728
v_add_f32 v31,v31,v37
v_mfma_f32_16x16x32_bf16 a[28:31],v[76:79],v[60:63],a[28:31]
ds_read_b64_tr_b16 v[72:73],v25 offset:10240
v_add_f32 v30,v30,v41
v_mfma_f32_16x16x32_bf16 a[24:27],v[76:79],v[56:59],a[24:27]
s_waitcnt lgkmcnt(11)
ds_read_b64_tr_b16 v[74:75],v25 offset:10752
v_add_f32 v31,v31,v45
v_mfma_f32_16x16x32_bf16 a[32:35],v[80:83],v[56:59],a[32:35]
ds_read_b64_tr_b16 v[76:77],v25 offset:11264
v_add_f32 v30,v30,v34
v_mfma_f32_16x16x32_bf16 a[36:39],v[80:83],v[60:63],a[36:39]
s_waitcnt lgkmcnt(11)
ds_read_b64_tr_b16 v[78:79],v25 offset:11776
v_add_f32 v31,v31,v38
v_mfma_f32_16x16x32_bf16 a[44:47],v[84:87],v[60:63],a[44:47]
ds_read_b64_tr_b16 v[80:81],v25 offset:12288
v_add_f32 v30,v30,v42
v_mfma_f32_16x16x32_bf16 a[40:43],v[84:87],v[56:59],a[40:43]
s_waitcnt lgkmcnt(11)
ds_read_b64_tr_b16 v[82:83],v25 offset:12800
v_add_f32 v31,v31,v46
v_mfma_f32_16x16x32_bf16 a[48:51],v[88:91],v[56:59],a[48:51]
ds_read_b64_tr_b16 v[84:85],v25 offset:13312
v_add_f32 v30,v30,v35
v_mfma_f32_16x16x32_bf16 a[52:55],v[88:91],v[60:63],a[52:55]
s_waitcnt lgkmcnt(11)
ds_read_b64_tr_b16 v[86:87],v25 offset:13824
v_add_f32 v31,v31,v39
v_mfma_f32_16x16x32_bf16 a[60:63],v[92:95],v[60:63],a[60:63]
ds_read_b64_tr_b16 v[88:89],v25 offset:14336
v_add_f32 v30,v30,v43
v_mfma_f32_16x16x32_bf16 a[56:59],v[92:95],v[56:59],a[56:59]
s_waitcnt lgkmcnt(11)
ds_read_b64_tr_b16 v[90:91],v25 offset:14848
v_add_f32 v31,v31,v47
v_mfma_f32_16x16x32_bf16 a[64:67],v[64:67],v[56:59],a[64:67]
ds_read_b64_tr_b16 v[92:93],v25 offset:15360
v_mov_b32 v16,v30
v_mfma_f32_16x16x32_bf16 a[68:71],v[64:67],v[60:63],a[68:71]
s_waitcnt lgkmcnt(11)
ds_read_b64_tr_b16 v[94:95],v25 offset:15872
v_mov_b32 v17,v31
v_mfma_f32_16x16x32_bf16 a[76:79],v[68:71],v[60:63],a[76:79]
ds_read_b64_tr_b16 v[64:65],v26 offset:0
v_permlane16_swap_b32 v30,v16
v_mfma_f32_16x16x32_bf16 a[72:75],v[68:71],v[56:59],a[72:75]
s_waitcnt lgkmcnt(11)
ds_read_b64_tr_b16 v[66:67],v26 offset:512
v_permlane16_swap_b32 v31,v17
v_mfma_f32_16x16x32_bf16 a[80:83],v[72:75],v[56:59],a[80:83]
ds_read_b64_tr_b16 v[68:69],v26 offset:1024
v_add_f32 v30,v30,v16
v_mfma_f32_16x16x32_bf16 a[84:87],v[72:75],v[60:63],a[84:87]
s_waitcnt lgkmcnt(11)
ds_read_b64_tr_b16 v[70:71],v26 offset:1536
v_add_f32 v31,v31,v17
v_mfma_f32_16x16x32_bf16 a[92:95],v[76:79],v[60:63],a[92:95]
ds_read_b64_tr_b16 v[72:73],v26 offset:2048
v_mov_b32 v16,v30
v_mfma_f32_16x16x32_bf16 a[88:91],v[76:79],v[56:59],a[88:91]
s_waitcnt lgkmcnt(11)
ds_read_b64_tr_b16 v[74:75],v26 offset:2560
v_mov_b32 v17,v31
v_mfma_f32_16x16x32_bf16 a[96:99],v[80:83],v[56:59],a[96:99]
ds_read_b64_tr_b16 v[76:77],v26 offset:3072
v_permlane32_swap_b32 v30,v16
v_mfma_f32_16x16x32_bf16 a[100:103],v[80:83],v[60:63],a[100:103]
s_waitcnt lgkmcnt(11)
ds_read_b64_tr_b16 v[78:79],v26 offset:3584
v_permlane32_swap_b32 v31,v17
v_mfma_f32_16x16x32_bf16 a[108:111],v[84:87],v[60:63],a[108:111]
ds_read_b64_tr_b16 v[80:81],v26 offset:4096
v_add_f32 v30,v30,v16
v_mfma_f32_16x16x32_bf16 a[104:107],v[84:87],v[56:59],a[104:107]
s_waitcnt lgkmcnt(11)
ds_read_b64_tr_b16 v[82:83],v26 offset:4608
v_add_f32 v31,v31,v17
v_mfma_f32_16x16x32_bf16 a[112:115],v[88:91],v[56:59],a[112:115]
ds_read_b64_tr_b16 v[84:85],v26 offset:5120
v_add_u32 v18,s52,%[vL16]
v_mfma_f32_16x16x32_bf16 a[116:119],v[88:91],v[60:63],a[116:119]
s_waitcnt lgkmcnt(11)
ds_read_b64_tr_b16 v[86:87],v26 offset:5632
v_mfma_f32_16x16x32_bf16 a[124:127],v[92:95],v[60:63],a[124:127]
ds_read_b64_tr_b16 v[88:89],v26 offset:6144
ds_read_b64_tr_b16 v[90:91],v26 offset:6656
v_mfma_f32_16x16x32_bf16 a[120:123],v[92:95],v[56:59],a[120:123]
s_waitcnt lgkmcnt(12)
s_waitcnt vmcnt(5)
ds_write_b128 v18,v[28:31]
s_waitcnt lgkmcnt(0)
s_barrier
v_mfma_f32_16x16x32_bf16 a[128:131],v[64:67],v[56:59],a[128:131]
ds_read_b64_tr_b16 v[92:93],v26 offset:7168
v_add_f32 v180,v180,v182
v_mfma_f32_16x16x32_bf16 a[132:135],v[64:67],v[60:63],a[132:135]
ds_read_b64_tr_b16 v[94:95],v26 offset:7680
v_add_f32 v184,v184,v186
v_mfma_f32_16x16x32_bf16 a[140:143],v[68:71],v[60:63],a[140:143]
ds_read_b64_tr_b16 v[64:65],v26 offset:8192
v_add_f32 v188,v188,v190
v_mfma_f32_16x16x32_bf16 a[136:139],v[68:71],v[56:59],a[136:139]
ds_read_b64_tr_b16 v[66:67],v26 offset:8704
v_add_f32 v181,v181,v183
v_mfma_f32_16x16x32_bf16 a[144:147],v[72:75],v[56:59],a[144:147]
ds_read_b64_tr_b16 v[68:69],v26 offset:9216
v_add_f32 v185,v185,v187
v_mfma_f32_16x16x32_bf16 a[148:151],v[72:75],v[60:63],a[148:151]
ds_read_b64_tr_b16 v[70:71],v26 offset:9728
v_add_f32 v189,v189,v191
v_mfma_f32_16x16x32_bf16 a[156:159],v[76:79],v[60:63],a[156:159]
ds_read_b64_tr_b16 v[72:73],v26 offset:10240
v_add_f32_dpp v180,v180,v180 quad_perm:[1,0,3,2] row_mask:0xf bank_mask:0xf bound_ctrl:1
v_mfma_f32_16x16x32_bf16 a[152:155],v[76:79],v[56:59],a[152:155]
ds_read_b64_tr_b16 v[74:75],v26 offset:10752
v_add_f32_dpp v184,v184,v184 quad_perm:[1,0,3,2] row_mask:0xf bank_mask:0xf bound_ctrl:1
v_mfma_f32_16x16x32_bf16 a[160:163],v[80:83],v[56:59],a[160:163]
ds_read_b64_tr_b16 v[76:77],v26 offset:11264
v_add_f32_dpp v188,v188,v188 quad_perm:[1,0,3,2] row_mask:0xf bank_mask:0xf bound_ctrl:1
v_mfma_f32_16x16x32_bf16 a[164:167],v[80:83],v[60:63],a[164:167]
ds_read_b64_tr_b16 v[78:79],v26 offset:11776
v_add_f32_dpp v181,v181,v181 quad_perm:[1,0,3,2] row_mask:0xf bank_mask:0xf bound_ctrl:1
v_mfma_f32_16x16x32_bf16 a[172:175],v[84:87],v[60:63],a[172:175]
ds_read_b64_tr_b16 v[80:81],v26 offset:12288
v_add_f32_dpp v185,v185,v185 quad_perm:[1,0,3,2] row_mask:0xf bank_mask:0xf bound_ctrl:1
v_mfma_f32_16x16x32_bf16 a[168:171],v[84:87],v[56:59],a[168:171]
ds_read_b64_tr_b16 v[82:83],v26 offset:12800
v_add_f32_dpp v189,v189,v189 quad_perm:[1,0,3,2] row_mask:0xf bank_mask:0xf bound_ctrl:1
v_mfma_f32_16x16x32_bf16 a[176:179],v[88:91],v[56:59],a[176:179]
ds_read_b64_tr_b16 v[84:85],v26 offset:13312
v_mul_f32 v180,0x3b000000,v180
v_mfma_f32_16x16x32_bf16 a[180:183],v[88:91],v[60:63],a[180:183]
s_waitcnt lgkmcnt(11)
ds_read_b64_tr_b16 v[86:87],v26 offset:13824
v_mul_f32 v184,0x3b000000,v184
v_mfma_f32_16x16x32_bf16 a[188:191],v[92:95],v[60:63],a[188:191]
ds_read_b64_tr_b16 v[88:89],v26 offset:14336
v_mul_f32 v188,0x3b000000,v188
v_mfma_f32_16x16x32_bf16 a[184:187],v[92:95],v[56:59],a[184:187]
s_waitcnt lgkmcnt(11)
ds_read_b64_tr_b16 v[90:91],v26 offset:14848
v_mul_f32_e64 v182,v180,-v180
v_mfma_f32_16x16x32_bf16 a[192:195],v[64:67],v[56:59],a[192:195]
ds_read_b64_tr_b16 v[92:93],v26 offset:15360
v_mul_f32_e64 v186,v184,-v184
v_mfma_f32_16x16x32_bf16 a[196:199],v[64:67],v[60:63],a[196:199]
s_waitcnt lgkmcnt(11)
ds_read_b64_tr_b16 v[94:95],v26 offset:15872
v_mul_f32_e64 v190,v188,-v188
v_mfma_f32_16x16x32_bf16 a[204:207],v[68:71],v[60:63],a[204:207]
v_fmac_f32 v182,0x3b000000,v181
v_mfma_f32_16x16x32_bf16 a[200:203],v[68:71],v[56:59],a[200:203]
s_waitcnt lgkmcnt(10)
v_fmac_f32 v186,0x3b000000,v185
v_mfma_f32_16x16x32_bf16 a[208:211],v[72:75],v[56:59],a[208:211]
v_fmac_f32 v190,0x3b000000,v189
v_mfma_f32_16x16x32_bf16 a[212:215],v[72:75],v[60:63],a[212:215]
s_waitcnt lgkmcnt(8)
v_max_f32 v182,0,v182
v_mfma_f32_16x16x32_bf16 a[220:223],v[76:79],v[60:63],a[220:223]
v_max_f32 v186,0,v186
v_mfma_f32_16x16x32_bf16 a[216:219],v[76:79],v[56:59],a[216:219]
s_waitcnt lgkmcnt(6)
v_max_f32 v190,0,v190
v_mfma_f32_16x16x32_bf16 a[224:227],v[80:83],v[56:59],a[224:227]
v_add_f32 v182,0x3727c5ac,v182
v_mfma_f32_16x16x32_bf16 a[228:231],v[80:83],v[60:63],a[228:231]
s_waitcnt lgkmcnt(4)
v_add_f32 v186,0x3727c5ac,v186
v_mfma_f32_16x16x32_bf16 a[236:239],v[84:87],v[60:63],a[236:239]
v_add_f32 v190,0x3727c5ac,v190
v_mfma_f32_16x16x32_bf16 a[232:235],v[84:87],v[56:59],a[232:235]
s_waitcnt lgkmcnt(2)
v_rsq_f32 v240,v182
v_mfma_f32_16x16x32_bf16 a[240:243],v[88:91],v[56:59],a[240:243]
v_rsq_f32 v241,v186
v_mfma_f32_16x16x32_bf16 a[244:247],v[88:91],v[60:63],a[244:247]
s_waitcnt lgkmcnt(0)
v_rsq_f32 v242,v190
v_mfma_f32_16x16x32_bf16 a[252:255],v[92:95],v[60:63],a[252:255]
v_mul_f32_e64 v181,v240,-v180
v_mfma_f32_16x16x32_bf16 a[248:251],v[92:95],v[56:59],a[248:251]
v_mul_f32_e64 v185,v241,-v184
v_mul_f32_e64 v189,v242,-v188
v_xor_b32 v97,16,v5
v_xor_b32 v98,32,v5
v_xor_b32 v99,48,v5
v_xor_b32 v101,16,v6
v_xor_b32 v102,32,v6
v_xor_b32 v103,48,v6
v_xor_b32 v105,16,v7
v_xor_b32 v106,32,v7
v_xor_b32 v107,48,v7
s_branch EPI_%=
EPI_%=:
s_bitcmp0_b32 s47,8
s_cbranch_scc1 NOX_%=
s_barrier
s_barrier
NOX_%=:
v_add_u32 v108,s53,%[vL16]
ds_read_b128 v[248:251],v108
ds_read_b128 v[128:131],v5
ds_read_b128 v[132:135],v97
ds_read_b128 v[136:139],v98
ds_read_b128 v[140:143],v99
ds_read_b128 v[144:147],v6
ds_read_b128 v[148:151],v101
)"
R"(ds_read_b128 v[152:155],v102
ds_read_b128 v[156:159],v103
ds_read_b128 v[160:163],v7
ds_read_b128 v[164:167],v105
ds_read_b128 v[168:171],v106
ds_read_b128 v[172:175],v107
v_and_b32 v109,16,%[vL16]
v_cmp_eq_u32 vcc,0,v109
v_add_u32 v110,s48,%[vL16]
v_add_u32 v111,s49,%[vL16]
s_bitcmp1_b32 s47,9
s_cselect_b32 s67,-1,0
v_cndmask_b32 v240,v181,v240,vcc
v_and_b32 v240,s67,v240
s_bitcmp1_b32 s47,10
s_cselect_b32 s67,-1,0
v_cndmask_b32 v241,v185,v241,vcc
v_and_b32 v241,s67,v241
s_bitcmp1_b32 s47,11
s_cselect_b32 s67,-1,0
v_cndmask_b32 v242,v189,v242,vcc
v_and_b32 v242,s67,v242
s_waitcnt lgkmcnt(12)
ds_read_b128 v[176:179],v98 offset:1024
ds_read_b128 v[180:183],v99 offset:1024
ds_read_b128 v[184:187],v6 offset:1024
ds_read_b128 v[188:191],v101 offset:1024
ds_read_b128 v[192:195],v102 offset:1024
ds_read_b128 v[196:199],v103 offset:1024
ds_read_b128 v[200:203],v7 offset:1024
ds_read_b128 v[204:207],v105 offset:1024
s_waitcnt vmcnt(0)
v_max_f32 v252,v28,v248
v_sub_f32 v16,v28,v252
v_sub_f32 v17,v248,v252
v_exp_f32 v16,v16
v_exp_f32 v17,v17
s_nop 0
v_mul_f32 v253,v17,v250
v_fmac_f32 v253,v16,v30
v_rcp_f32 v17,v253
s_nop 0
v_mul_f32 v244,v16,v17
buffer_store_dwordx2 v[252:253],v11,%[srdW],0 offen offset:0 sc1
v_max_f32 v254,v29,v249
v_sub_f32 v16,v29,v254
v_sub_f32 v17,v249,v254
v_exp_f32 v16,v16
v_exp_f32 v17,v17
s_nop 0
v_mul_f32 v255,v17,v251
v_fmac_f32 v255,v16,v31
v_rcp_f32 v17,v255
s_nop 0
v_mul_f32 v245,v16,v17
buffer_store_dwordx2 v[254:255],v11,%[srdW],0 offen offset:256 sc1
ds_write_b32 v10,v240
ds_write_b32 v10,v241 offset:544
ds_write_b32 v10,v242 offset:1088
s_waitcnt lgkmcnt(3)
v_accvgpr_read_b32 v32,a128
v_accvgpr_read_b32 v33,a129
v_accvgpr_read_b32 v34,a130
v_accvgpr_read_b32 v35,a131
v_accvgpr_read_b32 v36,a136
v_accvgpr_read_b32 v37,a137
v_accvgpr_read_b32 v38,a138
v_accvgpr_read_b32 v39,a139
v_pk_mul_f32 v[32:33],v[32:33],v[244:245] op_sel:[0,0] op_sel_hi:[1,0]
v_pk_mul_f32 v[34:35],v[34:35],v[244:245] op_sel:[0,0] op_sel_hi:[1,0]
v_pk_mul_f32 v[36:37],v[36:37],v[244:245] op_sel:[0,0] op_sel_hi:[1,0]
v_pk_mul_f32 v[38:39],v[38:39],v[244:245] op_sel:[0,0] op_sel_hi:[1,0]
v_cvt_pk_f16_f32 v40,v32,v33
v_cvt_pk_f16_f32 v41,v34,v35
v_cvt_pk_f16_f32 v42,v36,v37
v_cvt_pk_f16_f32 v43,v38,v39
ds_write_b128 v110,v[40:43] offset:0
v_accvgpr_read_b32 v44,a132
v_accvgpr_read_b32 v45,a133
v_accvgpr_read_b32 v46,a134
v_accvgpr_read_b32 v47,a135
v_accvgpr_read_b32 v48,a140
v_accvgpr_read_b32 v49,a141
v_accvgpr_read_b32 v50,a142
v_accvgpr_read_b32 v51,a143
v_pk_mul_f32 v[44:45],v[44:45],v[244:245] op_sel:[0,1] op_sel_hi:[1,1]
v_pk_mul_f32 v[46:47],v[46:47],v[244:245] op_sel:[0,1] op_sel_hi:[1,1]
v_pk_mul_f32 v[48:49],v[48:49],v[244:245] op_sel:[0,1] op_sel_hi:[1,1]
v_pk_mul_f32 v[50:51],v[50:51],v[244:245] op_sel:[0,1] op_sel_hi:[1,1]
v_cvt_pk_f16_f32 v52,v44,v45
v_cvt_pk_f16_f32 v53,v46,v47
v_cvt_pk_f16_f32 v54,v48,v49
v_cvt_pk_f16_f32 v55,v50,v51
ds_write_b128 v110,v[52:55] offset:1024
v_accvgpr_read_b32 v56,a144
v_accvgpr_read_b32 v57,a145
v_accvgpr_read_b32 v58,a146
v_accvgpr_read_b32 v59,a147
v_accvgpr_read_b32 v60,a152
v_accvgpr_read_b32 v61,a153
v_accvgpr_read_b32 v62,a154
v_accvgpr_read_b32 v63,a155
v_pk_mul_f32 v[56:57],v[56:57],v[244:245] op_sel:[0,0] op_sel_hi:[1,0]
v_pk_mul_f32 v[58:59],v[58:59],v[244:245] op_sel:[0,0] op_sel_hi:[1,0]
v_pk_mul_f32 v[60:61],v[60:61],v[244:245] op_sel:[0,0] op_sel_hi:[1,0]
v_pk_mul_f32 v[62:63],v[62:63],v[244:245] op_sel:[0,0] op_sel_hi:[1,0]
v_cvt_pk_f16_f32 v64,v56,v57
v_cvt_pk_f16_f32 v65,v58,v59
v_cvt_pk_f16_f32 v66,v60,v61
v_cvt_pk_f16_f32 v67,v62,v63
ds_write_b128 v110,v[64:67] offset:2048
v_accvgpr_read_b32 v68,a148
v_accvgpr_read_b32 v69,a149
v_accvgpr_read_b32 v70,a150
v_accvgpr_read_b32 v71,a151
v_accvgpr_read_b32 v72,a156
v_accvgpr_read_b32 v73,a157
v_accvgpr_read_b32 v74,a158
v_accvgpr_read_b32 v75,a159
v_pk_mul_f32 v[68:69],v[68:69],v[244:245] op_sel:[0,1] op_sel_hi:[1,1]
v_pk_mul_f32 v[70:71],v[70:71],v[244:245] op_sel:[0,1] op_sel_hi:[1,1]
v_pk_mul_f32 v[72:73],v[72:73],v[244:245] op_sel:[0,1] op_sel_hi:[1,1]
v_pk_mul_f32 v[74:75],v[74:75],v[244:245] op_sel:[0,1] op_sel_hi:[1,1]
v_cvt_pk_f16_f32 v76,v68,v69
v_cvt_pk_f16_f32 v77,v70,v71
v_cvt_pk_f16_f32 v78,v72,v73
v_cvt_pk_f16_f32 v79,v74,v75
ds_write_b128 v110,v[76:79] offset:3072
v_accvgpr_read_b32 v32,a160
v_accvgpr_read_b32 v33,a161
v_accvgpr_read_b32 v34,a162
v_accvgpr_read_b32 v35,a163
v_accvgpr_read_b32 v36,a168
v_accvgpr_read_b32 v37,a169
v_accvgpr_read_b32 v38,a170
v_accvgpr_read_b32 v39,a171
v_pk_mul_f32 v[32:33],v[32:33],v[244:245] op_sel:[0,0] op_sel_hi:[1,0]
v_pk_mul_f32 v[34:35],v[34:35],v[244:245] op_sel:[0,0] op_sel_hi:[1,0]
v_pk_mul_f32 v[36:37],v[36:37],v[244:245] op_sel:[0,0] op_sel_hi:[1,0]
v_pk_mul_f32 v[38:39],v[38:39],v[244:245] op_sel:[0,0] op_sel_hi:[1,0]
v_cvt_pk_f16_f32 v40,v32,v33
v_cvt_pk_f16_f32 v41,v34,v35
v_cvt_pk_f16_f32 v42,v36,v37
v_cvt_pk_f16_f32 v43,v38,v39
ds_write_b128 v110,v[40:43] offset:4096
v_accvgpr_read_b32 v44,a164
v_accvgpr_read_b32 v45,a165
v_accvgpr_read_b32 v46,a166
v_accvgpr_read_b32 v47,a167
v_accvgpr_read_b32 v48,a172
v_accvgpr_read_b32 v49,a173
v_accvgpr_read_b32 v50,a174
v_accvgpr_read_b32 v51,a175
v_pk_mul_f32 v[44:45],v[44:45],v[244:245] op_sel:[0,1] op_sel_hi:[1,1]
v_pk_mul_f32 v[46:47],v[46:47],v[244:245] op_sel:[0,1] op_sel_hi:[1,1]
v_pk_mul_f32 v[48:49],v[48:49],v[244:245] op_sel:[0,1] op_sel_hi:[1,1]
v_pk_mul_f32 v[50:51],v[50:51],v[244:245] op_sel:[0,1] op_sel_hi:[1,1]
v_cvt_pk_f16_f32 v52,v44,v45
v_cvt_pk_f16_f32 v53,v46,v47
v_cvt_pk_f16_f32 v54,v48,v49
v_cvt_pk_f16_f32 v55,v50,v51
ds_write_b128 v110,v[52:55] offset:5120
v_accvgpr_read_b32 v56,a176
v_accvgpr_read_b32 v57,a177
v_accvgpr_read_b32 v58,a178
v_accvgpr_read_b32 v59,a179
v_accvgpr_read_b32 v60,a184
v_accvgpr_read_b32 v61,a185
v_accvgpr_read_b32 v62,a186
v_accvgpr_read_b32 v63,a187
v_pk_mul_f32 v[56:57],v[56:57],v[244:245] op_sel:[0,0] op_sel_hi:[1,0]
v_pk_mul_f32 v[58:59],v[58:59],v[244:245] op_sel:[0,0] op_sel_hi:[1,0]
v_pk_mul_f32 v[60:61],v[60:61],v[244:245] op_sel:[0,0] op_sel_hi:[1,0]
v_pk_mul_f32 v[62:63],v[62:63],v[244:245] op_sel:[0,0] op_sel_hi:[1,0]
v_cvt_pk_f16_f32 v64,v56,v57
v_cvt_pk_f16_f32 v65,v58,v59
v_cvt_pk_f16_f32 v66,v60,v61
v_cvt_pk_f16_f32 v67,v62,v63
ds_write_b128 v110,v[64:67] offset:6144
v_accvgpr_read_b32 v68,a180
v_accvgpr_read_b32 v69,a181
v_accvgpr_read_b32 v70,a182
v_accvgpr_read_b32 v71,a183
v_accvgpr_read_b32 v72,a188
v_accvgpr_read_b32 v73,a189
v_accvgpr_read_b32 v74,a190
v_accvgpr_read_b32 v75,a191
v_pk_mul_f32 v[68:69],v[68:69],v[244:245] op_sel:[0,1] op_sel_hi:[1,1]
v_pk_mul_f32 v[70:71],v[70:71],v[244:245] op_sel:[0,1] op_sel_hi:[1,1]
v_pk_mul_f32 v[72:73],v[72:73],v[244:245] op_sel:[0,1] op_sel_hi:[1,1]
v_pk_mul_f32 v[74:75],v[74:75],v[244:245] op_sel:[0,1] op_sel_hi:[1,1]
v_cvt_pk_f16_f32 v76,v68,v69
v_cvt_pk_f16_f32 v77,v70,v71
v_cvt_pk_f16_f32 v78,v72,v73
v_cvt_pk_f16_f32 v79,v74,v75
ds_write_b128 v110,v[76:79] offset:7168
v_accvgpr_read_b32 v32,a192
v_accvgpr_read_b32 v33,a193
v_accvgpr_read_b32 v34,a194
v_accvgpr_read_b32 v35,a195
v_accvgpr_read_b32 v36,a200
v_accvgpr_read_b32 v37,a201
v_accvgpr_read_b32 v38,a202
v_accvgpr_read_b32 v39,a203
v_pk_mul_f32 v[32:33],v[32:33],v[244:245] op_sel:[0,0] op_sel_hi:[1,0]
v_pk_mul_f32 v[34:35],v[34:35],v[244:245] op_sel:[0,0] op_sel_hi:[1,0]
v_pk_mul_f32 v[36:37],v[36:37],v[244:245] op_sel:[0,0] op_sel_hi:[1,0]
v_pk_mul_f32 v[38:39],v[38:39],v[244:245] op_sel:[0,0] op_sel_hi:[1,0]
v_cvt_pk_f16_f32 v40,v32,v33
v_cvt_pk_f16_f32 v41,v34,v35
v_cvt_pk_f16_f32 v42,v36,v37
v_cvt_pk_f16_f32 v43,v38,v39
ds_write_b128 v111,v[40:43] offset:0
v_accvgpr_read_b32 v44,a196
v_accvgpr_read_b32 v45,a197
v_accvgpr_read_b32 v46,a198
v_accvgpr_read_b32 v47,a199
v_accvgpr_read_b32 v48,a204
v_accvgpr_read_b32 v49,a205
v_accvgpr_read_b32 v50,a206
v_accvgpr_read_b32 v51,a207
v_pk_mul_f32 v[44:45],v[44:45],v[244:245] op_sel:[0,1] op_sel_hi:[1,1]
v_pk_mul_f32 v[46:47],v[46:47],v[244:245] op_sel:[0,1] op_sel_hi:[1,1]
v_pk_mul_f32 v[48:49],v[48:49],v[244:245] op_sel:[0,1] op_sel_hi:[1,1]
v_pk_mul_f32 v[50:51],v[50:51],v[244:245] op_sel:[0,1] op_sel_hi:[1,1]
v_cvt_pk_f16_f32 v52,v44,v45
v_cvt_pk_f16_f32 v53,v46,v47
v_cvt_pk_f16_f32 v54,v48,v49
v_cvt_pk_f16_f32 v55,v50,v51
ds_write_b128 v111,v[52:55] offset:1024
v_accvgpr_read_b32 v56,a208
v_accvgpr_read_b32 v57,a209
v_accvgpr_read_b32 v58,a210
v_accvgpr_read_b32 v59,a211
v_accvgpr_read_b32 v60,a216
v_accvgpr_read_b32 v61,a217
v_accvgpr_read_b32 v62,a218
v_accvgpr_read_b32 v63,a219
v_pk_mul_f32 v[56:57],v[56:57],v[244:245] op_sel:[0,0] op_sel_hi:[1,0]
v_pk_mul_f32 v[58:59],v[58:59],v[244:245] op_sel:[0,0] op_sel_hi:[1,0]
v_pk_mul_f32 v[60:61],v[60:61],v[244:245] op_sel:[0,0] op_sel_hi:[1,0]
v_pk_mul_f32 v[62:63],v[62:63],v[244:245] op_sel:[0,0] op_sel_hi:[1,0]
v_cvt_pk_f16_f32 v64,v56,v57
v_cvt_pk_f16_f32 v65,v58,v59
v_cvt_pk_f16_f32 v66,v60,v61
v_cvt_pk_f16_f32 v67,v62,v63
ds_write_b128 v111,v[64:67] offset:2048
v_accvgpr_read_b32 v68,a212
v_accvgpr_read_b32 v69,a213
v_accvgpr_read_b32 v70,a214
v_accvgpr_read_b32 v71,a215
v_accvgpr_read_b32 v72,a220
v_accvgpr_read_b32 v73,a221
v_accvgpr_read_b32 v74,a222
v_accvgpr_read_b32 v75,a223
v_pk_mul_f32 v[68:69],v[68:69],v[244:245] op_sel:[0,1] op_sel_hi:[1,1]
v_pk_mul_f32 v[70:71],v[70:71],v[244:245] op_sel:[0,1] op_sel_hi:[1,1]
v_pk_mul_f32 v[72:73],v[72:73],v[244:245] op_sel:[0,1] op_sel_hi:[1,1]
v_pk_mul_f32 v[74:75],v[74:75],v[244:245] op_sel:[0,1] op_sel_hi:[1,1]
v_cvt_pk_f16_f32 v76,v68,v69
v_cvt_pk_f16_f32 v77,v70,v71
v_cvt_pk_f16_f32 v78,v72,v73
v_cvt_pk_f16_f32 v79,v74,v75
ds_write_b128 v111,v[76:79] offset:3072
v_accvgpr_read_b32 v32,a224
v_accvgpr_read_b32 v33,a225
v_accvgpr_read_b32 v34,a226
v_accvgpr_read_b32 v35,a227
v_accvgpr_read_b32 v36,a232
v_accvgpr_read_b32 v37,a233
v_accvgpr_read_b32 v38,a234
v_accvgpr_read_b32 v39,a235
v_pk_mul_f32 v[32:33],v[32:33],v[244:245] op_sel:[0,0] op_sel_hi:[1,0]
v_pk_mul_f32 v[34:35],v[34:35],v[244:245] op_sel:[0,0] op_sel_hi:[1,0]
v_pk_mul_f32 v[36:37],v[36:37],v[244:245] op_sel:[0,0] op_sel_hi:[1,0]
v_pk_mul_f32 v[38:39],v[38:39],v[244:245] op_sel:[0,0] op_sel_hi:[1,0]
v_cvt_pk_f16_f32 v40,v32,v33
v_cvt_pk_f16_f32 v41,v34,v35
v_cvt_pk_f16_f32 v42,v36,v37
v_cvt_pk_f16_f32 v43,v38,v39
ds_write_b128 v111,v[40:43] offset:4096
v_accvgpr_read_b32 v44,a228
v_accvgpr_read_b32 v45,a229
v_accvgpr_read_b32 v46,a230
v_accvgpr_read_b32 v47,a231
v_accvgpr_read_b32 v48,a236
v_accvgpr_read_b32 v49,a237
v_accvgpr_read_b32 v50,a238
v_accvgpr_read_b32 v51,a239
v_pk_mul_f32 v[44:45],v[44:45],v[244:245] op_sel:[0,1] op_sel_hi:[1,1]
v_pk_mul_f32 v[46:47],v[46:47],v[244:245] op_sel:[0,1] op_sel_hi:[1,1]
v_pk_mul_f32 v[48:49],v[48:49],v[244:245] op_sel:[0,1] op_sel_hi:[1,1]
v_pk_mul_f32 v[50:51],v[50:51],v[244:245] op_sel:[0,1] op_sel_hi:[1,1]
v_cvt_pk_f16_f32 v52,v44,v45
v_cvt_pk_f16_f32 v53,v46,v47
v_cvt_pk_f16_f32 v54,v48,v49
v_cvt_pk_f16_f32 v55,v50,v51
ds_write_b128 v111,v[52:55] offset:5120
v_accvgpr_read_b32 v56,a240
v_accvgpr_read_b32 v57,a241
v_accvgpr_read_b32 v58,a242
v_accvgpr_read_b32 v59,a243
v_accvgpr_read_b32 v60,a248
v_accvgpr_read_b32 v61,a249
v_accvgpr_read_b32 v62,a250
v_accvgpr_read_b32 v63,a251
v_pk_mul_f32 v[56:57],v[56:57],v[244:245] op_sel:[0,0] op_sel_hi:[1,0]
v_pk_mul_f32 v[58:59],v[58:59],v[244:245] op_sel:[0,0] op_sel_hi:[1,0]
v_pk_mul_f32 v[60:61],v[60:61],v[244:245] op_sel:[0,0] op_sel_hi:[1,0]
v_pk_mul_f32 v[62:63],v[62:63],v[244:245] op_sel:[0,0] op_sel_hi:[1,0]
v_cvt_pk_f16_f32 v64,v56,v57
v_cvt_pk_f16_f32 v65,v58,v59
v_cvt_pk_f16_f32 v66,v60,v61
v_cvt_pk_f16_f32 v67,v62,v63
ds_write_b128 v111,v[64:67] offset:6144
v_accvgpr_read_b32 v68,a244
v_accvgpr_read_b32 v69,a245
v_accvgpr_read_b32 v70,a246
v_accvgpr_read_b32 v71,a247
v_accvgpr_read_b32 v72,a252
v_accvgpr_read_b32 v73,a253
v_accvgpr_read_b32 v74,a254
v_accvgpr_read_b32 v75,a255
v_pk_mul_f32 v[68:69],v[68:69],v[244:245] op_sel:[0,1] op_sel_hi:[1,1]
v_pk_mul_f32 v[70:71],v[70:71],v[244:245] op_sel:[0,1] op_sel_hi:[1,1]
v_pk_mul_f32 v[72:73],v[72:73],v[244:245] op_sel:[0,1] op_sel_hi:[1,1]
v_pk_mul_f32 v[74:75],v[74:75],v[244:245] op_sel:[0,1] op_sel_hi:[1,1]
v_cvt_pk_f16_f32 v76,v68,v69
v_cvt_pk_f16_f32 v77,v70,v71
v_cvt_pk_f16_f32 v78,v72,v73
v_cvt_pk_f16_f32 v79,v74,v75
ds_write_b128 v111,v[76:79] offset:7168
v_add_u32 v110,s50,%[vL16]
v_add_u32 v111,s51,%[vL16]
v_add_u32 v108,s54,%[vL16]
v_add_u32 v109,20480,v108
v_add_u32 v16,-16,v13
v_cmp_gt_u32 vcc,4000,v16
s_cmp_eq_u64 vcc,-1
s_cbranch_scc1 NZ0_%=
v_cndmask_b32 v128,0,v128,vcc
v_cndmask_b32 v129,0,v129,vcc
v_cndmask_b32 v130,0,v130,vcc
v_cndmask_b32 v131,0,v131,vcc
v_cndmask_b32 v132,0,v132,vcc
v_cndmask_b32 v133,0,v133,vcc
v_cndmask_b32 v134,0,v134,vcc
v_cndmask_b32 v135,0,v135,vcc
v_cndmask_b32 v136,0,v136,vcc
v_cndmask_b32 v137,0,v137,vcc
v_cndmask_b32 v138,0,v138,vcc
v_cndmask_b32 v139,0,v139,vcc
v_cndmask_b32 v140,0,v140,vcc
v_cndmask_b32 v141,0,v141,vcc
v_cndmask_b32 v142,0,v142,vcc
v_cndmask_b32 v143,0,v143,vcc
NZ0_%=:
v_add_u32 v16,16,v13
v_cmp_gt_u32 vcc,4000,v16
s_cmp_eq_u64 vcc,-1
s_cbranch_scc1 NZ1_%=
v_cndmask_b32 v144,0,v144,vcc
v_cndmask_b32 v145,0,v145,vcc
v_cndmask_b32 v146,0,v146,vcc
v_cndmask_b32 v147,0,v147,vcc
v_cndmask_b32 v148,0,v148,vcc
v_cndmask_b32 v149,0,v149,vcc
v_cndmask_b32 v150,0,v150,vcc
v_cndmask_b32 v151,0,v151,vcc
v_cndmask_b32 v152,0,v152,vcc
v_cndmask_b32 v153,0,v153,vcc
v_cndmask_b32 v154,0,v154,vcc
v_cndmask_b32 v155,0,v155,vcc
v_cndmask_b32 v156,0,v156,vcc
v_cndmask_b32 v157,0,v157,vcc
v_cndmask_b32 v158,0,v158,vcc
v_cndmask_b32 v159,0,v159,vcc
NZ1_%=:
v_add_u32 v16,48,v13
v_cmp_gt_u32 vcc,4000,v16
s_cmp_eq_u64 vcc,-1
s_cbranch_scc1 NZ2_%=
v_cndmask_b32 v160,0,v160,vcc
v_cndmask_b32 v161,0,v161,vcc
v_cndmask_b32 v162,0,v162,vcc
v_cndmask_b32 v163,0,v163,vcc
v_cndmask_b32 v164,0,v164,vcc
v_cndmask_b32 v165,0,v165,vcc
v_cndmask_b32 v166,0,v166,vcc
v_cndmask_b32 v167,0,v167,vcc
v_cndmask_b32 v168,0,v168,vcc
v_cndmask_b32 v169,0,v169,vcc
v_cndmask_b32 v170,0,v170,vcc
v_cndmask_b32 v171,0,v171,vcc
v_cndmask_b32 v172,0,v172,vcc
v_cndmask_b32 v173,0,v173,vcc
v_cndmask_b32 v174,0,v174,vcc
v_cndmask_b32 v175,0,v175,vcc
NZ2_%=:
v_mov_b32 v20,0x40000000
)"
R"(s_waitcnt lgkmcnt(0)
s_barrier
ds_read_b128 v[112:115],v110 offset:0
ds_read_b128 v[116:119],v110 offset:1024
ds_read_b128 v[120:123],v110 offset:2048
ds_read_b128 v[32:35],v8 offset:0
ds_read_b128 v[36:39],v8 offset:1664
ds_read_b128 v[40:43],v8 offset:16
ds_read_b128 v[44:47],v8 offset:1680
ds_read_b128 v[124:127],v110 offset:3072
v_accvgpr_read_b32 v56,a0
v_accvgpr_read_b32 v57,a1
v_accvgpr_read_b32 v58,a2
v_accvgpr_read_b32 v59,a3
v_accvgpr_read_b32 v60,a8
v_accvgpr_read_b32 v61,a9
v_accvgpr_read_b32 v62,a10
v_accvgpr_read_b32 v63,a11
s_waitcnt lgkmcnt(7)
v_fma_mix_f32 v56,v56,v244,v112 op_sel_hi:[0,0,1]
v_fma_mix_f32 v57,v57,v244,v112 op_sel:[0,0,1] op_sel_hi:[0,0,1]
v_fma_mix_f32 v58,v58,v244,v113 op_sel_hi:[0,0,1]
v_fma_mix_f32 v59,v59,v244,v113 op_sel:[0,0,1] op_sel_hi:[0,0,1]
v_fma_mix_f32 v60,v60,v244,v114 op_sel_hi:[0,0,1]
v_fma_mix_f32 v61,v61,v244,v114 op_sel:[0,0,1] op_sel_hi:[0,0,1]
v_fma_mix_f32 v62,v62,v244,v115 op_sel_hi:[0,0,1]
v_fma_mix_f32 v63,v63,v244,v115 op_sel:[0,0,1] op_sel_hi:[0,0,1]
v_cvt_pk_f16_f32 v112,v56,v57
v_cvt_pk_f16_f32 v113,v58,v59
v_cvt_pk_f16_f32 v114,v60,v61
v_cvt_pk_f16_f32 v115,v62,v63
buffer_store_dwordx4 v[112:115],v108,%[srdW],0 offen offset:0 sc1
ds_read_b128 v[112:115],v110 offset:4096
v_accvgpr_read_b32 v56,a4
v_accvgpr_read_b32 v57,a5
v_accvgpr_read_b32 v58,a6
v_accvgpr_read_b32 v59,a7
v_accvgpr_read_b32 v60,a12
v_accvgpr_read_b32 v61,a13
v_accvgpr_read_b32 v62,a14
v_accvgpr_read_b32 v63,a15
s_waitcnt lgkmcnt(7)
v_fma_mix_f32 v56,v56,v245,v116 op_sel_hi:[0,0,1]
v_fma_mix_f32 v57,v57,v245,v116 op_sel:[0,0,1] op_sel_hi:[0,0,1]
v_fma_mix_f32 v58,v58,v245,v117 op_sel_hi:[0,0,1]
v_fma_mix_f32 v59,v59,v245,v117 op_sel:[0,0,1] op_sel_hi:[0,0,1]
v_fma_mix_f32 v60,v60,v245,v118 op_sel_hi:[0,0,1]
v_fma_mix_f32 v61,v61,v245,v118 op_sel:[0,0,1] op_sel_hi:[0,0,1]
v_fma_mix_f32 v62,v62,v245,v119 op_sel_hi:[0,0,1]
v_fma_mix_f32 v63,v63,v245,v119 op_sel:[0,0,1] op_sel_hi:[0,0,1]
v_cvt_pk_f16_f32 v116,v56,v57
v_cvt_pk_f16_f32 v117,v58,v59
v_cvt_pk_f16_f32 v118,v60,v61
v_cvt_pk_f16_f32 v119,v62,v63
buffer_store_dwordx4 v[116:119],v109,%[srdW],0 offen offset:0 sc1
ds_read_b128 v[116:119],v110 offset:5120
v_accvgpr_read_b32 v56,a16
v_accvgpr_read_b32 v57,a17
v_accvgpr_read_b32 v58,a18
v_accvgpr_read_b32 v59,a19
v_accvgpr_read_b32 v60,a24
v_accvgpr_read_b32 v61,a25
v_accvgpr_read_b32 v62,a26
v_accvgpr_read_b32 v63,a27
s_waitcnt lgkmcnt(7)
v_fma_mix_f32 v56,v56,v244,v120 op_sel_hi:[0,0,1]
v_fma_mix_f32 v57,v57,v244,v120 op_sel:[0,0,1] op_sel_hi:[0,0,1]
v_fma_mix_f32 v58,v58,v244,v121 op_sel_hi:[0,0,1]
v_fma_mix_f32 v59,v59,v244,v121 op_sel:[0,0,1] op_sel_hi:[0,0,1]
v_fma_mix_f32 v60,v60,v244,v122 op_sel_hi:[0,0,1]
v_fma_mix_f32 v61,v61,v244,v122 op_sel:[0,0,1] op_sel_hi:[0,0,1]
v_fma_mix_f32 v62,v62,v244,v123 op_sel_hi:[0,0,1]
v_fma_mix_f32 v63,v63,v244,v123 op_sel:[0,0,1] op_sel_hi:[0,0,1]
v_cvt_pk_f16_f32 v120,v56,v57
v_cvt_pk_f16_f32 v121,v58,v59
v_cvt_pk_f16_f32 v122,v60,v61
v_cvt_pk_f16_f32 v123,v62,v63
buffer_store_dwordx4 v[120:123],v108,%[srdW],0 offen offset:1024 sc1
ds_read_b128 v[120:123],v110 offset:6144
v_accvgpr_read_b32 v56,a20
v_accvgpr_read_b32 v57,a21
v_accvgpr_read_b32 v58,a22
v_accvgpr_read_b32 v59,a23
v_accvgpr_read_b32 v60,a28
v_accvgpr_read_b32 v61,a29
v_accvgpr_read_b32 v62,a30
v_accvgpr_read_b32 v63,a31
s_waitcnt lgkmcnt(3)
v_fma_mix_f32 v56,v56,v245,v124 op_sel_hi:[0,0,1]
v_fma_mix_f32 v57,v57,v245,v124 op_sel:[0,0,1] op_sel_hi:[0,0,1]
v_fma_mix_f32 v58,v58,v245,v125 op_sel_hi:[0,0,1]
v_fma_mix_f32 v59,v59,v245,v125 op_sel:[0,0,1] op_sel_hi:[0,0,1]
v_fma_mix_f32 v60,v60,v245,v126 op_sel_hi:[0,0,1]
v_fma_mix_f32 v61,v61,v245,v126 op_sel:[0,0,1] op_sel_hi:[0,0,1]
v_fma_mix_f32 v62,v62,v245,v127 op_sel_hi:[0,0,1]
v_fma_mix_f32 v63,v63,v245,v127 op_sel:[0,0,1] op_sel_hi:[0,0,1]
v_cvt_pk_f16_f32 v124,v56,v57
v_cvt_pk_f16_f32 v125,v58,v59
v_cvt_pk_f16_f32 v126,v60,v61
v_cvt_pk_f16_f32 v127,v62,v63
buffer_store_dwordx4 v[124:127],v109,%[srdW],0 offen offset:1024 sc1
ds_read_b128 v[124:127],v110 offset:7168
v_accvgpr_read_b32 v56,a32
v_accvgpr_read_b32 v57,a33
v_accvgpr_read_b32 v58,a34
v_accvgpr_read_b32 v59,a35
v_accvgpr_read_b32 v60,a40
v_accvgpr_read_b32 v61,a41
v_accvgpr_read_b32 v62,a42
v_accvgpr_read_b32 v63,a43
s_waitcnt lgkmcnt(3)
v_fma_mix_f32 v56,v56,v244,v112 op_sel_hi:[0,0,1]
v_fma_mix_f32 v57,v57,v244,v112 op_sel:[0,0,1] op_sel_hi:[0,0,1]
v_fma_mix_f32 v58,v58,v244,v113 op_sel_hi:[0,0,1]
v_fma_mix_f32 v59,v59,v244,v113 op_sel:[0,0,1] op_sel_hi:[0,0,1]
v_fma_mix_f32 v60,v60,v244,v114 op_sel_hi:[0,0,1]
v_fma_mix_f32 v61,v61,v244,v114 op_sel:[0,0,1] op_sel_hi:[0,0,1]
v_fma_mix_f32 v62,v62,v244,v115 op_sel_hi:[0,0,1]
v_fma_mix_f32 v63,v63,v244,v115 op_sel:[0,0,1] op_sel_hi:[0,0,1]
v_cvt_pk_f16_f32 v112,v56,v57
v_cvt_pk_f16_f32 v113,v58,v59
v_cvt_pk_f16_f32 v114,v60,v61
v_cvt_pk_f16_f32 v115,v62,v63
buffer_store_dwordx4 v[112:115],v108,%[srdW],0 offen offset:2048 sc1
ds_read_b128 v[112:115],v111 offset:0
v_accvgpr_read_b32 v56,a36
v_accvgpr_read_b32 v57,a37
v_accvgpr_read_b32 v58,a38
v_accvgpr_read_b32 v59,a39
v_accvgpr_read_b32 v60,a44
v_accvgpr_read_b32 v61,a45
v_accvgpr_read_b32 v62,a46
v_accvgpr_read_b32 v63,a47
s_waitcnt lgkmcnt(3)
v_fma_mix_f32 v56,v56,v245,v116 op_sel_hi:[0,0,1]
v_fma_mix_f32 v57,v57,v245,v116 op_sel:[0,0,1] op_sel_hi:[0,0,1]
v_fma_mix_f32 v58,v58,v245,v117 op_sel_hi:[0,0,1]
v_fma_mix_f32 v59,v59,v245,v117 op_sel:[0,0,1] op_sel_hi:[0,0,1]
v_fma_mix_f32 v60,v60,v245,v118 op_sel_hi:[0,0,1]
v_fma_mix_f32 v61,v61,v245,v118 op_sel:[0,0,1] op_sel_hi:[0,0,1]
v_fma_mix_f32 v62,v62,v245,v119 op_sel_hi:[0,0,1]
v_fma_mix_f32 v63,v63,v245,v119 op_sel:[0,0,1] op_sel_hi:[0,0,1]
v_cvt_pk_f16_f32 v116,v56,v57
v_cvt_pk_f16_f32 v117,v58,v59
v_cvt_pk_f16_f32 v118,v60,v61
v_cvt_pk_f16_f32 v119,v62,v63
buffer_store_dwordx4 v[116:119],v109,%[srdW],0 offen offset:2048 sc1
ds_read_b128 v[116:119],v111 offset:1024
v_accvgpr_read_b32 v56,a48
v_accvgpr_read_b32 v57,a49
v_accvgpr_read_b32 v58,a50
v_accvgpr_read_b32 v59,a51
v_accvgpr_read_b32 v60,a56
v_accvgpr_read_b32 v61,a57
v_accvgpr_read_b32 v62,a58
v_accvgpr_read_b32 v63,a59
s_waitcnt lgkmcnt(3)
v_fma_mix_f32 v56,v56,v244,v120 op_sel_hi:[0,0,1]
v_fma_mix_f32 v57,v57,v244,v120 op_sel:[0,0,1] op_sel_hi:[0,0,1]
v_fma_mix_f32 v58,v58,v244,v121 op_sel_hi:[0,0,1]
v_fma_mix_f32 v59,v59,v244,v121 op_sel:[0,0,1] op_sel_hi:[0,0,1]
v_fma_mix_f32 v60,v60,v244,v122 op_sel_hi:[0,0,1]
v_fma_mix_f32 v61,v61,v244,v122 op_sel:[0,0,1] op_sel_hi:[0,0,1]
v_fma_mix_f32 v62,v62,v244,v123 op_sel_hi:[0,0,1]
v_fma_mix_f32 v63,v63,v244,v123 op_sel:[0,0,1] op_sel_hi:[0,0,1]
v_cvt_pk_f16_f32 v120,v56,v57
v_cvt_pk_f16_f32 v121,v58,v59
v_cvt_pk_f16_f32 v122,v60,v61
v_cvt_pk_f16_f32 v123,v62,v63
buffer_store_dwordx4 v[120:123],v108,%[srdW],0 offen offset:3072 sc1
ds_read_b128 v[120:123],v111 offset:2048
v_accvgpr_read_b32 v56,a52
v_accvgpr_read_b32 v57,a53
v_accvgpr_read_b32 v58,a54
v_accvgpr_read_b32 v59,a55
v_accvgpr_read_b32 v60,a60
v_accvgpr_read_b32 v61,a61
v_accvgpr_read_b32 v62,a62
v_accvgpr_read_b32 v63,a63
s_waitcnt lgkmcnt(3)
v_fma_mix_f32 v56,v56,v245,v124 op_sel_hi:[0,0,1]
v_fma_mix_f32 v57,v57,v245,v124 op_sel:[0,0,1] op_sel_hi:[0,0,1]
v_fma_mix_f32 v58,v58,v245,v125 op_sel_hi:[0,0,1]
v_fma_mix_f32 v59,v59,v245,v125 op_sel:[0,0,1] op_sel_hi:[0,0,1]
v_fma_mix_f32 v60,v60,v245,v126 op_sel_hi:[0,0,1]
v_fma_mix_f32 v61,v61,v245,v126 op_sel:[0,0,1] op_sel_hi:[0,0,1]
v_fma_mix_f32 v62,v62,v245,v127 op_sel_hi:[0,0,1]
v_fma_mix_f32 v63,v63,v245,v127 op_sel:[0,0,1] op_sel_hi:[0,0,1]
v_cvt_pk_f16_f32 v124,v56,v57
v_cvt_pk_f16_f32 v125,v58,v59
v_cvt_pk_f16_f32 v126,v60,v61
v_cvt_pk_f16_f32 v127,v62,v63
buffer_store_dwordx4 v[124:127],v109,%[srdW],0 offen offset:3072 sc1
ds_read_b128 v[124:127],v111 offset:3072
v_accvgpr_read_b32 v56,a64
v_accvgpr_read_b32 v57,a65
v_accvgpr_read_b32 v58,a66
v_accvgpr_read_b32 v59,a67
v_accvgpr_read_b32 v60,a72
v_accvgpr_read_b32 v61,a73
v_accvgpr_read_b32 v62,a74
v_accvgpr_read_b32 v63,a75
s_waitcnt lgkmcnt(3)
v_fma_mix_f32 v56,v56,v244,v112 op_sel_hi:[0,0,1]
v_fma_mix_f32 v57,v57,v244,v112 op_sel:[0,0,1] op_sel_hi:[0,0,1]
v_fma_mix_f32 v58,v58,v244,v113 op_sel_hi:[0,0,1]
v_fma_mix_f32 v59,v59,v244,v113 op_sel:[0,0,1] op_sel_hi:[0,0,1]
v_fma_mix_f32 v60,v60,v244,v114 op_sel_hi:[0,0,1]
v_fma_mix_f32 v61,v61,v244,v114 op_sel:[0,0,1] op_sel_hi:[0,0,1]
v_fma_mix_f32 v62,v62,v244,v115 op_sel_hi:[0,0,1]
v_fma_mix_f32 v63,v63,v244,v115 op_sel:[0,0,1] op_sel_hi:[0,0,1]
v_cvt_pk_f16_f32 v112,v56,v57
v_cvt_pk_f16_f32 v113,v58,v59
v_cvt_pk_f16_f32 v114,v60,v61
v_cvt_pk_f16_f32 v115,v62,v63
v_add_u32 v108,0x1000,v108
v_add_u32 v109,0x1000,v109
buffer_store_dwordx4 v[112:115],v108,%[srdW],0 offen offset:0 sc1
ds_read_b128 v[112:115],v111 offset:4096
v_accvgpr_read_b32 v56,a68
v_accvgpr_read_b32 v57,a69
v_accvgpr_read_b32 v58,a70
v_accvgpr_read_b32 v59,a71
v_accvgpr_read_b32 v60,a76
v_accvgpr_read_b32 v61,a77
v_accvgpr_read_b32 v62,a78
v_accvgpr_read_b32 v63,a79
s_waitcnt lgkmcnt(3)
v_fma_mix_f32 v56,v56,v245,v116 op_sel_hi:[0,0,1]
v_fma_mix_f32 v57,v57,v245,v116 op_sel:[0,0,1] op_sel_hi:[0,0,1]
v_fma_mix_f32 v58,v58,v245,v117 op_sel_hi:[0,0,1]
v_fma_mix_f32 v59,v59,v245,v117 op_sel:[0,0,1] op_sel_hi:[0,0,1]
v_fma_mix_f32 v60,v60,v245,v118 op_sel_hi:[0,0,1]
v_fma_mix_f32 v61,v61,v245,v118 op_sel:[0,0,1] op_sel_hi:[0,0,1]
v_fma_mix_f32 v62,v62,v245,v119 op_sel_hi:[0,0,1]
v_fma_mix_f32 v63,v63,v245,v119 op_sel:[0,0,1] op_sel_hi:[0,0,1]
v_cvt_pk_f16_f32 v116,v56,v57
v_cvt_pk_f16_f32 v117,v58,v59
v_cvt_pk_f16_f32 v118,v60,v61
v_cvt_pk_f16_f32 v119,v62,v63
buffer_store_dwordx4 v[116:119],v109,%[srdW],0 offen offset:0 sc1
ds_read_b128 v[116:119],v111 offset:5120
v_accvgpr_read_b32 v56,a80
v_accvgpr_read_b32 v57,a81
v_accvgpr_read_b32 v58,a82
v_accvgpr_read_b32 v59,a83
v_accvgpr_read_b32 v60,a88
v_accvgpr_read_b32 v61,a89
v_accvgpr_read_b32 v62,a90
v_accvgpr_read_b32 v63,a91
s_waitcnt lgkmcnt(3)
v_fma_mix_f32 v56,v56,v244,v120 op_sel_hi:[0,0,1]
v_fma_mix_f32 v57,v57,v244,v120 op_sel:[0,0,1] op_sel_hi:[0,0,1]
v_fma_mix_f32 v58,v58,v244,v121 op_sel_hi:[0,0,1]
v_fma_mix_f32 v59,v59,v244,v121 op_sel:[0,0,1] op_sel_hi:[0,0,1]
v_fma_mix_f32 v60,v60,v244,v122 op_sel_hi:[0,0,1]
v_fma_mix_f32 v61,v61,v244,v122 op_sel:[0,0,1] op_sel_hi:[0,0,1]
v_fma_mix_f32 v62,v62,v244,v123 op_sel_hi:[0,0,1]
v_fma_mix_f32 v63,v63,v244,v123 op_sel:[0,0,1] op_sel_hi:[0,0,1]
v_cvt_pk_f16_f32 v120,v56,v57
v_cvt_pk_f16_f32 v121,v58,v59
v_cvt_pk_f16_f32 v122,v60,v61
v_cvt_pk_f16_f32 v123,v62,v63
buffer_store_dwordx4 v[120:123],v108,%[srdW],0 offen offset:1024 sc1
ds_read_b128 v[120:123],v111 offset:6144
v_accvgpr_read_b32 v56,a84
v_accvgpr_read_b32 v57,a85
v_accvgpr_read_b32 v58,a86
v_accvgpr_read_b32 v59,a87
v_accvgpr_read_b32 v60,a92
v_accvgpr_read_b32 v61,a93
v_accvgpr_read_b32 v62,a94
v_accvgpr_read_b32 v63,a95
s_waitcnt lgkmcnt(3)
v_fma_mix_f32 v56,v56,v245,v124 op_sel_hi:[0,0,1]
v_fma_mix_f32 v57,v57,v245,v124 op_sel:[0,0,1] op_sel_hi:[0,0,1]
v_fma_mix_f32 v58,v58,v245,v125 op_sel_hi:[0,0,1]
v_fma_mix_f32 v59,v59,v245,v125 op_sel:[0,0,1] op_sel_hi:[0,0,1]
v_fma_mix_f32 v60,v60,v245,v126 op_sel_hi:[0,0,1]
v_fma_mix_f32 v61,v61,v245,v126 op_sel:[0,0,1] op_sel_hi:[0,0,1]
v_fma_mix_f32 v62,v62,v245,v127 op_sel_hi:[0,0,1]
v_fma_mix_f32 v63,v63,v245,v127 op_sel:[0,0,1] op_sel_hi:[0,0,1]
v_cvt_pk_f16_f32 v124,v56,v57
v_cvt_pk_f16_f32 v125,v58,v59
v_cvt_pk_f16_f32 v126,v60,v61
v_cvt_pk_f16_f32 v127,v62,v63
buffer_store_dwordx4 v[124:127],v109,%[srdW],0 offen offset:1024 sc1
ds_read_b128 v[124:127],v111 offset:7168
v_accvgpr_read_b32 v56,a96
v_accvgpr_read_b32 v57,a97
v_accvgpr_read_b32 v58,a98
v_accvgpr_read_b32 v59,a99
v_accvgpr_read_b32 v60,a104
v_accvgpr_read_b32 v61,a105
v_accvgpr_read_b32 v62,a106
v_accvgpr_read_b32 v63,a107
s_waitcnt lgkmcnt(3)
v_fma_mix_f32 v56,v56,v244,v112 op_sel_hi:[0,0,1]
v_fma_mix_f32 v57,v57,v244,v112 op_sel:[0,0,1] op_sel_hi:[0,0,1]
v_fma_mix_f32 v58,v58,v244,v113 op_sel_hi:[0,0,1]
v_fma_mix_f32 v59,v59,v244,v113 op_sel:[0,0,1] op_sel_hi:[0,0,1]
v_fma_mix_f32 v60,v60,v244,v114 op_sel_hi:[0,0,1]
v_fma_mix_f32 v61,v61,v244,v114 op_sel:[0,0,1] op_sel_hi:[0,0,1]
v_fma_mix_f32 v62,v62,v244,v115 op_sel_hi:[0,0,1]
v_fma_mix_f32 v63,v63,v244,v115 op_sel:[0,0,1] op_sel_hi:[0,0,1]
v_cvt_pk_f16_f32 v112,v56,v57
v_cvt_pk_f16_f32 v113,v58,v59
v_cvt_pk_f16_f32 v114,v60,v61
v_cvt_pk_f16_f32 v115,v62,v63
buffer_store_dwordx4 v[112:115],v108,%[srdW],0 offen offset:2048 sc1
v_accvgpr_read_b32 v56,a100
v_accvgpr_read_b32 v57,a101
v_accvgpr_read_b32 v58,a102
v_accvgpr_read_b32 v59,a103
v_accvgpr_read_b32 v60,a108
v_accvgpr_read_b32 v61,a109
v_accvgpr_read_b32 v62,a110
v_accvgpr_read_b32 v63,a111
s_waitcnt lgkmcnt(2)
v_fma_mix_f32 v56,v56,v245,v116 op_sel_hi:[0,0,1]
v_fma_mix_f32 v57,v57,v245,v116 op_sel:[0,0,1] op_sel_hi:[0,0,1]
v_fma_mix_f32 v58,v58,v245,v117 op_sel_hi:[0,0,1]
v_fma_mix_f32 v59,v59,v245,v117 op_sel:[0,0,1] op_sel_hi:[0,0,1]
v_fma_mix_f32 v60,v60,v245,v118 op_sel_hi:[0,0,1]
v_fma_mix_f32 v61,v61,v245,v118 op_sel:[0,0,1] op_sel_hi:[0,0,1]
v_fma_mix_f32 v62,v62,v245,v119 op_sel_hi:[0,0,1]
v_fma_mix_f32 v63,v63,v245,v119 op_sel:[0,0,1] op_sel_hi:[0,0,1]
v_cvt_pk_f16_f32 v116,v56,v57
v_cvt_pk_f16_f32 v117,v58,v59
v_cvt_pk_f16_f32 v118,v60,v61
v_cvt_pk_f16_f32 v119,v62,v63
buffer_store_dwordx4 v[116:119],v109,%[srdW],0 offen offset:2048 sc1
v_accvgpr_read_b32 v56,a112
v_accvgpr_read_b32 v57,a113
v_accvgpr_read_b32 v58,a114
v_accvgpr_read_b32 v59,a115
v_accvgpr_read_b32 v60,a120
v_accvgpr_read_b32 v61,a121
v_accvgpr_read_b32 v62,a122
v_accvgpr_read_b32 v63,a123
s_waitcnt lgkmcnt(1)
v_fma_mix_f32 v56,v56,v244,v120 op_sel_hi:[0,0,1]
v_fma_mix_f32 v57,v57,v244,v120 op_sel:[0,0,1] op_sel_hi:[0,0,1]
v_fma_mix_f32 v58,v58,v244,v121 op_sel_hi:[0,0,1]
v_fma_mix_f32 v59,v59,v244,v121 op_sel:[0,0,1] op_sel_hi:[0,0,1]
v_fma_mix_f32 v60,v60,v244,v122 op_sel_hi:[0,0,1]
v_fma_mix_f32 v61,v61,v244,v122 op_sel:[0,0,1] op_sel_hi:[0,0,1]
v_fma_mix_f32 v62,v62,v244,v123 op_sel_hi:[0,0,1]
v_fma_mix_f32 v63,v63,v244,v123 op_sel:[0,0,1] op_sel_hi:[0,0,1]
v_cvt_pk_f16_f32 v120,v56,v57
v_cvt_pk_f16_f32 v121,v58,v59
v_cvt_pk_f16_f32 v122,v60,v61
v_cvt_pk_f16_f32 v123,v62,v63
buffer_store_dwordx4 v[120:123],v108,%[srdW],0 offen offset:3072 sc1
v_accvgpr_read_b32 v56,a116
v_accvgpr_read_b32 v57,a117
v_accvgpr_read_b32 v58,a118
v_accvgpr_read_b32 v59,a119
v_accvgpr_read_b32 v60,a124
v_accvgpr_read_b32 v61,a125
v_accvgpr_read_b32 v62,a126
v_accvgpr_read_b32 v63,a127
s_waitcnt lgkmcnt(0)
v_fma_mix_f32 v56,v56,v245,v124 op_sel_hi:[0,0,1]
v_fma_mix_f32 v57,v57,v245,v124 op_sel:[0,0,1] op_sel_hi:[0,0,1]
v_fma_mix_f32 v58,v58,v245,v125 op_sel_hi:[0,0,1]
v_fma_mix_f32 v59,v59,v245,v125 op_sel:[0,0,1] op_sel_hi:[0,0,1]
v_fma_mix_f32 v60,v60,v245,v126 op_sel_hi:[0,0,1]
v_fma_mix_f32 v61,v61,v245,v126 op_sel:[0,0,1] op_sel_hi:[0,0,1]
v_fma_mix_f32 v62,v62,v245,v127 op_sel_hi:[0,0,1]
v_fma_mix_f32 v63,v63,v245,v127 op_sel:[0,0,1] op_sel_hi:[0,0,1]
v_cvt_pk_f16_f32 v124,v56,v57
v_cvt_pk_f16_f32 v125,v58,v59
v_cvt_pk_f16_f32 v126,v60,v61
v_cvt_pk_f16_f32 v127,v62,v63
buffer_store_dwordx4 v[124:127],v109,%[srdW],0 offen offset:3072 sc1
ds_read_b128 v[48:51],v8 offset:32
ds_read_b128 v[52:55],v8 offset:1696
v_lshlrev_b32 v96,16,v128
v_and_b32 v97,0xffff0000,v128
v_lshlrev_b32 v98,16,v129
v_and_b32 v99,0xffff0000,v129
v_pk_fma_f32 v[96:97],v[96:97],v[32:33],v[36:37]
v_pk_fma_f32 v[98:99],v[98:99],v[34:35],v[38:39]
v_cvt_pk_bf16_f32 v128,v96,v97
v_cvt_pk_bf16_f32 v129,v98,v99
ds_read_b128 v[32:35],v8 offset:48
ds_read_b128 v[36:39],v8 offset:1712
v_lshlrev_b32 v100,16,v130
v_and_b32 v101,0xffff0000,v130
v_lshlrev_b32 v102,16,v131
v_and_b32 v103,0xffff0000,v131
v_pk_fma_f32 v[100:101],v[100:101],v[40:41],v[44:45]
v_pk_fma_f32 v[102:103],v[102:103],v[42:43],v[46:47]
v_cvt_pk_bf16_f32 v130,v100,v101
v_cvt_pk_bf16_f32 v131,v102,v103
ds_read_b128 v[40:43],v8 offset:64
ds_read_b128 v[44:47],v8 offset:1728
v_lshlrev_b32 v96,16,v132
v_and_b32 v97,0xffff0000,v132
)"
R"(v_lshlrev_b32 v98,16,v133
v_and_b32 v99,0xffff0000,v133
s_waitcnt lgkmcnt(4)
v_pk_fma_f32 v[96:97],v[96:97],v[48:49],v[52:53]
v_pk_fma_f32 v[98:99],v[98:99],v[50:51],v[54:55]
v_cvt_pk_bf16_f32 v132,v96,v97
v_cvt_pk_bf16_f32 v133,v98,v99
ds_read_b128 v[48:51],v8 offset:80
ds_read_b128 v[52:55],v8 offset:1744
v_lshlrev_b32 v100,16,v134
v_and_b32 v101,0xffff0000,v134
v_lshlrev_b32 v102,16,v135
v_and_b32 v103,0xffff0000,v135
s_waitcnt lgkmcnt(4)
v_pk_fma_f32 v[100:101],v[100:101],v[32:33],v[36:37]
v_pk_fma_f32 v[102:103],v[102:103],v[34:35],v[38:39]
v_cvt_pk_bf16_f32 v134,v100,v101
v_cvt_pk_bf16_f32 v135,v102,v103
ds_read_b128 v[32:35],v8 offset:96
ds_read_b128 v[36:39],v8 offset:1760
v_lshlrev_b32 v96,16,v136
v_and_b32 v97,0xffff0000,v136
v_lshlrev_b32 v98,16,v137
v_and_b32 v99,0xffff0000,v137
s_waitcnt lgkmcnt(4)
v_pk_fma_f32 v[96:97],v[96:97],v[40:41],v[44:45]
v_pk_fma_f32 v[98:99],v[98:99],v[42:43],v[46:47]
v_cvt_pk_bf16_f32 v136,v96,v97
v_cvt_pk_bf16_f32 v137,v98,v99
ds_read_b128 v[40:43],v8 offset:112
ds_read_b128 v[44:47],v8 offset:1776
v_lshlrev_b32 v100,16,v138
v_and_b32 v101,0xffff0000,v138
v_lshlrev_b32 v102,16,v139
v_and_b32 v103,0xffff0000,v139
s_waitcnt lgkmcnt(4)
v_pk_fma_f32 v[100:101],v[100:101],v[48:49],v[52:53]
v_pk_fma_f32 v[102:103],v[102:103],v[50:51],v[54:55]
v_cvt_pk_bf16_f32 v138,v100,v101
v_cvt_pk_bf16_f32 v139,v102,v103
v_readlane_b32 s60,%[pv],20
v_readlane_b32 s61,%[pv],21
v_readlane_b32 s71,%[pv],22
v_readlane_b32 s72,%[pv],23
v_readlane_b32 s73,%[pv],24
v_readlane_b32 s74,%[pv],25
v_readlane_b32 s75,%[pv],26
v_readlane_b32 s69,%[pv],27
v_readlane_b32 s62,%[pv],28
v_readlane_b32 s63,%[pv],29
v_readlane_b32 s64,%[pv],30
v_readlane_b32 s65,%[pv],31
v_readlane_b32 s66,%[pv],32
v_lshlrev_b32 v14,3,%[vL16]
v_min_u32 v14,s71,v14
v_add_u32 v14,s61,v14
v_bfe_u32 v5,%[vL16],4,2
v_mul_u32_u24 v5,544,v5
v_lshrrev_b32 v6,9,%[vL16]
v_lshl_add_u32 v5,v6,8,v5
v_bfe_u32 v7,%[vL16],6,3
v_lshl_add_u32 v5,v7,4,v5
v_add_u32 v5,s48,v5
v_lshlrev_b32 v13,6,v6
v_add_u32 v13,s72,v13
v_and_b32 v7,0x1f0,%[vL16]
v_mul_u32_u24 v12,81920,v6
v_add3_u32 v12,v12,v7,s73
v_mul_u32_u24 v6,544,v6
v_add3_u32 v6,v6,v7,s48
s_cmp_eq_u32 s74,2
s_cselect_b32 s70,0x40000000,0
v_add_u32 v10,s70,%[vL16]
v_and_b32 v11,0xf0,%[vL16]
v_add3_u32 v11,v11,s75,0
v_add_u32 v11,s70,v11
s_waitcnt vmcnt(0)
s_barrier
v_mov_b32 v16,s55
v_mov_b32 v17,s56
buffer_store_dword v16,v17,%[srdW],0 offen sc1
ds_read_b128 v[48:51],v8 offset:128
ds_read_b128 v[52:55],v8 offset:1792
v_lshlrev_b32 v96,16,v140
v_and_b32 v97,0xffff0000,v140
v_lshlrev_b32 v98,16,v141
buffer_load_dwordx4 a[32:35],%[vL16],%[srdW],s60 offen offset:0 sc1
v_and_b32 v99,0xffff0000,v141
s_waitcnt lgkmcnt(4)
v_pk_fma_f32 v[96:97],v[96:97],v[32:33],v[36:37]
v_pk_fma_f32 v[98:99],v[98:99],v[34:35],v[38:39]
v_cvt_pk_bf16_f32 v140,v96,v97
buffer_load_dwordx4 a[36:39],%[vL16],%[srdW],s60 offen offset:1024 sc1
v_cvt_pk_bf16_f32 v141,v98,v99
ds_read_b128 v[32:35],v8 offset:144
ds_read_b128 v[36:39],v8 offset:1808
v_lshlrev_b32 v100,16,v142
v_and_b32 v101,0xffff0000,v142
buffer_load_dwordx4 a[40:43],%[vL16],%[srdW],s60 offen offset:2048 sc1
v_lshlrev_b32 v102,16,v143
v_and_b32 v103,0xffff0000,v143
s_waitcnt lgkmcnt(4)
v_pk_fma_f32 v[100:101],v[100:101],v[40:41],v[44:45]
v_pk_fma_f32 v[102:103],v[102:103],v[42:43],v[46:47]
buffer_load_dwordx4 a[44:47],%[vL16],%[srdW],s60 offen offset:3072 sc1
v_cvt_pk_bf16_f32 v142,v100,v101
v_cvt_pk_bf16_f32 v143,v102,v103
ds_read_b128 v[40:43],v8 offset:160
ds_read_b128 v[44:47],v8 offset:1824
v_lshlrev_b32 v96,16,v144
s_add_u32 s60,s60,0x1000
v_and_b32 v97,0xffff0000,v144
v_lshlrev_b32 v98,16,v145
v_and_b32 v99,0xffff0000,v145
s_waitcnt lgkmcnt(4)
v_pk_fma_f32 v[96:97],v[96:97],v[48:49],v[52:53]
buffer_load_dwordx4 a[48:51],%[vL16],%[srdW],s60 offen offset:0 sc1
v_pk_fma_f32 v[98:99],v[98:99],v[50:51],v[54:55]
v_cvt_pk_bf16_f32 v144,v96,v97
v_cvt_pk_bf16_f32 v145,v98,v99
ds_read_b128 v[48:51],v8 offset:176
ds_read_b128 v[52:55],v8 offset:1840
buffer_load_dwordx4 a[52:55],%[vL16],%[srdW],s60 offen offset:1024 sc1
v_lshlrev_b32 v100,16,v146
v_and_b32 v101,0xffff0000,v146
v_lshlrev_b32 v102,16,v147
v_and_b32 v103,0xffff0000,v147
s_waitcnt lgkmcnt(4)
buffer_load_dwordx4 a[56:59],%[vL16],%[srdW],s60 offen offset:2048 sc1
v_pk_fma_f32 v[100:101],v[100:101],v[32:33],v[36:37]
v_pk_fma_f32 v[102:103],v[102:103],v[34:35],v[38:39]
v_cvt_pk_bf16_f32 v146,v100,v101
v_cvt_pk_bf16_f32 v147,v102,v103
ds_read_b128 v[32:35],v8 offset:192
buffer_load_dwordx4 a[60:63],%[vL16],%[srdW],s60 offen offset:3072 sc1
ds_read_b128 v[36:39],v8 offset:1856
v_lshlrev_b32 v96,16,v148
v_and_b32 v97,0xffff0000,v148
v_lshlrev_b32 v98,16,v149
v_and_b32 v99,0xffff0000,v149
v_readlane_b32 s67,%[pv],33
s_waitcnt lgkmcnt(4)
v_pk_fma_f32 v[96:97],v[96:97],v[40:41],v[44:45]
v_pk_fma_f32 v[98:99],v[98:99],v[42:43],v[46:47]
v_cvt_pk_bf16_f32 v148,v96,v97
v_cvt_pk_bf16_f32 v149,v98,v99
s_add_u32 s68,s67,0
ds_read_b128 v[40:43],v8 offset:208
ds_read_b128 v[44:47],v8 offset:1872
v_lshlrev_b32 v100,16,v150
v_and_b32 v101,0xffff0000,v150
v_lshlrev_b32 v102,16,v151
buffer_load_dwordx4 a[96:99],%[vL16],%[srdW],s68 offen offset:0 sc1
v_and_b32 v103,0xffff0000,v151
s_waitcnt lgkmcnt(4)
v_pk_fma_f32 v[100:101],v[100:101],v[48:49],v[52:53]
v_pk_fma_f32 v[102:103],v[102:103],v[50:51],v[54:55]
v_cvt_pk_bf16_f32 v150,v100,v101
buffer_load_dwordx4 a[100:103],%[vL16],%[srdW],s68 offen offset:1024 sc1
v_cvt_pk_bf16_f32 v151,v102,v103
ds_read_b128 v[48:51],v8 offset:224
ds_read_b128 v[52:55],v8 offset:1888
v_lshlrev_b32 v96,16,v152
v_and_b32 v97,0xffff0000,v152
buffer_load_dwordx4 a[104:107],%[vL16],%[srdW],s68 offen offset:2048 sc1
v_lshlrev_b32 v98,16,v153
v_and_b32 v99,0xffff0000,v153
s_waitcnt lgkmcnt(4)
v_pk_fma_f32 v[96:97],v[96:97],v[32:33],v[36:37]
v_pk_fma_f32 v[98:99],v[98:99],v[34:35],v[38:39]
buffer_load_dwordx4 a[108:111],%[vL16],%[srdW],s68 offen offset:3072 sc1
v_cvt_pk_bf16_f32 v152,v96,v97
v_cvt_pk_bf16_f32 v153,v98,v99
ds_read_b128 v[32:35],v8 offset:240
ds_read_b128 v[36:39],v8 offset:1904
v_lshlrev_b32 v100,16,v154
s_add_u32 s68,s67,4096
v_and_b32 v101,0xffff0000,v154
v_lshlrev_b32 v102,16,v155
v_and_b32 v103,0xffff0000,v155
s_waitcnt lgkmcnt(4)
v_pk_fma_f32 v[100:101],v[100:101],v[40:41],v[44:45]
buffer_load_dwordx4 a[112:115],%[vL16],%[srdW],s68 offen offset:0 sc1
v_pk_fma_f32 v[102:103],v[102:103],v[42:43],v[46:47]
v_cvt_pk_bf16_f32 v154,v100,v101
v_cvt_pk_bf16_f32 v155,v102,v103
ds_read_b128 v[40:43],v8 offset:272
ds_read_b128 v[44:47],v8 offset:1936
buffer_load_dwordx4 a[116:119],%[vL16],%[srdW],s68 offen offset:1024 sc1
v_lshlrev_b32 v96,16,v156
v_and_b32 v97,0xffff0000,v156
v_lshlrev_b32 v98,16,v157
v_and_b32 v99,0xffff0000,v157
s_waitcnt lgkmcnt(4)
buffer_load_dwordx4 a[120:123],%[vL16],%[srdW],s68 offen offset:2048 sc1
v_pk_fma_f32 v[96:97],v[96:97],v[48:49],v[52:53]
v_pk_fma_f32 v[98:99],v[98:99],v[50:51],v[54:55]
v_cvt_pk_bf16_f32 v156,v96,v97
v_cvt_pk_bf16_f32 v157,v98,v99
ds_read_b128 v[48:51],v8 offset:288
buffer_load_dwordx4 a[124:127],%[vL16],%[srdW],s68 offen offset:3072 sc1
ds_read_b128 v[52:55],v8 offset:1952
v_lshlrev_b32 v100,16,v158
v_and_b32 v101,0xffff0000,v158
v_lshlrev_b32 v102,16,v159
v_and_b32 v103,0xffff0000,v159
s_add_u32 s68,s67,8192
s_waitcnt lgkmcnt(4)
v_pk_fma_f32 v[100:101],v[100:101],v[32:33],v[36:37]
v_pk_fma_f32 v[102:103],v[102:103],v[34:35],v[38:39]
v_cvt_pk_bf16_f32 v158,v100,v101
v_cvt_pk_bf16_f32 v159,v102,v103
buffer_load_dwordx4 a[128:131],%[vL16],%[srdW],s68 offen offset:0 sc1
ds_read_b128 v[32:35],v8 offset:304
ds_read_b128 v[36:39],v8 offset:1968
v_lshlrev_b32 v96,16,v160
v_and_b32 v97,0xffff0000,v160
v_lshlrev_b32 v98,16,v161
buffer_load_dwordx4 a[132:135],%[vL16],%[srdW],s68 offen offset:1024 sc1
v_and_b32 v99,0xffff0000,v161
s_waitcnt lgkmcnt(4)
v_pk_fma_f32 v[96:97],v[96:97],v[40:41],v[44:45]
v_pk_fma_f32 v[98:99],v[98:99],v[42:43],v[46:47]
v_cvt_pk_bf16_f32 v160,v96,v97
buffer_load_dwordx4 a[136:139],%[vL16],%[srdW],s68 offen offset:2048 sc1
v_cvt_pk_bf16_f32 v161,v98,v99
ds_read_b128 v[40:43],v8 offset:320
ds_read_b128 v[44:47],v8 offset:1984
v_lshlrev_b32 v100,16,v162
v_and_b32 v101,0xffff0000,v162
buffer_load_dwordx4 a[140:143],%[vL16],%[srdW],s68 offen offset:3072 sc1
v_lshlrev_b32 v102,16,v163
v_and_b32 v103,0xffff0000,v163
s_waitcnt lgkmcnt(4)
v_pk_fma_f32 v[100:101],v[100:101],v[48:49],v[52:53]
v_pk_fma_f32 v[102:103],v[102:103],v[50:51],v[54:55]
s_add_u32 s68,s67,12288
v_cvt_pk_bf16_f32 v162,v100,v101
v_cvt_pk_bf16_f32 v163,v102,v103
ds_read_b128 v[48:51],v8 offset:336
ds_read_b128 v[52:55],v8 offset:2000
v_lshlrev_b32 v96,16,v164
buffer_load_dwordx4 a[144:147],%[vL16],%[srdW],s68 offen offset:0 sc1
v_and_b32 v97,0xffff0000,v164
v_lshlrev_b32 v98,16,v165
v_and_b32 v99,0xffff0000,v165
s_waitcnt lgkmcnt(4)
v_pk_fma_f32 v[96:97],v[96:97],v[32:33],v[36:37]
buffer_load_dwordx4 a[148:151],%[vL16],%[srdW],s68 offen offset:1024 sc1
v_pk_fma_f32 v[98:99],v[98:99],v[34:35],v[38:39]
v_cvt_pk_bf16_f32 v164,v96,v97
v_cvt_pk_bf16_f32 v165,v98,v99
ds_read_b128 v[32:35],v8 offset:352
ds_read_b128 v[36:39],v8 offset:2016
buffer_load_dwordx4 a[152:155],%[vL16],%[srdW],s68 offen offset:2048 sc1
v_lshlrev_b32 v100,16,v166
v_and_b32 v101,0xffff0000,v166
v_lshlrev_b32 v102,16,v167
v_and_b32 v103,0xffff0000,v167
s_waitcnt lgkmcnt(4)
buffer_load_dwordx4 a[156:159],%[vL16],%[srdW],s68 offen offset:3072 sc1
v_pk_fma_f32 v[100:101],v[100:101],v[40:41],v[44:45]
v_pk_fma_f32 v[102:103],v[102:103],v[42:43],v[46:47]
v_cvt_pk_bf16_f32 v166,v100,v101
v_cvt_pk_bf16_f32 v167,v102,v103
ds_read_b128 v[40:43],v8 offset:368
s_add_u32 s68,s67,16384
ds_read_b128 v[44:47],v8 offset:2032
v_lshlrev_b32 v96,16,v168
v_and_b32 v97,0xffff0000,v168
v_lshlrev_b32 v98,16,v169
v_and_b32 v99,0xffff0000,v169
buffer_load_dwordx4 a[160:163],%[vL16],%[srdW],s68 offen offset:0 sc1
s_waitcnt lgkmcnt(4)
v_pk_fma_f32 v[96:97],v[96:97],v[48:49],v[52:53]
v_pk_fma_f32 v[98:99],v[98:99],v[50:51],v[54:55]
v_cvt_pk_bf16_f32 v168,v96,v97
v_cvt_pk_bf16_f32 v169,v98,v99
buffer_load_dwordx4 a[164:167],%[vL16],%[srdW],s68 offen offset:1024 sc1
buffer_load_dwordx4 a[168:171],%[vL16],%[srdW],s68 offen offset:2048 sc1
buffer_load_dwordx4 a[172:175],%[vL16],%[srdW],s68 offen offset:3072 sc1
s_nop 1
v_mfma_f32_4x4x4_16b_bf16 v[112:115],v[208:209],v[128:129],0
buffer_load_dword v15,v14,%[srdW],0 offen sc1
v_mfma_f32_4x4x4_16b_bf16 v[116:119],v[208:209],v[130:131],0
ds_read_b128 v[48:51],v8 offset:384
v_mfma_f32_4x4x4_16b_bf16 v[120:123],v[208:209],v[132:133],0
ds_read_b128 v[52:55],v8 offset:2048
v_mfma_f32_4x4x4_16b_bf16 v[124:127],v[208:209],v[134:135],0
v_lshlrev_b32 v100,16,v170
v_mfma_f32_4x4x4_16b_bf16 v[112:115],v[210:211],v[130:131],v[112:115]
v_and_b32 v101,0xffff0000,v170
v_mfma_f32_4x4x4_16b_bf16 v[116:119],v[210:211],v[132:133],v[116:119]
v_lshlrev_b32 v102,16,v171
v_mfma_f32_4x4x4_16b_bf16 v[120:123],v[210:211],v[134:135],v[120:123]
v_and_b32 v103,0xffff0000,v171
v_mfma_f32_4x4x4_16b_bf16 v[124:127],v[210:211],v[136:137],v[124:127]
s_waitcnt lgkmcnt(4)
v_fma_f32 v100,v100,v32,v36
v_mfma_f32_4x4x4_16b_bf16 v[112:115],v[212:213],v[132:133],v[112:115]
v_fma_f32 v101,v101,v33,v37
v_mfma_f32_4x4x4_16b_bf16 v[116:119],v[212:213],v[134:135],v[116:119]
v_fma_f32 v102,v102,v34,v38
v_mfma_f32_4x4x4_16b_bf16 v[120:123],v[212:213],v[136:137],v[120:123]
v_fma_f32 v103,v103,v35,v39
v_mfma_f32_4x4x4_16b_bf16 v[124:127],v[212:213],v[138:139],v[124:127]
v_cvt_pk_bf16_f32 v170,v100,v101
v_mfma_f32_4x4x4_16b_bf16 v[112:115],v[214:215],v[134:135],v[112:115]
v_cvt_pk_bf16_f32 v171,v102,v103
v_mfma_f32_4x4x4_16b_bf16 v[116:119],v[214:215],v[136:137],v[116:119]
v_lshlrev_b32 v96,16,v172
v_mfma_f32_4x4x4_16b_bf16 v[120:123],v[214:215],v[138:139],v[120:123]
v_and_b32 v97,0xffff0000,v172
v_mfma_f32_4x4x4_16b_bf16 v[124:127],v[214:215],v[140:141],v[124:127]
v_lshlrev_b32 v98,16,v173
v_mfma_f32_4x4x4_16b_bf16 v[112:115],v[216:217],v[136:137],v[112:115]
v_and_b32 v99,0xffff0000,v173
v_mfma_f32_4x4x4_16b_bf16 v[116:119],v[216:217],v[138:139],v[116:119]
s_waitcnt lgkmcnt(2)
v_fma_f32 v96,v96,v40,v44
v_mfma_f32_4x4x4_16b_bf16 v[120:123],v[216:217],v[140:141],v[120:123]
v_fma_f32 v97,v97,v41,v45
v_mfma_f32_4x4x4_16b_bf16 v[124:127],v[216:217],v[142:143],v[124:127]
v_fma_f32 v98,v98,v42,v46
v_mfma_f32_4x4x4_16b_bf16 v[112:115],v[218:219],v[138:139],v[112:115]
v_fma_f32 v99,v99,v43,v47
v_mfma_f32_4x4x4_16b_bf16 v[116:119],v[218:219],v[140:141],v[116:119]
v_cvt_pk_bf16_f32 v172,v96,v97
v_mfma_f32_4x4x4_16b_bf16 v[120:123],v[218:219],v[142:143],v[120:123]
v_cvt_pk_bf16_f32 v173,v98,v99
v_mfma_f32_4x4x4_16b_bf16 v[124:127],v[218:219],v[144:145],v[124:127]
v_lshlrev_b32 v100,16,v174
v_mfma_f32_4x4x4_16b_bf16 v[112:115],v[220:221],v[140:141],v[112:115]
v_and_b32 v101,0xffff0000,v174
v_mfma_f32_4x4x4_16b_bf16 v[116:119],v[220:221],v[142:143],v[116:119]
v_lshlrev_b32 v102,16,v175
v_mfma_f32_4x4x4_16b_bf16 v[120:123],v[220:221],v[144:145],v[120:123]
v_and_b32 v103,0xffff0000,v175
v_mfma_f32_4x4x4_16b_bf16 v[124:127],v[220:221],v[146:147],v[124:127]
s_waitcnt lgkmcnt(0)
v_fma_f32 v100,v100,v48,v52
v_mfma_f32_4x4x4_16b_bf16 v[112:115],v[222:223],v[142:143],v[112:115]
v_fma_f32 v101,v101,v49,v53
v_mfma_f32_4x4x4_16b_bf16 v[116:119],v[222:223],v[144:145],v[116:119]
v_fma_f32 v102,v102,v50,v54
v_mfma_f32_4x4x4_16b_bf16 v[120:123],v[222:223],v[146:147],v[120:123]
v_fma_f32 v103,v103,v51,v55
v_mfma_f32_4x4x4_16b_bf16 v[124:127],v[222:223],v[148:149],v[124:127]
v_cvt_pk_bf16_f32 v174,v100,v101
v_mfma_f32_4x4x4_16b_bf16 v[112:115],v[224:225],v[144:145],v[112:115]
v_cvt_pk_bf16_f32 v175,v102,v103
v_mfma_f32_4x4x4_16b_bf16 v[116:119],v[224:225],v[146:147],v[116:119]
v_mfma_f32_4x4x4_16b_bf16 v[120:123],v[224:225],v[148:149],v[120:123]
v_mfma_f32_4x4x4_16b_bf16 v[124:127],v[224:225],v[150:151],v[124:127]
s_nop 1
v_mfma_f32_4x4x4_16b_bf16 v[232:235],v[208:209],v[136:137],0
v_lshlrev_b32 v96,16,v176
v_mfma_f32_4x4x4_16b_bf16 v[236:239],v[208:209],v[138:139],0
v_and_b32 v97,0xffff0000,v176
v_mfma_f32_4x4x4_16b_bf16 v[240:243],v[208:209],v[140:141],0
v_lshlrev_b32 v98,16,v177
v_mfma_f32_4x4x4_16b_bf16 v[244:247],v[208:209],v[142:143],0
v_and_b32 v99,0xffff0000,v177
v_mfma_f32_4x4x4_16b_bf16 v[232:235],v[210:211],v[138:139],v[232:235]
v_lshlrev_b32 v100,16,v178
v_mfma_f32_4x4x4_16b_bf16 v[236:239],v[210:211],v[140:141],v[236:239]
v_and_b32 v101,0xffff0000,v178
v_mfma_f32_4x4x4_16b_bf16 v[240:243],v[210:211],v[142:143],v[240:243]
v_lshlrev_b32 v102,16,v179
v_mfma_f32_4x4x4_16b_bf16 v[244:247],v[210:211],v[144:145],v[244:247]
v_and_b32 v103,0xffff0000,v179
v_mfma_f32_4x4x4_16b_bf16 v[232:235],v[212:213],v[140:141],v[232:235]
v_mul_f32 v96,v96,v112
v_mfma_f32_4x4x4_16b_bf16 v[236:239],v[212:213],v[142:143],v[236:239]
v_mul_f32 v97,v97,v113
v_mfma_f32_4x4x4_16b_bf16 v[240:243],v[212:213],v[144:145],v[240:243]
v_mul_f32 v98,v98,v114
v_mfma_f32_4x4x4_16b_bf16 v[244:247],v[212:213],v[146:147],v[244:247]
v_mul_f32 v99,v99,v115
v_mfma_f32_4x4x4_16b_bf16 v[232:235],v[214:215],v[142:143],v[232:235]
v_mul_f32 v100,v100,v116
v_mfma_f32_4x4x4_16b_bf16 v[236:239],v[214:215],v[144:145],v[236:239]
v_mul_f32 v101,v101,v117
v_mfma_f32_4x4x4_16b_bf16 v[240:243],v[214:215],v[146:147],v[240:243]
v_mul_f32 v102,v102,v118
v_mfma_f32_4x4x4_16b_bf16 v[244:247],v[214:215],v[148:149],v[244:247]
v_mul_f32 v103,v103,v119
v_mfma_f32_4x4x4_16b_bf16 v[232:235],v[216:217],v[144:145],v[232:235]
v_cvt_pk_bf16_f32 v176,v96,v97
v_mfma_f32_4x4x4_16b_bf16 v[236:239],v[216:217],v[146:147],v[236:239]
v_cvt_pk_bf16_f32 v177,v98,v99
v_mfma_f32_4x4x4_16b_bf16 v[240:243],v[216:217],v[148:149],v[240:243]
v_cvt_pk_bf16_f32 v178,v100,v101
v_mfma_f32_4x4x4_16b_bf16 v[244:247],v[216:217],v[150:151],v[244:247]
v_cvt_pk_bf16_f32 v179,v102,v103
v_mfma_f32_4x4x4_16b_bf16 v[232:235],v[218:219],v[146:147],v[232:235]
ds_write_b128 v5,v[176:179] offset:0
v_mfma_f32_4x4x4_16b_bf16 v[236:239],v[218:219],v[148:149],v[236:239]
v_lshlrev_b32 v96,16,v180
v_mfma_f32_4x4x4_16b_bf16 v[240:243],v[218:219],v[150:151],v[240:243]
)"
R"(v_and_b32 v97,0xffff0000,v180
v_mfma_f32_4x4x4_16b_bf16 v[244:247],v[218:219],v[152:153],v[244:247]
v_lshlrev_b32 v98,16,v181
v_mfma_f32_4x4x4_16b_bf16 v[232:235],v[220:221],v[148:149],v[232:235]
v_and_b32 v99,0xffff0000,v181
v_mfma_f32_4x4x4_16b_bf16 v[236:239],v[220:221],v[150:151],v[236:239]
v_lshlrev_b32 v100,16,v182
v_mfma_f32_4x4x4_16b_bf16 v[240:243],v[220:221],v[152:153],v[240:243]
v_and_b32 v101,0xffff0000,v182
v_mfma_f32_4x4x4_16b_bf16 v[244:247],v[220:221],v[154:155],v[244:247]
v_lshlrev_b32 v102,16,v183
v_mfma_f32_4x4x4_16b_bf16 v[232:235],v[222:223],v[150:151],v[232:235]
v_and_b32 v103,0xffff0000,v183
v_mfma_f32_4x4x4_16b_bf16 v[236:239],v[222:223],v[152:153],v[236:239]
v_mul_f32 v96,v96,v120
v_mfma_f32_4x4x4_16b_bf16 v[240:243],v[222:223],v[154:155],v[240:243]
v_mul_f32 v97,v97,v121
v_mfma_f32_4x4x4_16b_bf16 v[244:247],v[222:223],v[156:157],v[244:247]
v_mul_f32 v98,v98,v122
v_mfma_f32_4x4x4_16b_bf16 v[232:235],v[224:225],v[152:153],v[232:235]
v_mul_f32 v99,v99,v123
v_mfma_f32_4x4x4_16b_bf16 v[236:239],v[224:225],v[154:155],v[236:239]
v_mul_f32 v100,v100,v124
v_mfma_f32_4x4x4_16b_bf16 v[240:243],v[224:225],v[156:157],v[240:243]
v_mul_f32 v101,v101,v125
v_mfma_f32_4x4x4_16b_bf16 v[244:247],v[224:225],v[158:159],v[244:247]
v_mul_f32 v102,v102,v126
v_mul_f32 v103,v103,v127
v_cvt_pk_bf16_f32 v180,v96,v97
v_cvt_pk_bf16_f32 v181,v98,v99
v_cvt_pk_bf16_f32 v182,v100,v101
v_cvt_pk_bf16_f32 v183,v102,v103
ds_write_b128 v5,v[180:183] offset:128
ds_read_b128 v[176:179],v6 offset:0
ds_read_b128 v[180:183],v6 offset:1088
s_waitcnt vmcnt(0)
v_cmp_ne_u32 vcc,s55,v15
s_cmp_eq_u32 s74,1
s_cselect_b32 s70,vcc_lo,0
s_cmp_eq_u32 s70,0
s_cbranch_scc1 OK3G_%=
s_mov_b32 s97,0x40000
POLL3G_%=:
buffer_load_dword v15,v14,%[srdW],0 offen sc1
s_waitcnt vmcnt(0)
v_cmp_ne_u32 vcc,s55,v15
s_cbranch_vccz GO3G_%=
s_sub_u32 s97,s97,1
s_cbranch_scc1 GO3G_%=
s_sleep 4
s_branch POLL3G_%=
GO3G_%=:
OK3G_%=:
s_barrier
s_nop 1
v_mfma_f32_4x4x4_16b_bf16 v[112:115],v[208:209],v[144:145],0
v_add_u32 v16,0,v13
v_mfma_f32_4x4x4_16b_bf16 v[116:119],v[208:209],v[146:147],0
v_cmp_gt_u32 vcc,4000,v16
v_mfma_f32_4x4x4_16b_bf16 v[120:123],v[208:209],v[148:149],0
v_add_u32 v17,0,v12
v_mfma_f32_4x4x4_16b_bf16 v[124:127],v[208:209],v[150:151],0
s_waitcnt lgkmcnt(0)
v_cndmask_b32 v18,v20,v17,vcc
v_mfma_f32_4x4x4_16b_bf16 v[112:115],v[210:211],v[146:147],v[112:115]
buffer_store_dwordx4 v[176:179],v18,%[srdW],0 offen sc1
v_mfma_f32_4x4x4_16b_bf16 v[116:119],v[210:211],v[148:149],v[116:119]
v_add_u32 v16,128,v13
v_mfma_f32_4x4x4_16b_bf16 v[120:123],v[210:211],v[150:151],v[120:123]
v_cmp_gt_u32 vcc,4000,v16
v_mfma_f32_4x4x4_16b_bf16 v[124:127],v[210:211],v[152:153],v[124:127]
v_add_u32 v17,163840,v12
v_mfma_f32_4x4x4_16b_bf16 v[112:115],v[212:213],v[148:149],v[112:115]
v_cndmask_b32 v19,v20,v17,vcc
v_mfma_f32_4x4x4_16b_bf16 v[116:119],v[212:213],v[150:151],v[116:119]
buffer_store_dwordx4 v[180:183],v19,%[srdW],0 offen sc1
v_mfma_f32_4x4x4_16b_bf16 v[120:123],v[212:213],v[152:153],v[120:123]
v_lshlrev_b32 v96,16,v184
v_mfma_f32_4x4x4_16b_bf16 v[124:127],v[212:213],v[154:155],v[124:127]
v_and_b32 v97,0xffff0000,v184
v_mfma_f32_4x4x4_16b_bf16 v[112:115],v[214:215],v[150:151],v[112:115]
v_lshlrev_b32 v98,16,v185
v_mfma_f32_4x4x4_16b_bf16 v[116:119],v[214:215],v[152:153],v[116:119]
v_and_b32 v99,0xffff0000,v185
v_mfma_f32_4x4x4_16b_bf16 v[120:123],v[214:215],v[154:155],v[120:123]
s_lshl_b32 s61,s62,9
v_mfma_f32_4x4x4_16b_bf16 v[124:127],v[214:215],v[156:157],v[124:127]
buffer_load_dwordx4 v[128:131],v11,%[srdW],s61 offen sc1
v_mfma_f32_4x4x4_16b_bf16 v[112:115],v[216:217],v[152:153],v[112:115]
s_mul_i32 s62,s62,40960
v_mfma_f32_4x4x4_16b_bf16 v[116:119],v[216:217],v[154:155],v[116:119]
s_add_u32 s62,s62,s69
v_mfma_f32_4x4x4_16b_bf16 v[120:123],v[216:217],v[156:157],v[120:123]
v_lshlrev_b32 v100,16,v186
v_mfma_f32_4x4x4_16b_bf16 v[124:127],v[216:217],v[158:159],v[124:127]
v_and_b32 v101,0xffff0000,v186
v_mfma_f32_4x4x4_16b_bf16 v[112:115],v[218:219],v[154:155],v[112:115]
v_lshlrev_b32 v102,16,v187
v_mfma_f32_4x4x4_16b_bf16 v[116:119],v[218:219],v[156:157],v[116:119]
v_and_b32 v103,0xffff0000,v187
v_mfma_f32_4x4x4_16b_bf16 v[120:123],v[218:219],v[158:159],v[120:123]
s_lshl_b32 s61,s63,9
v_mfma_f32_4x4x4_16b_bf16 v[124:127],v[218:219],v[160:161],v[124:127]
buffer_load_dwordx4 v[132:135],v11,%[srdW],s61 offen sc1
v_mfma_f32_4x4x4_16b_bf16 v[112:115],v[220:221],v[156:157],v[112:115]
s_mul_i32 s63,s63,40960
v_mfma_f32_4x4x4_16b_bf16 v[116:119],v[220:221],v[158:159],v[116:119]
s_add_u32 s63,s63,s69
v_mfma_f32_4x4x4_16b_bf16 v[120:123],v[220:221],v[160:161],v[120:123]
v_mul_f32 v96,v96,v232
v_mfma_f32_4x4x4_16b_bf16 v[124:127],v[220:221],v[162:163],v[124:127]
v_mul_f32 v97,v97,v233
v_mfma_f32_4x4x4_16b_bf16 v[112:115],v[222:223],v[158:159],v[112:115]
v_mul_f32 v98,v98,v234
v_mfma_f32_4x4x4_16b_bf16 v[116:119],v[222:223],v[160:161],v[116:119]
v_mul_f32 v99,v99,v235
v_mfma_f32_4x4x4_16b_bf16 v[120:123],v[222:223],v[162:163],v[120:123]
s_lshl_b32 s61,s64,9
v_mfma_f32_4x4x4_16b_bf16 v[124:127],v[222:223],v[164:165],v[124:127]
buffer_load_dwordx4 v[136:139],v11,%[srdW],s61 offen sc1
v_mfma_f32_4x4x4_16b_bf16 v[112:115],v[224:225],v[160:161],v[112:115]
s_mul_i32 s64,s64,40960
v_mfma_f32_4x4x4_16b_bf16 v[116:119],v[224:225],v[162:163],v[116:119]
s_add_u32 s64,s64,s69
v_mfma_f32_4x4x4_16b_bf16 v[120:123],v[224:225],v[164:165],v[120:123]
v_mul_f32 v100,v100,v236
v_mfma_f32_4x4x4_16b_bf16 v[124:127],v[224:225],v[166:167],v[124:127]
v_mul_f32 v101,v101,v237
v_mul_f32 v102,v102,v238
v_mul_f32 v103,v103,v239
s_lshl_b32 s61,s65,9
buffer_load_dwordx4 v[140:143],v11,%[srdW],s61 offen sc1
s_mul_i32 s65,s65,40960
s_add_u32 s65,s65,s69
v_cvt_pk_bf16_f32 v184,v96,v97
v_cvt_pk_bf16_f32 v185,v98,v99
v_cvt_pk_bf16_f32 v186,v100,v101
v_cvt_pk_bf16_f32 v187,v102,v103
s_lshl_b32 s61,s66,9
buffer_load_dwordx4 v[104:107],v11,%[srdW],s61 offen sc1
s_mul_i32 s66,s66,40960
s_add_u32 s66,s66,s69
ds_write_b128 v5,v[184:187] offset:2176
v_lshlrev_b32 v96,16,v188
v_and_b32 v97,0xffff0000,v188
v_lshlrev_b32 v98,16,v189
s_add_u32 s61,s62,0
buffer_load_dwordx4 v[32:35],v10,%[srdW],s61 offen sc1
v_and_b32 v99,0xffff0000,v189
v_lshlrev_b32 v100,16,v190
v_and_b32 v101,0xffff0000,v190
v_lshlrev_b32 v102,16,v191
s_add_u32 s61,s62,10240000
buffer_load_dwordx4 v[36:39],v10,%[srdW],s61 offen sc1
v_and_b32 v103,0xffff0000,v191
v_mul_f32 v96,v96,v240
v_mul_f32 v97,v97,v241
v_mul_f32 v98,v98,v242
s_add_u32 s61,s63,0
buffer_load_dwordx4 v[40:43],v10,%[srdW],s61 offen sc1
v_mul_f32 v99,v99,v243
v_mul_f32 v100,v100,v244
v_mul_f32 v101,v101,v245
v_mul_f32 v102,v102,v246
s_add_u32 s61,s63,10240000
buffer_load_dwordx4 v[44:47],v10,%[srdW],s61 offen sc1
v_mul_f32 v103,v103,v247
v_cvt_pk_bf16_f32 v188,v96,v97
v_cvt_pk_bf16_f32 v189,v98,v99
v_cvt_pk_bf16_f32 v190,v100,v101
s_add_u32 s61,s64,0
buffer_load_dwordx4 v[48:51],v10,%[srdW],s61 offen sc1
v_cvt_pk_bf16_f32 v191,v102,v103
ds_write_b128 v5,v[188:191] offset:2304
ds_read_b128 v[184:187],v6 offset:2176
ds_read_b128 v[188:191],v6 offset:3264
s_add_u32 s61,s64,10240000
buffer_load_dwordx4 v[52:55],v10,%[srdW],s61 offen sc1
s_nop 1
v_mfma_f32_4x4x4_16b_bf16 v[232:235],v[208:209],v[152:153],0
v_add_u32 v16,16,v13
v_mfma_f32_4x4x4_16b_bf16 v[236:239],v[208:209],v[154:155],0
v_cmp_gt_u32 vcc,4000,v16
v_mfma_f32_4x4x4_16b_bf16 v[240:243],v[208:209],v[156:157],0
v_add_u32 v17,20480,v12
v_mfma_f32_4x4x4_16b_bf16 v[244:247],v[208:209],v[158:159],0
s_waitcnt lgkmcnt(0)
v_cndmask_b32 v18,v20,v17,vcc
v_mfma_f32_4x4x4_16b_bf16 v[232:235],v[210:211],v[154:155],v[232:235]
buffer_store_dwordx4 v[184:187],v18,%[srdW],0 offen sc1
v_mfma_f32_4x4x4_16b_bf16 v[236:239],v[210:211],v[156:157],v[236:239]
v_add_u32 v16,144,v13
v_mfma_f32_4x4x4_16b_bf16 v[240:243],v[210:211],v[158:159],v[240:243]
v_cmp_gt_u32 vcc,4000,v16
v_mfma_f32_4x4x4_16b_bf16 v[244:247],v[210:211],v[160:161],v[244:247]
v_add_u32 v17,184320,v12
v_mfma_f32_4x4x4_16b_bf16 v[232:235],v[212:213],v[156:157],v[232:235]
v_cndmask_b32 v19,v20,v17,vcc
v_mfma_f32_4x4x4_16b_bf16 v[236:239],v[212:213],v[158:159],v[236:239]
buffer_store_dwordx4 v[188:191],v19,%[srdW],0 offen sc1
v_mfma_f32_4x4x4_16b_bf16 v[240:243],v[212:213],v[160:161],v[240:243]
v_lshlrev_b32 v96,16,v192
v_mfma_f32_4x4x4_16b_bf16 v[244:247],v[212:213],v[162:163],v[244:247]
v_and_b32 v97,0xffff0000,v192
v_mfma_f32_4x4x4_16b_bf16 v[232:235],v[214:215],v[158:159],v[232:235]
v_lshlrev_b32 v98,16,v193
v_mfma_f32_4x4x4_16b_bf16 v[236:239],v[214:215],v[160:161],v[236:239]
v_and_b32 v99,0xffff0000,v193
v_mfma_f32_4x4x4_16b_bf16 v[240:243],v[214:215],v[162:163],v[240:243]
s_add_u32 s61,s65,0
v_mfma_f32_4x4x4_16b_bf16 v[244:247],v[214:215],v[164:165],v[244:247]
buffer_load_dwordx4 v[56:59],v10,%[srdW],s61 offen sc1
v_mfma_f32_4x4x4_16b_bf16 v[232:235],v[216:217],v[160:161],v[232:235]
v_lshlrev_b32 v100,16,v194
v_mfma_f32_4x4x4_16b_bf16 v[236:239],v[216:217],v[162:163],v[236:239]
v_and_b32 v101,0xffff0000,v194
v_mfma_f32_4x4x4_16b_bf16 v[240:243],v[216:217],v[164:165],v[240:243]
v_lshlrev_b32 v102,16,v195
v_mfma_f32_4x4x4_16b_bf16 v[244:247],v[216:217],v[166:167],v[244:247]
v_and_b32 v103,0xffff0000,v195
v_mfma_f32_4x4x4_16b_bf16 v[232:235],v[218:219],v[162:163],v[232:235]
s_add_u32 s61,s65,10240000
v_mfma_f32_4x4x4_16b_bf16 v[236:239],v[218:219],v[164:165],v[236:239]
buffer_load_dwordx4 v[60:63],v10,%[srdW],s61 offen sc1
v_mfma_f32_4x4x4_16b_bf16 v[240:243],v[218:219],v[166:167],v[240:243]
v_mul_f32 v96,v96,v112
v_mfma_f32_4x4x4_16b_bf16 v[244:247],v[218:219],v[168:169],v[244:247]
v_mul_f32 v97,v97,v113
v_mfma_f32_4x4x4_16b_bf16 v[232:235],v[220:221],v[164:165],v[232:235]
v_mul_f32 v98,v98,v114
v_mfma_f32_4x4x4_16b_bf16 v[236:239],v[220:221],v[166:167],v[236:239]
v_mul_f32 v99,v99,v115
v_mfma_f32_4x4x4_16b_bf16 v[240:243],v[220:221],v[168:169],v[240:243]
s_add_u32 s61,s66,0
v_mfma_f32_4x4x4_16b_bf16 v[244:247],v[220:221],v[170:171],v[244:247]
buffer_load_dwordx4 v[64:67],v10,%[srdW],s61 offen sc1
v_mfma_f32_4x4x4_16b_bf16 v[232:235],v[222:223],v[166:167],v[232:235]
v_mul_f32 v100,v100,v116
v_mfma_f32_4x4x4_16b_bf16 v[236:239],v[222:223],v[168:169],v[236:239]
v_mul_f32 v101,v101,v117
v_mfma_f32_4x4x4_16b_bf16 v[240:243],v[222:223],v[170:171],v[240:243]
v_mul_f32 v102,v102,v118
v_mfma_f32_4x4x4_16b_bf16 v[244:247],v[222:223],v[172:173],v[244:247]
v_mul_f32 v103,v103,v119
v_mfma_f32_4x4x4_16b_bf16 v[232:235],v[224:225],v[168:169],v[232:235]
s_add_u32 s61,s66,10240000
v_mfma_f32_4x4x4_16b_bf16 v[236:239],v[224:225],v[170:171],v[236:239]
buffer_load_dwordx4 v[68:71],v10,%[srdW],s61 offen sc1
v_mfma_f32_4x4x4_16b_bf16 v[240:243],v[224:225],v[172:173],v[240:243]
v_cvt_pk_bf16_f32 v192,v96,v97
v_mfma_f32_4x4x4_16b_bf16 v[244:247],v[224:225],v[174:175],v[244:247]
v_cvt_pk_bf16_f32 v193,v98,v99
v_cvt_pk_bf16_f32 v194,v100,v101
v_cvt_pk_bf16_f32 v195,v102,v103
s_add_u32 s61,s62,2048
buffer_load_dwordx4 v[72:75],v10,%[srdW],s61 offen sc1
ds_write_b128 v5,v[192:195] offset:0
v_lshlrev_b32 v96,16,v196
v_and_b32 v97,0xffff0000,v196
v_lshlrev_b32 v98,16,v197
s_add_u32 s61,s62,10242048
buffer_load_dwordx4 v[76:79],v10,%[srdW],s61 offen sc1
v_and_b32 v99,0xffff0000,v197
v_lshlrev_b32 v100,16,v198
v_and_b32 v101,0xffff0000,v198
v_lshlrev_b32 v102,16,v199
s_add_u32 s61,s63,2048
buffer_load_dwordx4 v[80:83],v10,%[srdW],s61 offen sc1
v_and_b32 v103,0xffff0000,v199
v_mul_f32 v96,v96,v120
v_mul_f32 v97,v97,v121
v_mul_f32 v98,v98,v122
s_add_u32 s61,s63,10242048
buffer_load_dwordx4 v[84:87],v10,%[srdW],s61 offen sc1
v_mul_f32 v99,v99,v123
v_mul_f32 v100,v100,v124
v_mul_f32 v101,v101,v125
v_mul_f32 v102,v102,v126
s_add_u32 s61,s64,2048
buffer_load_dwordx4 v[88:91],v10,%[srdW],s61 offen sc1
v_mul_f32 v103,v103,v127
v_cvt_pk_bf16_f32 v196,v96,v97
v_cvt_pk_bf16_f32 v197,v98,v99
v_cvt_pk_bf16_f32 v198,v100,v101
s_add_u32 s61,s64,10242048
buffer_load_dwordx4 v[92:95],v10,%[srdW],s61 offen sc1
v_cvt_pk_bf16_f32 v199,v102,v103
ds_write_b128 v5,v[196:199] offset:128
ds_read_b128 v[192:195],v6 offset:0
ds_read_b128 v[196:199],v6 offset:1088
s_nop 1
v_lshlrev_b32 v96,16,v200
v_and_b32 v97,0xffff0000,v200
v_lshlrev_b32 v98,16,v201
v_and_b32 v99,0xffff0000,v201
v_lshlrev_b32 v100,16,v202
v_and_b32 v101,0xffff0000,v202
v_lshlrev_b32 v102,16,v203
v_and_b32 v103,0xffff0000,v203
v_pk_mul_f32 v[96:97],v[96:97],v[232:233]
v_pk_mul_f32 v[98:99],v[98:99],v[234:235]
v_pk_mul_f32 v[100:101],v[100:101],v[236:237]
v_pk_mul_f32 v[102:103],v[102:103],v[238:239]
v_cvt_pk_bf16_f32 v200,v96,v97
v_cvt_pk_bf16_f32 v201,v98,v99
v_cvt_pk_bf16_f32 v202,v100,v101
v_cvt_pk_bf16_f32 v203,v102,v103
ds_write_b128 v5,v[200:203] offset:2176
v_add_u32 v16,32,v13
v_cmp_gt_u32 vcc,4000,v16
v_add_u32 v17,40960,v12
s_waitcnt lgkmcnt(1)
v_cndmask_b32 v18,v20,v17,vcc
buffer_store_dwordx4 v[192:195],v18,%[srdW],0 offen sc1
v_add_u32 v16,160,v13
v_cmp_gt_u32 vcc,4000,v16
v_add_u32 v17,204800,v12
s_nop 0
v_cndmask_b32 v19,v20,v17,vcc
buffer_store_dwordx4 v[196:199],v19,%[srdW],0 offen sc1
v_lshlrev_b32 v96,16,v204
v_and_b32 v97,0xffff0000,v204
v_lshlrev_b32 v98,16,v205
v_and_b32 v99,0xffff0000,v205
v_lshlrev_b32 v100,16,v206
v_and_b32 v101,0xffff0000,v206
v_lshlrev_b32 v102,16,v207
v_and_b32 v103,0xffff0000,v207
v_pk_mul_f32 v[96:97],v[96:97],v[240:241]
v_pk_mul_f32 v[98:99],v[98:99],v[242:243]
v_pk_mul_f32 v[100:101],v[100:101],v[244:245]
v_pk_mul_f32 v[102:103],v[102:103],v[246:247]
v_cvt_pk_bf16_f32 v204,v96,v97
v_cvt_pk_bf16_f32 v205,v98,v99
v_cvt_pk_bf16_f32 v206,v100,v101
v_cvt_pk_bf16_f32 v207,v102,v103
ds_write_b128 v5,v[204:207] offset:2304
ds_read_b128 v[200:203],v6 offset:2176
ds_read_b128 v[204:207],v6 offset:3264
v_add_u32 v16,48,v13
v_cmp_gt_u32 vcc,4000,v16
v_add_u32 v17,61440,v12
s_waitcnt lgkmcnt(0)
v_cndmask_b32 v18,v20,v17,vcc
buffer_store_dwordx4 v[200:203],v18,%[srdW],0 offen sc1
v_add_u32 v16,176,v13
v_cmp_gt_u32 vcc,4000,v16
v_add_u32 v17,225280,v12
s_nop 0
v_cndmask_b32 v19,v20,v17,vcc
buffer_store_dwordx4 v[204:207],v19,%[srdW],0 offen sc1
v_readlane_b32 s67,%[pv],33
v_readlane_b32 s70,%[pv],1
v_readlane_b32 s61,%[pv],34
s_nop 0
s_cmp_eq_u32 s61,0
s_nop 3
s_cbranch_scc0 P4L_%=
v_mov_b32 v10,%[vL16]
v_add_u32 v11,0x10000,v10
v_add_u32 v12,0x20000,v10
v_readlane_b32 s73,%[pv],35
v_readlane_b32 s77,%[pv],0
v_mov_b32 v20,s70
v_mov_b32 v21,s73
v_readlane_b32 s68,%[pv],36
v_readlane_b32 s71,%[pv],37
s_nop 0
v_add_u32 v13,s68,%[vL16]
v_add_u32 v14,0x10000,v13
v_add_u32 v15,0x20000,v13
s_waitcnt vmcnt(26)
v_max_f32 v16,v128,v130
v_sub_f32 v128,v128,v16
v_sub_f32 v130,v130,v16
v_exp_f32 v128,v128
v_exp_f32 v130,v130
s_nop 0
v_mul_f32 v128,v128,v129
v_mul_f32 v130,v130,v131
v_add_f32 v16,v128,v130
v_rcp_f32 v16,v16
s_nop 0
s_bitcmp1_b32 s71,0
s_cselect_b32 s61,-1,0
v_and_b32 v16,s61,v16
v_mul_f32 v22,v128,v16
v_mul_f32 v23,v130,v16
v_cvt_f16_f32 v22,v22
s_waitcnt vmcnt(25)
v_max_f32 v16,v132,v134
v_sub_f32 v132,v132,v16
v_sub_f32 v134,v134,v16
v_exp_f32 v132,v132
v_exp_f32 v134,v134
s_nop 0
v_mul_f32 v132,v132,v133
v_mul_f32 v134,v134,v135
v_add_f32 v16,v132,v134
v_rcp_f32 v16,v16
)"
R"(s_nop 0
s_bitcmp1_b32 s71,1
s_cselect_b32 s61,-1,0
v_and_b32 v16,s61,v16
v_mul_f32 v24,v132,v16
v_mul_f32 v25,v134,v16
v_cvt_f16_f32 v24,v24
s_waitcnt vmcnt(24)
v_max_f32 v16,v136,v138
v_sub_f32 v136,v136,v16
v_sub_f32 v138,v138,v16
v_exp_f32 v136,v136
v_exp_f32 v138,v138
s_nop 0
v_mul_f32 v136,v136,v137
v_mul_f32 v138,v138,v139
v_add_f32 v16,v136,v138
v_rcp_f32 v16,v16
s_nop 0
s_bitcmp1_b32 s71,2
s_cselect_b32 s61,-1,0
v_and_b32 v16,s61,v16
v_mul_f32 v26,v136,v16
v_mul_f32 v27,v138,v16
v_cvt_f16_f32 v26,v26
s_waitcnt vmcnt(23)
v_max_f32 v16,v140,v142
v_sub_f32 v140,v140,v16
v_sub_f32 v142,v142,v16
v_exp_f32 v140,v140
v_exp_f32 v142,v142
s_nop 0
v_mul_f32 v140,v140,v141
v_mul_f32 v142,v142,v143
v_add_f32 v16,v140,v142
v_rcp_f32 v16,v16
s_nop 0
s_bitcmp1_b32 s71,3
s_cselect_b32 s61,-1,0
v_and_b32 v16,s61,v16
v_mul_f32 v28,v140,v16
v_mul_f32 v29,v142,v16
v_cvt_f16_f32 v28,v28
s_waitcnt vmcnt(22)
v_max_f32 v16,v104,v106
v_sub_f32 v104,v104,v16
v_sub_f32 v106,v106,v16
v_exp_f32 v104,v104
v_exp_f32 v106,v106
s_nop 0
v_mul_f32 v104,v104,v105
v_mul_f32 v106,v106,v107
v_add_f32 v16,v104,v106
v_rcp_f32 v16,v16
s_nop 0
s_bitcmp1_b32 s71,4
s_cselect_b32 s61,-1,0
v_and_b32 v16,s61,v16
v_mul_f32 v30,v104,v16
v_mul_f32 v31,v106,v16
v_cvt_f16_f32 v30,v30
s_waitcnt vmcnt(0)
s_barrier
buffer_store_dword v20,v21,%[srdW],0 offen sc1
v_pk_mul_f16 v32,v32,v22 op_sel_hi:[1,0]
v_pk_mul_f16 v33,v33,v22 op_sel_hi:[1,0]
v_pk_mul_f16 v34,v34,v22 op_sel_hi:[1,0]
v_pk_mul_f16 v35,v35,v22 op_sel_hi:[1,0]
v_fma_mix_f32 v16,v36,v23,v32 op_sel_hi:[1,0,1]
v_fma_mix_f32 v17,v36,v23,v32 op_sel:[1,0,1] op_sel_hi:[1,0,1]
v_cvt_pk_bf16_f32 v32,v16,v17
v_fma_mix_f32 v18,v37,v23,v33 op_sel_hi:[1,0,1]
v_fma_mix_f32 v19,v37,v23,v33 op_sel:[1,0,1] op_sel_hi:[1,0,1]
v_cvt_pk_bf16_f32 v33,v18,v19
v_fma_mix_f32 v16,v38,v23,v34 op_sel_hi:[1,0,1]
v_fma_mix_f32 v17,v38,v23,v34 op_sel:[1,0,1] op_sel_hi:[1,0,1]
v_cvt_pk_bf16_f32 v34,v16,v17
v_fma_mix_f32 v18,v39,v23,v35 op_sel_hi:[1,0,1]
v_fma_mix_f32 v19,v39,v23,v35 op_sel:[1,0,1] op_sel_hi:[1,0,1]
v_cvt_pk_bf16_f32 v35,v18,v19
ds_write_b128 v13,v[32:35] offset:0
s_add_u32 s61,s65,2048
buffer_load_dwordx4 v[32:35],%[vL16],%[srdW],s61 offen sc1
s_add_u32 s61,s65,10242048
buffer_load_dwordx4 v[36:39],%[vL16],%[srdW],s61 offen sc1
v_pk_mul_f16 v40,v40,v24 op_sel_hi:[1,0]
v_pk_mul_f16 v41,v41,v24 op_sel_hi:[1,0]
v_pk_mul_f16 v42,v42,v24 op_sel_hi:[1,0]
v_pk_mul_f16 v43,v43,v24 op_sel_hi:[1,0]
v_fma_mix_f32 v16,v44,v25,v40 op_sel_hi:[1,0,1]
v_fma_mix_f32 v17,v44,v25,v40 op_sel:[1,0,1] op_sel_hi:[1,0,1]
v_cvt_pk_bf16_f32 v40,v16,v17
v_fma_mix_f32 v18,v45,v25,v41 op_sel_hi:[1,0,1]
v_fma_mix_f32 v19,v45,v25,v41 op_sel:[1,0,1] op_sel_hi:[1,0,1]
v_cvt_pk_bf16_f32 v41,v18,v19
v_fma_mix_f32 v16,v46,v25,v42 op_sel_hi:[1,0,1]
v_fma_mix_f32 v17,v46,v25,v42 op_sel:[1,0,1] op_sel_hi:[1,0,1]
v_cvt_pk_bf16_f32 v42,v16,v17
v_fma_mix_f32 v18,v47,v25,v43 op_sel_hi:[1,0,1]
v_fma_mix_f32 v19,v47,v25,v43 op_sel:[1,0,1] op_sel_hi:[1,0,1]
v_cvt_pk_bf16_f32 v43,v18,v19
ds_write_b128 v13,v[40:43] offset:32768
s_add_u32 s61,s66,2048
buffer_load_dwordx4 v[40:43],%[vL16],%[srdW],s61 offen sc1
s_add_u32 s61,s66,10242048
buffer_load_dwordx4 v[44:47],%[vL16],%[srdW],s61 offen sc1
v_pk_mul_f16 v48,v48,v26 op_sel_hi:[1,0]
v_pk_mul_f16 v49,v49,v26 op_sel_hi:[1,0]
v_pk_mul_f16 v50,v50,v26 op_sel_hi:[1,0]
v_pk_mul_f16 v51,v51,v26 op_sel_hi:[1,0]
v_fma_mix_f32 v16,v52,v27,v48 op_sel_hi:[1,0,1]
v_fma_mix_f32 v17,v52,v27,v48 op_sel:[1,0,1] op_sel_hi:[1,0,1]
v_cvt_pk_bf16_f32 v48,v16,v17
v_fma_mix_f32 v18,v53,v27,v49 op_sel_hi:[1,0,1]
v_fma_mix_f32 v19,v53,v27,v49 op_sel:[1,0,1] op_sel_hi:[1,0,1]
v_cvt_pk_bf16_f32 v49,v18,v19
v_fma_mix_f32 v16,v54,v27,v50 op_sel_hi:[1,0,1]
v_fma_mix_f32 v17,v54,v27,v50 op_sel:[1,0,1] op_sel_hi:[1,0,1]
v_cvt_pk_bf16_f32 v50,v16,v17
v_fma_mix_f32 v18,v55,v27,v51 op_sel_hi:[1,0,1]
v_fma_mix_f32 v19,v55,v27,v51 op_sel:[1,0,1] op_sel_hi:[1,0,1]
v_cvt_pk_bf16_f32 v51,v18,v19
ds_write_b128 v14,v[48:51] offset:0
s_add_u32 s61,s62,4096
buffer_load_dwordx4 v[48:51],%[vL16],%[srdW],s61 offen sc1
s_add_u32 s61,s62,10244096
buffer_load_dwordx4 v[52:55],%[vL16],%[srdW],s61 offen sc1
v_pk_mul_f16 v56,v56,v28 op_sel_hi:[1,0]
v_pk_mul_f16 v57,v57,v28 op_sel_hi:[1,0]
v_pk_mul_f16 v58,v58,v28 op_sel_hi:[1,0]
v_pk_mul_f16 v59,v59,v28 op_sel_hi:[1,0]
v_fma_mix_f32 v16,v60,v29,v56 op_sel_hi:[1,0,1]
v_fma_mix_f32 v17,v60,v29,v56 op_sel:[1,0,1] op_sel_hi:[1,0,1]
v_cvt_pk_bf16_f32 v56,v16,v17
v_fma_mix_f32 v18,v61,v29,v57 op_sel_hi:[1,0,1]
v_fma_mix_f32 v19,v61,v29,v57 op_sel:[1,0,1] op_sel_hi:[1,0,1]
v_cvt_pk_bf16_f32 v57,v18,v19
v_fma_mix_f32 v16,v62,v29,v58 op_sel_hi:[1,0,1]
v_fma_mix_f32 v17,v62,v29,v58 op_sel:[1,0,1] op_sel_hi:[1,0,1]
v_cvt_pk_bf16_f32 v58,v16,v17
v_fma_mix_f32 v18,v63,v29,v59 op_sel_hi:[1,0,1]
v_fma_mix_f32 v19,v63,v29,v59 op_sel:[1,0,1] op_sel_hi:[1,0,1]
v_cvt_pk_bf16_f32 v59,v18,v19
ds_write_b128 v14,v[56:59] offset:32768
s_add_u32 s61,s63,4096
buffer_load_dwordx4 v[56:59],%[vL16],%[srdW],s61 offen sc1
s_add_u32 s61,s63,10244096
buffer_load_dwordx4 v[60:63],%[vL16],%[srdW],s61 offen sc1
v_pk_mul_f16 v64,v64,v30 op_sel_hi:[1,0]
v_pk_mul_f16 v65,v65,v30 op_sel_hi:[1,0]
v_pk_mul_f16 v66,v66,v30 op_sel_hi:[1,0]
v_pk_mul_f16 v67,v67,v30 op_sel_hi:[1,0]
v_fma_mix_f32 v16,v68,v31,v64 op_sel_hi:[1,0,1]
v_fma_mix_f32 v17,v68,v31,v64 op_sel:[1,0,1] op_sel_hi:[1,0,1]
v_cvt_pk_bf16_f32 v64,v16,v17
v_fma_mix_f32 v18,v69,v31,v65 op_sel_hi:[1,0,1]
v_fma_mix_f32 v19,v69,v31,v65 op_sel:[1,0,1] op_sel_hi:[1,0,1]
v_cvt_pk_bf16_f32 v65,v18,v19
v_fma_mix_f32 v16,v70,v31,v66 op_sel_hi:[1,0,1]
v_fma_mix_f32 v17,v70,v31,v66 op_sel:[1,0,1] op_sel_hi:[1,0,1]
v_cvt_pk_bf16_f32 v66,v16,v17
v_fma_mix_f32 v18,v71,v31,v67 op_sel_hi:[1,0,1]
v_fma_mix_f32 v19,v71,v31,v67 op_sel:[1,0,1] op_sel_hi:[1,0,1]
v_cvt_pk_bf16_f32 v67,v18,v19
ds_write_b128 v15,v[64:67] offset:0
s_add_u32 s61,s64,4096
buffer_load_dwordx4 v[64:67],%[vL16],%[srdW],s61 offen sc1
s_add_u32 s61,s64,10244096
buffer_load_dwordx4 v[68:71],%[vL16],%[srdW],s61 offen sc1
s_waitcnt lgkmcnt(0)
s_barrier
ds_read_b128 a[0:3],v10 offset:0
ds_read_b128 a[4:7],v10 offset:16384
ds_read_b128 a[8:11],v10 offset:32768
s_waitcnt lgkmcnt(2)
v_mfma_f32_16x16x32_bf16 v[96:99],a[0:3],a[32:35],0
s_add_u32 s60,s60,0x1000
ds_read_b128 a[12:15],v10 offset:49152
v_pk_mul_f16 v72,v72,v22 op_sel_hi:[1,0]
v_mfma_f32_16x16x32_bf16 v[100:103],a[0:3],a[36:39],0
v_pk_mul_f16 v73,v73,v22 op_sel_hi:[1,0]
v_pk_mul_f16 v74,v74,v22 op_sel_hi:[1,0]
v_mfma_f32_16x16x32_bf16 v[104:107],a[0:3],a[40:43],0
buffer_load_dwordx4 a[64:67],%[vL16],%[srdW],s60 offen offset:0 sc1
v_mfma_f32_16x16x32_bf16 v[108:111],a[0:3],a[44:47],0
s_waitcnt lgkmcnt(2)
v_pk_mul_f16 v75,v75,v22 op_sel_hi:[1,0]
v_fma_mix_f32 v16,v76,v23,v72 op_sel_hi:[1,0,1]
v_mfma_f32_16x16x32_bf16 v[112:115],a[4:7],a[32:35],0
ds_read_b128 a[0:3],v11 offset:0
v_fma_mix_f32 v17,v76,v23,v72 op_sel:[1,0,1] op_sel_hi:[1,0,1]
v_mfma_f32_16x16x32_bf16 v[116:119],a[4:7],a[36:39],0
buffer_load_dwordx4 a[68:71],%[vL16],%[srdW],s60 offen offset:1024 sc1
v_mfma_f32_16x16x32_bf16 v[120:123],a[4:7],a[40:43],0
v_cvt_pk_bf16_f32 v72,v16,v17
v_fma_mix_f32 v18,v77,v23,v73 op_sel_hi:[1,0,1]
v_mfma_f32_16x16x32_bf16 v[124:127],a[4:7],a[44:47],0
s_waitcnt lgkmcnt(2)
v_fma_mix_f32 v19,v77,v23,v73 op_sel:[1,0,1] op_sel_hi:[1,0,1]
v_cvt_pk_bf16_f32 v73,v18,v19
v_mfma_f32_16x16x32_bf16 v[128:131],a[8:11],a[32:35],0
ds_read_b128 a[4:7],v11 offset:16384
v_fma_mix_f32 v16,v78,v23,v74 op_sel_hi:[1,0,1]
v_mfma_f32_16x16x32_bf16 v[132:135],a[8:11],a[36:39],0
buffer_load_dwordx4 a[72:75],%[vL16],%[srdW],s60 offen offset:2048 sc1
v_mfma_f32_16x16x32_bf16 v[136:139],a[8:11],a[40:43],0
v_fma_mix_f32 v17,v78,v23,v74 op_sel:[1,0,1] op_sel_hi:[1,0,1]
v_cvt_pk_bf16_f32 v74,v16,v17
v_mfma_f32_16x16x32_bf16 v[140:143],a[8:11],a[44:47],0
s_waitcnt lgkmcnt(2)
v_fma_mix_f32 v18,v79,v23,v75 op_sel_hi:[1,0,1]
v_fma_mix_f32 v19,v79,v23,v75 op_sel:[1,0,1] op_sel_hi:[1,0,1]
v_mfma_f32_16x16x32_bf16 v[144:147],a[12:15],a[32:35],0
ds_read_b128 a[8:11],v11 offset:32768
v_cvt_pk_bf16_f32 v75,v18,v19
v_mfma_f32_16x16x32_bf16 v[148:151],a[12:15],a[36:39],0
buffer_load_dwordx4 a[76:79],%[vL16],%[srdW],s60 offen offset:3072 sc1
v_mfma_f32_16x16x32_bf16 v[152:155],a[12:15],a[40:43],0
ds_write_b128 v13,v[72:75] offset:2048
s_add_u32 s61,s65,4096
v_mfma_f32_16x16x32_bf16 v[156:159],a[12:15],a[44:47],0
s_waitcnt lgkmcnt(3)
v_pk_mul_f16 v80,v80,v24 op_sel_hi:[1,0]
v_pk_mul_f16 v81,v81,v24 op_sel_hi:[1,0]
v_mfma_f32_16x16x32_bf16 v[160:163],a[0:3],a[32:35],0
ds_read_b128 a[12:15],v11 offset:49152
v_pk_mul_f16 v82,v82,v24 op_sel_hi:[1,0]
v_mfma_f32_16x16x32_bf16 v[164:167],a[0:3],a[36:39],0
buffer_load_dwordx4 v[72:75],%[vL16],%[srdW],s61 offen sc1
v_mfma_f32_16x16x32_bf16 v[168:171],a[0:3],a[40:43],0
s_add_u32 s61,s65,10244096
v_pk_mul_f16 v83,v83,v24 op_sel_hi:[1,0]
v_fma_mix_f32 v16,v84,v25,v80 op_sel_hi:[1,0,1]
v_mfma_f32_16x16x32_bf16 v[172:175],a[0:3],a[44:47],0
s_waitcnt lgkmcnt(3)
v_fma_mix_f32 v17,v84,v25,v80 op_sel:[1,0,1] op_sel_hi:[1,0,1]
v_cvt_pk_bf16_f32 v80,v16,v17
v_mfma_f32_16x16x32_bf16 v[176:179],a[4:7],a[32:35],0
ds_read_b128 a[0:3],v12 offset:0
v_fma_mix_f32 v18,v85,v25,v81 op_sel_hi:[1,0,1]
v_mfma_f32_16x16x32_bf16 v[180:183],a[4:7],a[36:39],0
buffer_load_dwordx4 v[76:79],%[vL16],%[srdW],s61 offen sc1
v_mfma_f32_16x16x32_bf16 v[184:187],a[4:7],a[40:43],0
v_fma_mix_f32 v19,v85,v25,v81 op_sel:[1,0,1] op_sel_hi:[1,0,1]
v_cvt_pk_bf16_f32 v81,v18,v19
v_mfma_f32_16x16x32_bf16 v[188:191],a[4:7],a[44:47],0
s_waitcnt lgkmcnt(3)
v_fma_mix_f32 v16,v86,v25,v82 op_sel_hi:[1,0,1]
v_fma_mix_f32 v17,v86,v25,v82 op_sel:[1,0,1] op_sel_hi:[1,0,1]
v_mfma_f32_16x16x32_bf16 v[192:195],a[8:11],a[32:35],0
ds_read_b128 a[4:7],v12 offset:16384
v_cvt_pk_bf16_f32 v82,v16,v17
v_mfma_f32_16x16x32_bf16 v[196:199],a[8:11],a[36:39],0
v_fma_mix_f32 v18,v87,v25,v83 op_sel_hi:[1,0,1]
v_fma_mix_f32 v19,v87,v25,v83 op_sel:[1,0,1] op_sel_hi:[1,0,1]
v_mfma_f32_16x16x32_bf16 v[200:203],a[8:11],a[40:43],0
v_cvt_pk_bf16_f32 v83,v18,v19
v_mfma_f32_16x16x32_bf16 v[204:207],a[8:11],a[44:47],0
s_waitcnt lgkmcnt(2)
ds_write_b128 v13,v[80:83] offset:34816
s_add_u32 s61,s66,4096
v_mfma_f32_16x16x32_bf16 v[208:211],a[12:15],a[32:35],0
ds_read_b128 a[8:11],v10 offset:1024
v_pk_mul_f16 v88,v88,v26 op_sel_hi:[1,0]
v_mfma_f32_16x16x32_bf16 v[212:215],a[12:15],a[36:39],0
buffer_load_dwordx4 v[80:83],%[vL16],%[srdW],s61 offen sc1
v_mfma_f32_16x16x32_bf16 v[216:219],a[12:15],a[40:43],0
s_add_u32 s61,s66,10244096
v_pk_mul_f16 v89,v89,v26 op_sel_hi:[1,0]
v_pk_mul_f16 v90,v90,v26 op_sel_hi:[1,0]
v_mfma_f32_16x16x32_bf16 v[220:223],a[12:15],a[44:47],0
s_waitcnt lgkmcnt(3)
v_pk_mul_f16 v91,v91,v26 op_sel_hi:[1,0]
v_fma_mix_f32 v16,v92,v27,v88 op_sel_hi:[1,0,1]
v_mfma_f32_16x16x32_bf16 v[224:227],a[0:3],a[32:35],0
ds_read_b128 a[12:15],v10 offset:17408
v_fma_mix_f32 v17,v92,v27,v88 op_sel:[1,0,1] op_sel_hi:[1,0,1]
v_mfma_f32_16x16x32_bf16 v[228:231],a[0:3],a[36:39],0
buffer_load_dwordx4 v[84:87],%[vL16],%[srdW],s61 offen sc1
v_mfma_f32_16x16x32_bf16 v[232:235],a[0:3],a[40:43],0
v_cvt_pk_bf16_f32 v88,v16,v17
v_fma_mix_f32 v18,v93,v27,v89 op_sel_hi:[1,0,1]
v_mfma_f32_16x16x32_bf16 v[236:239],a[0:3],a[44:47],0
s_waitcnt lgkmcnt(3)
v_fma_mix_f32 v19,v93,v27,v89 op_sel:[1,0,1] op_sel_hi:[1,0,1]
v_cvt_pk_bf16_f32 v89,v18,v19
v_mfma_f32_16x16x32_bf16 v[240:243],a[4:7],a[32:35],0
ds_read_b128 a[0:3],v10 offset:33792
v_fma_mix_f32 v16,v94,v27,v90 op_sel_hi:[1,0,1]
v_mfma_f32_16x16x32_bf16 v[244:247],a[4:7],a[36:39],0
v_fma_mix_f32 v17,v94,v27,v90 op_sel:[1,0,1] op_sel_hi:[1,0,1]
v_cvt_pk_bf16_f32 v90,v16,v17
v_mfma_f32_16x16x32_bf16 v[248:251],a[4:7],a[40:43],0
v_fma_mix_f32 v18,v95,v27,v91 op_sel_hi:[1,0,1]
v_fma_mix_f32 v19,v95,v27,v91 op_sel:[1,0,1] op_sel_hi:[1,0,1]
v_mfma_f32_16x16x32_bf16 v[252:255],a[4:7],a[44:47],0
s_waitcnt lgkmcnt(2)
v_cvt_pk_bf16_f32 v91,v18,v19
v_mfma_f32_16x16x32_bf16 v[96:99],a[8:11],a[48:51],v[96:99]
s_add_u32 s60,s60,0x1000
ds_read_b128 a[4:7],v10 offset:50176
ds_write_b128 v14,v[88:91] offset:2048
v_mfma_f32_16x16x32_bf16 v[100:103],a[8:11],a[52:55],v[100:103]
s_waitcnt vmcnt(16)
buffer_load_dwordx4 a[80:83],%[vL16],%[srdW],s60 offen offset:0 sc1
v_mfma_f32_16x16x32_bf16 v[104:107],a[8:11],a[56:59],v[104:107]
v_pk_mul_f16 v32,v32,v28 op_sel_hi:[1,0]
v_pk_mul_f16 v33,v33,v28 op_sel_hi:[1,0]
v_mfma_f32_16x16x32_bf16 v[108:111],a[8:11],a[60:63],v[108:111]
s_waitcnt lgkmcnt(3)
v_pk_mul_f16 v34,v34,v28 op_sel_hi:[1,0]
v_pk_mul_f16 v35,v35,v28 op_sel_hi:[1,0]
v_mfma_f32_16x16x32_bf16 v[112:115],a[12:15],a[48:51],v[112:115]
ds_read_b128 a[8:11],v11 offset:1024
v_fma_mix_f32 v16,v36,v29,v32 op_sel_hi:[1,0,1]
v_mfma_f32_16x16x32_bf16 v[116:119],a[12:15],a[52:55],v[116:119]
buffer_load_dwordx4 a[84:87],%[vL16],%[srdW],s60 offen offset:1024 sc1
v_mfma_f32_16x16x32_bf16 v[120:123],a[12:15],a[56:59],v[120:123]
v_fma_mix_f32 v17,v36,v29,v32 op_sel:[1,0,1] op_sel_hi:[1,0,1]
v_cvt_pk_bf16_f32 v32,v16,v17
v_mfma_f32_16x16x32_bf16 v[124:127],a[12:15],a[60:63],v[124:127]
s_waitcnt lgkmcnt(3)
v_fma_mix_f32 v18,v37,v29,v33 op_sel_hi:[1,0,1]
v_fma_mix_f32 v19,v37,v29,v33 op_sel:[1,0,1] op_sel_hi:[1,0,1]
v_mfma_f32_16x16x32_bf16 v[128:131],a[0:3],a[48:51],v[128:131]
ds_read_b128 a[12:15],v11 offset:17408
v_cvt_pk_bf16_f32 v33,v18,v19
v_mfma_f32_16x16x32_bf16 v[132:135],a[0:3],a[52:55],v[132:135]
buffer_load_dwordx4 a[88:91],%[vL16],%[srdW],s60 offen offset:2048 sc1
v_mfma_f32_16x16x32_bf16 v[136:139],a[0:3],a[56:59],v[136:139]
v_fma_mix_f32 v16,v38,v29,v34 op_sel_hi:[1,0,1]
v_fma_mix_f32 v17,v38,v29,v34 op_sel:[1,0,1] op_sel_hi:[1,0,1]
v_mfma_f32_16x16x32_bf16 v[140:143],a[0:3],a[60:63],v[140:143]
s_waitcnt lgkmcnt(3)
v_cvt_pk_bf16_f32 v34,v16,v17
v_fma_mix_f32 v18,v39,v29,v35 op_sel_hi:[1,0,1]
v_mfma_f32_16x16x32_bf16 v[144:147],a[4:7],a[48:51],v[144:147]
ds_read_b128 a[0:3],v11 offset:33792
v_fma_mix_f32 v19,v39,v29,v35 op_sel:[1,0,1] op_sel_hi:[1,0,1]
v_mfma_f32_16x16x32_bf16 v[148:151],a[4:7],a[52:55],v[148:151]
buffer_load_dwordx4 a[92:95],%[vL16],%[srdW],s60 offen offset:3072 sc1
v_mfma_f32_16x16x32_bf16 v[152:155],a[4:7],a[56:59],v[152:155]
s_add_u32 s61,s62,6144
v_cvt_pk_bf16_f32 v35,v18,v19
v_mfma_f32_16x16x32_bf16 v[156:159],a[4:7],a[60:63],v[156:159]
s_waitcnt lgkmcnt(2)
ds_write_b128 v14,v[32:35] offset:34816
s_waitcnt vmcnt(18)
v_mfma_f32_16x16x32_bf16 v[160:163],a[8:11],a[48:51],v[160:163]
ds_read_b128 a[4:7],v11 offset:50176
v_pk_mul_f16 v40,v40,v30 op_sel_hi:[1,0]
v_mfma_f32_16x16x32_bf16 v[164:167],a[8:11],a[52:55],v[164:167]
buffer_load_dwordx4 v[88:91],%[vL16],%[srdW],s61 offen sc1
v_mfma_f32_16x16x32_bf16 v[168:171],a[8:11],a[56:59],v[168:171]
s_add_u32 s61,s62,10246144
v_pk_mul_f16 v41,v41,v30 op_sel_hi:[1,0]
v_pk_mul_f16 v42,v42,v30 op_sel_hi:[1,0]
v_mfma_f32_16x16x32_bf16 v[172:175],a[8:11],a[60:63],v[172:175]
s_waitcnt lgkmcnt(3)
v_pk_mul_f16 v43,v43,v30 op_sel_hi:[1,0]
v_fma_mix_f32 v16,v44,v31,v40 op_sel_hi:[1,0,1]
v_mfma_f32_16x16x32_bf16 v[176:179],a[12:15],a[48:51],v[176:179]
ds_read_b128 a[8:11],v12 offset:1024
v_fma_mix_f32 v17,v44,v31,v40 op_sel:[1,0,1] op_sel_hi:[1,0,1]
v_mfma_f32_16x16x32_bf16 v[180:183],a[12:15],a[52:55],v[180:183]
buffer_load_dwordx4 v[92:95],%[vL16],%[srdW],s61 offen sc1
v_mfma_f32_16x16x32_bf16 v[184:187],a[12:15],a[56:59],v[184:187]
s_add_u32 s61,s63,6144
v_cvt_pk_bf16_f32 v40,v16,v17
v_fma_mix_f32 v18,v45,v31,v41 op_sel_hi:[1,0,1]
v_mfma_f32_16x16x32_bf16 v[188:191],a[12:15],a[60:63],v[188:191]
s_waitcnt lgkmcnt(3)
v_fma_mix_f32 v19,v45,v31,v41 op_sel:[1,0,1] op_sel_hi:[1,0,1]
v_cvt_pk_bf16_f32 v41,v18,v19
v_mfma_f32_16x16x32_bf16 v[192:195],a[0:3],a[48:51],v[192:195]
ds_read_b128 a[12:15],v12 offset:17408
v_fma_mix_f32 v16,v46,v31,v42 op_sel_hi:[1,0,1]
v_mfma_f32_16x16x32_bf16 v[196:199],a[0:3],a[52:55],v[196:199]
buffer_load_dwordx4 v[32:35],%[vL16],%[srdW],s61 offen sc1
v_mfma_f32_16x16x32_bf16 v[200:203],a[0:3],a[56:59],v[200:203]
s_add_u32 s61,s63,10246144
v_fma_mix_f32 v17,v46,v31,v42 op_sel:[1,0,1] op_sel_hi:[1,0,1]
v_cvt_pk_bf16_f32 v42,v16,v17
v_mfma_f32_16x16x32_bf16 v[204:207],a[0:3],a[60:63],v[204:207]
s_waitcnt lgkmcnt(2)
v_fma_mix_f32 v18,v47,v31,v43 op_sel_hi:[1,0,1]
v_fma_mix_f32 v19,v47,v31,v43 op_sel:[1,0,1] op_sel_hi:[1,0,1]
v_mfma_f32_16x16x32_bf16 v[208:211],a[4:7],a[48:51],v[208:211]
v_cvt_pk_bf16_f32 v43,v18,v19
ds_write_b128 v15,v[40:43] offset:2048
s_waitcnt lgkmcnt(0)
s_barrier
ds_read_b128 a[0:3],v10 offset:2048
v_mfma_f32_16x16x32_bf16 v[212:215],a[4:7],a[52:55],v[212:215]
buffer_load_dwordx4 v[36:39],%[vL16],%[srdW],s61 offen sc1
v_mfma_f32_16x16x32_bf16 v[216:219],a[4:7],a[56:59],v[216:219]
s_add_u32 s61,s64,6144
v_mfma_f32_16x16x32_bf16 v[220:223],a[4:7],a[60:63],v[220:223]
v_mfma_f32_16x16x32_bf16 v[224:227],a[8:11],a[48:51],v[224:227]
ds_read_b128 a[4:7],v10 offset:18432
)"
R"(v_mfma_f32_16x16x32_bf16 v[228:231],a[8:11],a[52:55],v[228:231]
buffer_load_dwordx4 v[40:43],%[vL16],%[srdW],s61 offen sc1
v_mfma_f32_16x16x32_bf16 v[232:235],a[8:11],a[56:59],v[232:235]
s_add_u32 s61,s64,10246144
v_mfma_f32_16x16x32_bf16 v[236:239],a[8:11],a[60:63],v[236:239]
v_mfma_f32_16x16x32_bf16 v[240:243],a[12:15],a[48:51],v[240:243]
ds_read_b128 a[8:11],v10 offset:34816
v_mfma_f32_16x16x32_bf16 v[244:247],a[12:15],a[52:55],v[244:247]
buffer_load_dwordx4 v[44:47],%[vL16],%[srdW],s61 offen sc1
v_mfma_f32_16x16x32_bf16 v[248:251],a[12:15],a[56:59],v[248:251]
v_mfma_f32_16x16x32_bf16 v[252:255],a[12:15],a[60:63],v[252:255]
s_waitcnt lgkmcnt(2)
s_waitcnt vmcnt(17)
v_mfma_f32_16x16x32_bf16 v[96:99],a[0:3],a[64:67],v[96:99]
s_waitcnt vmcnt(16)
ds_read_b128 a[12:15],v10 offset:51200
s_add_u32 s60,s60,0x1000
v_mfma_f32_16x16x32_bf16 v[100:103],a[0:3],a[68:71],v[100:103]
s_waitcnt vmcnt(15)
buffer_load_dwordx4 a[32:35],%[vL16],%[srdW],s60 offen offset:0 sc1
v_mfma_f32_16x16x32_bf16 v[104:107],a[0:3],a[72:75],v[104:107]
s_waitcnt vmcnt(15)
v_pk_mul_f16 v48,v48,v22 op_sel_hi:[1,0]
v_pk_mul_f16 v49,v49,v22 op_sel_hi:[1,0]
v_mfma_f32_16x16x32_bf16 v[108:111],a[0:3],a[76:79],v[108:111]
s_waitcnt lgkmcnt(2)
v_pk_mul_f16 v50,v50,v22 op_sel_hi:[1,0]
v_pk_mul_f16 v51,v51,v22 op_sel_hi:[1,0]
v_mfma_f32_16x16x32_bf16 v[112:115],a[4:7],a[64:67],v[112:115]
ds_read_b128 a[0:3],v11 offset:2048
v_fma_mix_f32 v16,v52,v23,v48 op_sel_hi:[1,0,1]
v_mfma_f32_16x16x32_bf16 v[116:119],a[4:7],a[68:71],v[116:119]
buffer_load_dwordx4 a[36:39],%[vL16],%[srdW],s60 offen offset:1024 sc1
v_mfma_f32_16x16x32_bf16 v[120:123],a[4:7],a[72:75],v[120:123]
v_fma_mix_f32 v17,v52,v23,v48 op_sel:[1,0,1] op_sel_hi:[1,0,1]
v_cvt_pk_bf16_f32 v48,v16,v17
v_mfma_f32_16x16x32_bf16 v[124:127],a[4:7],a[76:79],v[124:127]
s_waitcnt lgkmcnt(2)
v_fma_mix_f32 v18,v53,v23,v49 op_sel_hi:[1,0,1]
v_fma_mix_f32 v19,v53,v23,v49 op_sel:[1,0,1] op_sel_hi:[1,0,1]
v_mfma_f32_16x16x32_bf16 v[128:131],a[8:11],a[64:67],v[128:131]
ds_read_b128 a[4:7],v11 offset:18432
v_cvt_pk_bf16_f32 v49,v18,v19
v_mfma_f32_16x16x32_bf16 v[132:135],a[8:11],a[68:71],v[132:135]
buffer_load_dwordx4 a[40:43],%[vL16],%[srdW],s60 offen offset:2048 sc1
v_mfma_f32_16x16x32_bf16 v[136:139],a[8:11],a[72:75],v[136:139]
v_fma_mix_f32 v16,v54,v23,v50 op_sel_hi:[1,0,1]
v_fma_mix_f32 v17,v54,v23,v50 op_sel:[1,0,1] op_sel_hi:[1,0,1]
v_mfma_f32_16x16x32_bf16 v[140:143],a[8:11],a[76:79],v[140:143]
s_waitcnt lgkmcnt(2)
v_cvt_pk_bf16_f32 v50,v16,v17
v_fma_mix_f32 v18,v55,v23,v51 op_sel_hi:[1,0,1]
v_mfma_f32_16x16x32_bf16 v[144:147],a[12:15],a[64:67],v[144:147]
ds_read_b128 a[8:11],v11 offset:34816
v_fma_mix_f32 v19,v55,v23,v51 op_sel:[1,0,1] op_sel_hi:[1,0,1]
v_mfma_f32_16x16x32_bf16 v[148:151],a[12:15],a[68:71],v[148:151]
buffer_load_dwordx4 a[44:47],%[vL16],%[srdW],s60 offen offset:3072 sc1
v_mfma_f32_16x16x32_bf16 v[152:155],a[12:15],a[72:75],v[152:155]
v_cvt_pk_bf16_f32 v51,v18,v19
v_mfma_f32_16x16x32_bf16 v[156:159],a[12:15],a[76:79],v[156:159]
s_waitcnt lgkmcnt(2)
ds_write_b128 v13,v[48:51] offset:4096
s_add_u32 s61,s65,6144
v_mfma_f32_16x16x32_bf16 v[160:163],a[0:3],a[64:67],v[160:163]
ds_read_b128 a[12:15],v11 offset:51200
v_pk_mul_f16 v56,v56,v24 op_sel_hi:[1,0]
v_mfma_f32_16x16x32_bf16 v[164:167],a[0:3],a[68:71],v[164:167]
buffer_load_dwordx4 v[48:51],%[vL16],%[srdW],s61 offen sc1
v_mfma_f32_16x16x32_bf16 v[168:171],a[0:3],a[72:75],v[168:171]
s_add_u32 s61,s65,10246144
v_pk_mul_f16 v57,v57,v24 op_sel_hi:[1,0]
v_pk_mul_f16 v58,v58,v24 op_sel_hi:[1,0]
v_mfma_f32_16x16x32_bf16 v[172:175],a[0:3],a[76:79],v[172:175]
s_waitcnt lgkmcnt(3)
v_pk_mul_f16 v59,v59,v24 op_sel_hi:[1,0]
v_fma_mix_f32 v16,v60,v25,v56 op_sel_hi:[1,0,1]
v_mfma_f32_16x16x32_bf16 v[176:179],a[4:7],a[64:67],v[176:179]
ds_read_b128 a[0:3],v12 offset:2048
v_fma_mix_f32 v17,v60,v25,v56 op_sel:[1,0,1] op_sel_hi:[1,0,1]
v_mfma_f32_16x16x32_bf16 v[180:183],a[4:7],a[68:71],v[180:183]
buffer_load_dwordx4 v[52:55],%[vL16],%[srdW],s61 offen sc1
v_mfma_f32_16x16x32_bf16 v[184:187],a[4:7],a[72:75],v[184:187]
v_cvt_pk_bf16_f32 v56,v16,v17
v_fma_mix_f32 v18,v61,v25,v57 op_sel_hi:[1,0,1]
v_mfma_f32_16x16x32_bf16 v[188:191],a[4:7],a[76:79],v[188:191]
s_waitcnt lgkmcnt(3)
v_fma_mix_f32 v19,v61,v25,v57 op_sel:[1,0,1] op_sel_hi:[1,0,1]
v_cvt_pk_bf16_f32 v57,v18,v19
v_mfma_f32_16x16x32_bf16 v[192:195],a[8:11],a[64:67],v[192:195]
ds_read_b128 a[4:7],v12 offset:18432
v_fma_mix_f32 v16,v62,v25,v58 op_sel_hi:[1,0,1]
v_mfma_f32_16x16x32_bf16 v[196:199],a[8:11],a[68:71],v[196:199]
v_fma_mix_f32 v17,v62,v25,v58 op_sel:[1,0,1] op_sel_hi:[1,0,1]
v_cvt_pk_bf16_f32 v58,v16,v17
v_mfma_f32_16x16x32_bf16 v[200:203],a[8:11],a[72:75],v[200:203]
v_fma_mix_f32 v18,v63,v25,v59 op_sel_hi:[1,0,1]
v_fma_mix_f32 v19,v63,v25,v59 op_sel:[1,0,1] op_sel_hi:[1,0,1]
v_mfma_f32_16x16x32_bf16 v[204:207],a[8:11],a[76:79],v[204:207]
s_waitcnt lgkmcnt(2)
v_cvt_pk_bf16_f32 v59,v18,v19
v_mfma_f32_16x16x32_bf16 v[208:211],a[12:15],a[64:67],v[208:211]
ds_read_b128 a[8:11],v10 offset:3072
ds_write_b128 v13,v[56:59] offset:36864
v_mfma_f32_16x16x32_bf16 v[212:215],a[12:15],a[68:71],v[212:215]
s_add_u32 s61,s66,6144
v_pk_mul_f16 v64,v64,v26 op_sel_hi:[1,0]
v_mfma_f32_16x16x32_bf16 v[216:219],a[12:15],a[72:75],v[216:219]
buffer_load_dwordx4 v[56:59],%[vL16],%[srdW],s61 offen sc1
v_mfma_f32_16x16x32_bf16 v[220:223],a[12:15],a[76:79],v[220:223]
s_waitcnt lgkmcnt(3)
s_add_u32 s61,s66,10246144
v_pk_mul_f16 v65,v65,v26 op_sel_hi:[1,0]
v_mfma_f32_16x16x32_bf16 v[224:227],a[0:3],a[64:67],v[224:227]
ds_read_b128 a[12:15],v10 offset:19456
v_pk_mul_f16 v66,v66,v26 op_sel_hi:[1,0]
v_mfma_f32_16x16x32_bf16 v[228:231],a[0:3],a[68:71],v[228:231]
buffer_load_dwordx4 v[60:63],%[vL16],%[srdW],s61 offen sc1
v_mfma_f32_16x16x32_bf16 v[232:235],a[0:3],a[72:75],v[232:235]
v_pk_mul_f16 v67,v67,v26 op_sel_hi:[1,0]
v_fma_mix_f32 v16,v68,v27,v64 op_sel_hi:[1,0,1]
v_mfma_f32_16x16x32_bf16 v[236:239],a[0:3],a[76:79],v[236:239]
s_waitcnt lgkmcnt(3)
v_fma_mix_f32 v17,v68,v27,v64 op_sel:[1,0,1] op_sel_hi:[1,0,1]
v_cvt_pk_bf16_f32 v64,v16,v17
v_mfma_f32_16x16x32_bf16 v[240:243],a[4:7],a[64:67],v[240:243]
ds_read_b128 a[0:3],v10 offset:35840
v_fma_mix_f32 v18,v69,v27,v65 op_sel_hi:[1,0,1]
v_mfma_f32_16x16x32_bf16 v[244:247],a[4:7],a[68:71],v[244:247]
v_fma_mix_f32 v19,v69,v27,v65 op_sel:[1,0,1] op_sel_hi:[1,0,1]
v_cvt_pk_bf16_f32 v65,v18,v19
v_mfma_f32_16x16x32_bf16 v[248:251],a[4:7],a[72:75],v[248:251]
v_fma_mix_f32 v16,v70,v27,v66 op_sel_hi:[1,0,1]
v_fma_mix_f32 v17,v70,v27,v66 op_sel:[1,0,1] op_sel_hi:[1,0,1]
v_mfma_f32_16x16x32_bf16 v[252:255],a[4:7],a[76:79],v[252:255]
s_waitcnt lgkmcnt(3)
s_waitcnt vmcnt(17)
v_cvt_pk_bf16_f32 v66,v16,v17
v_mfma_f32_16x16x32_bf16 v[96:99],a[8:11],a[80:83],v[96:99]
s_waitcnt vmcnt(16)
ds_read_b128 a[4:7],v10 offset:52224
s_add_u32 s60,s60,0x1000
v_mfma_f32_16x16x32_bf16 v[100:103],a[8:11],a[84:87],v[100:103]
s_waitcnt vmcnt(15)
buffer_load_dwordx4 a[48:51],%[vL16],%[srdW],s60 offen offset:0 sc1
v_mfma_f32_16x16x32_bf16 v[104:107],a[8:11],a[88:91],v[104:107]
s_waitcnt vmcnt(15)
v_fma_mix_f32 v18,v71,v27,v67 op_sel_hi:[1,0,1]
v_fma_mix_f32 v19,v71,v27,v67 op_sel:[1,0,1] op_sel_hi:[1,0,1]
v_mfma_f32_16x16x32_bf16 v[108:111],a[8:11],a[92:95],v[108:111]
s_waitcnt lgkmcnt(2)
v_cvt_pk_bf16_f32 v67,v18,v19
v_mfma_f32_16x16x32_bf16 v[112:115],a[12:15],a[80:83],v[112:115]
ds_read_b128 a[8:11],v11 offset:3072
ds_write_b128 v14,v[64:67] offset:4096
v_mfma_f32_16x16x32_bf16 v[116:119],a[12:15],a[84:87],v[116:119]
buffer_load_dwordx4 a[52:55],%[vL16],%[srdW],s60 offen offset:1024 sc1
v_mfma_f32_16x16x32_bf16 v[120:123],a[12:15],a[88:91],v[120:123]
v_pk_mul_f16 v72,v72,v28 op_sel_hi:[1,0]
v_pk_mul_f16 v73,v73,v28 op_sel_hi:[1,0]
v_mfma_f32_16x16x32_bf16 v[124:127],a[12:15],a[92:95],v[124:127]
s_waitcnt lgkmcnt(3)
v_pk_mul_f16 v74,v74,v28 op_sel_hi:[1,0]
v_pk_mul_f16 v75,v75,v28 op_sel_hi:[1,0]
v_mfma_f32_16x16x32_bf16 v[128:131],a[0:3],a[80:83],v[128:131]
ds_read_b128 a[12:15],v11 offset:19456
v_fma_mix_f32 v16,v76,v29,v72 op_sel_hi:[1,0,1]
v_mfma_f32_16x16x32_bf16 v[132:135],a[0:3],a[84:87],v[132:135]
buffer_load_dwordx4 a[56:59],%[vL16],%[srdW],s60 offen offset:2048 sc1
v_mfma_f32_16x16x32_bf16 v[136:139],a[0:3],a[88:91],v[136:139]
v_fma_mix_f32 v17,v76,v29,v72 op_sel:[1,0,1] op_sel_hi:[1,0,1]
v_cvt_pk_bf16_f32 v72,v16,v17
v_mfma_f32_16x16x32_bf16 v[140:143],a[0:3],a[92:95],v[140:143]
s_waitcnt lgkmcnt(3)
v_fma_mix_f32 v18,v77,v29,v73 op_sel_hi:[1,0,1]
v_fma_mix_f32 v19,v77,v29,v73 op_sel:[1,0,1] op_sel_hi:[1,0,1]
v_mfma_f32_16x16x32_bf16 v[144:147],a[4:7],a[80:83],v[144:147]
ds_read_b128 a[0:3],v11 offset:35840
v_cvt_pk_bf16_f32 v73,v18,v19
v_mfma_f32_16x16x32_bf16 v[148:151],a[4:7],a[84:87],v[148:151]
buffer_load_dwordx4 a[60:63],%[vL16],%[srdW],s60 offen offset:3072 sc1
v_mfma_f32_16x16x32_bf16 v[152:155],a[4:7],a[88:91],v[152:155]
s_add_u32 s61,s62,8192
v_fma_mix_f32 v16,v78,v29,v74 op_sel_hi:[1,0,1]
v_fma_mix_f32 v17,v78,v29,v74 op_sel:[1,0,1] op_sel_hi:[1,0,1]
v_mfma_f32_16x16x32_bf16 v[156:159],a[4:7],a[92:95],v[156:159]
s_waitcnt lgkmcnt(3)
v_cvt_pk_bf16_f32 v74,v16,v17
v_fma_mix_f32 v18,v79,v29,v75 op_sel_hi:[1,0,1]
v_mfma_f32_16x16x32_bf16 v[160:163],a[8:11],a[80:83],v[160:163]
ds_read_b128 a[4:7],v11 offset:52224
v_fma_mix_f32 v19,v79,v29,v75 op_sel:[1,0,1] op_sel_hi:[1,0,1]
v_mfma_f32_16x16x32_bf16 v[164:167],a[8:11],a[84:87],v[164:167]
buffer_load_dwordx4 v[64:67],%[vL16],%[srdW],s61 offen sc1
v_mfma_f32_16x16x32_bf16 v[168:171],a[8:11],a[88:91],v[168:171]
s_add_u32 s61,s62,10248192
v_cvt_pk_bf16_f32 v75,v18,v19
v_mfma_f32_16x16x32_bf16 v[172:175],a[8:11],a[92:95],v[172:175]
s_waitcnt lgkmcnt(2)
ds_write_b128 v14,v[72:75] offset:36864
v_pk_mul_f16 v80,v80,v30 op_sel_hi:[1,0]
v_mfma_f32_16x16x32_bf16 v[176:179],a[12:15],a[80:83],v[176:179]
ds_read_b128 a[8:11],v12 offset:3072
v_pk_mul_f16 v81,v81,v30 op_sel_hi:[1,0]
v_mfma_f32_16x16x32_bf16 v[180:183],a[12:15],a[84:87],v[180:183]
buffer_load_dwordx4 v[68:71],%[vL16],%[srdW],s61 offen sc1
v_mfma_f32_16x16x32_bf16 v[184:187],a[12:15],a[88:91],v[184:187]
s_add_u32 s61,s63,8192
v_pk_mul_f16 v82,v82,v30 op_sel_hi:[1,0]
v_pk_mul_f16 v83,v83,v30 op_sel_hi:[1,0]
v_mfma_f32_16x16x32_bf16 v[188:191],a[12:15],a[92:95],v[188:191]
s_waitcnt lgkmcnt(3)
v_fma_mix_f32 v16,v84,v31,v80 op_sel_hi:[1,0,1]
v_fma_mix_f32 v17,v84,v31,v80 op_sel:[1,0,1] op_sel_hi:[1,0,1]
v_mfma_f32_16x16x32_bf16 v[192:195],a[0:3],a[80:83],v[192:195]
ds_read_b128 a[12:15],v12 offset:19456
v_cvt_pk_bf16_f32 v80,v16,v17
v_mfma_f32_16x16x32_bf16 v[196:199],a[0:3],a[84:87],v[196:199]
buffer_load_dwordx4 v[72:75],%[vL16],%[srdW],s61 offen sc1
v_mfma_f32_16x16x32_bf16 v[200:203],a[0:3],a[88:91],v[200:203]
s_add_u32 s61,s63,10248192
v_fma_mix_f32 v18,v85,v31,v81 op_sel_hi:[1,0,1]
v_fma_mix_f32 v19,v85,v31,v81 op_sel:[1,0,1] op_sel_hi:[1,0,1]
v_mfma_f32_16x16x32_bf16 v[204:207],a[0:3],a[92:95],v[204:207]
s_waitcnt lgkmcnt(3)
v_cvt_pk_bf16_f32 v81,v18,v19
v_fma_mix_f32 v16,v86,v31,v82 op_sel_hi:[1,0,1]
v_mfma_f32_16x16x32_bf16 v[208:211],a[4:7],a[80:83],v[208:211]
v_fma_mix_f32 v17,v86,v31,v82 op_sel:[1,0,1] op_sel_hi:[1,0,1]
v_cvt_pk_bf16_f32 v82,v16,v17
v_fma_mix_f32 v18,v87,v31,v83 op_sel_hi:[1,0,1]
v_fma_mix_f32 v19,v87,v31,v83 op_sel:[1,0,1] op_sel_hi:[1,0,1]
v_cvt_pk_bf16_f32 v83,v18,v19
ds_write_b128 v15,v[80:83] offset:4096
s_waitcnt lgkmcnt(0)
s_barrier
ds_read_b128 a[0:3],v10 offset:4096
v_mfma_f32_16x16x32_bf16 v[212:215],a[4:7],a[84:87],v[212:215]
buffer_load_dwordx4 v[76:79],%[vL16],%[srdW],s61 offen sc1
v_mfma_f32_16x16x32_bf16 v[216:219],a[4:7],a[88:91],v[216:219]
s_add_u32 s61,s64,8192
v_mfma_f32_16x16x32_bf16 v[220:223],a[4:7],a[92:95],v[220:223]
v_mfma_f32_16x16x32_bf16 v[224:227],a[8:11],a[80:83],v[224:227]
ds_read_b128 a[4:7],v10 offset:20480
v_mfma_f32_16x16x32_bf16 v[228:231],a[8:11],a[84:87],v[228:231]
buffer_load_dwordx4 v[80:83],%[vL16],%[srdW],s61 offen sc1
v_mfma_f32_16x16x32_bf16 v[232:235],a[8:11],a[88:91],v[232:235]
s_add_u32 s61,s64,10248192
v_mfma_f32_16x16x32_bf16 v[236:239],a[8:11],a[92:95],v[236:239]
v_mfma_f32_16x16x32_bf16 v[240:243],a[12:15],a[80:83],v[240:243]
ds_read_b128 a[8:11],v10 offset:36864
v_mfma_f32_16x16x32_bf16 v[244:247],a[12:15],a[84:87],v[244:247]
buffer_load_dwordx4 v[84:87],%[vL16],%[srdW],s61 offen sc1
v_mfma_f32_16x16x32_bf16 v[248:251],a[12:15],a[88:91],v[248:251]
v_mfma_f32_16x16x32_bf16 v[252:255],a[12:15],a[92:95],v[252:255]
s_waitcnt lgkmcnt(2)
s_waitcnt vmcnt(17)
v_mfma_f32_16x16x32_bf16 v[96:99],a[0:3],a[32:35],v[96:99]
s_waitcnt vmcnt(16)
ds_read_b128 a[12:15],v10 offset:53248
s_add_u32 s60,s60,0x1000
v_mfma_f32_16x16x32_bf16 v[100:103],a[0:3],a[36:39],v[100:103]
s_waitcnt vmcnt(15)
buffer_load_dwordx4 a[64:67],%[vL16],%[srdW],s60 offen offset:0 sc1
v_mfma_f32_16x16x32_bf16 v[104:107],a[0:3],a[40:43],v[104:107]
s_waitcnt vmcnt(15)
v_pk_mul_f16 v88,v88,v22 op_sel_hi:[1,0]
v_pk_mul_f16 v89,v89,v22 op_sel_hi:[1,0]
v_mfma_f32_16x16x32_bf16 v[108:111],a[0:3],a[44:47],v[108:111]
s_waitcnt lgkmcnt(2)
v_pk_mul_f16 v90,v90,v22 op_sel_hi:[1,0]
v_pk_mul_f16 v91,v91,v22 op_sel_hi:[1,0]
v_mfma_f32_16x16x32_bf16 v[112:115],a[4:7],a[32:35],v[112:115]
ds_read_b128 a[0:3],v11 offset:4096
v_fma_mix_f32 v16,v92,v23,v88 op_sel_hi:[1,0,1]
v_mfma_f32_16x16x32_bf16 v[116:119],a[4:7],a[36:39],v[116:119]
buffer_load_dwordx4 a[68:71],%[vL16],%[srdW],s60 offen offset:1024 sc1
v_mfma_f32_16x16x32_bf16 v[120:123],a[4:7],a[40:43],v[120:123]
v_fma_mix_f32 v17,v92,v23,v88 op_sel:[1,0,1] op_sel_hi:[1,0,1]
v_cvt_pk_bf16_f32 v88,v16,v17
v_mfma_f32_16x16x32_bf16 v[124:127],a[4:7],a[44:47],v[124:127]
s_waitcnt lgkmcnt(2)
v_fma_mix_f32 v18,v93,v23,v89 op_sel_hi:[1,0,1]
v_fma_mix_f32 v19,v93,v23,v89 op_sel:[1,0,1] op_sel_hi:[1,0,1]
v_mfma_f32_16x16x32_bf16 v[128:131],a[8:11],a[32:35],v[128:131]
ds_read_b128 a[4:7],v11 offset:20480
v_cvt_pk_bf16_f32 v89,v18,v19
v_mfma_f32_16x16x32_bf16 v[132:135],a[8:11],a[36:39],v[132:135]
buffer_load_dwordx4 a[72:75],%[vL16],%[srdW],s60 offen offset:2048 sc1
v_mfma_f32_16x16x32_bf16 v[136:139],a[8:11],a[40:43],v[136:139]
v_fma_mix_f32 v16,v94,v23,v90 op_sel_hi:[1,0,1]
v_fma_mix_f32 v17,v94,v23,v90 op_sel:[1,0,1] op_sel_hi:[1,0,1]
v_mfma_f32_16x16x32_bf16 v[140:143],a[8:11],a[44:47],v[140:143]
s_waitcnt lgkmcnt(2)
v_cvt_pk_bf16_f32 v90,v16,v17
v_fma_mix_f32 v18,v95,v23,v91 op_sel_hi:[1,0,1]
v_mfma_f32_16x16x32_bf16 v[144:147],a[12:15],a[32:35],v[144:147]
ds_read_b128 a[8:11],v11 offset:36864
v_fma_mix_f32 v19,v95,v23,v91 op_sel:[1,0,1] op_sel_hi:[1,0,1]
v_mfma_f32_16x16x32_bf16 v[148:151],a[12:15],a[36:39],v[148:151]
buffer_load_dwordx4 a[76:79],%[vL16],%[srdW],s60 offen offset:3072 sc1
v_mfma_f32_16x16x32_bf16 v[152:155],a[12:15],a[40:43],v[152:155]
v_cvt_pk_bf16_f32 v91,v18,v19
v_mfma_f32_16x16x32_bf16 v[156:159],a[12:15],a[44:47],v[156:159]
s_waitcnt lgkmcnt(2)
ds_write_b128 v13,v[88:91] offset:6144
s_add_u32 s61,s65,8192
v_mfma_f32_16x16x32_bf16 v[160:163],a[0:3],a[32:35],v[160:163]
ds_read_b128 a[12:15],v11 offset:53248
v_pk_mul_f16 v32,v32,v24 op_sel_hi:[1,0]
v_mfma_f32_16x16x32_bf16 v[164:167],a[0:3],a[36:39],v[164:167]
buffer_load_dwordx4 v[88:91],%[vL16],%[srdW],s61 offen sc1
v_mfma_f32_16x16x32_bf16 v[168:171],a[0:3],a[40:43],v[168:171]
s_add_u32 s61,s65,10248192
v_pk_mul_f16 v33,v33,v24 op_sel_hi:[1,0]
v_pk_mul_f16 v34,v34,v24 op_sel_hi:[1,0]
v_mfma_f32_16x16x32_bf16 v[172:175],a[0:3],a[44:47],v[172:175]
s_waitcnt lgkmcnt(3)
v_pk_mul_f16 v35,v35,v24 op_sel_hi:[1,0]
v_fma_mix_f32 v16,v36,v25,v32 op_sel_hi:[1,0,1]
v_mfma_f32_16x16x32_bf16 v[176:179],a[4:7],a[32:35],v[176:179]
ds_read_b128 a[0:3],v12 offset:4096
v_fma_mix_f32 v17,v36,v25,v32 op_sel:[1,0,1] op_sel_hi:[1,0,1]
v_mfma_f32_16x16x32_bf16 v[180:183],a[4:7],a[36:39],v[180:183]
buffer_load_dwordx4 v[92:95],%[vL16],%[srdW],s61 offen sc1
v_mfma_f32_16x16x32_bf16 v[184:187],a[4:7],a[40:43],v[184:187]
v_cvt_pk_bf16_f32 v32,v16,v17
v_fma_mix_f32 v18,v37,v25,v33 op_sel_hi:[1,0,1]
v_mfma_f32_16x16x32_bf16 v[188:191],a[4:7],a[44:47],v[188:191]
s_waitcnt lgkmcnt(3)
v_fma_mix_f32 v19,v37,v25,v33 op_sel:[1,0,1] op_sel_hi:[1,0,1]
v_cvt_pk_bf16_f32 v33,v18,v19
v_mfma_f32_16x16x32_bf16 v[192:195],a[8:11],a[32:35],v[192:195]
ds_read_b128 a[4:7],v12 offset:20480
v_fma_mix_f32 v16,v38,v25,v34 op_sel_hi:[1,0,1]
v_mfma_f32_16x16x32_bf16 v[196:199],a[8:11],a[36:39],v[196:199]
v_fma_mix_f32 v17,v38,v25,v34 op_sel:[1,0,1] op_sel_hi:[1,0,1]
v_cvt_pk_bf16_f32 v34,v16,v17
v_mfma_f32_16x16x32_bf16 v[200:203],a[8:11],a[40:43],v[200:203]
v_fma_mix_f32 v18,v39,v25,v35 op_sel_hi:[1,0,1]
v_fma_mix_f32 v19,v39,v25,v35 op_sel:[1,0,1] op_sel_hi:[1,0,1]
v_mfma_f32_16x16x32_bf16 v[204:207],a[8:11],a[44:47],v[204:207]
s_waitcnt lgkmcnt(2)
v_cvt_pk_bf16_f32 v35,v18,v19
v_mfma_f32_16x16x32_bf16 v[208:211],a[12:15],a[32:35],v[208:211]
ds_read_b128 a[8:11],v10 offset:5120
ds_write_b128 v13,v[32:35] offset:38912
v_mfma_f32_16x16x32_bf16 v[212:215],a[12:15],a[36:39],v[212:215]
s_add_u32 s61,s66,8192
v_pk_mul_f16 v40,v40,v26 op_sel_hi:[1,0]
v_mfma_f32_16x16x32_bf16 v[216:219],a[12:15],a[40:43],v[216:219]
buffer_load_dwordx4 v[32:35],%[vL16],%[srdW],s61 offen sc1
v_mfma_f32_16x16x32_bf16 v[220:223],a[12:15],a[44:47],v[220:223]
s_waitcnt lgkmcnt(3)
s_add_u32 s61,s66,10248192
v_pk_mul_f16 v41,v41,v26 op_sel_hi:[1,0]
v_mfma_f32_16x16x32_bf16 v[224:227],a[0:3],a[32:35],v[224:227]
ds_read_b128 a[12:15],v10 offset:21504
v_pk_mul_f16 v42,v42,v26 op_sel_hi:[1,0]
v_mfma_f32_16x16x32_bf16 v[228:231],a[0:3],a[36:39],v[228:231]
buffer_load_dwordx4 v[36:39],%[vL16],%[srdW],s61 offen sc1
v_mfma_f32_16x16x32_bf16 v[232:235],a[0:3],a[40:43],v[232:235]
v_pk_mul_f16 v43,v43,v26 op_sel_hi:[1,0]
v_fma_mix_f32 v16,v44,v27,v40 op_sel_hi:[1,0,1]
v_mfma_f32_16x16x32_bf16 v[236:239],a[0:3],a[44:47],v[236:239]
s_waitcnt lgkmcnt(3)
v_fma_mix_f32 v17,v44,v27,v40 op_sel:[1,0,1] op_sel_hi:[1,0,1]
v_cvt_pk_bf16_f32 v40,v16,v17
v_mfma_f32_16x16x32_bf16 v[240:243],a[4:7],a[32:35],v[240:243]
ds_read_b128 a[0:3],v10 offset:37888
v_fma_mix_f32 v18,v45,v27,v41 op_sel_hi:[1,0,1]
v_mfma_f32_16x16x32_bf16 v[244:247],a[4:7],a[36:39],v[244:247]
v_fma_mix_f32 v19,v45,v27,v41 op_sel:[1,0,1] op_sel_hi:[1,0,1]
v_cvt_pk_bf16_f32 v41,v18,v19
v_mfma_f32_16x16x32_bf16 v[248:251],a[4:7],a[40:43],v[248:251]
v_fma_mix_f32 v16,v46,v27,v42 op_sel_hi:[1,0,1]
v_fma_mix_f32 v17,v46,v27,v42 op_sel:[1,0,1] op_sel_hi:[1,0,1]
v_mfma_f32_16x16x32_bf16 v[252:255],a[4:7],a[44:47],v[252:255]
s_waitcnt lgkmcnt(3)
s_waitcnt vmcnt(17)
v_cvt_pk_bf16_f32 v42,v16,v17
v_mfma_f32_16x16x32_bf16 v[96:99],a[8:11],a[48:51],v[96:99]
s_waitcnt vmcnt(16)
ds_read_b128 a[4:7],v10 offset:54272
s_add_u32 s60,s60,0x1000
v_mfma_f32_16x16x32_bf16 v[100:103],a[8:11],a[52:55],v[100:103]
s_waitcnt vmcnt(15)
buffer_load_dwordx4 a[80:83],%[vL16],%[srdW],s60 offen offset:0 sc1
v_mfma_f32_16x16x32_bf16 v[104:107],a[8:11],a[56:59],v[104:107]
s_waitcnt vmcnt(15)
v_fma_mix_f32 v18,v47,v27,v43 op_sel_hi:[1,0,1]
v_fma_mix_f32 v19,v47,v27,v43 op_sel:[1,0,1] op_sel_hi:[1,0,1]
v_mfma_f32_16x16x32_bf16 v[108:111],a[8:11],a[60:63],v[108:111]
s_waitcnt lgkmcnt(2)
v_cvt_pk_bf16_f32 v43,v18,v19
v_mfma_f32_16x16x32_bf16 v[112:115],a[12:15],a[48:51],v[112:115]
ds_read_b128 a[8:11],v11 offset:5120
ds_write_b128 v14,v[40:43] offset:6144
v_mfma_f32_16x16x32_bf16 v[116:119],a[12:15],a[52:55],v[116:119]
buffer_load_dwordx4 a[84:87],%[vL16],%[srdW],s60 offen offset:1024 sc1
v_mfma_f32_16x16x32_bf16 v[120:123],a[12:15],a[56:59],v[120:123]
)"
R"(v_pk_mul_f16 v48,v48,v28 op_sel_hi:[1,0]
v_pk_mul_f16 v49,v49,v28 op_sel_hi:[1,0]
v_mfma_f32_16x16x32_bf16 v[124:127],a[12:15],a[60:63],v[124:127]
s_waitcnt lgkmcnt(3)
v_pk_mul_f16 v50,v50,v28 op_sel_hi:[1,0]
v_pk_mul_f16 v51,v51,v28 op_sel_hi:[1,0]
v_mfma_f32_16x16x32_bf16 v[128:131],a[0:3],a[48:51],v[128:131]
ds_read_b128 a[12:15],v11 offset:21504
v_fma_mix_f32 v16,v52,v29,v48 op_sel_hi:[1,0,1]
v_mfma_f32_16x16x32_bf16 v[132:135],a[0:3],a[52:55],v[132:135]
buffer_load_dwordx4 a[88:91],%[vL16],%[srdW],s60 offen offset:2048 sc1
v_mfma_f32_16x16x32_bf16 v[136:139],a[0:3],a[56:59],v[136:139]
v_fma_mix_f32 v17,v52,v29,v48 op_sel:[1,0,1] op_sel_hi:[1,0,1]
v_cvt_pk_bf16_f32 v48,v16,v17
v_mfma_f32_16x16x32_bf16 v[140:143],a[0:3],a[60:63],v[140:143]
s_waitcnt lgkmcnt(3)
v_fma_mix_f32 v18,v53,v29,v49 op_sel_hi:[1,0,1]
v_fma_mix_f32 v19,v53,v29,v49 op_sel:[1,0,1] op_sel_hi:[1,0,1]
v_mfma_f32_16x16x32_bf16 v[144:147],a[4:7],a[48:51],v[144:147]
ds_read_b128 a[0:3],v11 offset:37888
v_cvt_pk_bf16_f32 v49,v18,v19
v_mfma_f32_16x16x32_bf16 v[148:151],a[4:7],a[52:55],v[148:151]
buffer_load_dwordx4 a[92:95],%[vL16],%[srdW],s60 offen offset:3072 sc1
v_mfma_f32_16x16x32_bf16 v[152:155],a[4:7],a[56:59],v[152:155]
s_add_u32 s61,s62,10240
v_fma_mix_f32 v16,v54,v29,v50 op_sel_hi:[1,0,1]
v_fma_mix_f32 v17,v54,v29,v50 op_sel:[1,0,1] op_sel_hi:[1,0,1]
v_mfma_f32_16x16x32_bf16 v[156:159],a[4:7],a[60:63],v[156:159]
s_waitcnt lgkmcnt(3)
v_cvt_pk_bf16_f32 v50,v16,v17
v_fma_mix_f32 v18,v55,v29,v51 op_sel_hi:[1,0,1]
v_mfma_f32_16x16x32_bf16 v[160:163],a[8:11],a[48:51],v[160:163]
ds_read_b128 a[4:7],v11 offset:54272
v_fma_mix_f32 v19,v55,v29,v51 op_sel:[1,0,1] op_sel_hi:[1,0,1]
v_mfma_f32_16x16x32_bf16 v[164:167],a[8:11],a[52:55],v[164:167]
buffer_load_dwordx4 v[40:43],%[vL16],%[srdW],s61 offen sc1
v_mfma_f32_16x16x32_bf16 v[168:171],a[8:11],a[56:59],v[168:171]
s_add_u32 s61,s62,10250240
v_cvt_pk_bf16_f32 v51,v18,v19
v_mfma_f32_16x16x32_bf16 v[172:175],a[8:11],a[60:63],v[172:175]
s_waitcnt lgkmcnt(2)
ds_write_b128 v14,v[48:51] offset:38912
v_pk_mul_f16 v56,v56,v30 op_sel_hi:[1,0]
v_mfma_f32_16x16x32_bf16 v[176:179],a[12:15],a[48:51],v[176:179]
ds_read_b128 a[8:11],v12 offset:5120
v_pk_mul_f16 v57,v57,v30 op_sel_hi:[1,0]
v_mfma_f32_16x16x32_bf16 v[180:183],a[12:15],a[52:55],v[180:183]
buffer_load_dwordx4 v[44:47],%[vL16],%[srdW],s61 offen sc1
v_mfma_f32_16x16x32_bf16 v[184:187],a[12:15],a[56:59],v[184:187]
s_add_u32 s61,s63,10240
v_pk_mul_f16 v58,v58,v30 op_sel_hi:[1,0]
v_pk_mul_f16 v59,v59,v30 op_sel_hi:[1,0]
v_mfma_f32_16x16x32_bf16 v[188:191],a[12:15],a[60:63],v[188:191]
s_waitcnt lgkmcnt(3)
v_fma_mix_f32 v16,v60,v31,v56 op_sel_hi:[1,0,1]
v_fma_mix_f32 v17,v60,v31,v56 op_sel:[1,0,1] op_sel_hi:[1,0,1]
v_mfma_f32_16x16x32_bf16 v[192:195],a[0:3],a[48:51],v[192:195]
ds_read_b128 a[12:15],v12 offset:21504
v_cvt_pk_bf16_f32 v56,v16,v17
v_mfma_f32_16x16x32_bf16 v[196:199],a[0:3],a[52:55],v[196:199]
buffer_load_dwordx4 v[48:51],%[vL16],%[srdW],s61 offen sc1
v_mfma_f32_16x16x32_bf16 v[200:203],a[0:3],a[56:59],v[200:203]
s_add_u32 s61,s63,10250240
v_fma_mix_f32 v18,v61,v31,v57 op_sel_hi:[1,0,1]
v_fma_mix_f32 v19,v61,v31,v57 op_sel:[1,0,1] op_sel_hi:[1,0,1]
v_mfma_f32_16x16x32_bf16 v[204:207],a[0:3],a[60:63],v[204:207]
s_waitcnt lgkmcnt(3)
v_cvt_pk_bf16_f32 v57,v18,v19
v_fma_mix_f32 v16,v62,v31,v58 op_sel_hi:[1,0,1]
v_mfma_f32_16x16x32_bf16 v[208:211],a[4:7],a[48:51],v[208:211]
v_fma_mix_f32 v17,v62,v31,v58 op_sel:[1,0,1] op_sel_hi:[1,0,1]
v_cvt_pk_bf16_f32 v58,v16,v17
v_fma_mix_f32 v18,v63,v31,v59 op_sel_hi:[1,0,1]
v_fma_mix_f32 v19,v63,v31,v59 op_sel:[1,0,1] op_sel_hi:[1,0,1]
v_cvt_pk_bf16_f32 v59,v18,v19
ds_write_b128 v15,v[56:59] offset:6144
s_waitcnt lgkmcnt(0)
s_barrier
ds_read_b128 a[0:3],v10 offset:6144
v_mfma_f32_16x16x32_bf16 v[212:215],a[4:7],a[52:55],v[212:215]
buffer_load_dwordx4 v[52:55],%[vL16],%[srdW],s61 offen sc1
v_mfma_f32_16x16x32_bf16 v[216:219],a[4:7],a[56:59],v[216:219]
s_add_u32 s61,s64,10240
v_mfma_f32_16x16x32_bf16 v[220:223],a[4:7],a[60:63],v[220:223]
v_mfma_f32_16x16x32_bf16 v[224:227],a[8:11],a[48:51],v[224:227]
ds_read_b128 a[4:7],v10 offset:22528
v_mfma_f32_16x16x32_bf16 v[228:231],a[8:11],a[52:55],v[228:231]
buffer_load_dwordx4 v[56:59],%[vL16],%[srdW],s61 offen sc1
v_mfma_f32_16x16x32_bf16 v[232:235],a[8:11],a[56:59],v[232:235]
s_add_u32 s61,s64,10250240
v_mfma_f32_16x16x32_bf16 v[236:239],a[8:11],a[60:63],v[236:239]
v_mfma_f32_16x16x32_bf16 v[240:243],a[12:15],a[48:51],v[240:243]
ds_read_b128 a[8:11],v10 offset:38912
v_mfma_f32_16x16x32_bf16 v[244:247],a[12:15],a[52:55],v[244:247]
buffer_load_dwordx4 v[60:63],%[vL16],%[srdW],s61 offen sc1
v_mfma_f32_16x16x32_bf16 v[248:251],a[12:15],a[56:59],v[248:251]
v_mfma_f32_16x16x32_bf16 v[252:255],a[12:15],a[60:63],v[252:255]
s_waitcnt lgkmcnt(2)
s_waitcnt vmcnt(17)
v_mfma_f32_16x16x32_bf16 v[96:99],a[0:3],a[64:67],v[96:99]
s_waitcnt vmcnt(16)
ds_read_b128 a[12:15],v10 offset:55296
s_add_u32 s60,s60,0x1000
v_mfma_f32_16x16x32_bf16 v[100:103],a[0:3],a[68:71],v[100:103]
s_waitcnt vmcnt(15)
buffer_load_dwordx4 a[32:35],%[vL16],%[srdW],s60 offen offset:0 sc1
v_mfma_f32_16x16x32_bf16 v[104:107],a[0:3],a[72:75],v[104:107]
s_waitcnt vmcnt(15)
v_pk_mul_f16 v64,v64,v22 op_sel_hi:[1,0]
v_pk_mul_f16 v65,v65,v22 op_sel_hi:[1,0]
v_mfma_f32_16x16x32_bf16 v[108:111],a[0:3],a[76:79],v[108:111]
s_waitcnt lgkmcnt(2)
v_pk_mul_f16 v66,v66,v22 op_sel_hi:[1,0]
v_pk_mul_f16 v67,v67,v22 op_sel_hi:[1,0]
v_mfma_f32_16x16x32_bf16 v[112:115],a[4:7],a[64:67],v[112:115]
ds_read_b128 a[0:3],v11 offset:6144
v_fma_mix_f32 v16,v68,v23,v64 op_sel_hi:[1,0,1]
v_mfma_f32_16x16x32_bf16 v[116:119],a[4:7],a[68:71],v[116:119]
buffer_load_dwordx4 a[36:39],%[vL16],%[srdW],s60 offen offset:1024 sc1
v_mfma_f32_16x16x32_bf16 v[120:123],a[4:7],a[72:75],v[120:123]
v_fma_mix_f32 v17,v68,v23,v64 op_sel:[1,0,1] op_sel_hi:[1,0,1]
v_cvt_pk_bf16_f32 v64,v16,v17
v_mfma_f32_16x16x32_bf16 v[124:127],a[4:7],a[76:79],v[124:127]
s_waitcnt lgkmcnt(2)
v_fma_mix_f32 v18,v69,v23,v65 op_sel_hi:[1,0,1]
v_fma_mix_f32 v19,v69,v23,v65 op_sel:[1,0,1] op_sel_hi:[1,0,1]
v_mfma_f32_16x16x32_bf16 v[128:131],a[8:11],a[64:67],v[128:131]
ds_read_b128 a[4:7],v11 offset:22528
v_cvt_pk_bf16_f32 v65,v18,v19
v_mfma_f32_16x16x32_bf16 v[132:135],a[8:11],a[68:71],v[132:135]
buffer_load_dwordx4 a[40:43],%[vL16],%[srdW],s60 offen offset:2048 sc1
v_mfma_f32_16x16x32_bf16 v[136:139],a[8:11],a[72:75],v[136:139]
v_fma_mix_f32 v16,v70,v23,v66 op_sel_hi:[1,0,1]
v_fma_mix_f32 v17,v70,v23,v66 op_sel:[1,0,1] op_sel_hi:[1,0,1]
v_mfma_f32_16x16x32_bf16 v[140:143],a[8:11],a[76:79],v[140:143]
s_waitcnt lgkmcnt(2)
v_cvt_pk_bf16_f32 v66,v16,v17
v_fma_mix_f32 v18,v71,v23,v67 op_sel_hi:[1,0,1]
v_mfma_f32_16x16x32_bf16 v[144:147],a[12:15],a[64:67],v[144:147]
ds_read_b128 a[8:11],v11 offset:38912
v_fma_mix_f32 v19,v71,v23,v67 op_sel:[1,0,1] op_sel_hi:[1,0,1]
v_mfma_f32_16x16x32_bf16 v[148:151],a[12:15],a[68:71],v[148:151]
buffer_load_dwordx4 a[44:47],%[vL16],%[srdW],s60 offen offset:3072 sc1
v_mfma_f32_16x16x32_bf16 v[152:155],a[12:15],a[72:75],v[152:155]
v_cvt_pk_bf16_f32 v67,v18,v19
v_mfma_f32_16x16x32_bf16 v[156:159],a[12:15],a[76:79],v[156:159]
s_waitcnt lgkmcnt(2)
ds_write_b128 v13,v[64:67] offset:8192
s_add_u32 s61,s65,10240
v_mfma_f32_16x16x32_bf16 v[160:163],a[0:3],a[64:67],v[160:163]
ds_read_b128 a[12:15],v11 offset:55296
v_pk_mul_f16 v72,v72,v24 op_sel_hi:[1,0]
v_mfma_f32_16x16x32_bf16 v[164:167],a[0:3],a[68:71],v[164:167]
buffer_load_dwordx4 v[64:67],%[vL16],%[srdW],s61 offen sc1
v_mfma_f32_16x16x32_bf16 v[168:171],a[0:3],a[72:75],v[168:171]
s_add_u32 s61,s65,10250240
v_pk_mul_f16 v73,v73,v24 op_sel_hi:[1,0]
v_pk_mul_f16 v74,v74,v24 op_sel_hi:[1,0]
v_mfma_f32_16x16x32_bf16 v[172:175],a[0:3],a[76:79],v[172:175]
s_waitcnt lgkmcnt(3)
v_pk_mul_f16 v75,v75,v24 op_sel_hi:[1,0]
v_fma_mix_f32 v16,v76,v25,v72 op_sel_hi:[1,0,1]
v_mfma_f32_16x16x32_bf16 v[176:179],a[4:7],a[64:67],v[176:179]
ds_read_b128 a[0:3],v12 offset:6144
v_fma_mix_f32 v17,v76,v25,v72 op_sel:[1,0,1] op_sel_hi:[1,0,1]
v_mfma_f32_16x16x32_bf16 v[180:183],a[4:7],a[68:71],v[180:183]
buffer_load_dwordx4 v[68:71],%[vL16],%[srdW],s61 offen sc1
v_mfma_f32_16x16x32_bf16 v[184:187],a[4:7],a[72:75],v[184:187]
v_cvt_pk_bf16_f32 v72,v16,v17
v_fma_mix_f32 v18,v77,v25,v73 op_sel_hi:[1,0,1]
v_mfma_f32_16x16x32_bf16 v[188:191],a[4:7],a[76:79],v[188:191]
s_waitcnt lgkmcnt(3)
v_fma_mix_f32 v19,v77,v25,v73 op_sel:[1,0,1] op_sel_hi:[1,0,1]
v_cvt_pk_bf16_f32 v73,v18,v19
v_mfma_f32_16x16x32_bf16 v[192:195],a[8:11],a[64:67],v[192:195]
ds_read_b128 a[4:7],v12 offset:22528
v_fma_mix_f32 v16,v78,v25,v74 op_sel_hi:[1,0,1]
v_mfma_f32_16x16x32_bf16 v[196:199],a[8:11],a[68:71],v[196:199]
v_fma_mix_f32 v17,v78,v25,v74 op_sel:[1,0,1] op_sel_hi:[1,0,1]
v_cvt_pk_bf16_f32 v74,v16,v17
v_mfma_f32_16x16x32_bf16 v[200:203],a[8:11],a[72:75],v[200:203]
v_fma_mix_f32 v18,v79,v25,v75 op_sel_hi:[1,0,1]
v_fma_mix_f32 v19,v79,v25,v75 op_sel:[1,0,1] op_sel_hi:[1,0,1]
v_mfma_f32_16x16x32_bf16 v[204:207],a[8:11],a[76:79],v[204:207]
s_waitcnt lgkmcnt(2)
v_cvt_pk_bf16_f32 v75,v18,v19
v_mfma_f32_16x16x32_bf16 v[208:211],a[12:15],a[64:67],v[208:211]
ds_read_b128 a[8:11],v10 offset:7168
ds_write_b128 v13,v[72:75] offset:40960
v_mfma_f32_16x16x32_bf16 v[212:215],a[12:15],a[68:71],v[212:215]
s_add_u32 s61,s66,10240
v_pk_mul_f16 v80,v80,v26 op_sel_hi:[1,0]
v_mfma_f32_16x16x32_bf16 v[216:219],a[12:15],a[72:75],v[216:219]
buffer_load_dwordx4 v[72:75],%[vL16],%[srdW],s61 offen sc1
v_mfma_f32_16x16x32_bf16 v[220:223],a[12:15],a[76:79],v[220:223]
s_waitcnt lgkmcnt(3)
s_add_u32 s61,s66,10250240
v_pk_mul_f16 v81,v81,v26 op_sel_hi:[1,0]
v_mfma_f32_16x16x32_bf16 v[224:227],a[0:3],a[64:67],v[224:227]
ds_read_b128 a[12:15],v10 offset:23552
v_pk_mul_f16 v82,v82,v26 op_sel_hi:[1,0]
v_mfma_f32_16x16x32_bf16 v[228:231],a[0:3],a[68:71],v[228:231]
buffer_load_dwordx4 v[76:79],%[vL16],%[srdW],s61 offen sc1
v_mfma_f32_16x16x32_bf16 v[232:235],a[0:3],a[72:75],v[232:235]
v_pk_mul_f16 v83,v83,v26 op_sel_hi:[1,0]
v_fma_mix_f32 v16,v84,v27,v80 op_sel_hi:[1,0,1]
v_mfma_f32_16x16x32_bf16 v[236:239],a[0:3],a[76:79],v[236:239]
s_waitcnt lgkmcnt(3)
v_fma_mix_f32 v17,v84,v27,v80 op_sel:[1,0,1] op_sel_hi:[1,0,1]
v_cvt_pk_bf16_f32 v80,v16,v17
v_mfma_f32_16x16x32_bf16 v[240:243],a[4:7],a[64:67],v[240:243]
ds_read_b128 a[0:3],v10 offset:39936
v_fma_mix_f32 v18,v85,v27,v81 op_sel_hi:[1,0,1]
v_mfma_f32_16x16x32_bf16 v[244:247],a[4:7],a[68:71],v[244:247]
v_fma_mix_f32 v19,v85,v27,v81 op_sel:[1,0,1] op_sel_hi:[1,0,1]
v_cvt_pk_bf16_f32 v81,v18,v19
v_mfma_f32_16x16x32_bf16 v[248:251],a[4:7],a[72:75],v[248:251]
v_fma_mix_f32 v16,v86,v27,v82 op_sel_hi:[1,0,1]
v_fma_mix_f32 v17,v86,v27,v82 op_sel:[1,0,1] op_sel_hi:[1,0,1]
v_mfma_f32_16x16x32_bf16 v[252:255],a[4:7],a[76:79],v[252:255]
s_waitcnt lgkmcnt(3)
s_waitcnt vmcnt(17)
v_cvt_pk_bf16_f32 v82,v16,v17
v_mfma_f32_16x16x32_bf16 v[96:99],a[8:11],a[80:83],v[96:99]
s_waitcnt vmcnt(16)
ds_read_b128 a[4:7],v10 offset:56320
s_add_u32 s60,s60,0x1000
v_mfma_f32_16x16x32_bf16 v[100:103],a[8:11],a[84:87],v[100:103]
s_waitcnt vmcnt(15)
buffer_load_dwordx4 a[48:51],%[vL16],%[srdW],s60 offen offset:0 sc1
v_mfma_f32_16x16x32_bf16 v[104:107],a[8:11],a[88:91],v[104:107]
s_waitcnt vmcnt(15)
v_fma_mix_f32 v18,v87,v27,v83 op_sel_hi:[1,0,1]
v_fma_mix_f32 v19,v87,v27,v83 op_sel:[1,0,1] op_sel_hi:[1,0,1]
v_mfma_f32_16x16x32_bf16 v[108:111],a[8:11],a[92:95],v[108:111]
s_waitcnt lgkmcnt(2)
v_cvt_pk_bf16_f32 v83,v18,v19
v_mfma_f32_16x16x32_bf16 v[112:115],a[12:15],a[80:83],v[112:115]
ds_read_b128 a[8:11],v11 offset:7168
ds_write_b128 v14,v[80:83] offset:8192
v_mfma_f32_16x16x32_bf16 v[116:119],a[12:15],a[84:87],v[116:119]
buffer_load_dwordx4 a[52:55],%[vL16],%[srdW],s60 offen offset:1024 sc1
v_mfma_f32_16x16x32_bf16 v[120:123],a[12:15],a[88:91],v[120:123]
v_pk_mul_f16 v88,v88,v28 op_sel_hi:[1,0]
v_pk_mul_f16 v89,v89,v28 op_sel_hi:[1,0]
v_mfma_f32_16x16x32_bf16 v[124:127],a[12:15],a[92:95],v[124:127]
s_waitcnt lgkmcnt(3)
v_pk_mul_f16 v90,v90,v28 op_sel_hi:[1,0]
v_pk_mul_f16 v91,v91,v28 op_sel_hi:[1,0]
v_mfma_f32_16x16x32_bf16 v[128:131],a[0:3],a[80:83],v[128:131]
ds_read_b128 a[12:15],v11 offset:23552
v_fma_mix_f32 v16,v92,v29,v88 op_sel_hi:[1,0,1]
v_mfma_f32_16x16x32_bf16 v[132:135],a[0:3],a[84:87],v[132:135]
buffer_load_dwordx4 a[56:59],%[vL16],%[srdW],s60 offen offset:2048 sc1
v_mfma_f32_16x16x32_bf16 v[136:139],a[0:3],a[88:91],v[136:139]
v_fma_mix_f32 v17,v92,v29,v88 op_sel:[1,0,1] op_sel_hi:[1,0,1]
v_cvt_pk_bf16_f32 v88,v16,v17
v_mfma_f32_16x16x32_bf16 v[140:143],a[0:3],a[92:95],v[140:143]
s_waitcnt lgkmcnt(3)
v_fma_mix_f32 v18,v93,v29,v89 op_sel_hi:[1,0,1]
v_fma_mix_f32 v19,v93,v29,v89 op_sel:[1,0,1] op_sel_hi:[1,0,1]
v_mfma_f32_16x16x32_bf16 v[144:147],a[4:7],a[80:83],v[144:147]
ds_read_b128 a[0:3],v11 offset:39936
v_cvt_pk_bf16_f32 v89,v18,v19
v_mfma_f32_16x16x32_bf16 v[148:151],a[4:7],a[84:87],v[148:151]
buffer_load_dwordx4 a[60:63],%[vL16],%[srdW],s60 offen offset:3072 sc1
v_mfma_f32_16x16x32_bf16 v[152:155],a[4:7],a[88:91],v[152:155]
s_add_u32 s61,s62,12288
v_fma_mix_f32 v16,v94,v29,v90 op_sel_hi:[1,0,1]
v_fma_mix_f32 v17,v94,v29,v90 op_sel:[1,0,1] op_sel_hi:[1,0,1]
v_mfma_f32_16x16x32_bf16 v[156:159],a[4:7],a[92:95],v[156:159]
s_waitcnt lgkmcnt(3)
v_cvt_pk_bf16_f32 v90,v16,v17
v_fma_mix_f32 v18,v95,v29,v91 op_sel_hi:[1,0,1]
v_mfma_f32_16x16x32_bf16 v[160:163],a[8:11],a[80:83],v[160:163]
ds_read_b128 a[4:7],v11 offset:56320
v_fma_mix_f32 v19,v95,v29,v91 op_sel:[1,0,1] op_sel_hi:[1,0,1]
v_mfma_f32_16x16x32_bf16 v[164:167],a[8:11],a[84:87],v[164:167]
buffer_load_dwordx4 v[80:83],%[vL16],%[srdW],s61 offen sc1
v_mfma_f32_16x16x32_bf16 v[168:171],a[8:11],a[88:91],v[168:171]
s_add_u32 s61,s62,10252288
v_cvt_pk_bf16_f32 v91,v18,v19
v_mfma_f32_16x16x32_bf16 v[172:175],a[8:11],a[92:95],v[172:175]
s_waitcnt lgkmcnt(2)
ds_write_b128 v14,v[88:91] offset:40960
v_pk_mul_f16 v32,v32,v30 op_sel_hi:[1,0]
v_mfma_f32_16x16x32_bf16 v[176:179],a[12:15],a[80:83],v[176:179]
ds_read_b128 a[8:11],v12 offset:7168
v_pk_mul_f16 v33,v33,v30 op_sel_hi:[1,0]
v_mfma_f32_16x16x32_bf16 v[180:183],a[12:15],a[84:87],v[180:183]
buffer_load_dwordx4 v[84:87],%[vL16],%[srdW],s61 offen sc1
v_mfma_f32_16x16x32_bf16 v[184:187],a[12:15],a[88:91],v[184:187]
s_add_u32 s61,s63,12288
v_pk_mul_f16 v34,v34,v30 op_sel_hi:[1,0]
v_pk_mul_f16 v35,v35,v30 op_sel_hi:[1,0]
v_mfma_f32_16x16x32_bf16 v[188:191],a[12:15],a[92:95],v[188:191]
s_waitcnt lgkmcnt(3)
v_fma_mix_f32 v16,v36,v31,v32 op_sel_hi:[1,0,1]
v_fma_mix_f32 v17,v36,v31,v32 op_sel:[1,0,1] op_sel_hi:[1,0,1]
v_mfma_f32_16x16x32_bf16 v[192:195],a[0:3],a[80:83],v[192:195]
ds_read_b128 a[12:15],v12 offset:23552
v_cvt_pk_bf16_f32 v32,v16,v17
v_mfma_f32_16x16x32_bf16 v[196:199],a[0:3],a[84:87],v[196:199]
buffer_load_dwordx4 v[88:91],%[vL16],%[srdW],s61 offen sc1
v_mfma_f32_16x16x32_bf16 v[200:203],a[0:3],a[88:91],v[200:203]
s_add_u32 s61,s63,10252288
v_fma_mix_f32 v18,v37,v31,v33 op_sel_hi:[1,0,1]
v_fma_mix_f32 v19,v37,v31,v33 op_sel:[1,0,1] op_sel_hi:[1,0,1]
v_mfma_f32_16x16x32_bf16 v[204:207],a[0:3],a[92:95],v[204:207]
s_waitcnt lgkmcnt(3)
v_cvt_pk_bf16_f32 v33,v18,v19
v_fma_mix_f32 v16,v38,v31,v34 op_sel_hi:[1,0,1]
v_mfma_f32_16x16x32_bf16 v[208:211],a[4:7],a[80:83],v[208:211]
v_fma_mix_f32 v17,v38,v31,v34 op_sel:[1,0,1] op_sel_hi:[1,0,1]
v_cvt_pk_bf16_f32 v34,v16,v17
v_fma_mix_f32 v18,v39,v31,v35 op_sel_hi:[1,0,1]
v_fma_mix_f32 v19,v39,v31,v35 op_sel:[1,0,1] op_sel_hi:[1,0,1]
v_cvt_pk_bf16_f32 v35,v18,v19
ds_write_b128 v15,v[32:35] offset:8192
s_waitcnt lgkmcnt(0)
s_barrier
ds_read_b128 a[0:3],v10 offset:8192
v_mfma_f32_16x16x32_bf16 v[212:215],a[4:7],a[84:87],v[212:215]
buffer_load_dwordx4 v[92:95],%[vL16],%[srdW],s61 offen sc1
v_mfma_f32_16x16x32_bf16 v[216:219],a[4:7],a[88:91],v[216:219]
s_add_u32 s61,s64,12288
v_mfma_f32_16x16x32_bf16 v[220:223],a[4:7],a[92:95],v[220:223]
v_mfma_f32_16x16x32_bf16 v[224:227],a[8:11],a[80:83],v[224:227]
ds_read_b128 a[4:7],v10 offset:24576
v_mfma_f32_16x16x32_bf16 v[228:231],a[8:11],a[84:87],v[228:231]
buffer_load_dwordx4 v[32:35],%[vL16],%[srdW],s61 offen sc1
v_mfma_f32_16x16x32_bf16 v[232:235],a[8:11],a[88:91],v[232:235]
s_add_u32 s61,s64,10252288
v_mfma_f32_16x16x32_bf16 v[236:239],a[8:11],a[92:95],v[236:239]
v_mfma_f32_16x16x32_bf16 v[240:243],a[12:15],a[80:83],v[240:243]
ds_read_b128 a[8:11],v10 offset:40960
v_mfma_f32_16x16x32_bf16 v[244:247],a[12:15],a[84:87],v[244:247]
buffer_load_dwordx4 v[36:39],%[vL16],%[srdW],s61 offen sc1
v_mfma_f32_16x16x32_bf16 v[248:251],a[12:15],a[88:91],v[248:251]
v_mfma_f32_16x16x32_bf16 v[252:255],a[12:15],a[92:95],v[252:255]
s_waitcnt lgkmcnt(2)
s_waitcnt vmcnt(17)
v_mfma_f32_16x16x32_bf16 v[96:99],a[0:3],a[32:35],v[96:99]
s_waitcnt vmcnt(16)
ds_read_b128 a[12:15],v10 offset:57344
s_add_u32 s60,s60,0x1000
v_mfma_f32_16x16x32_bf16 v[100:103],a[0:3],a[36:39],v[100:103]
s_waitcnt vmcnt(15)
buffer_load_dwordx4 a[64:67],%[vL16],%[srdW],s60 offen offset:0 sc1
v_mfma_f32_16x16x32_bf16 v[104:107],a[0:3],a[40:43],v[104:107]
s_waitcnt vmcnt(15)
v_pk_mul_f16 v40,v40,v22 op_sel_hi:[1,0]
v_pk_mul_f16 v41,v41,v22 op_sel_hi:[1,0]
v_mfma_f32_16x16x32_bf16 v[108:111],a[0:3],a[44:47],v[108:111]
s_waitcnt lgkmcnt(2)
v_pk_mul_f16 v42,v42,v22 op_sel_hi:[1,0]
v_pk_mul_f16 v43,v43,v22 op_sel_hi:[1,0]
v_mfma_f32_16x16x32_bf16 v[112:115],a[4:7],a[32:35],v[112:115]
ds_read_b128 a[0:3],v11 offset:8192
v_fma_mix_f32 v16,v44,v23,v40 op_sel_hi:[1,0,1]
v_mfma_f32_16x16x32_bf16 v[116:119],a[4:7],a[36:39],v[116:119]
buffer_load_dwordx4 a[68:71],%[vL16],%[srdW],s60 offen offset:1024 sc1
v_mfma_f32_16x16x32_bf16 v[120:123],a[4:7],a[40:43],v[120:123]
v_fma_mix_f32 v17,v44,v23,v40 op_sel:[1,0,1] op_sel_hi:[1,0,1]
v_cvt_pk_bf16_f32 v40,v16,v17
v_mfma_f32_16x16x32_bf16 v[124:127],a[4:7],a[44:47],v[124:127]
s_waitcnt lgkmcnt(2)
v_fma_mix_f32 v18,v45,v23,v41 op_sel_hi:[1,0,1]
v_fma_mix_f32 v19,v45,v23,v41 op_sel:[1,0,1] op_sel_hi:[1,0,1]
v_mfma_f32_16x16x32_bf16 v[128:131],a[8:11],a[32:35],v[128:131]
ds_read_b128 a[4:7],v11 offset:24576
v_cvt_pk_bf16_f32 v41,v18,v19
v_mfma_f32_16x16x32_bf16 v[132:135],a[8:11],a[36:39],v[132:135]
buffer_load_dwordx4 a[72:75],%[vL16],%[srdW],s60 offen offset:2048 sc1
v_mfma_f32_16x16x32_bf16 v[136:139],a[8:11],a[40:43],v[136:139]
v_fma_mix_f32 v16,v46,v23,v42 op_sel_hi:[1,0,1]
v_fma_mix_f32 v17,v46,v23,v42 op_sel:[1,0,1] op_sel_hi:[1,0,1]
v_mfma_f32_16x16x32_bf16 v[140:143],a[8:11],a[44:47],v[140:143]
s_waitcnt lgkmcnt(2)
v_cvt_pk_bf16_f32 v42,v16,v17
v_fma_mix_f32 v18,v47,v23,v43 op_sel_hi:[1,0,1]
v_mfma_f32_16x16x32_bf16 v[144:147],a[12:15],a[32:35],v[144:147]
ds_read_b128 a[8:11],v11 offset:40960
v_fma_mix_f32 v19,v47,v23,v43 op_sel:[1,0,1] op_sel_hi:[1,0,1]
v_mfma_f32_16x16x32_bf16 v[148:151],a[12:15],a[36:39],v[148:151]
buffer_load_dwordx4 a[76:79],%[vL16],%[srdW],s60 offen offset:3072 sc1
v_mfma_f32_16x16x32_bf16 v[152:155],a[12:15],a[40:43],v[152:155]
v_cvt_pk_bf16_f32 v43,v18,v19
v_mfma_f32_16x16x32_bf16 v[156:159],a[12:15],a[44:47],v[156:159]
s_waitcnt lgkmcnt(2)
ds_write_b128 v13,v[40:43] offset:10240
s_add_u32 s61,s65,12288
v_mfma_f32_16x16x32_bf16 v[160:163],a[0:3],a[32:35],v[160:163]
ds_read_b128 a[12:15],v11 offset:57344
v_pk_mul_f16 v48,v48,v24 op_sel_hi:[1,0]
v_mfma_f32_16x16x32_bf16 v[164:167],a[0:3],a[36:39],v[164:167]
buffer_load_dwordx4 v[40:43],%[vL16],%[srdW],s61 offen sc1
v_mfma_f32_16x16x32_bf16 v[168:171],a[0:3],a[40:43],v[168:171]
s_add_u32 s61,s65,10252288
v_pk_mul_f16 v49,v49,v24 op_sel_hi:[1,0]
)"
R"(v_pk_mul_f16 v50,v50,v24 op_sel_hi:[1,0]
v_mfma_f32_16x16x32_bf16 v[172:175],a[0:3],a[44:47],v[172:175]
s_waitcnt lgkmcnt(3)
v_pk_mul_f16 v51,v51,v24 op_sel_hi:[1,0]
v_fma_mix_f32 v16,v52,v25,v48 op_sel_hi:[1,0,1]
v_mfma_f32_16x16x32_bf16 v[176:179],a[4:7],a[32:35],v[176:179]
ds_read_b128 a[0:3],v12 offset:8192
v_fma_mix_f32 v17,v52,v25,v48 op_sel:[1,0,1] op_sel_hi:[1,0,1]
v_mfma_f32_16x16x32_bf16 v[180:183],a[4:7],a[36:39],v[180:183]
buffer_load_dwordx4 v[44:47],%[vL16],%[srdW],s61 offen sc1
v_mfma_f32_16x16x32_bf16 v[184:187],a[4:7],a[40:43],v[184:187]
v_cvt_pk_bf16_f32 v48,v16,v17
v_fma_mix_f32 v18,v53,v25,v49 op_sel_hi:[1,0,1]
v_mfma_f32_16x16x32_bf16 v[188:191],a[4:7],a[44:47],v[188:191]
s_waitcnt lgkmcnt(3)
v_fma_mix_f32 v19,v53,v25,v49 op_sel:[1,0,1] op_sel_hi:[1,0,1]
v_cvt_pk_bf16_f32 v49,v18,v19
v_mfma_f32_16x16x32_bf16 v[192:195],a[8:11],a[32:35],v[192:195]
ds_read_b128 a[4:7],v12 offset:24576
v_fma_mix_f32 v16,v54,v25,v50 op_sel_hi:[1,0,1]
v_mfma_f32_16x16x32_bf16 v[196:199],a[8:11],a[36:39],v[196:199]
v_fma_mix_f32 v17,v54,v25,v50 op_sel:[1,0,1] op_sel_hi:[1,0,1]
v_cvt_pk_bf16_f32 v50,v16,v17
v_mfma_f32_16x16x32_bf16 v[200:203],a[8:11],a[40:43],v[200:203]
v_fma_mix_f32 v18,v55,v25,v51 op_sel_hi:[1,0,1]
v_fma_mix_f32 v19,v55,v25,v51 op_sel:[1,0,1] op_sel_hi:[1,0,1]
v_mfma_f32_16x16x32_bf16 v[204:207],a[8:11],a[44:47],v[204:207]
s_waitcnt lgkmcnt(2)
v_cvt_pk_bf16_f32 v51,v18,v19
v_mfma_f32_16x16x32_bf16 v[208:211],a[12:15],a[32:35],v[208:211]
ds_read_b128 a[8:11],v10 offset:9216
ds_write_b128 v13,v[48:51] offset:43008
v_mfma_f32_16x16x32_bf16 v[212:215],a[12:15],a[36:39],v[212:215]
s_add_u32 s61,s66,12288
v_pk_mul_f16 v56,v56,v26 op_sel_hi:[1,0]
v_mfma_f32_16x16x32_bf16 v[216:219],a[12:15],a[40:43],v[216:219]
buffer_load_dwordx4 v[48:51],%[vL16],%[srdW],s61 offen sc1
v_mfma_f32_16x16x32_bf16 v[220:223],a[12:15],a[44:47],v[220:223]
s_waitcnt lgkmcnt(3)
s_add_u32 s61,s66,10252288
v_pk_mul_f16 v57,v57,v26 op_sel_hi:[1,0]
v_mfma_f32_16x16x32_bf16 v[224:227],a[0:3],a[32:35],v[224:227]
ds_read_b128 a[12:15],v10 offset:25600
v_pk_mul_f16 v58,v58,v26 op_sel_hi:[1,0]
v_mfma_f32_16x16x32_bf16 v[228:231],a[0:3],a[36:39],v[228:231]
buffer_load_dwordx4 v[52:55],%[vL16],%[srdW],s61 offen sc1
v_mfma_f32_16x16x32_bf16 v[232:235],a[0:3],a[40:43],v[232:235]
v_pk_mul_f16 v59,v59,v26 op_sel_hi:[1,0]
v_fma_mix_f32 v16,v60,v27,v56 op_sel_hi:[1,0,1]
v_mfma_f32_16x16x32_bf16 v[236:239],a[0:3],a[44:47],v[236:239]
s_waitcnt lgkmcnt(3)
v_fma_mix_f32 v17,v60,v27,v56 op_sel:[1,0,1] op_sel_hi:[1,0,1]
v_cvt_pk_bf16_f32 v56,v16,v17
v_mfma_f32_16x16x32_bf16 v[240:243],a[4:7],a[32:35],v[240:243]
ds_read_b128 a[0:3],v10 offset:41984
v_fma_mix_f32 v18,v61,v27,v57 op_sel_hi:[1,0,1]
v_mfma_f32_16x16x32_bf16 v[244:247],a[4:7],a[36:39],v[244:247]
v_fma_mix_f32 v19,v61,v27,v57 op_sel:[1,0,1] op_sel_hi:[1,0,1]
v_cvt_pk_bf16_f32 v57,v18,v19
v_mfma_f32_16x16x32_bf16 v[248:251],a[4:7],a[40:43],v[248:251]
v_fma_mix_f32 v16,v62,v27,v58 op_sel_hi:[1,0,1]
v_fma_mix_f32 v17,v62,v27,v58 op_sel:[1,0,1] op_sel_hi:[1,0,1]
v_mfma_f32_16x16x32_bf16 v[252:255],a[4:7],a[44:47],v[252:255]
s_waitcnt lgkmcnt(3)
s_waitcnt vmcnt(17)
v_cvt_pk_bf16_f32 v58,v16,v17
v_mfma_f32_16x16x32_bf16 v[96:99],a[8:11],a[48:51],v[96:99]
s_waitcnt vmcnt(16)
ds_read_b128 a[4:7],v10 offset:58368
s_add_u32 s60,s60,0x1000
v_mfma_f32_16x16x32_bf16 v[100:103],a[8:11],a[52:55],v[100:103]
s_waitcnt vmcnt(15)
buffer_load_dwordx4 a[80:83],%[vL16],%[srdW],s60 offen offset:0 sc1
v_mfma_f32_16x16x32_bf16 v[104:107],a[8:11],a[56:59],v[104:107]
s_waitcnt vmcnt(15)
v_fma_mix_f32 v18,v63,v27,v59 op_sel_hi:[1,0,1]
v_fma_mix_f32 v19,v63,v27,v59 op_sel:[1,0,1] op_sel_hi:[1,0,1]
v_mfma_f32_16x16x32_bf16 v[108:111],a[8:11],a[60:63],v[108:111]
s_waitcnt lgkmcnt(2)
v_cvt_pk_bf16_f32 v59,v18,v19
v_mfma_f32_16x16x32_bf16 v[112:115],a[12:15],a[48:51],v[112:115]
ds_read_b128 a[8:11],v11 offset:9216
ds_write_b128 v14,v[56:59] offset:10240
v_mfma_f32_16x16x32_bf16 v[116:119],a[12:15],a[52:55],v[116:119]
buffer_load_dwordx4 a[84:87],%[vL16],%[srdW],s60 offen offset:1024 sc1
v_mfma_f32_16x16x32_bf16 v[120:123],a[12:15],a[56:59],v[120:123]
v_pk_mul_f16 v64,v64,v28 op_sel_hi:[1,0]
v_pk_mul_f16 v65,v65,v28 op_sel_hi:[1,0]
v_mfma_f32_16x16x32_bf16 v[124:127],a[12:15],a[60:63],v[124:127]
s_waitcnt lgkmcnt(3)
v_pk_mul_f16 v66,v66,v28 op_sel_hi:[1,0]
v_pk_mul_f16 v67,v67,v28 op_sel_hi:[1,0]
v_mfma_f32_16x16x32_bf16 v[128:131],a[0:3],a[48:51],v[128:131]
ds_read_b128 a[12:15],v11 offset:25600
v_fma_mix_f32 v16,v68,v29,v64 op_sel_hi:[1,0,1]
v_mfma_f32_16x16x32_bf16 v[132:135],a[0:3],a[52:55],v[132:135]
buffer_load_dwordx4 a[88:91],%[vL16],%[srdW],s60 offen offset:2048 sc1
v_mfma_f32_16x16x32_bf16 v[136:139],a[0:3],a[56:59],v[136:139]
v_fma_mix_f32 v17,v68,v29,v64 op_sel:[1,0,1] op_sel_hi:[1,0,1]
v_cvt_pk_bf16_f32 v64,v16,v17
v_mfma_f32_16x16x32_bf16 v[140:143],a[0:3],a[60:63],v[140:143]
s_waitcnt lgkmcnt(3)
v_fma_mix_f32 v18,v69,v29,v65 op_sel_hi:[1,0,1]
v_fma_mix_f32 v19,v69,v29,v65 op_sel:[1,0,1] op_sel_hi:[1,0,1]
v_mfma_f32_16x16x32_bf16 v[144:147],a[4:7],a[48:51],v[144:147]
ds_read_b128 a[0:3],v11 offset:41984
v_cvt_pk_bf16_f32 v65,v18,v19
v_mfma_f32_16x16x32_bf16 v[148:151],a[4:7],a[52:55],v[148:151]
buffer_load_dwordx4 a[92:95],%[vL16],%[srdW],s60 offen offset:3072 sc1
v_mfma_f32_16x16x32_bf16 v[152:155],a[4:7],a[56:59],v[152:155]
s_add_u32 s61,s62,14336
v_fma_mix_f32 v16,v70,v29,v66 op_sel_hi:[1,0,1]
v_fma_mix_f32 v17,v70,v29,v66 op_sel:[1,0,1] op_sel_hi:[1,0,1]
v_mfma_f32_16x16x32_bf16 v[156:159],a[4:7],a[60:63],v[156:159]
s_waitcnt lgkmcnt(3)
v_cvt_pk_bf16_f32 v66,v16,v17
v_fma_mix_f32 v18,v71,v29,v67 op_sel_hi:[1,0,1]
v_mfma_f32_16x16x32_bf16 v[160:163],a[8:11],a[48:51],v[160:163]
ds_read_b128 a[4:7],v11 offset:58368
v_fma_mix_f32 v19,v71,v29,v67 op_sel:[1,0,1] op_sel_hi:[1,0,1]
v_mfma_f32_16x16x32_bf16 v[164:167],a[8:11],a[52:55],v[164:167]
buffer_load_dwordx4 v[56:59],%[vL16],%[srdW],s61 offen sc1
v_mfma_f32_16x16x32_bf16 v[168:171],a[8:11],a[56:59],v[168:171]
s_add_u32 s61,s62,10254336
v_cvt_pk_bf16_f32 v67,v18,v19
v_mfma_f32_16x16x32_bf16 v[172:175],a[8:11],a[60:63],v[172:175]
s_waitcnt lgkmcnt(2)
ds_write_b128 v14,v[64:67] offset:43008
v_pk_mul_f16 v72,v72,v30 op_sel_hi:[1,0]
v_mfma_f32_16x16x32_bf16 v[176:179],a[12:15],a[48:51],v[176:179]
ds_read_b128 a[8:11],v12 offset:9216
v_pk_mul_f16 v73,v73,v30 op_sel_hi:[1,0]
v_mfma_f32_16x16x32_bf16 v[180:183],a[12:15],a[52:55],v[180:183]
buffer_load_dwordx4 v[60:63],%[vL16],%[srdW],s61 offen sc1
v_mfma_f32_16x16x32_bf16 v[184:187],a[12:15],a[56:59],v[184:187]
s_add_u32 s61,s63,14336
v_pk_mul_f16 v74,v74,v30 op_sel_hi:[1,0]
v_pk_mul_f16 v75,v75,v30 op_sel_hi:[1,0]
v_mfma_f32_16x16x32_bf16 v[188:191],a[12:15],a[60:63],v[188:191]
s_waitcnt lgkmcnt(3)
v_fma_mix_f32 v16,v76,v31,v72 op_sel_hi:[1,0,1]
v_fma_mix_f32 v17,v76,v31,v72 op_sel:[1,0,1] op_sel_hi:[1,0,1]
v_mfma_f32_16x16x32_bf16 v[192:195],a[0:3],a[48:51],v[192:195]
ds_read_b128 a[12:15],v12 offset:25600
v_cvt_pk_bf16_f32 v72,v16,v17
v_mfma_f32_16x16x32_bf16 v[196:199],a[0:3],a[52:55],v[196:199]
buffer_load_dwordx4 v[64:67],%[vL16],%[srdW],s61 offen sc1
v_mfma_f32_16x16x32_bf16 v[200:203],a[0:3],a[56:59],v[200:203]
s_add_u32 s61,s63,10254336
v_fma_mix_f32 v18,v77,v31,v73 op_sel_hi:[1,0,1]
v_fma_mix_f32 v19,v77,v31,v73 op_sel:[1,0,1] op_sel_hi:[1,0,1]
v_mfma_f32_16x16x32_bf16 v[204:207],a[0:3],a[60:63],v[204:207]
s_waitcnt lgkmcnt(3)
v_cvt_pk_bf16_f32 v73,v18,v19
v_fma_mix_f32 v16,v78,v31,v74 op_sel_hi:[1,0,1]
v_mfma_f32_16x16x32_bf16 v[208:211],a[4:7],a[48:51],v[208:211]
v_fma_mix_f32 v17,v78,v31,v74 op_sel:[1,0,1] op_sel_hi:[1,0,1]
v_cvt_pk_bf16_f32 v74,v16,v17
v_fma_mix_f32 v18,v79,v31,v75 op_sel_hi:[1,0,1]
v_fma_mix_f32 v19,v79,v31,v75 op_sel:[1,0,1] op_sel_hi:[1,0,1]
v_cvt_pk_bf16_f32 v75,v18,v19
ds_write_b128 v15,v[72:75] offset:10240
s_waitcnt lgkmcnt(0)
s_barrier
ds_read_b128 a[0:3],v10 offset:10240
v_mfma_f32_16x16x32_bf16 v[212:215],a[4:7],a[52:55],v[212:215]
buffer_load_dwordx4 v[68:71],%[vL16],%[srdW],s61 offen sc1
v_mfma_f32_16x16x32_bf16 v[216:219],a[4:7],a[56:59],v[216:219]
s_add_u32 s61,s64,14336
v_mfma_f32_16x16x32_bf16 v[220:223],a[4:7],a[60:63],v[220:223]
v_mfma_f32_16x16x32_bf16 v[224:227],a[8:11],a[48:51],v[224:227]
ds_read_b128 a[4:7],v10 offset:26624
v_mfma_f32_16x16x32_bf16 v[228:231],a[8:11],a[52:55],v[228:231]
buffer_load_dwordx4 v[72:75],%[vL16],%[srdW],s61 offen sc1
v_mfma_f32_16x16x32_bf16 v[232:235],a[8:11],a[56:59],v[232:235]
s_add_u32 s61,s64,10254336
v_mfma_f32_16x16x32_bf16 v[236:239],a[8:11],a[60:63],v[236:239]
v_mfma_f32_16x16x32_bf16 v[240:243],a[12:15],a[48:51],v[240:243]
ds_read_b128 a[8:11],v10 offset:43008
v_mfma_f32_16x16x32_bf16 v[244:247],a[12:15],a[52:55],v[244:247]
buffer_load_dwordx4 v[76:79],%[vL16],%[srdW],s61 offen sc1
v_mfma_f32_16x16x32_bf16 v[248:251],a[12:15],a[56:59],v[248:251]
v_mfma_f32_16x16x32_bf16 v[252:255],a[12:15],a[60:63],v[252:255]
s_waitcnt lgkmcnt(2)
s_waitcnt vmcnt(17)
v_mfma_f32_16x16x32_bf16 v[96:99],a[0:3],a[64:67],v[96:99]
s_waitcnt vmcnt(16)
ds_read_b128 a[12:15],v10 offset:59392
s_add_u32 s60,s60,0x1000
v_mfma_f32_16x16x32_bf16 v[100:103],a[0:3],a[68:71],v[100:103]
s_waitcnt vmcnt(15)
buffer_load_dwordx4 a[32:35],%[vL16],%[srdW],s60 offen offset:0 sc1
v_mfma_f32_16x16x32_bf16 v[104:107],a[0:3],a[72:75],v[104:107]
s_waitcnt vmcnt(15)
v_pk_mul_f16 v80,v80,v22 op_sel_hi:[1,0]
v_pk_mul_f16 v81,v81,v22 op_sel_hi:[1,0]
v_mfma_f32_16x16x32_bf16 v[108:111],a[0:3],a[76:79],v[108:111]
s_waitcnt lgkmcnt(2)
v_pk_mul_f16 v82,v82,v22 op_sel_hi:[1,0]
v_pk_mul_f16 v83,v83,v22 op_sel_hi:[1,0]
v_mfma_f32_16x16x32_bf16 v[112:115],a[4:7],a[64:67],v[112:115]
ds_read_b128 a[0:3],v11 offset:10240
v_fma_mix_f32 v16,v84,v23,v80 op_sel_hi:[1,0,1]
v_mfma_f32_16x16x32_bf16 v[116:119],a[4:7],a[68:71],v[116:119]
buffer_load_dwordx4 a[36:39],%[vL16],%[srdW],s60 offen offset:1024 sc1
v_mfma_f32_16x16x32_bf16 v[120:123],a[4:7],a[72:75],v[120:123]
v_fma_mix_f32 v17,v84,v23,v80 op_sel:[1,0,1] op_sel_hi:[1,0,1]
v_cvt_pk_bf16_f32 v80,v16,v17
v_mfma_f32_16x16x32_bf16 v[124:127],a[4:7],a[76:79],v[124:127]
s_waitcnt lgkmcnt(2)
v_fma_mix_f32 v18,v85,v23,v81 op_sel_hi:[1,0,1]
v_fma_mix_f32 v19,v85,v23,v81 op_sel:[1,0,1] op_sel_hi:[1,0,1]
v_mfma_f32_16x16x32_bf16 v[128:131],a[8:11],a[64:67],v[128:131]
ds_read_b128 a[4:7],v11 offset:26624
v_cvt_pk_bf16_f32 v81,v18,v19
v_mfma_f32_16x16x32_bf16 v[132:135],a[8:11],a[68:71],v[132:135]
buffer_load_dwordx4 a[40:43],%[vL16],%[srdW],s60 offen offset:2048 sc1
v_mfma_f32_16x16x32_bf16 v[136:139],a[8:11],a[72:75],v[136:139]
v_fma_mix_f32 v16,v86,v23,v82 op_sel_hi:[1,0,1]
v_fma_mix_f32 v17,v86,v23,v82 op_sel:[1,0,1] op_sel_hi:[1,0,1]
v_mfma_f32_16x16x32_bf16 v[140:143],a[8:11],a[76:79],v[140:143]
s_waitcnt lgkmcnt(2)
v_cvt_pk_bf16_f32 v82,v16,v17
v_fma_mix_f32 v18,v87,v23,v83 op_sel_hi:[1,0,1]
v_mfma_f32_16x16x32_bf16 v[144:147],a[12:15],a[64:67],v[144:147]
ds_read_b128 a[8:11],v11 offset:43008
v_fma_mix_f32 v19,v87,v23,v83 op_sel:[1,0,1] op_sel_hi:[1,0,1]
v_mfma_f32_16x16x32_bf16 v[148:151],a[12:15],a[68:71],v[148:151]
buffer_load_dwordx4 a[44:47],%[vL16],%[srdW],s60 offen offset:3072 sc1
v_mfma_f32_16x16x32_bf16 v[152:155],a[12:15],a[72:75],v[152:155]
v_cvt_pk_bf16_f32 v83,v18,v19
v_mfma_f32_16x16x32_bf16 v[156:159],a[12:15],a[76:79],v[156:159]
s_waitcnt lgkmcnt(2)
ds_write_b128 v13,v[80:83] offset:12288
s_add_u32 s61,s65,14336
v_mfma_f32_16x16x32_bf16 v[160:163],a[0:3],a[64:67],v[160:163]
ds_read_b128 a[12:15],v11 offset:59392
v_pk_mul_f16 v88,v88,v24 op_sel_hi:[1,0]
v_mfma_f32_16x16x32_bf16 v[164:167],a[0:3],a[68:71],v[164:167]
buffer_load_dwordx4 v[80:83],%[vL16],%[srdW],s61 offen sc1
v_mfma_f32_16x16x32_bf16 v[168:171],a[0:3],a[72:75],v[168:171]
s_add_u32 s61,s65,10254336
v_pk_mul_f16 v89,v89,v24 op_sel_hi:[1,0]
v_pk_mul_f16 v90,v90,v24 op_sel_hi:[1,0]
v_mfma_f32_16x16x32_bf16 v[172:175],a[0:3],a[76:79],v[172:175]
s_waitcnt lgkmcnt(3)
v_pk_mul_f16 v91,v91,v24 op_sel_hi:[1,0]
v_fma_mix_f32 v16,v92,v25,v88 op_sel_hi:[1,0,1]
v_mfma_f32_16x16x32_bf16 v[176:179],a[4:7],a[64:67],v[176:179]
ds_read_b128 a[0:3],v12 offset:10240
v_fma_mix_f32 v17,v92,v25,v88 op_sel:[1,0,1] op_sel_hi:[1,0,1]
v_mfma_f32_16x16x32_bf16 v[180:183],a[4:7],a[68:71],v[180:183]
buffer_load_dwordx4 v[84:87],%[vL16],%[srdW],s61 offen sc1
v_mfma_f32_16x16x32_bf16 v[184:187],a[4:7],a[72:75],v[184:187]
v_cvt_pk_bf16_f32 v88,v16,v17
v_fma_mix_f32 v18,v93,v25,v89 op_sel_hi:[1,0,1]
v_mfma_f32_16x16x32_bf16 v[188:191],a[4:7],a[76:79],v[188:191]
s_waitcnt lgkmcnt(3)
v_fma_mix_f32 v19,v93,v25,v89 op_sel:[1,0,1] op_sel_hi:[1,0,1]
v_cvt_pk_bf16_f32 v89,v18,v19
v_mfma_f32_16x16x32_bf16 v[192:195],a[8:11],a[64:67],v[192:195]
ds_read_b128 a[4:7],v12 offset:26624
v_fma_mix_f32 v16,v94,v25,v90 op_sel_hi:[1,0,1]
v_mfma_f32_16x16x32_bf16 v[196:199],a[8:11],a[68:71],v[196:199]
v_fma_mix_f32 v17,v94,v25,v90 op_sel:[1,0,1] op_sel_hi:[1,0,1]
v_cvt_pk_bf16_f32 v90,v16,v17
v_mfma_f32_16x16x32_bf16 v[200:203],a[8:11],a[72:75],v[200:203]
v_fma_mix_f32 v18,v95,v25,v91 op_sel_hi:[1,0,1]
v_fma_mix_f32 v19,v95,v25,v91 op_sel:[1,0,1] op_sel_hi:[1,0,1]
v_mfma_f32_16x16x32_bf16 v[204:207],a[8:11],a[76:79],v[204:207]
s_waitcnt lgkmcnt(2)
v_cvt_pk_bf16_f32 v91,v18,v19
v_mfma_f32_16x16x32_bf16 v[208:211],a[12:15],a[64:67],v[208:211]
ds_read_b128 a[8:11],v10 offset:11264
ds_write_b128 v13,v[88:91] offset:45056
v_mfma_f32_16x16x32_bf16 v[212:215],a[12:15],a[68:71],v[212:215]
s_add_u32 s61,s66,14336
v_pk_mul_f16 v32,v32,v26 op_sel_hi:[1,0]
v_mfma_f32_16x16x32_bf16 v[216:219],a[12:15],a[72:75],v[216:219]
buffer_load_dwordx4 v[88:91],%[vL16],%[srdW],s61 offen sc1
v_mfma_f32_16x16x32_bf16 v[220:223],a[12:15],a[76:79],v[220:223]
s_waitcnt lgkmcnt(3)
s_add_u32 s61,s66,10254336
v_pk_mul_f16 v33,v33,v26 op_sel_hi:[1,0]
v_mfma_f32_16x16x32_bf16 v[224:227],a[0:3],a[64:67],v[224:227]
ds_read_b128 a[12:15],v10 offset:27648
v_pk_mul_f16 v34,v34,v26 op_sel_hi:[1,0]
v_mfma_f32_16x16x32_bf16 v[228:231],a[0:3],a[68:71],v[228:231]
buffer_load_dwordx4 v[92:95],%[vL16],%[srdW],s61 offen sc1
v_mfma_f32_16x16x32_bf16 v[232:235],a[0:3],a[72:75],v[232:235]
v_pk_mul_f16 v35,v35,v26 op_sel_hi:[1,0]
v_fma_mix_f32 v16,v36,v27,v32 op_sel_hi:[1,0,1]
v_mfma_f32_16x16x32_bf16 v[236:239],a[0:3],a[76:79],v[236:239]
s_waitcnt lgkmcnt(3)
v_fma_mix_f32 v17,v36,v27,v32 op_sel:[1,0,1] op_sel_hi:[1,0,1]
v_cvt_pk_bf16_f32 v32,v16,v17
v_mfma_f32_16x16x32_bf16 v[240:243],a[4:7],a[64:67],v[240:243]
ds_read_b128 a[0:3],v10 offset:44032
v_fma_mix_f32 v18,v37,v27,v33 op_sel_hi:[1,0,1]
v_mfma_f32_16x16x32_bf16 v[244:247],a[4:7],a[68:71],v[244:247]
v_fma_mix_f32 v19,v37,v27,v33 op_sel:[1,0,1] op_sel_hi:[1,0,1]
v_cvt_pk_bf16_f32 v33,v18,v19
v_mfma_f32_16x16x32_bf16 v[248:251],a[4:7],a[72:75],v[248:251]
v_fma_mix_f32 v16,v38,v27,v34 op_sel_hi:[1,0,1]
v_fma_mix_f32 v17,v38,v27,v34 op_sel:[1,0,1] op_sel_hi:[1,0,1]
v_mfma_f32_16x16x32_bf16 v[252:255],a[4:7],a[76:79],v[252:255]
s_waitcnt lgkmcnt(3)
s_waitcnt vmcnt(17)
v_cvt_pk_bf16_f32 v34,v16,v17
v_mfma_f32_16x16x32_bf16 v[96:99],a[8:11],a[80:83],v[96:99]
s_waitcnt vmcnt(16)
ds_read_b128 a[4:7],v10 offset:60416
s_add_u32 s60,s60,0x1000
v_mfma_f32_16x16x32_bf16 v[100:103],a[8:11],a[84:87],v[100:103]
s_waitcnt vmcnt(15)
buffer_load_dwordx4 a[48:51],%[vL16],%[srdW],s60 offen offset:0 sc1
v_mfma_f32_16x16x32_bf16 v[104:107],a[8:11],a[88:91],v[104:107]
s_waitcnt vmcnt(15)
v_fma_mix_f32 v18,v39,v27,v35 op_sel_hi:[1,0,1]
v_fma_mix_f32 v19,v39,v27,v35 op_sel:[1,0,1] op_sel_hi:[1,0,1]
v_mfma_f32_16x16x32_bf16 v[108:111],a[8:11],a[92:95],v[108:111]
s_waitcnt lgkmcnt(2)
v_cvt_pk_bf16_f32 v35,v18,v19
v_mfma_f32_16x16x32_bf16 v[112:115],a[12:15],a[80:83],v[112:115]
ds_read_b128 a[8:11],v11 offset:11264
ds_write_b128 v14,v[32:35] offset:12288
v_mfma_f32_16x16x32_bf16 v[116:119],a[12:15],a[84:87],v[116:119]
buffer_load_dwordx4 a[52:55],%[vL16],%[srdW],s60 offen offset:1024 sc1
v_mfma_f32_16x16x32_bf16 v[120:123],a[12:15],a[88:91],v[120:123]
v_pk_mul_f16 v40,v40,v28 op_sel_hi:[1,0]
v_pk_mul_f16 v41,v41,v28 op_sel_hi:[1,0]
v_mfma_f32_16x16x32_bf16 v[124:127],a[12:15],a[92:95],v[124:127]
s_waitcnt lgkmcnt(3)
v_pk_mul_f16 v42,v42,v28 op_sel_hi:[1,0]
v_pk_mul_f16 v43,v43,v28 op_sel_hi:[1,0]
v_mfma_f32_16x16x32_bf16 v[128:131],a[0:3],a[80:83],v[128:131]
ds_read_b128 a[12:15],v11 offset:27648
v_fma_mix_f32 v16,v44,v29,v40 op_sel_hi:[1,0,1]
v_mfma_f32_16x16x32_bf16 v[132:135],a[0:3],a[84:87],v[132:135]
buffer_load_dwordx4 a[56:59],%[vL16],%[srdW],s60 offen offset:2048 sc1
v_mfma_f32_16x16x32_bf16 v[136:139],a[0:3],a[88:91],v[136:139]
v_fma_mix_f32 v17,v44,v29,v40 op_sel:[1,0,1] op_sel_hi:[1,0,1]
v_cvt_pk_bf16_f32 v40,v16,v17
v_mfma_f32_16x16x32_bf16 v[140:143],a[0:3],a[92:95],v[140:143]
s_waitcnt lgkmcnt(3)
v_fma_mix_f32 v18,v45,v29,v41 op_sel_hi:[1,0,1]
v_fma_mix_f32 v19,v45,v29,v41 op_sel:[1,0,1] op_sel_hi:[1,0,1]
v_mfma_f32_16x16x32_bf16 v[144:147],a[4:7],a[80:83],v[144:147]
ds_read_b128 a[0:3],v11 offset:44032
v_cvt_pk_bf16_f32 v41,v18,v19
v_mfma_f32_16x16x32_bf16 v[148:151],a[4:7],a[84:87],v[148:151]
buffer_load_dwordx4 a[60:63],%[vL16],%[srdW],s60 offen offset:3072 sc1
v_mfma_f32_16x16x32_bf16 v[152:155],a[4:7],a[88:91],v[152:155]
v_fma_mix_f32 v16,v46,v29,v42 op_sel_hi:[1,0,1]
v_fma_mix_f32 v17,v46,v29,v42 op_sel:[1,0,1] op_sel_hi:[1,0,1]
v_mfma_f32_16x16x32_bf16 v[156:159],a[4:7],a[92:95],v[156:159]
s_waitcnt lgkmcnt(3)
v_cvt_pk_bf16_f32 v42,v16,v17
v_fma_mix_f32 v18,v47,v29,v43 op_sel_hi:[1,0,1]
v_mfma_f32_16x16x32_bf16 v[160:163],a[8:11],a[80:83],v[160:163]
ds_read_b128 a[4:7],v11 offset:60416
v_fma_mix_f32 v19,v47,v29,v43 op_sel:[1,0,1] op_sel_hi:[1,0,1]
v_mfma_f32_16x16x32_bf16 v[164:167],a[8:11],a[84:87],v[164:167]
v_cvt_pk_bf16_f32 v43,v18,v19
v_mfma_f32_16x16x32_bf16 v[168:171],a[8:11],a[88:91],v[168:171]
ds_write_b128 v14,v[40:43] offset:45056
v_pk_mul_f16 v48,v48,v30 op_sel_hi:[1,0]
v_mfma_f32_16x16x32_bf16 v[172:175],a[8:11],a[92:95],v[172:175]
s_waitcnt lgkmcnt(3)
v_pk_mul_f16 v49,v49,v30 op_sel_hi:[1,0]
v_pk_mul_f16 v50,v50,v30 op_sel_hi:[1,0]
v_mfma_f32_16x16x32_bf16 v[176:179],a[12:15],a[80:83],v[176:179]
ds_read_b128 a[8:11],v12 offset:11264
v_pk_mul_f16 v51,v51,v30 op_sel_hi:[1,0]
v_mfma_f32_16x16x32_bf16 v[180:183],a[12:15],a[84:87],v[180:183]
v_fma_mix_f32 v16,v52,v31,v48 op_sel_hi:[1,0,1]
v_fma_mix_f32 v17,v52,v31,v48 op_sel:[1,0,1] op_sel_hi:[1,0,1]
v_mfma_f32_16x16x32_bf16 v[184:187],a[12:15],a[88:91],v[184:187]
v_cvt_pk_bf16_f32 v48,v16,v17
v_fma_mix_f32 v18,v53,v31,v49 op_sel_hi:[1,0,1]
v_mfma_f32_16x16x32_bf16 v[188:191],a[12:15],a[92:95],v[188:191]
s_waitcnt lgkmcnt(3)
v_fma_mix_f32 v19,v53,v31,v49 op_sel:[1,0,1] op_sel_hi:[1,0,1]
v_cvt_pk_bf16_f32 v49,v18,v19
v_mfma_f32_16x16x32_bf16 v[192:195],a[0:3],a[80:83],v[192:195]
ds_read_b128 a[12:15],v12 offset:27648
v_fma_mix_f32 v16,v54,v31,v50 op_sel_hi:[1,0,1]
v_mfma_f32_16x16x32_bf16 v[196:199],a[0:3],a[84:87],v[196:199]
v_fma_mix_f32 v17,v54,v31,v50 op_sel:[1,0,1] op_sel_hi:[1,0,1]
v_cvt_pk_bf16_f32 v50,v16,v17
v_mfma_f32_16x16x32_bf16 v[200:203],a[0:3],a[88:91],v[200:203]
v_fma_mix_f32 v18,v55,v31,v51 op_sel_hi:[1,0,1]
v_fma_mix_f32 v19,v55,v31,v51 op_sel:[1,0,1] op_sel_hi:[1,0,1]
v_mfma_f32_16x16x32_bf16 v[204:207],a[0:3],a[92:95],v[204:207]
s_waitcnt lgkmcnt(3)
v_cvt_pk_bf16_f32 v51,v18,v19
v_mfma_f32_16x16x32_bf16 v[208:211],a[4:7],a[80:83],v[208:211]
ds_write_b128 v15,v[48:51] offset:12288
s_waitcnt lgkmcnt(0)
s_barrier
)"
R"(ds_read_b128 a[0:3],v10 offset:12288
v_mfma_f32_16x16x32_bf16 v[212:215],a[4:7],a[84:87],v[212:215]
v_mfma_f32_16x16x32_bf16 v[216:219],a[4:7],a[88:91],v[216:219]
v_mfma_f32_16x16x32_bf16 v[220:223],a[4:7],a[92:95],v[220:223]
v_mfma_f32_16x16x32_bf16 v[224:227],a[8:11],a[80:83],v[224:227]
ds_read_b128 a[4:7],v10 offset:28672
v_mfma_f32_16x16x32_bf16 v[228:231],a[8:11],a[84:87],v[228:231]
v_mfma_f32_16x16x32_bf16 v[232:235],a[8:11],a[88:91],v[232:235]
v_mfma_f32_16x16x32_bf16 v[236:239],a[8:11],a[92:95],v[236:239]
v_mfma_f32_16x16x32_bf16 v[240:243],a[12:15],a[80:83],v[240:243]
ds_read_b128 a[8:11],v10 offset:45056
v_mfma_f32_16x16x32_bf16 v[244:247],a[12:15],a[84:87],v[244:247]
v_mfma_f32_16x16x32_bf16 v[248:251],a[12:15],a[88:91],v[248:251]
v_mfma_f32_16x16x32_bf16 v[252:255],a[12:15],a[92:95],v[252:255]
s_waitcnt lgkmcnt(2)
s_waitcnt vmcnt(11)
v_mfma_f32_16x16x32_bf16 v[96:99],a[0:3],a[32:35],v[96:99]
s_waitcnt vmcnt(10)
ds_read_b128 a[12:15],v10 offset:61440
s_add_u32 s60,s60,0x1000
v_mfma_f32_16x16x32_bf16 v[100:103],a[0:3],a[36:39],v[100:103]
s_waitcnt vmcnt(9)
buffer_load_dwordx4 a[64:67],%[vL16],%[srdW],s60 offen offset:0 sc1
v_mfma_f32_16x16x32_bf16 v[104:107],a[0:3],a[40:43],v[104:107]
s_waitcnt vmcnt(9)
v_pk_mul_f16 v56,v56,v22 op_sel_hi:[1,0]
v_pk_mul_f16 v57,v57,v22 op_sel_hi:[1,0]
v_mfma_f32_16x16x32_bf16 v[108:111],a[0:3],a[44:47],v[108:111]
s_waitcnt lgkmcnt(2)
v_pk_mul_f16 v58,v58,v22 op_sel_hi:[1,0]
v_pk_mul_f16 v59,v59,v22 op_sel_hi:[1,0]
v_mfma_f32_16x16x32_bf16 v[112:115],a[4:7],a[32:35],v[112:115]
ds_read_b128 a[0:3],v11 offset:12288
v_fma_mix_f32 v16,v60,v23,v56 op_sel_hi:[1,0,1]
v_mfma_f32_16x16x32_bf16 v[116:119],a[4:7],a[36:39],v[116:119]
buffer_load_dwordx4 a[68:71],%[vL16],%[srdW],s60 offen offset:1024 sc1
v_mfma_f32_16x16x32_bf16 v[120:123],a[4:7],a[40:43],v[120:123]
v_fma_mix_f32 v17,v60,v23,v56 op_sel:[1,0,1] op_sel_hi:[1,0,1]
v_cvt_pk_bf16_f32 v56,v16,v17
v_mfma_f32_16x16x32_bf16 v[124:127],a[4:7],a[44:47],v[124:127]
s_waitcnt lgkmcnt(2)
v_fma_mix_f32 v18,v61,v23,v57 op_sel_hi:[1,0,1]
v_fma_mix_f32 v19,v61,v23,v57 op_sel:[1,0,1] op_sel_hi:[1,0,1]
v_mfma_f32_16x16x32_bf16 v[128:131],a[8:11],a[32:35],v[128:131]
ds_read_b128 a[4:7],v11 offset:28672
v_cvt_pk_bf16_f32 v57,v18,v19
v_mfma_f32_16x16x32_bf16 v[132:135],a[8:11],a[36:39],v[132:135]
buffer_load_dwordx4 a[72:75],%[vL16],%[srdW],s60 offen offset:2048 sc1
v_mfma_f32_16x16x32_bf16 v[136:139],a[8:11],a[40:43],v[136:139]
v_fma_mix_f32 v16,v62,v23,v58 op_sel_hi:[1,0,1]
v_fma_mix_f32 v17,v62,v23,v58 op_sel:[1,0,1] op_sel_hi:[1,0,1]
v_mfma_f32_16x16x32_bf16 v[140:143],a[8:11],a[44:47],v[140:143]
s_waitcnt lgkmcnt(2)
v_cvt_pk_bf16_f32 v58,v16,v17
v_fma_mix_f32 v18,v63,v23,v59 op_sel_hi:[1,0,1]
v_mfma_f32_16x16x32_bf16 v[144:147],a[12:15],a[32:35],v[144:147]
ds_read_b128 a[8:11],v11 offset:45056
v_fma_mix_f32 v19,v63,v23,v59 op_sel:[1,0,1] op_sel_hi:[1,0,1]
v_mfma_f32_16x16x32_bf16 v[148:151],a[12:15],a[36:39],v[148:151]
buffer_load_dwordx4 a[76:79],%[vL16],%[srdW],s60 offen offset:3072 sc1
v_mfma_f32_16x16x32_bf16 v[152:155],a[12:15],a[40:43],v[152:155]
v_readlane_b32 s83,%[pv],38
v_readlane_b32 s74,%[pv],39
v_mfma_f32_16x16x32_bf16 v[156:159],a[12:15],a[44:47],v[156:159]
s_waitcnt lgkmcnt(2)
v_readlane_b32 s75,%[pv],40
v_readlane_b32 s76,%[pv],41
v_mfma_f32_16x16x32_bf16 v[160:163],a[0:3],a[32:35],v[160:163]
ds_read_b128 a[12:15],v11 offset:61440
v_cvt_pk_bf16_f32 v59,v18,v19
v_mfma_f32_16x16x32_bf16 v[164:167],a[0:3],a[36:39],v[164:167]
ds_write_b128 v13,v[56:59] offset:14336
v_pk_mul_f16 v64,v64,v24 op_sel_hi:[1,0]
v_mfma_f32_16x16x32_bf16 v[168:171],a[0:3],a[40:43],v[168:171]
v_pk_mul_f16 v65,v65,v24 op_sel_hi:[1,0]
v_pk_mul_f16 v66,v66,v24 op_sel_hi:[1,0]
v_mfma_f32_16x16x32_bf16 v[172:175],a[0:3],a[44:47],v[172:175]
s_waitcnt lgkmcnt(3)
v_pk_mul_f16 v67,v67,v24 op_sel_hi:[1,0]
v_fma_mix_f32 v16,v68,v25,v64 op_sel_hi:[1,0,1]
v_mfma_f32_16x16x32_bf16 v[176:179],a[4:7],a[32:35],v[176:179]
ds_read_b128 a[0:3],v12 offset:12288
v_fma_mix_f32 v17,v68,v25,v64 op_sel:[1,0,1] op_sel_hi:[1,0,1]
v_mfma_f32_16x16x32_bf16 v[180:183],a[4:7],a[36:39],v[180:183]
v_cvt_pk_bf16_f32 v64,v16,v17
v_fma_mix_f32 v18,v69,v25,v65 op_sel_hi:[1,0,1]
v_mfma_f32_16x16x32_bf16 v[184:187],a[4:7],a[40:43],v[184:187]
v_fma_mix_f32 v19,v69,v25,v65 op_sel:[1,0,1] op_sel_hi:[1,0,1]
v_cvt_pk_bf16_f32 v65,v18,v19
v_mfma_f32_16x16x32_bf16 v[188:191],a[4:7],a[44:47],v[188:191]
s_waitcnt lgkmcnt(3)
v_fma_mix_f32 v16,v70,v25,v66 op_sel_hi:[1,0,1]
v_fma_mix_f32 v17,v70,v25,v66 op_sel:[1,0,1] op_sel_hi:[1,0,1]
v_mfma_f32_16x16x32_bf16 v[192:195],a[8:11],a[32:35],v[192:195]
ds_read_b128 a[4:7],v12 offset:28672
v_cvt_pk_bf16_f32 v66,v16,v17
v_mfma_f32_16x16x32_bf16 v[196:199],a[8:11],a[36:39],v[196:199]
v_fma_mix_f32 v18,v71,v25,v67 op_sel_hi:[1,0,1]
v_fma_mix_f32 v19,v71,v25,v67 op_sel:[1,0,1] op_sel_hi:[1,0,1]
v_mfma_f32_16x16x32_bf16 v[200:203],a[8:11],a[40:43],v[200:203]
v_cvt_pk_bf16_f32 v67,v18,v19
v_mfma_f32_16x16x32_bf16 v[204:207],a[8:11],a[44:47],v[204:207]
s_waitcnt lgkmcnt(3)
ds_write_b128 v13,v[64:67] offset:47104
v_pk_mul_f16 v72,v72,v26 op_sel_hi:[1,0]
v_mfma_f32_16x16x32_bf16 v[208:211],a[12:15],a[32:35],v[208:211]
ds_read_b128 a[8:11],v10 offset:13312
v_pk_mul_f16 v73,v73,v26 op_sel_hi:[1,0]
v_mfma_f32_16x16x32_bf16 v[212:215],a[12:15],a[36:39],v[212:215]
v_pk_mul_f16 v74,v74,v26 op_sel_hi:[1,0]
v_pk_mul_f16 v75,v75,v26 op_sel_hi:[1,0]
v_mfma_f32_16x16x32_bf16 v[216:219],a[12:15],a[40:43],v[216:219]
v_fma_mix_f32 v16,v76,v27,v72 op_sel_hi:[1,0,1]
v_fma_mix_f32 v17,v76,v27,v72 op_sel:[1,0,1] op_sel_hi:[1,0,1]
v_mfma_f32_16x16x32_bf16 v[220:223],a[12:15],a[44:47],v[220:223]
s_waitcnt lgkmcnt(3)
v_cvt_pk_bf16_f32 v72,v16,v17
v_fma_mix_f32 v18,v77,v27,v73 op_sel_hi:[1,0,1]
v_mfma_f32_16x16x32_bf16 v[224:227],a[0:3],a[32:35],v[224:227]
ds_read_b128 a[12:15],v10 offset:29696
v_fma_mix_f32 v19,v77,v27,v73 op_sel:[1,0,1] op_sel_hi:[1,0,1]
v_mfma_f32_16x16x32_bf16 v[228:231],a[0:3],a[36:39],v[228:231]
v_cvt_pk_bf16_f32 v73,v18,v19
v_fma_mix_f32 v16,v78,v27,v74 op_sel_hi:[1,0,1]
v_mfma_f32_16x16x32_bf16 v[232:235],a[0:3],a[40:43],v[232:235]
v_fma_mix_f32 v17,v78,v27,v74 op_sel:[1,0,1] op_sel_hi:[1,0,1]
v_cvt_pk_bf16_f32 v74,v16,v17
v_mfma_f32_16x16x32_bf16 v[236:239],a[0:3],a[44:47],v[236:239]
s_waitcnt lgkmcnt(3)
v_fma_mix_f32 v18,v79,v27,v75 op_sel_hi:[1,0,1]
v_fma_mix_f32 v19,v79,v27,v75 op_sel:[1,0,1] op_sel_hi:[1,0,1]
v_mfma_f32_16x16x32_bf16 v[240:243],a[4:7],a[32:35],v[240:243]
ds_read_b128 a[0:3],v10 offset:46080
v_cvt_pk_bf16_f32 v75,v18,v19
v_mfma_f32_16x16x32_bf16 v[244:247],a[4:7],a[36:39],v[244:247]
ds_write_b128 v14,v[72:75] offset:14336
s_waitcnt vmcnt(10)
v_mfma_f32_16x16x32_bf16 v[248:251],a[4:7],a[40:43],v[248:251]
v_pk_mul_f16 v80,v80,v28 op_sel_hi:[1,0]
v_pk_mul_f16 v81,v81,v28 op_sel_hi:[1,0]
v_mfma_f32_16x16x32_bf16 v[252:255],a[4:7],a[44:47],v[252:255]
s_waitcnt lgkmcnt(3)
s_waitcnt vmcnt(7)
v_pk_mul_f16 v82,v82,v28 op_sel_hi:[1,0]
v_mfma_f32_16x16x32_bf16 v[96:99],a[8:11],a[48:51],v[96:99]
s_waitcnt vmcnt(6)
ds_read_b128 a[4:7],v10 offset:62464
s_add_u32 s60,s60,0x1000
v_mfma_f32_16x16x32_bf16 v[100:103],a[8:11],a[52:55],v[100:103]
s_waitcnt vmcnt(5)
buffer_load_dwordx4 a[80:83],%[vL16],%[srdW],s60 offen offset:0 sc1
v_mfma_f32_16x16x32_bf16 v[104:107],a[8:11],a[56:59],v[104:107]
s_waitcnt vmcnt(5)
v_pk_mul_f16 v83,v83,v28 op_sel_hi:[1,0]
v_fma_mix_f32 v16,v84,v29,v80 op_sel_hi:[1,0,1]
v_mfma_f32_16x16x32_bf16 v[108:111],a[8:11],a[60:63],v[108:111]
s_waitcnt lgkmcnt(3)
v_fma_mix_f32 v17,v84,v29,v80 op_sel:[1,0,1] op_sel_hi:[1,0,1]
v_cvt_pk_bf16_f32 v80,v16,v17
v_mfma_f32_16x16x32_bf16 v[112:115],a[12:15],a[48:51],v[112:115]
ds_read_b128 a[8:11],v11 offset:13312
v_fma_mix_f32 v18,v85,v29,v81 op_sel_hi:[1,0,1]
v_mfma_f32_16x16x32_bf16 v[116:119],a[12:15],a[52:55],v[116:119]
buffer_load_dwordx4 a[84:87],%[vL16],%[srdW],s60 offen offset:1024 sc1
v_mfma_f32_16x16x32_bf16 v[120:123],a[12:15],a[56:59],v[120:123]
v_fma_mix_f32 v19,v85,v29,v81 op_sel:[1,0,1] op_sel_hi:[1,0,1]
v_cvt_pk_bf16_f32 v81,v18,v19
v_mfma_f32_16x16x32_bf16 v[124:127],a[12:15],a[60:63],v[124:127]
s_waitcnt lgkmcnt(3)
v_fma_mix_f32 v16,v86,v29,v82 op_sel_hi:[1,0,1]
v_fma_mix_f32 v17,v86,v29,v82 op_sel:[1,0,1] op_sel_hi:[1,0,1]
v_mfma_f32_16x16x32_bf16 v[128:131],a[0:3],a[48:51],v[128:131]
ds_read_b128 a[12:15],v11 offset:29696
v_cvt_pk_bf16_f32 v82,v16,v17
v_mfma_f32_16x16x32_bf16 v[132:135],a[0:3],a[52:55],v[132:135]
buffer_load_dwordx4 a[88:91],%[vL16],%[srdW],s60 offen offset:2048 sc1
v_mfma_f32_16x16x32_bf16 v[136:139],a[0:3],a[56:59],v[136:139]
v_fma_mix_f32 v18,v87,v29,v83 op_sel_hi:[1,0,1]
v_fma_mix_f32 v19,v87,v29,v83 op_sel:[1,0,1] op_sel_hi:[1,0,1]
v_mfma_f32_16x16x32_bf16 v[140:143],a[0:3],a[60:63],v[140:143]
s_waitcnt lgkmcnt(2)
v_cvt_pk_bf16_f32 v83,v18,v19
v_mfma_f32_16x16x32_bf16 v[144:147],a[4:7],a[48:51],v[144:147]
ds_read_b128 a[0:3],v11 offset:46080
ds_write_b128 v14,v[80:83] offset:47104
v_mfma_f32_16x16x32_bf16 v[148:151],a[4:7],a[52:55],v[148:151]
buffer_load_dwordx4 a[92:95],%[vL16],%[srdW],s60 offen offset:3072 sc1
v_mfma_f32_16x16x32_bf16 v[152:155],a[4:7],a[56:59],v[152:155]
v_pk_mul_f16 v88,v88,v30 op_sel_hi:[1,0]
v_pk_mul_f16 v89,v89,v30 op_sel_hi:[1,0]
v_mfma_f32_16x16x32_bf16 v[156:159],a[4:7],a[60:63],v[156:159]
s_waitcnt lgkmcnt(3)
v_pk_mul_f16 v90,v90,v30 op_sel_hi:[1,0]
v_pk_mul_f16 v91,v91,v30 op_sel_hi:[1,0]
v_mfma_f32_16x16x32_bf16 v[160:163],a[8:11],a[48:51],v[160:163]
ds_read_b128 a[4:7],v11 offset:62464
v_fma_mix_f32 v16,v92,v31,v88 op_sel_hi:[1,0,1]
v_mfma_f32_16x16x32_bf16 v[164:167],a[8:11],a[52:55],v[164:167]
v_fma_mix_f32 v17,v92,v31,v88 op_sel:[1,0,1] op_sel_hi:[1,0,1]
v_cvt_pk_bf16_f32 v88,v16,v17
v_mfma_f32_16x16x32_bf16 v[168:171],a[8:11],a[56:59],v[168:171]
v_fma_mix_f32 v18,v93,v31,v89 op_sel_hi:[1,0,1]
v_fma_mix_f32 v19,v93,v31,v89 op_sel:[1,0,1] op_sel_hi:[1,0,1]
v_mfma_f32_16x16x32_bf16 v[172:175],a[8:11],a[60:63],v[172:175]
s_waitcnt lgkmcnt(3)
v_cvt_pk_bf16_f32 v89,v18,v19
v_fma_mix_f32 v16,v94,v31,v90 op_sel_hi:[1,0,1]
v_mfma_f32_16x16x32_bf16 v[176:179],a[12:15],a[48:51],v[176:179]
ds_read_b128 a[8:11],v12 offset:13312
v_fma_mix_f32 v17,v94,v31,v90 op_sel:[1,0,1] op_sel_hi:[1,0,1]
v_mfma_f32_16x16x32_bf16 v[180:183],a[12:15],a[52:55],v[180:183]
v_cvt_pk_bf16_f32 v90,v16,v17
v_fma_mix_f32 v18,v95,v31,v91 op_sel_hi:[1,0,1]
v_mfma_f32_16x16x32_bf16 v[184:187],a[12:15],a[56:59],v[184:187]
v_fma_mix_f32 v19,v95,v31,v91 op_sel:[1,0,1] op_sel_hi:[1,0,1]
v_cvt_pk_bf16_f32 v91,v18,v19
v_mfma_f32_16x16x32_bf16 v[188:191],a[12:15],a[60:63],v[188:191]
s_waitcnt lgkmcnt(3)
ds_write_b128 v15,v[88:91] offset:14336
v_mfma_f32_16x16x32_bf16 v[192:195],a[0:3],a[48:51],v[192:195]
ds_read_b128 a[12:15],v12 offset:29696
v_mfma_f32_16x16x32_bf16 v[196:199],a[0:3],a[52:55],v[196:199]
v_mfma_f32_16x16x32_bf16 v[200:203],a[0:3],a[56:59],v[200:203]
v_mfma_f32_16x16x32_bf16 v[204:207],a[0:3],a[60:63],v[204:207]
s_waitcnt lgkmcnt(3)
v_mfma_f32_16x16x32_bf16 v[208:211],a[4:7],a[48:51],v[208:211]
s_waitcnt lgkmcnt(1)
s_barrier
ds_read_b128 a[0:3],v10 offset:14336
v_mfma_f32_16x16x32_bf16 v[212:215],a[4:7],a[52:55],v[212:215]
v_mfma_f32_16x16x32_bf16 v[216:219],a[4:7],a[56:59],v[216:219]
v_mfma_f32_16x16x32_bf16 v[220:223],a[4:7],a[60:63],v[220:223]
v_mfma_f32_16x16x32_bf16 v[224:227],a[8:11],a[48:51],v[224:227]
ds_read_b128 a[4:7],v10 offset:30720
v_mfma_f32_16x16x32_bf16 v[228:231],a[8:11],a[52:55],v[228:231]
v_mfma_f32_16x16x32_bf16 v[232:235],a[8:11],a[56:59],v[232:235]
v_mfma_f32_16x16x32_bf16 v[236:239],a[8:11],a[60:63],v[236:239]
s_waitcnt lgkmcnt(2)
v_mfma_f32_16x16x32_bf16 v[240:243],a[12:15],a[48:51],v[240:243]
ds_read_b128 a[8:11],v10 offset:47104
v_mfma_f32_16x16x32_bf16 v[244:247],a[12:15],a[52:55],v[244:247]
v_mfma_f32_16x16x32_bf16 v[248:251],a[12:15],a[56:59],v[248:251]
v_mfma_f32_16x16x32_bf16 v[252:255],a[12:15],a[60:63],v[252:255]
s_waitcnt lgkmcnt(2)
s_waitcnt vmcnt(7)
v_mfma_f32_16x16x32_bf16 v[96:99],a[0:3],a[64:67],v[96:99]
s_waitcnt vmcnt(6)
ds_read_b128 a[12:15],v10 offset:63488
s_add_u32 s82,s83,0
v_mfma_f32_16x16x32_bf16 v[100:103],a[0:3],a[68:71],v[100:103]
s_waitcnt vmcnt(5)
buffer_load_dwordx4 a[176:179],%[vL16],%[srdW],s82 offen offset:0 sc1
v_mfma_f32_16x16x32_bf16 v[104:107],a[0:3],a[72:75],v[104:107]
s_waitcnt vmcnt(5)
ds_read_b128 a[216:219],v10 offset:15360
v_readlane_b32 s78,%[pv],42
v_mfma_f32_16x16x32_bf16 v[108:111],a[0:3],a[76:79],v[108:111]
s_waitcnt lgkmcnt(3)
v_readlane_b32 s79,%[pv],43
v_readlane_b32 s72,%[pv],44
v_mfma_f32_16x16x32_bf16 v[112:115],a[4:7],a[64:67],v[112:115]
ds_read_b128 a[0:3],v11 offset:14336
ds_read_b128 a[220:223],v10 offset:31744
v_mfma_f32_16x16x32_bf16 v[116:119],a[4:7],a[68:71],v[116:119]
buffer_load_dwordx4 a[180:183],%[vL16],%[srdW],s82 offen offset:1024 sc1
v_mfma_f32_16x16x32_bf16 v[120:123],a[4:7],a[72:75],v[120:123]
s_add_u32 s82,s83,2048
v_lshrrev_b32 v32,8,%[vL16]
v_bfe_u32 v33,%[vL16],4,4
v_mfma_f32_16x16x32_bf16 v[124:127],a[4:7],a[76:79],v[124:127]
s_waitcnt lgkmcnt(4)
v_mul_u32_u24 v2,336,v33
v_lshl_add_u32 v2,v32,4,v2
v_mfma_f32_16x16x32_bf16 v[128:131],a[8:11],a[64:67],v[128:131]
ds_read_b128 a[4:7],v11 offset:30720
ds_read_b128 a[224:227],v10 offset:48128
v_mfma_f32_16x16x32_bf16 v[132:135],a[8:11],a[68:71],v[132:135]
buffer_load_dwordx4 a[184:187],%[vL16],%[srdW],s82 offen offset:0 sc1
v_mfma_f32_16x16x32_bf16 v[136:139],a[8:11],a[72:75],v[136:139]
v_add_u32 v2,s78,v2
v_add_u32 v3,8,v2
v_mfma_f32_16x16x32_bf16 v[140:143],a[8:11],a[76:79],v[140:143]
s_waitcnt lgkmcnt(5)
v_lshrrev_b32 v32,6,%[vL16]
v_bfe_u32 v33,%[vL16],4,2
v_mfma_f32_16x16x32_bf16 v[144:147],a[12:15],a[64:67],v[144:147]
ds_read_b128 a[8:11],v11 offset:47104
ds_read_b128 a[228:231],v10 offset:64512
v_mfma_f32_16x16x32_bf16 v[148:151],a[12:15],a[68:71],v[148:151]
buffer_load_dwordx4 a[188:191],%[vL16],%[srdW],s82 offen offset:1024 sc1
v_mfma_f32_16x16x32_bf16 v[152:155],a[12:15],a[72:75],v[152:155]
s_add_u32 s82,s83,4096
v_mul_u32_u24 v4,336,v32
v_lshl_add_u32 v4,v33,6,v4
v_mfma_f32_16x16x32_bf16 v[156:159],a[12:15],a[76:79],v[156:159]
s_waitcnt lgkmcnt(5)
v_add_u32 v4,s78,v4
v_and_b32 v34,1,v33
v_mfma_f32_16x16x32_bf16 v[160:163],a[0:3],a[64:67],v[160:163]
ds_read_b128 a[12:15],v11 offset:63488
ds_read_b128 a[232:235],v11 offset:15360
v_mfma_f32_16x16x32_bf16 v[164:167],a[0:3],a[68:71],v[164:167]
buffer_load_dwordx4 a[192:195],%[vL16],%[srdW],s82 offen offset:0 sc1
v_mfma_f32_16x16x32_bf16 v[168:171],a[0:3],a[72:75],v[168:171]
v_lshlrev_b32 v34,4,v34
v_mov_b32 v35,0x3f80
v_mfma_f32_16x16x32_bf16 v[172:175],a[0:3],a[76:79],v[172:175]
s_waitcnt lgkmcnt(5)
v_lshlrev_b32 v34,v34,v35
v_cmp_gt_u32 vcc,2,v33
v_mfma_f32_16x16x32_bf16 v[176:179],a[4:7],a[64:67],v[176:179]
ds_read_b128 a[0:3],v12 offset:14336
ds_read_b128 a[236:239],v11 offset:31744
v_mfma_f32_16x16x32_bf16 v[180:183],a[4:7],a[68:71],v[180:183]
buffer_load_dwordx4 a[196:199],%[vL16],%[srdW],s82 offen offset:1024 sc1
v_mfma_f32_16x16x32_bf16 v[184:187],a[4:7],a[72:75],v[184:187]
s_add_u32 s82,s83,6144
v_add_u32 v5,s79,%[vL16]
v_add_u32 v8,s72,%[vL16]
v_mfma_f32_16x16x32_bf16 v[188:191],a[4:7],a[76:79],v[188:191]
s_waitcnt lgkmcnt(5)
v_cndmask_b32 v6,0,v34,vcc
v_cndmask_b32 v7,v34,0,vcc
v_mfma_f32_16x16x32_bf16 v[192:195],a[8:11],a[64:67],v[192:195]
ds_read_b128 a[4:7],v12 offset:30720
ds_read_b128 a[240:243],v11 offset:48128
v_mfma_f32_16x16x32_bf16 v[196:199],a[8:11],a[68:71],v[196:199]
buffer_load_dwordx4 a[200:203],%[vL16],%[srdW],s82 offen offset:0 sc1
v_mfma_f32_16x16x32_bf16 v[200:203],a[8:11],a[72:75],v[200:203]
v_add_u32 v20,0x1000,v8
v_and_b32 v32,0x100,%[vL16]
v_mfma_f32_16x16x32_bf16 v[204:207],a[8:11],a[76:79],v[204:207]
s_waitcnt lgkmcnt(5)
v_cmp_ne_u32 vcc,0,v32
v_mfma_f32_16x16x32_bf16 v[208:211],a[12:15],a[64:67],v[208:211]
buffer_load_dwordx4 a[204:207],%[vL16],%[srdW],s82 offen offset:1024 sc1
v_mfma_f32_16x16x32_bf16 v[212:215],a[12:15],a[68:71],v[212:215]
s_add_u32 s82,s83,8192
ds_read_b128 a[244:247],v11 offset:64512
v_mfma_f32_16x16x32_bf16 v[216:219],a[12:15],a[72:75],v[216:219]
v_mfma_f32_16x16x32_bf16 v[220:223],a[12:15],a[76:79],v[220:223]
s_waitcnt lgkmcnt(4)
buffer_load_dwordx4 a[208:211],%[vL16],%[srdW],s82 offen offset:0 sc1
v_mfma_f32_16x16x32_bf16 v[224:227],a[0:3],a[64:67],v[224:227]
ds_read_b128 a[248:251],v12 offset:15360
v_mfma_f32_16x16x32_bf16 v[228:231],a[0:3],a[68:71],v[228:231]
v_mfma_f32_16x16x32_bf16 v[232:235],a[0:3],a[72:75],v[232:235]
buffer_load_dwordx4 a[212:215],%[vL16],%[srdW],s82 offen offset:1024 sc1
v_mfma_f32_16x16x32_bf16 v[236:239],a[0:3],a[76:79],v[236:239]
s_waitcnt lgkmcnt(3)
v_mfma_f32_16x16x32_bf16 v[240:243],a[4:7],a[64:67],v[240:243]
ds_read_b128 a[252:255],v12 offset:31744
v_mfma_f32_16x16x32_bf16 v[244:247],a[4:7],a[68:71],v[244:247]
v_mfma_f32_16x16x32_bf16 v[248:251],a[4:7],a[72:75],v[248:251]
v_mfma_f32_16x16x32_bf16 v[252:255],a[4:7],a[76:79],v[252:255]
s_waitcnt vmcnt(10)
s_branch P4E_%=
P4L_%=:
v_lshrrev_b32 v16,8,%[vL16]
v_bfe_u32 v17,%[vL16],4,4
v_lshrrev_b32 v18,1,v16
v_lshlrev_b32 v10,8,v18
v_and_b32 v18,1,v16
v_lshl_add_u32 v10,v18,6,v10
v_lshrrev_b32 v18,2,v17
v_lshl_add_u32 v10,v18,4,v10
v_bfe_u32 v18,v17,1,1
v_lshl_add_u32 v10,v18,7,v10
v_and_b32 v18,1,v17
v_lshl_add_u32 v10,v18,3,v10
v_add_u32 v11,0x10000,v10
v_add_u32 v12,0x20000,v10
v_readlane_b32 s73,%[pv],35
v_readlane_b32 s77,%[pv],0
v_mov_b32 v20,s70
v_mov_b32 v21,s73
s_waitcnt vmcnt(0)
s_barrier
buffer_store_dword v20,v21,%[srdW],0 offen sc1
buffer_load_dword v15,v14,%[srdW],0 offen sc1
s_waitcnt vmcnt(0)
v_cmp_ne_u32 vcc,s70,v15
s_nop 0
s_and_b32 vcc_lo,vcc_lo,s77
s_cbranch_scc0 OK3L_%=
s_mov_b32 s97,0x40000
POLL3L_%=:
buffer_load_dword v15,v14,%[srdW],0 offen sc1
s_waitcnt vmcnt(0)
v_cmp_ne_u32 vcc,s70,v15
s_cbranch_vccz GO3L_%=
s_sub_u32 s97,s97,1
s_cbranch_scc1 GO3L_%=
s_sleep 4
s_branch POLL3L_%=
GO3L_%=:
OK3L_%=:
s_barrier
v_readlane_b32 s68,%[pv],45
v_readlane_b32 s69,%[pv],46
)"
R"(v_readlane_b32 s71,%[pv],47
s_bitcmp1_b32 s71,0
s_cselect_b32 s61,0,0x40000000
v_add_u32 v22,s61,%[vL16]
s_bitcmp1_b32 s71,1
s_cselect_b32 s61,0,0x40000000
v_add_u32 v23,s61,%[vL16]
s_bitcmp1_b32 s71,2
s_cselect_b32 s61,0,0x40000000
v_add_u32 v24,s61,%[vL16]
s_bitcmp1_b32 s71,3
s_cselect_b32 s61,0,0x40000000
v_add_u32 v25,s61,%[vL16]
s_bitcmp1_b32 s71,4
s_cselect_b32 s61,0,0x40000000
v_add_u32 v26,s61,%[vL16]
s_add_u32 m0,s68,0
s_add_u32 s61,s69,0
buffer_load_dwordx4 v22,%[srdW],s61 offen sc1 lds
s_add_u32 m0,s68,16384
s_add_u32 s61,s69,20480
buffer_load_dwordx4 v23,%[srdW],s61 offen sc1 lds
s_add_u32 m0,s68,32768
s_add_u32 s61,s69,40960
buffer_load_dwordx4 v24,%[srdW],s61 offen sc1 lds
s_add_u32 m0,s68,49152
s_add_u32 s61,s69,61440
buffer_load_dwordx4 v25,%[srdW],s61 offen sc1 lds
s_add_u32 m0,s68,65536
s_add_u32 s61,s69,81920
buffer_load_dwordx4 v26,%[srdW],s61 offen sc1 lds
s_add_u32 m0,s68,2048
s_add_u32 s61,s69,2048
buffer_load_dwordx4 v22,%[srdW],s61 offen sc1 lds
s_add_u32 m0,s68,18432
s_add_u32 s61,s69,22528
buffer_load_dwordx4 v23,%[srdW],s61 offen sc1 lds
s_add_u32 m0,s68,34816
s_add_u32 s61,s69,43008
buffer_load_dwordx4 v24,%[srdW],s61 offen sc1 lds
s_add_u32 m0,s68,51200
s_add_u32 s61,s69,63488
buffer_load_dwordx4 v25,%[srdW],s61 offen sc1 lds
s_add_u32 m0,s68,67584
s_add_u32 s61,s69,83968
buffer_load_dwordx4 v26,%[srdW],s61 offen sc1 lds
s_waitcnt vmcnt(5)
s_barrier
ds_read_b64_tr_b16 a[0:1],v10 offset:0
ds_read_b64_tr_b16 a[2:3],v10 offset:512
ds_read_b64_tr_b16 a[4:5],v10 offset:16384
ds_read_b64_tr_b16 a[6:7],v10 offset:16896
ds_read_b64_tr_b16 a[8:9],v10 offset:32768
ds_read_b64_tr_b16 a[10:11],v10 offset:33280
s_waitcnt lgkmcnt(4)
v_mfma_f32_16x16x32_bf16 v[96:99],a[0:3],a[32:35],0
s_add_u32 s60,s60,0x1000
ds_read_b64_tr_b16 a[12:13],v10 offset:49152
ds_read_b64_tr_b16 a[14:15],v10 offset:49664
v_mfma_f32_16x16x32_bf16 v[100:103],a[0:3],a[36:39],0
v_mfma_f32_16x16x32_bf16 v[104:107],a[0:3],a[40:43],0
buffer_load_dwordx4 a[64:67],%[vL16],%[srdW],s60 offen offset:0 sc1
v_mfma_f32_16x16x32_bf16 v[108:111],a[0:3],a[44:47],0
s_waitcnt lgkmcnt(4)
v_mfma_f32_16x16x32_bf16 v[112:115],a[4:7],a[32:35],0
ds_read_b64_tr_b16 a[0:1],v11 offset:0
ds_read_b64_tr_b16 a[2:3],v11 offset:512
v_mfma_f32_16x16x32_bf16 v[116:119],a[4:7],a[36:39],0
buffer_load_dwordx4 a[68:71],%[vL16],%[srdW],s60 offen offset:1024 sc1
v_mfma_f32_16x16x32_bf16 v[120:123],a[4:7],a[40:43],0
v_mfma_f32_16x16x32_bf16 v[124:127],a[4:7],a[44:47],0
s_waitcnt lgkmcnt(4)
v_mfma_f32_16x16x32_bf16 v[128:131],a[8:11],a[32:35],0
ds_read_b64_tr_b16 a[4:5],v11 offset:16384
ds_read_b64_tr_b16 a[6:7],v11 offset:16896
v_mfma_f32_16x16x32_bf16 v[132:135],a[8:11],a[36:39],0
buffer_load_dwordx4 a[72:75],%[vL16],%[srdW],s60 offen offset:2048 sc1
v_mfma_f32_16x16x32_bf16 v[136:139],a[8:11],a[40:43],0
v_mfma_f32_16x16x32_bf16 v[140:143],a[8:11],a[44:47],0
s_waitcnt lgkmcnt(4)
v_mfma_f32_16x16x32_bf16 v[144:147],a[12:15],a[32:35],0
ds_read_b64_tr_b16 a[8:9],v11 offset:32768
ds_read_b64_tr_b16 a[10:11],v11 offset:33280
v_mfma_f32_16x16x32_bf16 v[148:151],a[12:15],a[36:39],0
buffer_load_dwordx4 a[76:79],%[vL16],%[srdW],s60 offen offset:3072 sc1
v_mfma_f32_16x16x32_bf16 v[152:155],a[12:15],a[40:43],0
s_add_u32 m0,s68,4096
s_add_u32 s61,s69,4096
v_mfma_f32_16x16x32_bf16 v[156:159],a[12:15],a[44:47],0
s_waitcnt lgkmcnt(4)
v_mfma_f32_16x16x32_bf16 v[160:163],a[0:3],a[32:35],0
ds_read_b64_tr_b16 a[12:13],v11 offset:49152
ds_read_b64_tr_b16 a[14:15],v11 offset:49664
v_mfma_f32_16x16x32_bf16 v[164:167],a[0:3],a[36:39],0
buffer_load_dwordx4 v22,%[srdW],s61 offen sc1 lds
v_mfma_f32_16x16x32_bf16 v[168:171],a[0:3],a[40:43],0
s_add_u32 m0,s68,20480
s_add_u32 s61,s69,24576
v_mfma_f32_16x16x32_bf16 v[172:175],a[0:3],a[44:47],0
s_waitcnt lgkmcnt(4)
v_mfma_f32_16x16x32_bf16 v[176:179],a[4:7],a[32:35],0
ds_read_b64_tr_b16 a[0:1],v12 offset:0
ds_read_b64_tr_b16 a[2:3],v12 offset:512
v_mfma_f32_16x16x32_bf16 v[180:183],a[4:7],a[36:39],0
buffer_load_dwordx4 v23,%[srdW],s61 offen sc1 lds
v_mfma_f32_16x16x32_bf16 v[184:187],a[4:7],a[40:43],0
s_add_u32 m0,s68,36864
s_add_u32 s61,s69,45056
v_mfma_f32_16x16x32_bf16 v[188:191],a[4:7],a[44:47],0
s_waitcnt lgkmcnt(4)
v_mfma_f32_16x16x32_bf16 v[192:195],a[8:11],a[32:35],0
ds_read_b64_tr_b16 a[4:5],v12 offset:16384
ds_read_b64_tr_b16 a[6:7],v12 offset:16896
v_mfma_f32_16x16x32_bf16 v[196:199],a[8:11],a[36:39],0
buffer_load_dwordx4 v24,%[srdW],s61 offen sc1 lds
v_mfma_f32_16x16x32_bf16 v[200:203],a[8:11],a[40:43],0
s_add_u32 m0,s68,53248
s_add_u32 s61,s69,65536
v_mfma_f32_16x16x32_bf16 v[204:207],a[8:11],a[44:47],0
s_waitcnt lgkmcnt(4)
v_mfma_f32_16x16x32_bf16 v[208:211],a[12:15],a[32:35],0
ds_read_b64_tr_b16 a[8:9],v10 offset:1024
ds_read_b64_tr_b16 a[10:11],v10 offset:1536
v_mfma_f32_16x16x32_bf16 v[212:215],a[12:15],a[36:39],0
buffer_load_dwordx4 v25,%[srdW],s61 offen sc1 lds
v_mfma_f32_16x16x32_bf16 v[216:219],a[12:15],a[40:43],0
s_add_u32 m0,s68,69632
s_add_u32 s61,s69,86016
v_mfma_f32_16x16x32_bf16 v[220:223],a[12:15],a[44:47],0
s_waitcnt lgkmcnt(4)
v_mfma_f32_16x16x32_bf16 v[224:227],a[0:3],a[32:35],0
ds_read_b64_tr_b16 a[12:13],v10 offset:17408
ds_read_b64_tr_b16 a[14:15],v10 offset:17920
v_mfma_f32_16x16x32_bf16 v[228:231],a[0:3],a[36:39],0
buffer_load_dwordx4 v26,%[srdW],s61 offen sc1 lds
v_mfma_f32_16x16x32_bf16 v[232:235],a[0:3],a[40:43],0
v_mfma_f32_16x16x32_bf16 v[236:239],a[0:3],a[44:47],0
s_waitcnt lgkmcnt(4)
v_mfma_f32_16x16x32_bf16 v[240:243],a[4:7],a[32:35],0
ds_read_b64_tr_b16 a[0:1],v10 offset:33792
ds_read_b64_tr_b16 a[2:3],v10 offset:34304
v_mfma_f32_16x16x32_bf16 v[244:247],a[4:7],a[36:39],0
v_mfma_f32_16x16x32_bf16 v[248:251],a[4:7],a[40:43],0
v_mfma_f32_16x16x32_bf16 v[252:255],a[4:7],a[44:47],0
s_waitcnt lgkmcnt(4)
v_mfma_f32_16x16x32_bf16 v[96:99],a[8:11],a[48:51],v[96:99]
s_add_u32 s60,s60,0x1000
ds_read_b64_tr_b16 a[4:5],v10 offset:50176
ds_read_b64_tr_b16 a[6:7],v10 offset:50688
v_mfma_f32_16x16x32_bf16 v[100:103],a[8:11],a[52:55],v[100:103]
buffer_load_dwordx4 a[80:83],%[vL16],%[srdW],s60 offen offset:0 sc1
v_mfma_f32_16x16x32_bf16 v[104:107],a[8:11],a[56:59],v[104:107]
v_mfma_f32_16x16x32_bf16 v[108:111],a[8:11],a[60:63],v[108:111]
s_waitcnt lgkmcnt(4)
v_mfma_f32_16x16x32_bf16 v[112:115],a[12:15],a[48:51],v[112:115]
ds_read_b64_tr_b16 a[8:9],v11 offset:1024
ds_read_b64_tr_b16 a[10:11],v11 offset:1536
v_mfma_f32_16x16x32_bf16 v[116:119],a[12:15],a[52:55],v[116:119]
buffer_load_dwordx4 a[84:87],%[vL16],%[srdW],s60 offen offset:1024 sc1
v_mfma_f32_16x16x32_bf16 v[120:123],a[12:15],a[56:59],v[120:123]
v_mfma_f32_16x16x32_bf16 v[124:127],a[12:15],a[60:63],v[124:127]
s_waitcnt lgkmcnt(4)
v_mfma_f32_16x16x32_bf16 v[128:131],a[0:3],a[48:51],v[128:131]
ds_read_b64_tr_b16 a[12:13],v11 offset:17408
ds_read_b64_tr_b16 a[14:15],v11 offset:17920
v_mfma_f32_16x16x32_bf16 v[132:135],a[0:3],a[52:55],v[132:135]
buffer_load_dwordx4 a[88:91],%[vL16],%[srdW],s60 offen offset:2048 sc1
v_mfma_f32_16x16x32_bf16 v[136:139],a[0:3],a[56:59],v[136:139]
v_mfma_f32_16x16x32_bf16 v[140:143],a[0:3],a[60:63],v[140:143]
s_waitcnt lgkmcnt(4)
v_mfma_f32_16x16x32_bf16 v[144:147],a[4:7],a[48:51],v[144:147]
ds_read_b64_tr_b16 a[0:1],v11 offset:33792
ds_read_b64_tr_b16 a[2:3],v11 offset:34304
v_mfma_f32_16x16x32_bf16 v[148:151],a[4:7],a[52:55],v[148:151]
buffer_load_dwordx4 a[92:95],%[vL16],%[srdW],s60 offen offset:3072 sc1
v_mfma_f32_16x16x32_bf16 v[152:155],a[4:7],a[56:59],v[152:155]
v_mfma_f32_16x16x32_bf16 v[156:159],a[4:7],a[60:63],v[156:159]
s_waitcnt lgkmcnt(4)
v_mfma_f32_16x16x32_bf16 v[160:163],a[8:11],a[48:51],v[160:163]
ds_read_b64_tr_b16 a[4:5],v11 offset:50176
ds_read_b64_tr_b16 a[6:7],v11 offset:50688
v_mfma_f32_16x16x32_bf16 v[164:167],a[8:11],a[52:55],v[164:167]
v_mfma_f32_16x16x32_bf16 v[168:171],a[8:11],a[56:59],v[168:171]
v_mfma_f32_16x16x32_bf16 v[172:175],a[8:11],a[60:63],v[172:175]
s_waitcnt lgkmcnt(4)
v_mfma_f32_16x16x32_bf16 v[176:179],a[12:15],a[48:51],v[176:179]
ds_read_b64_tr_b16 a[8:9],v12 offset:1024
ds_read_b64_tr_b16 a[10:11],v12 offset:1536
v_mfma_f32_16x16x32_bf16 v[180:183],a[12:15],a[52:55],v[180:183]
v_mfma_f32_16x16x32_bf16 v[184:187],a[12:15],a[56:59],v[184:187]
v_mfma_f32_16x16x32_bf16 v[188:191],a[12:15],a[60:63],v[188:191]
s_waitcnt lgkmcnt(4)
v_mfma_f32_16x16x32_bf16 v[192:195],a[0:3],a[48:51],v[192:195]
ds_read_b64_tr_b16 a[12:13],v12 offset:17408
ds_read_b64_tr_b16 a[14:15],v12 offset:17920
v_mfma_f32_16x16x32_bf16 v[196:199],a[0:3],a[52:55],v[196:199]
v_mfma_f32_16x16x32_bf16 v[200:203],a[0:3],a[56:59],v[200:203]
v_mfma_f32_16x16x32_bf16 v[204:207],a[0:3],a[60:63],v[204:207]
s_waitcnt lgkmcnt(4)
v_mfma_f32_16x16x32_bf16 v[208:211],a[4:7],a[48:51],v[208:211]
s_waitcnt vmcnt(13)
s_barrier
ds_read_b64_tr_b16 a[0:1],v10 offset:2048
ds_read_b64_tr_b16 a[2:3],v10 offset:2560
v_mfma_f32_16x16x32_bf16 v[212:215],a[4:7],a[52:55],v[212:215]
v_mfma_f32_16x16x32_bf16 v[216:219],a[4:7],a[56:59],v[216:219]
v_mfma_f32_16x16x32_bf16 v[220:223],a[4:7],a[60:63],v[220:223]
s_waitcnt lgkmcnt(4)
v_mfma_f32_16x16x32_bf16 v[224:227],a[8:11],a[48:51],v[224:227]
ds_read_b64_tr_b16 a[4:5],v10 offset:18432
ds_read_b64_tr_b16 a[6:7],v10 offset:18944
v_mfma_f32_16x16x32_bf16 v[228:231],a[8:11],a[52:55],v[228:231]
v_mfma_f32_16x16x32_bf16 v[232:235],a[8:11],a[56:59],v[232:235]
v_mfma_f32_16x16x32_bf16 v[236:239],a[8:11],a[60:63],v[236:239]
s_waitcnt lgkmcnt(4)
v_mfma_f32_16x16x32_bf16 v[240:243],a[12:15],a[48:51],v[240:243]
ds_read_b64_tr_b16 a[8:9],v10 offset:34816
ds_read_b64_tr_b16 a[10:11],v10 offset:35328
v_mfma_f32_16x16x32_bf16 v[244:247],a[12:15],a[52:55],v[244:247]
v_mfma_f32_16x16x32_bf16 v[248:251],a[12:15],a[56:59],v[248:251]
v_mfma_f32_16x16x32_bf16 v[252:255],a[12:15],a[60:63],v[252:255]
s_waitcnt lgkmcnt(4)
s_waitcnt vmcnt(12)
v_mfma_f32_16x16x32_bf16 v[96:99],a[0:3],a[64:67],v[96:99]
s_waitcnt vmcnt(11)
ds_read_b64_tr_b16 a[12:13],v10 offset:51200
ds_read_b64_tr_b16 a[14:15],v10 offset:51712
v_mfma_f32_16x16x32_bf16 v[100:103],a[0:3],a[68:71],v[100:103]
s_waitcnt vmcnt(10)
s_add_u32 s60,s60,0x1000
v_mfma_f32_16x16x32_bf16 v[104:107],a[0:3],a[72:75],v[104:107]
s_waitcnt vmcnt(9)
buffer_load_dwordx4 a[32:35],%[vL16],%[srdW],s60 offen offset:0 sc1
v_mfma_f32_16x16x32_bf16 v[108:111],a[0:3],a[76:79],v[108:111]
s_waitcnt lgkmcnt(4)
v_mfma_f32_16x16x32_bf16 v[112:115],a[4:7],a[64:67],v[112:115]
ds_read_b64_tr_b16 a[0:1],v11 offset:2048
ds_read_b64_tr_b16 a[2:3],v11 offset:2560
v_mfma_f32_16x16x32_bf16 v[116:119],a[4:7],a[68:71],v[116:119]
buffer_load_dwordx4 a[36:39],%[vL16],%[srdW],s60 offen offset:1024 sc1
v_mfma_f32_16x16x32_bf16 v[120:123],a[4:7],a[72:75],v[120:123]
v_mfma_f32_16x16x32_bf16 v[124:127],a[4:7],a[76:79],v[124:127]
s_waitcnt lgkmcnt(4)
v_mfma_f32_16x16x32_bf16 v[128:131],a[8:11],a[64:67],v[128:131]
ds_read_b64_tr_b16 a[4:5],v11 offset:18432
ds_read_b64_tr_b16 a[6:7],v11 offset:18944
v_mfma_f32_16x16x32_bf16 v[132:135],a[8:11],a[68:71],v[132:135]
buffer_load_dwordx4 a[40:43],%[vL16],%[srdW],s60 offen offset:2048 sc1
v_mfma_f32_16x16x32_bf16 v[136:139],a[8:11],a[72:75],v[136:139]
v_mfma_f32_16x16x32_bf16 v[140:143],a[8:11],a[76:79],v[140:143]
s_waitcnt lgkmcnt(4)
v_mfma_f32_16x16x32_bf16 v[144:147],a[12:15],a[64:67],v[144:147]
ds_read_b64_tr_b16 a[8:9],v11 offset:34816
ds_read_b64_tr_b16 a[10:11],v11 offset:35328
v_mfma_f32_16x16x32_bf16 v[148:151],a[12:15],a[68:71],v[148:151]
buffer_load_dwordx4 a[44:47],%[vL16],%[srdW],s60 offen offset:3072 sc1
v_mfma_f32_16x16x32_bf16 v[152:155],a[12:15],a[72:75],v[152:155]
s_add_u32 m0,s68,6144
s_add_u32 s61,s69,6144
v_mfma_f32_16x16x32_bf16 v[156:159],a[12:15],a[76:79],v[156:159]
s_waitcnt lgkmcnt(4)
v_mfma_f32_16x16x32_bf16 v[160:163],a[0:3],a[64:67],v[160:163]
ds_read_b64_tr_b16 a[12:13],v11 offset:51200
ds_read_b64_tr_b16 a[14:15],v11 offset:51712
v_mfma_f32_16x16x32_bf16 v[164:167],a[0:3],a[68:71],v[164:167]
buffer_load_dwordx4 v22,%[srdW],s61 offen sc1 lds
v_mfma_f32_16x16x32_bf16 v[168:171],a[0:3],a[72:75],v[168:171]
s_add_u32 m0,s68,22528
s_add_u32 s61,s69,26624
v_mfma_f32_16x16x32_bf16 v[172:175],a[0:3],a[76:79],v[172:175]
s_waitcnt lgkmcnt(4)
v_mfma_f32_16x16x32_bf16 v[176:179],a[4:7],a[64:67],v[176:179]
ds_read_b64_tr_b16 a[0:1],v12 offset:2048
ds_read_b64_tr_b16 a[2:3],v12 offset:2560
v_mfma_f32_16x16x32_bf16 v[180:183],a[4:7],a[68:71],v[180:183]
buffer_load_dwordx4 v23,%[srdW],s61 offen sc1 lds
v_mfma_f32_16x16x32_bf16 v[184:187],a[4:7],a[72:75],v[184:187]
s_add_u32 m0,s68,38912
s_add_u32 s61,s69,47104
v_mfma_f32_16x16x32_bf16 v[188:191],a[4:7],a[76:79],v[188:191]
s_waitcnt lgkmcnt(4)
v_mfma_f32_16x16x32_bf16 v[192:195],a[8:11],a[64:67],v[192:195]
ds_read_b64_tr_b16 a[4:5],v12 offset:18432
ds_read_b64_tr_b16 a[6:7],v12 offset:18944
v_mfma_f32_16x16x32_bf16 v[196:199],a[8:11],a[68:71],v[196:199]
buffer_load_dwordx4 v24,%[srdW],s61 offen sc1 lds
v_mfma_f32_16x16x32_bf16 v[200:203],a[8:11],a[72:75],v[200:203]
s_add_u32 m0,s68,55296
s_add_u32 s61,s69,67584
v_mfma_f32_16x16x32_bf16 v[204:207],a[8:11],a[76:79],v[204:207]
s_waitcnt lgkmcnt(4)
v_mfma_f32_16x16x32_bf16 v[208:211],a[12:15],a[64:67],v[208:211]
ds_read_b64_tr_b16 a[8:9],v10 offset:3072
ds_read_b64_tr_b16 a[10:11],v10 offset:3584
v_mfma_f32_16x16x32_bf16 v[212:215],a[12:15],a[68:71],v[212:215]
buffer_load_dwordx4 v25,%[srdW],s61 offen sc1 lds
v_mfma_f32_16x16x32_bf16 v[216:219],a[12:15],a[72:75],v[216:219]
s_add_u32 m0,s68,71680
s_add_u32 s61,s69,88064
v_mfma_f32_16x16x32_bf16 v[220:223],a[12:15],a[76:79],v[220:223]
s_waitcnt lgkmcnt(4)
v_mfma_f32_16x16x32_bf16 v[224:227],a[0:3],a[64:67],v[224:227]
ds_read_b64_tr_b16 a[12:13],v10 offset:19456
ds_read_b64_tr_b16 a[14:15],v10 offset:19968
v_mfma_f32_16x16x32_bf16 v[228:231],a[0:3],a[68:71],v[228:231]
buffer_load_dwordx4 v26,%[srdW],s61 offen sc1 lds
v_mfma_f32_16x16x32_bf16 v[232:235],a[0:3],a[72:75],v[232:235]
v_mfma_f32_16x16x32_bf16 v[236:239],a[0:3],a[76:79],v[236:239]
s_waitcnt lgkmcnt(4)
v_mfma_f32_16x16x32_bf16 v[240:243],a[4:7],a[64:67],v[240:243]
ds_read_b64_tr_b16 a[0:1],v10 offset:35840
ds_read_b64_tr_b16 a[2:3],v10 offset:36352
v_mfma_f32_16x16x32_bf16 v[244:247],a[4:7],a[68:71],v[244:247]
v_mfma_f32_16x16x32_bf16 v[248:251],a[4:7],a[72:75],v[248:251]
v_mfma_f32_16x16x32_bf16 v[252:255],a[4:7],a[76:79],v[252:255]
s_waitcnt lgkmcnt(4)
s_waitcnt vmcnt(12)
v_mfma_f32_16x16x32_bf16 v[96:99],a[8:11],a[80:83],v[96:99]
s_waitcnt vmcnt(11)
ds_read_b64_tr_b16 a[4:5],v10 offset:52224
ds_read_b64_tr_b16 a[6:7],v10 offset:52736
v_mfma_f32_16x16x32_bf16 v[100:103],a[8:11],a[84:87],v[100:103]
s_waitcnt vmcnt(10)
s_add_u32 s60,s60,0x1000
v_mfma_f32_16x16x32_bf16 v[104:107],a[8:11],a[88:91],v[104:107]
s_waitcnt vmcnt(9)
buffer_load_dwordx4 a[48:51],%[vL16],%[srdW],s60 offen offset:0 sc1
v_mfma_f32_16x16x32_bf16 v[108:111],a[8:11],a[92:95],v[108:111]
s_waitcnt lgkmcnt(4)
v_mfma_f32_16x16x32_bf16 v[112:115],a[12:15],a[80:83],v[112:115]
ds_read_b64_tr_b16 a[8:9],v11 offset:3072
ds_read_b64_tr_b16 a[10:11],v11 offset:3584
v_mfma_f32_16x16x32_bf16 v[116:119],a[12:15],a[84:87],v[116:119]
buffer_load_dwordx4 a[52:55],%[vL16],%[srdW],s60 offen offset:1024 sc1
v_mfma_f32_16x16x32_bf16 v[120:123],a[12:15],a[88:91],v[120:123]
v_mfma_f32_16x16x32_bf16 v[124:127],a[12:15],a[92:95],v[124:127]
s_waitcnt lgkmcnt(4)
v_mfma_f32_16x16x32_bf16 v[128:131],a[0:3],a[80:83],v[128:131]
ds_read_b64_tr_b16 a[12:13],v11 offset:19456
ds_read_b64_tr_b16 a[14:15],v11 offset:19968
v_mfma_f32_16x16x32_bf16 v[132:135],a[0:3],a[84:87],v[132:135]
buffer_load_dwordx4 a[56:59],%[vL16],%[srdW],s60 offen offset:2048 sc1
v_mfma_f32_16x16x32_bf16 v[136:139],a[0:3],a[88:91],v[136:139]
v_mfma_f32_16x16x32_bf16 v[140:143],a[0:3],a[92:95],v[140:143]
s_waitcnt lgkmcnt(4)
v_mfma_f32_16x16x32_bf16 v[144:147],a[4:7],a[80:83],v[144:147]
ds_read_b64_tr_b16 a[0:1],v11 offset:35840
ds_read_b64_tr_b16 a[2:3],v11 offset:36352
v_mfma_f32_16x16x32_bf16 v[148:151],a[4:7],a[84:87],v[148:151]
buffer_load_dwordx4 a[60:63],%[vL16],%[srdW],s60 offen offset:3072 sc1
v_mfma_f32_16x16x32_bf16 v[152:155],a[4:7],a[88:91],v[152:155]
v_mfma_f32_16x16x32_bf16 v[156:159],a[4:7],a[92:95],v[156:159]
s_waitcnt lgkmcnt(4)
v_mfma_f32_16x16x32_bf16 v[160:163],a[8:11],a[80:83],v[160:163]
ds_read_b64_tr_b16 a[4:5],v11 offset:52224
ds_read_b64_tr_b16 a[6:7],v11 offset:52736
v_mfma_f32_16x16x32_bf16 v[164:167],a[8:11],a[84:87],v[164:167]
v_mfma_f32_16x16x32_bf16 v[168:171],a[8:11],a[88:91],v[168:171]
v_mfma_f32_16x16x32_bf16 v[172:175],a[8:11],a[92:95],v[172:175]
s_waitcnt lgkmcnt(4)
v_mfma_f32_16x16x32_bf16 v[176:179],a[12:15],a[80:83],v[176:179]
ds_read_b64_tr_b16 a[8:9],v12 offset:3072
ds_read_b64_tr_b16 a[10:11],v12 offset:3584
v_mfma_f32_16x16x32_bf16 v[180:183],a[12:15],a[84:87],v[180:183]
v_mfma_f32_16x16x32_bf16 v[184:187],a[12:15],a[88:91],v[184:187]
v_mfma_f32_16x16x32_bf16 v[188:191],a[12:15],a[92:95],v[188:191]
s_waitcnt lgkmcnt(4)
v_mfma_f32_16x16x32_bf16 v[192:195],a[0:3],a[80:83],v[192:195]
ds_read_b64_tr_b16 a[12:13],v12 offset:19456
ds_read_b64_tr_b16 a[14:15],v12 offset:19968
v_mfma_f32_16x16x32_bf16 v[196:199],a[0:3],a[84:87],v[196:199]
v_mfma_f32_16x16x32_bf16 v[200:203],a[0:3],a[88:91],v[200:203]
v_mfma_f32_16x16x32_bf16 v[204:207],a[0:3],a[92:95],v[204:207]
s_waitcnt lgkmcnt(4)
v_mfma_f32_16x16x32_bf16 v[208:211],a[4:7],a[80:83],v[208:211]
s_barrier
ds_read_b64_tr_b16 a[0:1],v10 offset:4096
ds_read_b64_tr_b16 a[2:3],v10 offset:4608
v_mfma_f32_16x16x32_bf16 v[212:215],a[4:7],a[84:87],v[212:215]
v_mfma_f32_16x16x32_bf16 v[216:219],a[4:7],a[88:91],v[216:219]
v_mfma_f32_16x16x32_bf16 v[220:223],a[4:7],a[92:95],v[220:223]
s_waitcnt lgkmcnt(4)
v_mfma_f32_16x16x32_bf16 v[224:227],a[8:11],a[80:83],v[224:227]
ds_read_b64_tr_b16 a[4:5],v10 offset:20480
ds_read_b64_tr_b16 a[6:7],v10 offset:20992
v_mfma_f32_16x16x32_bf16 v[228:231],a[8:11],a[84:87],v[228:231]
v_mfma_f32_16x16x32_bf16 v[232:235],a[8:11],a[88:91],v[232:235]
v_mfma_f32_16x16x32_bf16 v[236:239],a[8:11],a[92:95],v[236:239]
s_waitcnt lgkmcnt(4)
v_mfma_f32_16x16x32_bf16 v[240:243],a[12:15],a[80:83],v[240:243]
ds_read_b64_tr_b16 a[8:9],v10 offset:36864
ds_read_b64_tr_b16 a[10:11],v10 offset:37376
v_mfma_f32_16x16x32_bf16 v[244:247],a[12:15],a[84:87],v[244:247]
v_mfma_f32_16x16x32_bf16 v[248:251],a[12:15],a[88:91],v[248:251]
v_mfma_f32_16x16x32_bf16 v[252:255],a[12:15],a[92:95],v[252:255]
s_waitcnt lgkmcnt(4)
s_waitcnt vmcnt(12)
v_mfma_f32_16x16x32_bf16 v[96:99],a[0:3],a[32:35],v[96:99]
s_waitcnt vmcnt(11)
ds_read_b64_tr_b16 a[12:13],v10 offset:53248
)"
R"(ds_read_b64_tr_b16 a[14:15],v10 offset:53760
v_mfma_f32_16x16x32_bf16 v[100:103],a[0:3],a[36:39],v[100:103]
s_waitcnt vmcnt(10)
s_add_u32 s60,s60,0x1000
v_mfma_f32_16x16x32_bf16 v[104:107],a[0:3],a[40:43],v[104:107]
s_waitcnt vmcnt(9)
buffer_load_dwordx4 a[64:67],%[vL16],%[srdW],s60 offen offset:0 sc1
v_mfma_f32_16x16x32_bf16 v[108:111],a[0:3],a[44:47],v[108:111]
s_waitcnt lgkmcnt(4)
v_mfma_f32_16x16x32_bf16 v[112:115],a[4:7],a[32:35],v[112:115]
ds_read_b64_tr_b16 a[0:1],v11 offset:4096
ds_read_b64_tr_b16 a[2:3],v11 offset:4608
v_mfma_f32_16x16x32_bf16 v[116:119],a[4:7],a[36:39],v[116:119]
buffer_load_dwordx4 a[68:71],%[vL16],%[srdW],s60 offen offset:1024 sc1
v_mfma_f32_16x16x32_bf16 v[120:123],a[4:7],a[40:43],v[120:123]
v_mfma_f32_16x16x32_bf16 v[124:127],a[4:7],a[44:47],v[124:127]
s_waitcnt lgkmcnt(4)
v_mfma_f32_16x16x32_bf16 v[128:131],a[8:11],a[32:35],v[128:131]
ds_read_b64_tr_b16 a[4:5],v11 offset:20480
ds_read_b64_tr_b16 a[6:7],v11 offset:20992
v_mfma_f32_16x16x32_bf16 v[132:135],a[8:11],a[36:39],v[132:135]
buffer_load_dwordx4 a[72:75],%[vL16],%[srdW],s60 offen offset:2048 sc1
v_mfma_f32_16x16x32_bf16 v[136:139],a[8:11],a[40:43],v[136:139]
v_mfma_f32_16x16x32_bf16 v[140:143],a[8:11],a[44:47],v[140:143]
s_waitcnt lgkmcnt(4)
v_mfma_f32_16x16x32_bf16 v[144:147],a[12:15],a[32:35],v[144:147]
ds_read_b64_tr_b16 a[8:9],v11 offset:36864
ds_read_b64_tr_b16 a[10:11],v11 offset:37376
v_mfma_f32_16x16x32_bf16 v[148:151],a[12:15],a[36:39],v[148:151]
buffer_load_dwordx4 a[76:79],%[vL16],%[srdW],s60 offen offset:3072 sc1
v_mfma_f32_16x16x32_bf16 v[152:155],a[12:15],a[40:43],v[152:155]
s_add_u32 m0,s68,8192
s_add_u32 s61,s69,8192
v_mfma_f32_16x16x32_bf16 v[156:159],a[12:15],a[44:47],v[156:159]
s_waitcnt lgkmcnt(4)
v_mfma_f32_16x16x32_bf16 v[160:163],a[0:3],a[32:35],v[160:163]
ds_read_b64_tr_b16 a[12:13],v11 offset:53248
ds_read_b64_tr_b16 a[14:15],v11 offset:53760
v_mfma_f32_16x16x32_bf16 v[164:167],a[0:3],a[36:39],v[164:167]
buffer_load_dwordx4 v22,%[srdW],s61 offen sc1 lds
v_mfma_f32_16x16x32_bf16 v[168:171],a[0:3],a[40:43],v[168:171]
s_add_u32 m0,s68,24576
s_add_u32 s61,s69,28672
v_mfma_f32_16x16x32_bf16 v[172:175],a[0:3],a[44:47],v[172:175]
s_waitcnt lgkmcnt(4)
v_mfma_f32_16x16x32_bf16 v[176:179],a[4:7],a[32:35],v[176:179]
ds_read_b64_tr_b16 a[0:1],v12 offset:4096
ds_read_b64_tr_b16 a[2:3],v12 offset:4608
v_mfma_f32_16x16x32_bf16 v[180:183],a[4:7],a[36:39],v[180:183]
buffer_load_dwordx4 v23,%[srdW],s61 offen sc1 lds
v_mfma_f32_16x16x32_bf16 v[184:187],a[4:7],a[40:43],v[184:187]
s_add_u32 m0,s68,40960
s_add_u32 s61,s69,49152
v_mfma_f32_16x16x32_bf16 v[188:191],a[4:7],a[44:47],v[188:191]
s_waitcnt lgkmcnt(4)
v_mfma_f32_16x16x32_bf16 v[192:195],a[8:11],a[32:35],v[192:195]
ds_read_b64_tr_b16 a[4:5],v12 offset:20480
ds_read_b64_tr_b16 a[6:7],v12 offset:20992
v_mfma_f32_16x16x32_bf16 v[196:199],a[8:11],a[36:39],v[196:199]
buffer_load_dwordx4 v24,%[srdW],s61 offen sc1 lds
v_mfma_f32_16x16x32_bf16 v[200:203],a[8:11],a[40:43],v[200:203]
s_add_u32 m0,s68,57344
s_add_u32 s61,s69,69632
v_mfma_f32_16x16x32_bf16 v[204:207],a[8:11],a[44:47],v[204:207]
s_waitcnt lgkmcnt(4)
v_mfma_f32_16x16x32_bf16 v[208:211],a[12:15],a[32:35],v[208:211]
ds_read_b64_tr_b16 a[8:9],v10 offset:5120
ds_read_b64_tr_b16 a[10:11],v10 offset:5632
v_mfma_f32_16x16x32_bf16 v[212:215],a[12:15],a[36:39],v[212:215]
buffer_load_dwordx4 v25,%[srdW],s61 offen sc1 lds
v_mfma_f32_16x16x32_bf16 v[216:219],a[12:15],a[40:43],v[216:219]
s_add_u32 m0,s68,73728
s_add_u32 s61,s69,90112
v_mfma_f32_16x16x32_bf16 v[220:223],a[12:15],a[44:47],v[220:223]
s_waitcnt lgkmcnt(4)
v_mfma_f32_16x16x32_bf16 v[224:227],a[0:3],a[32:35],v[224:227]
ds_read_b64_tr_b16 a[12:13],v10 offset:21504
ds_read_b64_tr_b16 a[14:15],v10 offset:22016
v_mfma_f32_16x16x32_bf16 v[228:231],a[0:3],a[36:39],v[228:231]
buffer_load_dwordx4 v26,%[srdW],s61 offen sc1 lds
v_mfma_f32_16x16x32_bf16 v[232:235],a[0:3],a[40:43],v[232:235]
v_mfma_f32_16x16x32_bf16 v[236:239],a[0:3],a[44:47],v[236:239]
s_waitcnt lgkmcnt(4)
v_mfma_f32_16x16x32_bf16 v[240:243],a[4:7],a[32:35],v[240:243]
ds_read_b64_tr_b16 a[0:1],v10 offset:37888
ds_read_b64_tr_b16 a[2:3],v10 offset:38400
v_mfma_f32_16x16x32_bf16 v[244:247],a[4:7],a[36:39],v[244:247]
v_mfma_f32_16x16x32_bf16 v[248:251],a[4:7],a[40:43],v[248:251]
v_mfma_f32_16x16x32_bf16 v[252:255],a[4:7],a[44:47],v[252:255]
s_waitcnt lgkmcnt(4)
s_waitcnt vmcnt(12)
v_mfma_f32_16x16x32_bf16 v[96:99],a[8:11],a[48:51],v[96:99]
s_waitcnt vmcnt(11)
ds_read_b64_tr_b16 a[4:5],v10 offset:54272
ds_read_b64_tr_b16 a[6:7],v10 offset:54784
v_mfma_f32_16x16x32_bf16 v[100:103],a[8:11],a[52:55],v[100:103]
s_waitcnt vmcnt(10)
s_add_u32 s60,s60,0x1000
v_mfma_f32_16x16x32_bf16 v[104:107],a[8:11],a[56:59],v[104:107]
s_waitcnt vmcnt(9)
buffer_load_dwordx4 a[80:83],%[vL16],%[srdW],s60 offen offset:0 sc1
v_mfma_f32_16x16x32_bf16 v[108:111],a[8:11],a[60:63],v[108:111]
s_waitcnt lgkmcnt(4)
v_mfma_f32_16x16x32_bf16 v[112:115],a[12:15],a[48:51],v[112:115]
ds_read_b64_tr_b16 a[8:9],v11 offset:5120
ds_read_b64_tr_b16 a[10:11],v11 offset:5632
v_mfma_f32_16x16x32_bf16 v[116:119],a[12:15],a[52:55],v[116:119]
buffer_load_dwordx4 a[84:87],%[vL16],%[srdW],s60 offen offset:1024 sc1
v_mfma_f32_16x16x32_bf16 v[120:123],a[12:15],a[56:59],v[120:123]
v_mfma_f32_16x16x32_bf16 v[124:127],a[12:15],a[60:63],v[124:127]
s_waitcnt lgkmcnt(4)
v_mfma_f32_16x16x32_bf16 v[128:131],a[0:3],a[48:51],v[128:131]
ds_read_b64_tr_b16 a[12:13],v11 offset:21504
ds_read_b64_tr_b16 a[14:15],v11 offset:22016
v_mfma_f32_16x16x32_bf16 v[132:135],a[0:3],a[52:55],v[132:135]
buffer_load_dwordx4 a[88:91],%[vL16],%[srdW],s60 offen offset:2048 sc1
v_mfma_f32_16x16x32_bf16 v[136:139],a[0:3],a[56:59],v[136:139]
v_mfma_f32_16x16x32_bf16 v[140:143],a[0:3],a[60:63],v[140:143]
s_waitcnt lgkmcnt(4)
v_mfma_f32_16x16x32_bf16 v[144:147],a[4:7],a[48:51],v[144:147]
ds_read_b64_tr_b16 a[0:1],v11 offset:37888
ds_read_b64_tr_b16 a[2:3],v11 offset:38400
v_mfma_f32_16x16x32_bf16 v[148:151],a[4:7],a[52:55],v[148:151]
buffer_load_dwordx4 a[92:95],%[vL16],%[srdW],s60 offen offset:3072 sc1
v_mfma_f32_16x16x32_bf16 v[152:155],a[4:7],a[56:59],v[152:155]
v_mfma_f32_16x16x32_bf16 v[156:159],a[4:7],a[60:63],v[156:159]
s_waitcnt lgkmcnt(4)
v_mfma_f32_16x16x32_bf16 v[160:163],a[8:11],a[48:51],v[160:163]
ds_read_b64_tr_b16 a[4:5],v11 offset:54272
ds_read_b64_tr_b16 a[6:7],v11 offset:54784
v_mfma_f32_16x16x32_bf16 v[164:167],a[8:11],a[52:55],v[164:167]
v_mfma_f32_16x16x32_bf16 v[168:171],a[8:11],a[56:59],v[168:171]
v_mfma_f32_16x16x32_bf16 v[172:175],a[8:11],a[60:63],v[172:175]
s_waitcnt lgkmcnt(4)
v_mfma_f32_16x16x32_bf16 v[176:179],a[12:15],a[48:51],v[176:179]
ds_read_b64_tr_b16 a[8:9],v12 offset:5120
ds_read_b64_tr_b16 a[10:11],v12 offset:5632
v_mfma_f32_16x16x32_bf16 v[180:183],a[12:15],a[52:55],v[180:183]
v_mfma_f32_16x16x32_bf16 v[184:187],a[12:15],a[56:59],v[184:187]
v_mfma_f32_16x16x32_bf16 v[188:191],a[12:15],a[60:63],v[188:191]
s_waitcnt lgkmcnt(4)
v_mfma_f32_16x16x32_bf16 v[192:195],a[0:3],a[48:51],v[192:195]
ds_read_b64_tr_b16 a[12:13],v12 offset:21504
ds_read_b64_tr_b16 a[14:15],v12 offset:22016
v_mfma_f32_16x16x32_bf16 v[196:199],a[0:3],a[52:55],v[196:199]
v_mfma_f32_16x16x32_bf16 v[200:203],a[0:3],a[56:59],v[200:203]
v_mfma_f32_16x16x32_bf16 v[204:207],a[0:3],a[60:63],v[204:207]
s_waitcnt lgkmcnt(4)
v_mfma_f32_16x16x32_bf16 v[208:211],a[4:7],a[48:51],v[208:211]
s_barrier
ds_read_b64_tr_b16 a[0:1],v10 offset:6144
ds_read_b64_tr_b16 a[2:3],v10 offset:6656
v_mfma_f32_16x16x32_bf16 v[212:215],a[4:7],a[52:55],v[212:215]
v_mfma_f32_16x16x32_bf16 v[216:219],a[4:7],a[56:59],v[216:219]
v_mfma_f32_16x16x32_bf16 v[220:223],a[4:7],a[60:63],v[220:223]
s_waitcnt lgkmcnt(4)
v_mfma_f32_16x16x32_bf16 v[224:227],a[8:11],a[48:51],v[224:227]
ds_read_b64_tr_b16 a[4:5],v10 offset:22528
ds_read_b64_tr_b16 a[6:7],v10 offset:23040
v_mfma_f32_16x16x32_bf16 v[228:231],a[8:11],a[52:55],v[228:231]
v_mfma_f32_16x16x32_bf16 v[232:235],a[8:11],a[56:59],v[232:235]
v_mfma_f32_16x16x32_bf16 v[236:239],a[8:11],a[60:63],v[236:239]
s_waitcnt lgkmcnt(4)
v_mfma_f32_16x16x32_bf16 v[240:243],a[12:15],a[48:51],v[240:243]
ds_read_b64_tr_b16 a[8:9],v10 offset:38912
ds_read_b64_tr_b16 a[10:11],v10 offset:39424
v_mfma_f32_16x16x32_bf16 v[244:247],a[12:15],a[52:55],v[244:247]
v_mfma_f32_16x16x32_bf16 v[248:251],a[12:15],a[56:59],v[248:251]
v_mfma_f32_16x16x32_bf16 v[252:255],a[12:15],a[60:63],v[252:255]
s_waitcnt lgkmcnt(4)
s_waitcnt vmcnt(12)
v_mfma_f32_16x16x32_bf16 v[96:99],a[0:3],a[64:67],v[96:99]
s_waitcnt vmcnt(11)
ds_read_b64_tr_b16 a[12:13],v10 offset:55296
ds_read_b64_tr_b16 a[14:15],v10 offset:55808
v_mfma_f32_16x16x32_bf16 v[100:103],a[0:3],a[68:71],v[100:103]
s_waitcnt vmcnt(10)
s_add_u32 s60,s60,0x1000
v_mfma_f32_16x16x32_bf16 v[104:107],a[0:3],a[72:75],v[104:107]
s_waitcnt vmcnt(9)
buffer_load_dwordx4 a[32:35],%[vL16],%[srdW],s60 offen offset:0 sc1
v_mfma_f32_16x16x32_bf16 v[108:111],a[0:3],a[76:79],v[108:111]
s_waitcnt lgkmcnt(4)
v_mfma_f32_16x16x32_bf16 v[112:115],a[4:7],a[64:67],v[112:115]
ds_read_b64_tr_b16 a[0:1],v11 offset:6144
ds_read_b64_tr_b16 a[2:3],v11 offset:6656
v_mfma_f32_16x16x32_bf16 v[116:119],a[4:7],a[68:71],v[116:119]
buffer_load_dwordx4 a[36:39],%[vL16],%[srdW],s60 offen offset:1024 sc1
v_mfma_f32_16x16x32_bf16 v[120:123],a[4:7],a[72:75],v[120:123]
v_mfma_f32_16x16x32_bf16 v[124:127],a[4:7],a[76:79],v[124:127]
s_waitcnt lgkmcnt(4)
v_mfma_f32_16x16x32_bf16 v[128:131],a[8:11],a[64:67],v[128:131]
ds_read_b64_tr_b16 a[4:5],v11 offset:22528
ds_read_b64_tr_b16 a[6:7],v11 offset:23040
v_mfma_f32_16x16x32_bf16 v[132:135],a[8:11],a[68:71],v[132:135]
buffer_load_dwordx4 a[40:43],%[vL16],%[srdW],s60 offen offset:2048 sc1
v_mfma_f32_16x16x32_bf16 v[136:139],a[8:11],a[72:75],v[136:139]
v_mfma_f32_16x16x32_bf16 v[140:143],a[8:11],a[76:79],v[140:143]
s_waitcnt lgkmcnt(4)
v_mfma_f32_16x16x32_bf16 v[144:147],a[12:15],a[64:67],v[144:147]
ds_read_b64_tr_b16 a[8:9],v11 offset:38912
ds_read_b64_tr_b16 a[10:11],v11 offset:39424
v_mfma_f32_16x16x32_bf16 v[148:151],a[12:15],a[68:71],v[148:151]
buffer_load_dwordx4 a[44:47],%[vL16],%[srdW],s60 offen offset:3072 sc1
v_mfma_f32_16x16x32_bf16 v[152:155],a[12:15],a[72:75],v[152:155]
s_add_u32 m0,s68,10240
s_add_u32 s61,s69,10240
v_mfma_f32_16x16x32_bf16 v[156:159],a[12:15],a[76:79],v[156:159]
s_waitcnt lgkmcnt(4)
v_mfma_f32_16x16x32_bf16 v[160:163],a[0:3],a[64:67],v[160:163]
ds_read_b64_tr_b16 a[12:13],v11 offset:55296
ds_read_b64_tr_b16 a[14:15],v11 offset:55808
v_mfma_f32_16x16x32_bf16 v[164:167],a[0:3],a[68:71],v[164:167]
buffer_load_dwordx4 v22,%[srdW],s61 offen sc1 lds
v_mfma_f32_16x16x32_bf16 v[168:171],a[0:3],a[72:75],v[168:171]
s_add_u32 m0,s68,26624
s_add_u32 s61,s69,30720
v_mfma_f32_16x16x32_bf16 v[172:175],a[0:3],a[76:79],v[172:175]
s_waitcnt lgkmcnt(4)
v_mfma_f32_16x16x32_bf16 v[176:179],a[4:7],a[64:67],v[176:179]
ds_read_b64_tr_b16 a[0:1],v12 offset:6144
ds_read_b64_tr_b16 a[2:3],v12 offset:6656
v_mfma_f32_16x16x32_bf16 v[180:183],a[4:7],a[68:71],v[180:183]
buffer_load_dwordx4 v23,%[srdW],s61 offen sc1 lds
v_mfma_f32_16x16x32_bf16 v[184:187],a[4:7],a[72:75],v[184:187]
s_add_u32 m0,s68,43008
s_add_u32 s61,s69,51200
v_mfma_f32_16x16x32_bf16 v[188:191],a[4:7],a[76:79],v[188:191]
s_waitcnt lgkmcnt(4)
v_mfma_f32_16x16x32_bf16 v[192:195],a[8:11],a[64:67],v[192:195]
ds_read_b64_tr_b16 a[4:5],v12 offset:22528
ds_read_b64_tr_b16 a[6:7],v12 offset:23040
v_mfma_f32_16x16x32_bf16 v[196:199],a[8:11],a[68:71],v[196:199]
buffer_load_dwordx4 v24,%[srdW],s61 offen sc1 lds
v_mfma_f32_16x16x32_bf16 v[200:203],a[8:11],a[72:75],v[200:203]
s_add_u32 m0,s68,59392
s_add_u32 s61,s69,71680
v_mfma_f32_16x16x32_bf16 v[204:207],a[8:11],a[76:79],v[204:207]
s_waitcnt lgkmcnt(4)
v_mfma_f32_16x16x32_bf16 v[208:211],a[12:15],a[64:67],v[208:211]
ds_read_b64_tr_b16 a[8:9],v10 offset:7168
ds_read_b64_tr_b16 a[10:11],v10 offset:7680
v_mfma_f32_16x16x32_bf16 v[212:215],a[12:15],a[68:71],v[212:215]
buffer_load_dwordx4 v25,%[srdW],s61 offen sc1 lds
v_mfma_f32_16x16x32_bf16 v[216:219],a[12:15],a[72:75],v[216:219]
s_add_u32 m0,s68,75776
s_add_u32 s61,s69,92160
v_mfma_f32_16x16x32_bf16 v[220:223],a[12:15],a[76:79],v[220:223]
s_waitcnt lgkmcnt(4)
v_mfma_f32_16x16x32_bf16 v[224:227],a[0:3],a[64:67],v[224:227]
ds_read_b64_tr_b16 a[12:13],v10 offset:23552
ds_read_b64_tr_b16 a[14:15],v10 offset:24064
v_mfma_f32_16x16x32_bf16 v[228:231],a[0:3],a[68:71],v[228:231]
buffer_load_dwordx4 v26,%[srdW],s61 offen sc1 lds
v_mfma_f32_16x16x32_bf16 v[232:235],a[0:3],a[72:75],v[232:235]
v_mfma_f32_16x16x32_bf16 v[236:239],a[0:3],a[76:79],v[236:239]
s_waitcnt lgkmcnt(4)
v_mfma_f32_16x16x32_bf16 v[240:243],a[4:7],a[64:67],v[240:243]
ds_read_b64_tr_b16 a[0:1],v10 offset:39936
ds_read_b64_tr_b16 a[2:3],v10 offset:40448
v_mfma_f32_16x16x32_bf16 v[244:247],a[4:7],a[68:71],v[244:247]
v_mfma_f32_16x16x32_bf16 v[248:251],a[4:7],a[72:75],v[248:251]
v_mfma_f32_16x16x32_bf16 v[252:255],a[4:7],a[76:79],v[252:255]
s_waitcnt lgkmcnt(4)
s_waitcnt vmcnt(12)
v_mfma_f32_16x16x32_bf16 v[96:99],a[8:11],a[80:83],v[96:99]
s_waitcnt vmcnt(11)
ds_read_b64_tr_b16 a[4:5],v10 offset:56320
ds_read_b64_tr_b16 a[6:7],v10 offset:56832
v_mfma_f32_16x16x32_bf16 v[100:103],a[8:11],a[84:87],v[100:103]
s_waitcnt vmcnt(10)
s_add_u32 s60,s60,0x1000
v_mfma_f32_16x16x32_bf16 v[104:107],a[8:11],a[88:91],v[104:107]
s_waitcnt vmcnt(9)
buffer_load_dwordx4 a[48:51],%[vL16],%[srdW],s60 offen offset:0 sc1
v_mfma_f32_16x16x32_bf16 v[108:111],a[8:11],a[92:95],v[108:111]
s_waitcnt lgkmcnt(4)
v_mfma_f32_16x16x32_bf16 v[112:115],a[12:15],a[80:83],v[112:115]
ds_read_b64_tr_b16 a[8:9],v11 offset:7168
ds_read_b64_tr_b16 a[10:11],v11 offset:7680
v_mfma_f32_16x16x32_bf16 v[116:119],a[12:15],a[84:87],v[116:119]
buffer_load_dwordx4 a[52:55],%[vL16],%[srdW],s60 offen offset:1024 sc1
v_mfma_f32_16x16x32_bf16 v[120:123],a[12:15],a[88:91],v[120:123]
v_mfma_f32_16x16x32_bf16 v[124:127],a[12:15],a[92:95],v[124:127]
s_waitcnt lgkmcnt(4)
v_mfma_f32_16x16x32_bf16 v[128:131],a[0:3],a[80:83],v[128:131]
ds_read_b64_tr_b16 a[12:13],v11 offset:23552
ds_read_b64_tr_b16 a[14:15],v11 offset:24064
v_mfma_f32_16x16x32_bf16 v[132:135],a[0:3],a[84:87],v[132:135]
buffer_load_dwordx4 a[56:59],%[vL16],%[srdW],s60 offen offset:2048 sc1
v_mfma_f32_16x16x32_bf16 v[136:139],a[0:3],a[88:91],v[136:139]
v_mfma_f32_16x16x32_bf16 v[140:143],a[0:3],a[92:95],v[140:143]
s_waitcnt lgkmcnt(4)
v_mfma_f32_16x16x32_bf16 v[144:147],a[4:7],a[80:83],v[144:147]
ds_read_b64_tr_b16 a[0:1],v11 offset:39936
ds_read_b64_tr_b16 a[2:3],v11 offset:40448
v_mfma_f32_16x16x32_bf16 v[148:151],a[4:7],a[84:87],v[148:151]
buffer_load_dwordx4 a[60:63],%[vL16],%[srdW],s60 offen offset:3072 sc1
v_mfma_f32_16x16x32_bf16 v[152:155],a[4:7],a[88:91],v[152:155]
v_mfma_f32_16x16x32_bf16 v[156:159],a[4:7],a[92:95],v[156:159]
s_waitcnt lgkmcnt(4)
v_mfma_f32_16x16x32_bf16 v[160:163],a[8:11],a[80:83],v[160:163]
ds_read_b64_tr_b16 a[4:5],v11 offset:56320
ds_read_b64_tr_b16 a[6:7],v11 offset:56832
v_mfma_f32_16x16x32_bf16 v[164:167],a[8:11],a[84:87],v[164:167]
v_mfma_f32_16x16x32_bf16 v[168:171],a[8:11],a[88:91],v[168:171]
v_mfma_f32_16x16x32_bf16 v[172:175],a[8:11],a[92:95],v[172:175]
s_waitcnt lgkmcnt(4)
v_mfma_f32_16x16x32_bf16 v[176:179],a[12:15],a[80:83],v[176:179]
ds_read_b64_tr_b16 a[8:9],v12 offset:7168
ds_read_b64_tr_b16 a[10:11],v12 offset:7680
v_mfma_f32_16x16x32_bf16 v[180:183],a[12:15],a[84:87],v[180:183]
v_mfma_f32_16x16x32_bf16 v[184:187],a[12:15],a[88:91],v[184:187]
v_mfma_f32_16x16x32_bf16 v[188:191],a[12:15],a[92:95],v[188:191]
s_waitcnt lgkmcnt(4)
v_mfma_f32_16x16x32_bf16 v[192:195],a[0:3],a[80:83],v[192:195]
ds_read_b64_tr_b16 a[12:13],v12 offset:23552
ds_read_b64_tr_b16 a[14:15],v12 offset:24064
v_mfma_f32_16x16x32_bf16 v[196:199],a[0:3],a[84:87],v[196:199]
v_mfma_f32_16x16x32_bf16 v[200:203],a[0:3],a[88:91],v[200:203]
v_mfma_f32_16x16x32_bf16 v[204:207],a[0:3],a[92:95],v[204:207]
s_waitcnt lgkmcnt(4)
v_mfma_f32_16x16x32_bf16 v[208:211],a[4:7],a[80:83],v[208:211]
s_barrier
ds_read_b64_tr_b16 a[0:1],v10 offset:8192
ds_read_b64_tr_b16 a[2:3],v10 offset:8704
v_mfma_f32_16x16x32_bf16 v[212:215],a[4:7],a[84:87],v[212:215]
v_mfma_f32_16x16x32_bf16 v[216:219],a[4:7],a[88:91],v[216:219]
v_mfma_f32_16x16x32_bf16 v[220:223],a[4:7],a[92:95],v[220:223]
s_waitcnt lgkmcnt(4)
v_mfma_f32_16x16x32_bf16 v[224:227],a[8:11],a[80:83],v[224:227]
ds_read_b64_tr_b16 a[4:5],v10 offset:24576
ds_read_b64_tr_b16 a[6:7],v10 offset:25088
v_mfma_f32_16x16x32_bf16 v[228:231],a[8:11],a[84:87],v[228:231]
v_mfma_f32_16x16x32_bf16 v[232:235],a[8:11],a[88:91],v[232:235]
v_mfma_f32_16x16x32_bf16 v[236:239],a[8:11],a[92:95],v[236:239]
s_waitcnt lgkmcnt(4)
v_mfma_f32_16x16x32_bf16 v[240:243],a[12:15],a[80:83],v[240:243]
ds_read_b64_tr_b16 a[8:9],v10 offset:40960
ds_read_b64_tr_b16 a[10:11],v10 offset:41472
v_mfma_f32_16x16x32_bf16 v[244:247],a[12:15],a[84:87],v[244:247]
v_mfma_f32_16x16x32_bf16 v[248:251],a[12:15],a[88:91],v[248:251]
v_mfma_f32_16x16x32_bf16 v[252:255],a[12:15],a[92:95],v[252:255]
s_waitcnt lgkmcnt(4)
s_waitcnt vmcnt(12)
v_mfma_f32_16x16x32_bf16 v[96:99],a[0:3],a[32:35],v[96:99]
s_waitcnt vmcnt(11)
ds_read_b64_tr_b16 a[12:13],v10 offset:57344
ds_read_b64_tr_b16 a[14:15],v10 offset:57856
v_mfma_f32_16x16x32_bf16 v[100:103],a[0:3],a[36:39],v[100:103]
s_waitcnt vmcnt(10)
s_add_u32 s60,s60,0x1000
v_mfma_f32_16x16x32_bf16 v[104:107],a[0:3],a[40:43],v[104:107]
s_waitcnt vmcnt(9)
buffer_load_dwordx4 a[64:67],%[vL16],%[srdW],s60 offen offset:0 sc1
v_mfma_f32_16x16x32_bf16 v[108:111],a[0:3],a[44:47],v[108:111]
s_waitcnt lgkmcnt(4)
v_mfma_f32_16x16x32_bf16 v[112:115],a[4:7],a[32:35],v[112:115]
ds_read_b64_tr_b16 a[0:1],v11 offset:8192
ds_read_b64_tr_b16 a[2:3],v11 offset:8704
v_mfma_f32_16x16x32_bf16 v[116:119],a[4:7],a[36:39],v[116:119]
buffer_load_dwordx4 a[68:71],%[vL16],%[srdW],s60 offen offset:1024 sc1
v_mfma_f32_16x16x32_bf16 v[120:123],a[4:7],a[40:43],v[120:123]
v_mfma_f32_16x16x32_bf16 v[124:127],a[4:7],a[44:47],v[124:127]
s_waitcnt lgkmcnt(4)
v_mfma_f32_16x16x32_bf16 v[128:131],a[8:11],a[32:35],v[128:131]
ds_read_b64_tr_b16 a[4:5],v11 offset:24576
ds_read_b64_tr_b16 a[6:7],v11 offset:25088
v_mfma_f32_16x16x32_bf16 v[132:135],a[8:11],a[36:39],v[132:135]
buffer_load_dwordx4 a[72:75],%[vL16],%[srdW],s60 offen offset:2048 sc1
v_mfma_f32_16x16x32_bf16 v[136:139],a[8:11],a[40:43],v[136:139]
v_mfma_f32_16x16x32_bf16 v[140:143],a[8:11],a[44:47],v[140:143]
s_waitcnt lgkmcnt(4)
v_mfma_f32_16x16x32_bf16 v[144:147],a[12:15],a[32:35],v[144:147]
ds_read_b64_tr_b16 a[8:9],v11 offset:40960
ds_read_b64_tr_b16 a[10:11],v11 offset:41472
v_mfma_f32_16x16x32_bf16 v[148:151],a[12:15],a[36:39],v[148:151]
buffer_load_dwordx4 a[76:79],%[vL16],%[srdW],s60 offen offset:3072 sc1
v_mfma_f32_16x16x32_bf16 v[152:155],a[12:15],a[40:43],v[152:155]
s_add_u32 m0,s68,12288
s_add_u32 s61,s69,12288
v_mfma_f32_16x16x32_bf16 v[156:159],a[12:15],a[44:47],v[156:159]
s_waitcnt lgkmcnt(4)
v_mfma_f32_16x16x32_bf16 v[160:163],a[0:3],a[32:35],v[160:163]
ds_read_b64_tr_b16 a[12:13],v11 offset:57344
ds_read_b64_tr_b16 a[14:15],v11 offset:57856
v_mfma_f32_16x16x32_bf16 v[164:167],a[0:3],a[36:39],v[164:167]
buffer_load_dwordx4 v22,%[srdW],s61 offen sc1 lds
v_mfma_f32_16x16x32_bf16 v[168:171],a[0:3],a[40:43],v[168:171]
s_add_u32 m0,s68,28672
s_add_u32 s61,s69,32768
v_mfma_f32_16x16x32_bf16 v[172:175],a[0:3],a[44:47],v[172:175]
s_waitcnt lgkmcnt(4)
v_mfma_f32_16x16x32_bf16 v[176:179],a[4:7],a[32:35],v[176:179]
ds_read_b64_tr_b16 a[0:1],v12 offset:8192
ds_read_b64_tr_b16 a[2:3],v12 offset:8704
v_mfma_f32_16x16x32_bf16 v[180:183],a[4:7],a[36:39],v[180:183]
buffer_load_dwordx4 v23,%[srdW],s61 offen sc1 lds
v_mfma_f32_16x16x32_bf16 v[184:187],a[4:7],a[40:43],v[184:187]
s_add_u32 m0,s68,45056
)"
R"(s_add_u32 s61,s69,53248
v_mfma_f32_16x16x32_bf16 v[188:191],a[4:7],a[44:47],v[188:191]
s_waitcnt lgkmcnt(4)
v_mfma_f32_16x16x32_bf16 v[192:195],a[8:11],a[32:35],v[192:195]
ds_read_b64_tr_b16 a[4:5],v12 offset:24576
ds_read_b64_tr_b16 a[6:7],v12 offset:25088
v_mfma_f32_16x16x32_bf16 v[196:199],a[8:11],a[36:39],v[196:199]
buffer_load_dwordx4 v24,%[srdW],s61 offen sc1 lds
v_mfma_f32_16x16x32_bf16 v[200:203],a[8:11],a[40:43],v[200:203]
s_add_u32 m0,s68,61440
s_add_u32 s61,s69,73728
v_mfma_f32_16x16x32_bf16 v[204:207],a[8:11],a[44:47],v[204:207]
s_waitcnt lgkmcnt(4)
v_mfma_f32_16x16x32_bf16 v[208:211],a[12:15],a[32:35],v[208:211]
ds_read_b64_tr_b16 a[8:9],v10 offset:9216
ds_read_b64_tr_b16 a[10:11],v10 offset:9728
v_mfma_f32_16x16x32_bf16 v[212:215],a[12:15],a[36:39],v[212:215]
buffer_load_dwordx4 v25,%[srdW],s61 offen sc1 lds
v_mfma_f32_16x16x32_bf16 v[216:219],a[12:15],a[40:43],v[216:219]
s_add_u32 m0,s68,77824
s_add_u32 s61,s69,94208
v_mfma_f32_16x16x32_bf16 v[220:223],a[12:15],a[44:47],v[220:223]
s_waitcnt lgkmcnt(4)
v_mfma_f32_16x16x32_bf16 v[224:227],a[0:3],a[32:35],v[224:227]
ds_read_b64_tr_b16 a[12:13],v10 offset:25600
ds_read_b64_tr_b16 a[14:15],v10 offset:26112
v_mfma_f32_16x16x32_bf16 v[228:231],a[0:3],a[36:39],v[228:231]
buffer_load_dwordx4 v26,%[srdW],s61 offen sc1 lds
v_mfma_f32_16x16x32_bf16 v[232:235],a[0:3],a[40:43],v[232:235]
v_mfma_f32_16x16x32_bf16 v[236:239],a[0:3],a[44:47],v[236:239]
s_waitcnt lgkmcnt(4)
v_mfma_f32_16x16x32_bf16 v[240:243],a[4:7],a[32:35],v[240:243]
ds_read_b64_tr_b16 a[0:1],v10 offset:41984
ds_read_b64_tr_b16 a[2:3],v10 offset:42496
v_mfma_f32_16x16x32_bf16 v[244:247],a[4:7],a[36:39],v[244:247]
v_mfma_f32_16x16x32_bf16 v[248:251],a[4:7],a[40:43],v[248:251]
v_mfma_f32_16x16x32_bf16 v[252:255],a[4:7],a[44:47],v[252:255]
s_waitcnt lgkmcnt(4)
s_waitcnt vmcnt(12)
v_mfma_f32_16x16x32_bf16 v[96:99],a[8:11],a[48:51],v[96:99]
s_waitcnt vmcnt(11)
ds_read_b64_tr_b16 a[4:5],v10 offset:58368
ds_read_b64_tr_b16 a[6:7],v10 offset:58880
v_mfma_f32_16x16x32_bf16 v[100:103],a[8:11],a[52:55],v[100:103]
s_waitcnt vmcnt(10)
s_add_u32 s60,s60,0x1000
v_mfma_f32_16x16x32_bf16 v[104:107],a[8:11],a[56:59],v[104:107]
s_waitcnt vmcnt(9)
buffer_load_dwordx4 a[80:83],%[vL16],%[srdW],s60 offen offset:0 sc1
v_mfma_f32_16x16x32_bf16 v[108:111],a[8:11],a[60:63],v[108:111]
s_waitcnt lgkmcnt(4)
v_mfma_f32_16x16x32_bf16 v[112:115],a[12:15],a[48:51],v[112:115]
ds_read_b64_tr_b16 a[8:9],v11 offset:9216
ds_read_b64_tr_b16 a[10:11],v11 offset:9728
v_mfma_f32_16x16x32_bf16 v[116:119],a[12:15],a[52:55],v[116:119]
buffer_load_dwordx4 a[84:87],%[vL16],%[srdW],s60 offen offset:1024 sc1
v_mfma_f32_16x16x32_bf16 v[120:123],a[12:15],a[56:59],v[120:123]
v_mfma_f32_16x16x32_bf16 v[124:127],a[12:15],a[60:63],v[124:127]
s_waitcnt lgkmcnt(4)
v_mfma_f32_16x16x32_bf16 v[128:131],a[0:3],a[48:51],v[128:131]
ds_read_b64_tr_b16 a[12:13],v11 offset:25600
ds_read_b64_tr_b16 a[14:15],v11 offset:26112
v_mfma_f32_16x16x32_bf16 v[132:135],a[0:3],a[52:55],v[132:135]
buffer_load_dwordx4 a[88:91],%[vL16],%[srdW],s60 offen offset:2048 sc1
v_mfma_f32_16x16x32_bf16 v[136:139],a[0:3],a[56:59],v[136:139]
v_mfma_f32_16x16x32_bf16 v[140:143],a[0:3],a[60:63],v[140:143]
s_waitcnt lgkmcnt(4)
v_mfma_f32_16x16x32_bf16 v[144:147],a[4:7],a[48:51],v[144:147]
ds_read_b64_tr_b16 a[0:1],v11 offset:41984
ds_read_b64_tr_b16 a[2:3],v11 offset:42496
v_mfma_f32_16x16x32_bf16 v[148:151],a[4:7],a[52:55],v[148:151]
buffer_load_dwordx4 a[92:95],%[vL16],%[srdW],s60 offen offset:3072 sc1
v_mfma_f32_16x16x32_bf16 v[152:155],a[4:7],a[56:59],v[152:155]
v_mfma_f32_16x16x32_bf16 v[156:159],a[4:7],a[60:63],v[156:159]
s_waitcnt lgkmcnt(4)
v_mfma_f32_16x16x32_bf16 v[160:163],a[8:11],a[48:51],v[160:163]
ds_read_b64_tr_b16 a[4:5],v11 offset:58368
ds_read_b64_tr_b16 a[6:7],v11 offset:58880
v_mfma_f32_16x16x32_bf16 v[164:167],a[8:11],a[52:55],v[164:167]
v_mfma_f32_16x16x32_bf16 v[168:171],a[8:11],a[56:59],v[168:171]
v_mfma_f32_16x16x32_bf16 v[172:175],a[8:11],a[60:63],v[172:175]
s_waitcnt lgkmcnt(4)
v_mfma_f32_16x16x32_bf16 v[176:179],a[12:15],a[48:51],v[176:179]
ds_read_b64_tr_b16 a[8:9],v12 offset:9216
ds_read_b64_tr_b16 a[10:11],v12 offset:9728
v_mfma_f32_16x16x32_bf16 v[180:183],a[12:15],a[52:55],v[180:183]
v_mfma_f32_16x16x32_bf16 v[184:187],a[12:15],a[56:59],v[184:187]
v_mfma_f32_16x16x32_bf16 v[188:191],a[12:15],a[60:63],v[188:191]
s_waitcnt lgkmcnt(4)
v_mfma_f32_16x16x32_bf16 v[192:195],a[0:3],a[48:51],v[192:195]
ds_read_b64_tr_b16 a[12:13],v12 offset:25600
ds_read_b64_tr_b16 a[14:15],v12 offset:26112
v_mfma_f32_16x16x32_bf16 v[196:199],a[0:3],a[52:55],v[196:199]
v_mfma_f32_16x16x32_bf16 v[200:203],a[0:3],a[56:59],v[200:203]
v_mfma_f32_16x16x32_bf16 v[204:207],a[0:3],a[60:63],v[204:207]
s_waitcnt lgkmcnt(4)
v_mfma_f32_16x16x32_bf16 v[208:211],a[4:7],a[48:51],v[208:211]
s_barrier
ds_read_b64_tr_b16 a[0:1],v10 offset:10240
ds_read_b64_tr_b16 a[2:3],v10 offset:10752
v_mfma_f32_16x16x32_bf16 v[212:215],a[4:7],a[52:55],v[212:215]
v_mfma_f32_16x16x32_bf16 v[216:219],a[4:7],a[56:59],v[216:219]
v_mfma_f32_16x16x32_bf16 v[220:223],a[4:7],a[60:63],v[220:223]
s_waitcnt lgkmcnt(4)
v_mfma_f32_16x16x32_bf16 v[224:227],a[8:11],a[48:51],v[224:227]
ds_read_b64_tr_b16 a[4:5],v10 offset:26624
ds_read_b64_tr_b16 a[6:7],v10 offset:27136
v_mfma_f32_16x16x32_bf16 v[228:231],a[8:11],a[52:55],v[228:231]
v_mfma_f32_16x16x32_bf16 v[232:235],a[8:11],a[56:59],v[232:235]
v_mfma_f32_16x16x32_bf16 v[236:239],a[8:11],a[60:63],v[236:239]
s_waitcnt lgkmcnt(4)
v_mfma_f32_16x16x32_bf16 v[240:243],a[12:15],a[48:51],v[240:243]
ds_read_b64_tr_b16 a[8:9],v10 offset:43008
ds_read_b64_tr_b16 a[10:11],v10 offset:43520
v_mfma_f32_16x16x32_bf16 v[244:247],a[12:15],a[52:55],v[244:247]
v_mfma_f32_16x16x32_bf16 v[248:251],a[12:15],a[56:59],v[248:251]
v_mfma_f32_16x16x32_bf16 v[252:255],a[12:15],a[60:63],v[252:255]
s_waitcnt lgkmcnt(4)
s_waitcnt vmcnt(12)
v_mfma_f32_16x16x32_bf16 v[96:99],a[0:3],a[64:67],v[96:99]
s_waitcnt vmcnt(11)
ds_read_b64_tr_b16 a[12:13],v10 offset:59392
ds_read_b64_tr_b16 a[14:15],v10 offset:59904
v_mfma_f32_16x16x32_bf16 v[100:103],a[0:3],a[68:71],v[100:103]
s_waitcnt vmcnt(10)
s_add_u32 s60,s60,0x1000
v_mfma_f32_16x16x32_bf16 v[104:107],a[0:3],a[72:75],v[104:107]
s_waitcnt vmcnt(9)
buffer_load_dwordx4 a[32:35],%[vL16],%[srdW],s60 offen offset:0 sc1
v_mfma_f32_16x16x32_bf16 v[108:111],a[0:3],a[76:79],v[108:111]
s_waitcnt lgkmcnt(4)
v_mfma_f32_16x16x32_bf16 v[112:115],a[4:7],a[64:67],v[112:115]
ds_read_b64_tr_b16 a[0:1],v11 offset:10240
ds_read_b64_tr_b16 a[2:3],v11 offset:10752
v_mfma_f32_16x16x32_bf16 v[116:119],a[4:7],a[68:71],v[116:119]
buffer_load_dwordx4 a[36:39],%[vL16],%[srdW],s60 offen offset:1024 sc1
v_mfma_f32_16x16x32_bf16 v[120:123],a[4:7],a[72:75],v[120:123]
v_mfma_f32_16x16x32_bf16 v[124:127],a[4:7],a[76:79],v[124:127]
s_waitcnt lgkmcnt(4)
v_mfma_f32_16x16x32_bf16 v[128:131],a[8:11],a[64:67],v[128:131]
ds_read_b64_tr_b16 a[4:5],v11 offset:26624
ds_read_b64_tr_b16 a[6:7],v11 offset:27136
v_mfma_f32_16x16x32_bf16 v[132:135],a[8:11],a[68:71],v[132:135]
buffer_load_dwordx4 a[40:43],%[vL16],%[srdW],s60 offen offset:2048 sc1
v_mfma_f32_16x16x32_bf16 v[136:139],a[8:11],a[72:75],v[136:139]
v_mfma_f32_16x16x32_bf16 v[140:143],a[8:11],a[76:79],v[140:143]
s_waitcnt lgkmcnt(4)
v_mfma_f32_16x16x32_bf16 v[144:147],a[12:15],a[64:67],v[144:147]
ds_read_b64_tr_b16 a[8:9],v11 offset:43008
ds_read_b64_tr_b16 a[10:11],v11 offset:43520
v_mfma_f32_16x16x32_bf16 v[148:151],a[12:15],a[68:71],v[148:151]
buffer_load_dwordx4 a[44:47],%[vL16],%[srdW],s60 offen offset:3072 sc1
v_mfma_f32_16x16x32_bf16 v[152:155],a[12:15],a[72:75],v[152:155]
s_add_u32 m0,s68,14336
s_add_u32 s61,s69,14336
v_mfma_f32_16x16x32_bf16 v[156:159],a[12:15],a[76:79],v[156:159]
s_waitcnt lgkmcnt(4)
v_mfma_f32_16x16x32_bf16 v[160:163],a[0:3],a[64:67],v[160:163]
ds_read_b64_tr_b16 a[12:13],v11 offset:59392
ds_read_b64_tr_b16 a[14:15],v11 offset:59904
v_mfma_f32_16x16x32_bf16 v[164:167],a[0:3],a[68:71],v[164:167]
buffer_load_dwordx4 v22,%[srdW],s61 offen sc1 lds
v_mfma_f32_16x16x32_bf16 v[168:171],a[0:3],a[72:75],v[168:171]
s_add_u32 m0,s68,30720
s_add_u32 s61,s69,34816
v_mfma_f32_16x16x32_bf16 v[172:175],a[0:3],a[76:79],v[172:175]
s_waitcnt lgkmcnt(4)
v_mfma_f32_16x16x32_bf16 v[176:179],a[4:7],a[64:67],v[176:179]
ds_read_b64_tr_b16 a[0:1],v12 offset:10240
ds_read_b64_tr_b16 a[2:3],v12 offset:10752
v_mfma_f32_16x16x32_bf16 v[180:183],a[4:7],a[68:71],v[180:183]
buffer_load_dwordx4 v23,%[srdW],s61 offen sc1 lds
v_mfma_f32_16x16x32_bf16 v[184:187],a[4:7],a[72:75],v[184:187]
s_add_u32 m0,s68,47104
s_add_u32 s61,s69,55296
v_mfma_f32_16x16x32_bf16 v[188:191],a[4:7],a[76:79],v[188:191]
s_waitcnt lgkmcnt(4)
v_mfma_f32_16x16x32_bf16 v[192:195],a[8:11],a[64:67],v[192:195]
ds_read_b64_tr_b16 a[4:5],v12 offset:26624
ds_read_b64_tr_b16 a[6:7],v12 offset:27136
v_mfma_f32_16x16x32_bf16 v[196:199],a[8:11],a[68:71],v[196:199]
buffer_load_dwordx4 v24,%[srdW],s61 offen sc1 lds
v_mfma_f32_16x16x32_bf16 v[200:203],a[8:11],a[72:75],v[200:203]
s_add_u32 m0,s68,63488
s_add_u32 s61,s69,75776
v_mfma_f32_16x16x32_bf16 v[204:207],a[8:11],a[76:79],v[204:207]
s_waitcnt lgkmcnt(4)
v_mfma_f32_16x16x32_bf16 v[208:211],a[12:15],a[64:67],v[208:211]
ds_read_b64_tr_b16 a[8:9],v10 offset:11264
ds_read_b64_tr_b16 a[10:11],v10 offset:11776
v_mfma_f32_16x16x32_bf16 v[212:215],a[12:15],a[68:71],v[212:215]
buffer_load_dwordx4 v25,%[srdW],s61 offen sc1 lds
v_mfma_f32_16x16x32_bf16 v[216:219],a[12:15],a[72:75],v[216:219]
s_add_u32 m0,s68,79872
s_add_u32 s61,s69,96256
v_mfma_f32_16x16x32_bf16 v[220:223],a[12:15],a[76:79],v[220:223]
s_waitcnt lgkmcnt(4)
v_mfma_f32_16x16x32_bf16 v[224:227],a[0:3],a[64:67],v[224:227]
ds_read_b64_tr_b16 a[12:13],v10 offset:27648
ds_read_b64_tr_b16 a[14:15],v10 offset:28160
v_mfma_f32_16x16x32_bf16 v[228:231],a[0:3],a[68:71],v[228:231]
buffer_load_dwordx4 v26,%[srdW],s61 offen sc1 lds
v_mfma_f32_16x16x32_bf16 v[232:235],a[0:3],a[72:75],v[232:235]
v_mfma_f32_16x16x32_bf16 v[236:239],a[0:3],a[76:79],v[236:239]
s_waitcnt lgkmcnt(4)
v_mfma_f32_16x16x32_bf16 v[240:243],a[4:7],a[64:67],v[240:243]
ds_read_b64_tr_b16 a[0:1],v10 offset:44032
ds_read_b64_tr_b16 a[2:3],v10 offset:44544
v_mfma_f32_16x16x32_bf16 v[244:247],a[4:7],a[68:71],v[244:247]
v_mfma_f32_16x16x32_bf16 v[248:251],a[4:7],a[72:75],v[248:251]
v_mfma_f32_16x16x32_bf16 v[252:255],a[4:7],a[76:79],v[252:255]
s_waitcnt lgkmcnt(4)
s_waitcnt vmcnt(12)
v_mfma_f32_16x16x32_bf16 v[96:99],a[8:11],a[80:83],v[96:99]
s_waitcnt vmcnt(11)
ds_read_b64_tr_b16 a[4:5],v10 offset:60416
ds_read_b64_tr_b16 a[6:7],v10 offset:60928
v_mfma_f32_16x16x32_bf16 v[100:103],a[8:11],a[84:87],v[100:103]
s_waitcnt vmcnt(10)
s_add_u32 s60,s60,0x1000
v_mfma_f32_16x16x32_bf16 v[104:107],a[8:11],a[88:91],v[104:107]
s_waitcnt vmcnt(9)
buffer_load_dwordx4 a[48:51],%[vL16],%[srdW],s60 offen offset:0 sc1
v_mfma_f32_16x16x32_bf16 v[108:111],a[8:11],a[92:95],v[108:111]
s_waitcnt lgkmcnt(4)
v_mfma_f32_16x16x32_bf16 v[112:115],a[12:15],a[80:83],v[112:115]
ds_read_b64_tr_b16 a[8:9],v11 offset:11264
ds_read_b64_tr_b16 a[10:11],v11 offset:11776
v_mfma_f32_16x16x32_bf16 v[116:119],a[12:15],a[84:87],v[116:119]
buffer_load_dwordx4 a[52:55],%[vL16],%[srdW],s60 offen offset:1024 sc1
v_mfma_f32_16x16x32_bf16 v[120:123],a[12:15],a[88:91],v[120:123]
v_mfma_f32_16x16x32_bf16 v[124:127],a[12:15],a[92:95],v[124:127]
s_waitcnt lgkmcnt(4)
v_mfma_f32_16x16x32_bf16 v[128:131],a[0:3],a[80:83],v[128:131]
ds_read_b64_tr_b16 a[12:13],v11 offset:27648
ds_read_b64_tr_b16 a[14:15],v11 offset:28160
v_mfma_f32_16x16x32_bf16 v[132:135],a[0:3],a[84:87],v[132:135]
buffer_load_dwordx4 a[56:59],%[vL16],%[srdW],s60 offen offset:2048 sc1
v_mfma_f32_16x16x32_bf16 v[136:139],a[0:3],a[88:91],v[136:139]
v_mfma_f32_16x16x32_bf16 v[140:143],a[0:3],a[92:95],v[140:143]
s_waitcnt lgkmcnt(4)
v_mfma_f32_16x16x32_bf16 v[144:147],a[4:7],a[80:83],v[144:147]
ds_read_b64_tr_b16 a[0:1],v11 offset:44032
ds_read_b64_tr_b16 a[2:3],v11 offset:44544
v_mfma_f32_16x16x32_bf16 v[148:151],a[4:7],a[84:87],v[148:151]
buffer_load_dwordx4 a[60:63],%[vL16],%[srdW],s60 offen offset:3072 sc1
v_mfma_f32_16x16x32_bf16 v[152:155],a[4:7],a[88:91],v[152:155]
v_mfma_f32_16x16x32_bf16 v[156:159],a[4:7],a[92:95],v[156:159]
s_waitcnt lgkmcnt(4)
v_mfma_f32_16x16x32_bf16 v[160:163],a[8:11],a[80:83],v[160:163]
ds_read_b64_tr_b16 a[4:5],v11 offset:60416
ds_read_b64_tr_b16 a[6:7],v11 offset:60928
v_mfma_f32_16x16x32_bf16 v[164:167],a[8:11],a[84:87],v[164:167]
v_mfma_f32_16x16x32_bf16 v[168:171],a[8:11],a[88:91],v[168:171]
v_mfma_f32_16x16x32_bf16 v[172:175],a[8:11],a[92:95],v[172:175]
s_waitcnt lgkmcnt(4)
v_mfma_f32_16x16x32_bf16 v[176:179],a[12:15],a[80:83],v[176:179]
ds_read_b64_tr_b16 a[8:9],v12 offset:11264
ds_read_b64_tr_b16 a[10:11],v12 offset:11776
v_mfma_f32_16x16x32_bf16 v[180:183],a[12:15],a[84:87],v[180:183]
v_mfma_f32_16x16x32_bf16 v[184:187],a[12:15],a[88:91],v[184:187]
v_mfma_f32_16x16x32_bf16 v[188:191],a[12:15],a[92:95],v[188:191]
s_waitcnt lgkmcnt(4)
v_mfma_f32_16x16x32_bf16 v[192:195],a[0:3],a[80:83],v[192:195]
ds_read_b64_tr_b16 a[12:13],v12 offset:27648
ds_read_b64_tr_b16 a[14:15],v12 offset:28160
v_mfma_f32_16x16x32_bf16 v[196:199],a[0:3],a[84:87],v[196:199]
v_mfma_f32_16x16x32_bf16 v[200:203],a[0:3],a[88:91],v[200:203]
v_mfma_f32_16x16x32_bf16 v[204:207],a[0:3],a[92:95],v[204:207]
s_waitcnt lgkmcnt(4)
v_mfma_f32_16x16x32_bf16 v[208:211],a[4:7],a[80:83],v[208:211]
s_barrier
ds_read_b64_tr_b16 a[0:1],v10 offset:12288
ds_read_b64_tr_b16 a[2:3],v10 offset:12800
v_mfma_f32_16x16x32_bf16 v[212:215],a[4:7],a[84:87],v[212:215]
v_mfma_f32_16x16x32_bf16 v[216:219],a[4:7],a[88:91],v[216:219]
v_mfma_f32_16x16x32_bf16 v[220:223],a[4:7],a[92:95],v[220:223]
s_waitcnt lgkmcnt(4)
v_mfma_f32_16x16x32_bf16 v[224:227],a[8:11],a[80:83],v[224:227]
ds_read_b64_tr_b16 a[4:5],v10 offset:28672
ds_read_b64_tr_b16 a[6:7],v10 offset:29184
v_mfma_f32_16x16x32_bf16 v[228:231],a[8:11],a[84:87],v[228:231]
v_mfma_f32_16x16x32_bf16 v[232:235],a[8:11],a[88:91],v[232:235]
v_mfma_f32_16x16x32_bf16 v[236:239],a[8:11],a[92:95],v[236:239]
s_waitcnt lgkmcnt(4)
v_mfma_f32_16x16x32_bf16 v[240:243],a[12:15],a[80:83],v[240:243]
ds_read_b64_tr_b16 a[8:9],v10 offset:45056
ds_read_b64_tr_b16 a[10:11],v10 offset:45568
v_mfma_f32_16x16x32_bf16 v[244:247],a[12:15],a[84:87],v[244:247]
v_mfma_f32_16x16x32_bf16 v[248:251],a[12:15],a[88:91],v[248:251]
v_mfma_f32_16x16x32_bf16 v[252:255],a[12:15],a[92:95],v[252:255]
s_waitcnt lgkmcnt(4)
s_waitcnt vmcnt(12)
v_mfma_f32_16x16x32_bf16 v[96:99],a[0:3],a[32:35],v[96:99]
s_waitcnt vmcnt(11)
ds_read_b64_tr_b16 a[12:13],v10 offset:61440
ds_read_b64_tr_b16 a[14:15],v10 offset:61952
v_mfma_f32_16x16x32_bf16 v[100:103],a[0:3],a[36:39],v[100:103]
s_waitcnt vmcnt(10)
s_add_u32 s60,s60,0x1000
v_mfma_f32_16x16x32_bf16 v[104:107],a[0:3],a[40:43],v[104:107]
s_waitcnt vmcnt(9)
buffer_load_dwordx4 a[64:67],%[vL16],%[srdW],s60 offen offset:0 sc1
v_mfma_f32_16x16x32_bf16 v[108:111],a[0:3],a[44:47],v[108:111]
s_waitcnt lgkmcnt(4)
v_mfma_f32_16x16x32_bf16 v[112:115],a[4:7],a[32:35],v[112:115]
ds_read_b64_tr_b16 a[0:1],v11 offset:12288
ds_read_b64_tr_b16 a[2:3],v11 offset:12800
v_mfma_f32_16x16x32_bf16 v[116:119],a[4:7],a[36:39],v[116:119]
buffer_load_dwordx4 a[68:71],%[vL16],%[srdW],s60 offen offset:1024 sc1
v_mfma_f32_16x16x32_bf16 v[120:123],a[4:7],a[40:43],v[120:123]
v_mfma_f32_16x16x32_bf16 v[124:127],a[4:7],a[44:47],v[124:127]
s_waitcnt lgkmcnt(4)
v_mfma_f32_16x16x32_bf16 v[128:131],a[8:11],a[32:35],v[128:131]
ds_read_b64_tr_b16 a[4:5],v11 offset:28672
ds_read_b64_tr_b16 a[6:7],v11 offset:29184
v_mfma_f32_16x16x32_bf16 v[132:135],a[8:11],a[36:39],v[132:135]
buffer_load_dwordx4 a[72:75],%[vL16],%[srdW],s60 offen offset:2048 sc1
v_mfma_f32_16x16x32_bf16 v[136:139],a[8:11],a[40:43],v[136:139]
v_mfma_f32_16x16x32_bf16 v[140:143],a[8:11],a[44:47],v[140:143]
s_waitcnt lgkmcnt(4)
v_mfma_f32_16x16x32_bf16 v[144:147],a[12:15],a[32:35],v[144:147]
ds_read_b64_tr_b16 a[8:9],v11 offset:45056
ds_read_b64_tr_b16 a[10:11],v11 offset:45568
v_mfma_f32_16x16x32_bf16 v[148:151],a[12:15],a[36:39],v[148:151]
buffer_load_dwordx4 a[76:79],%[vL16],%[srdW],s60 offen offset:3072 sc1
v_mfma_f32_16x16x32_bf16 v[152:155],a[12:15],a[40:43],v[152:155]
v_readlane_b32 s83,%[pv],38
v_readlane_b32 s74,%[pv],39
v_mfma_f32_16x16x32_bf16 v[156:159],a[12:15],a[44:47],v[156:159]
s_waitcnt lgkmcnt(4)
v_readlane_b32 s75,%[pv],40
v_readlane_b32 s76,%[pv],41
v_mfma_f32_16x16x32_bf16 v[160:163],a[0:3],a[32:35],v[160:163]
ds_read_b64_tr_b16 a[12:13],v11 offset:61440
ds_read_b64_tr_b16 a[14:15],v11 offset:61952
v_mfma_f32_16x16x32_bf16 v[164:167],a[0:3],a[36:39],v[164:167]
v_mfma_f32_16x16x32_bf16 v[168:171],a[0:3],a[40:43],v[168:171]
v_mfma_f32_16x16x32_bf16 v[172:175],a[0:3],a[44:47],v[172:175]
s_waitcnt lgkmcnt(4)
v_mfma_f32_16x16x32_bf16 v[176:179],a[4:7],a[32:35],v[176:179]
ds_read_b64_tr_b16 a[0:1],v12 offset:12288
ds_read_b64_tr_b16 a[2:3],v12 offset:12800
v_mfma_f32_16x16x32_bf16 v[180:183],a[4:7],a[36:39],v[180:183]
v_mfma_f32_16x16x32_bf16 v[184:187],a[4:7],a[40:43],v[184:187]
v_mfma_f32_16x16x32_bf16 v[188:191],a[4:7],a[44:47],v[188:191]
s_waitcnt lgkmcnt(4)
v_mfma_f32_16x16x32_bf16 v[192:195],a[8:11],a[32:35],v[192:195]
ds_read_b64_tr_b16 a[4:5],v12 offset:28672
ds_read_b64_tr_b16 a[6:7],v12 offset:29184
v_mfma_f32_16x16x32_bf16 v[196:199],a[8:11],a[36:39],v[196:199]
v_mfma_f32_16x16x32_bf16 v[200:203],a[8:11],a[40:43],v[200:203]
v_mfma_f32_16x16x32_bf16 v[204:207],a[8:11],a[44:47],v[204:207]
s_waitcnt lgkmcnt(4)
v_mfma_f32_16x16x32_bf16 v[208:211],a[12:15],a[32:35],v[208:211]
ds_read_b64_tr_b16 a[8:9],v10 offset:13312
ds_read_b64_tr_b16 a[10:11],v10 offset:13824
v_mfma_f32_16x16x32_bf16 v[212:215],a[12:15],a[36:39],v[212:215]
v_mfma_f32_16x16x32_bf16 v[216:219],a[12:15],a[40:43],v[216:219]
v_mfma_f32_16x16x32_bf16 v[220:223],a[12:15],a[44:47],v[220:223]
s_waitcnt lgkmcnt(4)
v_mfma_f32_16x16x32_bf16 v[224:227],a[0:3],a[32:35],v[224:227]
ds_read_b64_tr_b16 a[12:13],v10 offset:29696
ds_read_b64_tr_b16 a[14:15],v10 offset:30208
v_mfma_f32_16x16x32_bf16 v[228:231],a[0:3],a[36:39],v[228:231]
v_mfma_f32_16x16x32_bf16 v[232:235],a[0:3],a[40:43],v[232:235]
v_mfma_f32_16x16x32_bf16 v[236:239],a[0:3],a[44:47],v[236:239]
s_waitcnt lgkmcnt(4)
v_mfma_f32_16x16x32_bf16 v[240:243],a[4:7],a[32:35],v[240:243]
ds_read_b64_tr_b16 a[0:1],v10 offset:46080
ds_read_b64_tr_b16 a[2:3],v10 offset:46592
v_mfma_f32_16x16x32_bf16 v[244:247],a[4:7],a[36:39],v[244:247]
v_mfma_f32_16x16x32_bf16 v[248:251],a[4:7],a[40:43],v[248:251]
v_mfma_f32_16x16x32_bf16 v[252:255],a[4:7],a[44:47],v[252:255]
s_waitcnt lgkmcnt(4)
s_waitcnt vmcnt(7)
v_mfma_f32_16x16x32_bf16 v[96:99],a[8:11],a[48:51],v[96:99]
s_waitcnt vmcnt(6)
ds_read_b64_tr_b16 a[4:5],v10 offset:62464
ds_read_b64_tr_b16 a[6:7],v10 offset:62976
v_mfma_f32_16x16x32_bf16 v[100:103],a[8:11],a[52:55],v[100:103]
s_waitcnt vmcnt(5)
s_add_u32 s60,s60,0x1000
v_mfma_f32_16x16x32_bf16 v[104:107],a[8:11],a[56:59],v[104:107]
s_waitcnt vmcnt(4)
buffer_load_dwordx4 a[80:83],%[vL16],%[srdW],s60 offen offset:0 sc1
v_mfma_f32_16x16x32_bf16 v[108:111],a[8:11],a[60:63],v[108:111]
s_waitcnt lgkmcnt(4)
v_mfma_f32_16x16x32_bf16 v[112:115],a[12:15],a[48:51],v[112:115]
ds_read_b64_tr_b16 a[8:9],v11 offset:13312
ds_read_b64_tr_b16 a[10:11],v11 offset:13824
v_mfma_f32_16x16x32_bf16 v[116:119],a[12:15],a[52:55],v[116:119]
buffer_load_dwordx4 a[84:87],%[vL16],%[srdW],s60 offen offset:1024 sc1
v_mfma_f32_16x16x32_bf16 v[120:123],a[12:15],a[56:59],v[120:123]
v_mfma_f32_16x16x32_bf16 v[124:127],a[12:15],a[60:63],v[124:127]
s_waitcnt lgkmcnt(4)
v_mfma_f32_16x16x32_bf16 v[128:131],a[0:3],a[48:51],v[128:131]
ds_read_b64_tr_b16 a[12:13],v11 offset:29696
ds_read_b64_tr_b16 a[14:15],v11 offset:30208
v_mfma_f32_16x16x32_bf16 v[132:135],a[0:3],a[52:55],v[132:135]
)"
R"(buffer_load_dwordx4 a[88:91],%[vL16],%[srdW],s60 offen offset:2048 sc1
v_mfma_f32_16x16x32_bf16 v[136:139],a[0:3],a[56:59],v[136:139]
v_mfma_f32_16x16x32_bf16 v[140:143],a[0:3],a[60:63],v[140:143]
s_waitcnt lgkmcnt(4)
v_mfma_f32_16x16x32_bf16 v[144:147],a[4:7],a[48:51],v[144:147]
ds_read_b64_tr_b16 a[0:1],v11 offset:46080
ds_read_b64_tr_b16 a[2:3],v11 offset:46592
v_mfma_f32_16x16x32_bf16 v[148:151],a[4:7],a[52:55],v[148:151]
buffer_load_dwordx4 a[92:95],%[vL16],%[srdW],s60 offen offset:3072 sc1
v_mfma_f32_16x16x32_bf16 v[152:155],a[4:7],a[56:59],v[152:155]
v_mfma_f32_16x16x32_bf16 v[156:159],a[4:7],a[60:63],v[156:159]
s_waitcnt lgkmcnt(4)
v_mfma_f32_16x16x32_bf16 v[160:163],a[8:11],a[48:51],v[160:163]
ds_read_b64_tr_b16 a[4:5],v11 offset:62464
ds_read_b64_tr_b16 a[6:7],v11 offset:62976
v_mfma_f32_16x16x32_bf16 v[164:167],a[8:11],a[52:55],v[164:167]
v_mfma_f32_16x16x32_bf16 v[168:171],a[8:11],a[56:59],v[168:171]
v_mfma_f32_16x16x32_bf16 v[172:175],a[8:11],a[60:63],v[172:175]
s_waitcnt lgkmcnt(4)
v_mfma_f32_16x16x32_bf16 v[176:179],a[12:15],a[48:51],v[176:179]
ds_read_b64_tr_b16 a[8:9],v12 offset:13312
ds_read_b64_tr_b16 a[10:11],v12 offset:13824
v_mfma_f32_16x16x32_bf16 v[180:183],a[12:15],a[52:55],v[180:183]
v_mfma_f32_16x16x32_bf16 v[184:187],a[12:15],a[56:59],v[184:187]
v_mfma_f32_16x16x32_bf16 v[188:191],a[12:15],a[60:63],v[188:191]
s_waitcnt lgkmcnt(4)
v_mfma_f32_16x16x32_bf16 v[192:195],a[0:3],a[48:51],v[192:195]
ds_read_b64_tr_b16 a[12:13],v12 offset:29696
ds_read_b64_tr_b16 a[14:15],v12 offset:30208
v_mfma_f32_16x16x32_bf16 v[196:199],a[0:3],a[52:55],v[196:199]
v_mfma_f32_16x16x32_bf16 v[200:203],a[0:3],a[56:59],v[200:203]
v_mfma_f32_16x16x32_bf16 v[204:207],a[0:3],a[60:63],v[204:207]
s_waitcnt lgkmcnt(4)
v_mfma_f32_16x16x32_bf16 v[208:211],a[4:7],a[48:51],v[208:211]
s_barrier
ds_read_b64_tr_b16 a[0:1],v10 offset:14336
ds_read_b64_tr_b16 a[2:3],v10 offset:14848
v_mfma_f32_16x16x32_bf16 v[212:215],a[4:7],a[52:55],v[212:215]
v_mfma_f32_16x16x32_bf16 v[216:219],a[4:7],a[56:59],v[216:219]
v_mfma_f32_16x16x32_bf16 v[220:223],a[4:7],a[60:63],v[220:223]
s_waitcnt lgkmcnt(4)
v_mfma_f32_16x16x32_bf16 v[224:227],a[8:11],a[48:51],v[224:227]
ds_read_b64_tr_b16 a[4:5],v10 offset:30720
ds_read_b64_tr_b16 a[6:7],v10 offset:31232
v_mfma_f32_16x16x32_bf16 v[228:231],a[8:11],a[52:55],v[228:231]
v_mfma_f32_16x16x32_bf16 v[232:235],a[8:11],a[56:59],v[232:235]
v_mfma_f32_16x16x32_bf16 v[236:239],a[8:11],a[60:63],v[236:239]
s_waitcnt lgkmcnt(4)
v_mfma_f32_16x16x32_bf16 v[240:243],a[12:15],a[48:51],v[240:243]
ds_read_b64_tr_b16 a[8:9],v10 offset:47104
ds_read_b64_tr_b16 a[10:11],v10 offset:47616
v_mfma_f32_16x16x32_bf16 v[244:247],a[12:15],a[52:55],v[244:247]
v_mfma_f32_16x16x32_bf16 v[248:251],a[12:15],a[56:59],v[248:251]
v_mfma_f32_16x16x32_bf16 v[252:255],a[12:15],a[60:63],v[252:255]
s_waitcnt lgkmcnt(4)
s_waitcnt vmcnt(7)
v_mfma_f32_16x16x32_bf16 v[96:99],a[0:3],a[64:67],v[96:99]
s_waitcnt vmcnt(6)
ds_read_b64_tr_b16 a[12:13],v10 offset:63488
ds_read_b64_tr_b16 a[14:15],v10 offset:64000
v_mfma_f32_16x16x32_bf16 v[100:103],a[0:3],a[68:71],v[100:103]
s_waitcnt vmcnt(5)
s_add_u32 s82,s83,0
ds_read_b64_tr_b16 a[216:217],v10 offset:15360
v_mfma_f32_16x16x32_bf16 v[104:107],a[0:3],a[72:75],v[104:107]
s_waitcnt vmcnt(4)
buffer_load_dwordx4 a[176:179],%[vL16],%[srdW],s82 offen offset:0 sc1
v_mfma_f32_16x16x32_bf16 v[108:111],a[0:3],a[76:79],v[108:111]
s_waitcnt lgkmcnt(5)
ds_read_b64_tr_b16 a[218:219],v10 offset:15872
v_readlane_b32 s78,%[pv],42
v_mfma_f32_16x16x32_bf16 v[112:115],a[4:7],a[64:67],v[112:115]
ds_read_b64_tr_b16 a[0:1],v11 offset:14336
ds_read_b64_tr_b16 a[2:3],v11 offset:14848
v_mfma_f32_16x16x32_bf16 v[116:119],a[4:7],a[68:71],v[116:119]
buffer_load_dwordx4 a[180:183],%[vL16],%[srdW],s82 offen offset:1024 sc1
v_mfma_f32_16x16x32_bf16 v[120:123],a[4:7],a[72:75],v[120:123]
s_add_u32 s82,s83,2048
ds_read_b64_tr_b16 a[220:221],v10 offset:31744
ds_read_b64_tr_b16 a[222:223],v10 offset:32256
v_mfma_f32_16x16x32_bf16 v[124:127],a[4:7],a[76:79],v[124:127]
s_waitcnt lgkmcnt(8)
v_readlane_b32 s79,%[pv],43
v_readlane_b32 s72,%[pv],44
v_mfma_f32_16x16x32_bf16 v[128:131],a[8:11],a[64:67],v[128:131]
ds_read_b64_tr_b16 a[4:5],v11 offset:30720
ds_read_b64_tr_b16 a[6:7],v11 offset:31232
v_mfma_f32_16x16x32_bf16 v[132:135],a[8:11],a[68:71],v[132:135]
buffer_load_dwordx4 a[184:187],%[vL16],%[srdW],s82 offen offset:0 sc1
v_mfma_f32_16x16x32_bf16 v[136:139],a[8:11],a[72:75],v[136:139]
ds_read_b64_tr_b16 a[224:225],v10 offset:48128
ds_read_b64_tr_b16 a[226:227],v10 offset:48640
v_mfma_f32_16x16x32_bf16 v[140:143],a[8:11],a[76:79],v[140:143]
s_waitcnt lgkmcnt(10)
v_lshrrev_b32 v32,8,%[vL16]
v_bfe_u32 v33,%[vL16],4,4
v_mfma_f32_16x16x32_bf16 v[144:147],a[12:15],a[64:67],v[144:147]
ds_read_b64_tr_b16 a[8:9],v11 offset:47104
ds_read_b64_tr_b16 a[10:11],v11 offset:47616
v_mfma_f32_16x16x32_bf16 v[148:151],a[12:15],a[68:71],v[148:151]
buffer_load_dwordx4 a[188:191],%[vL16],%[srdW],s82 offen offset:1024 sc1
v_mfma_f32_16x16x32_bf16 v[152:155],a[12:15],a[72:75],v[152:155]
s_add_u32 s82,s83,4096
ds_read_b64_tr_b16 a[228:229],v10 offset:64512
ds_read_b64_tr_b16 a[230:231],v10 offset:65024
v_mfma_f32_16x16x32_bf16 v[156:159],a[12:15],a[76:79],v[156:159]
s_waitcnt lgkmcnt(10)
v_mul_u32_u24 v2,336,v33
v_lshl_add_u32 v2,v32,3,v2
v_mfma_f32_16x16x32_bf16 v[160:163],a[0:3],a[64:67],v[160:163]
ds_read_b64_tr_b16 a[12:13],v11 offset:63488
ds_read_b64_tr_b16 a[14:15],v11 offset:64000
v_mfma_f32_16x16x32_bf16 v[164:167],a[0:3],a[68:71],v[164:167]
buffer_load_dwordx4 a[192:195],%[vL16],%[srdW],s82 offen offset:0 sc1
v_mfma_f32_16x16x32_bf16 v[168:171],a[0:3],a[72:75],v[168:171]
ds_read_b64_tr_b16 a[232:233],v11 offset:15360
ds_read_b64_tr_b16 a[234:235],v11 offset:15872
v_mfma_f32_16x16x32_bf16 v[172:175],a[0:3],a[76:79],v[172:175]
s_waitcnt lgkmcnt(10)
v_add_u32 v2,s78,v2
v_add_u32 v3,32,v2
v_mfma_f32_16x16x32_bf16 v[176:179],a[4:7],a[64:67],v[176:179]
ds_read_b64_tr_b16 a[0:1],v12 offset:14336
ds_read_b64_tr_b16 a[2:3],v12 offset:14848
v_mfma_f32_16x16x32_bf16 v[180:183],a[4:7],a[68:71],v[180:183]
buffer_load_dwordx4 a[196:199],%[vL16],%[srdW],s82 offen offset:1024 sc1
v_mfma_f32_16x16x32_bf16 v[184:187],a[4:7],a[72:75],v[184:187]
s_add_u32 s82,s83,6144
ds_read_b64_tr_b16 a[236:237],v11 offset:31744
ds_read_b64_tr_b16 a[238:239],v11 offset:32256
v_mfma_f32_16x16x32_bf16 v[188:191],a[4:7],a[76:79],v[188:191]
s_waitcnt lgkmcnt(10)
v_lshrrev_b32 v32,6,%[vL16]
v_bfe_u32 v33,%[vL16],4,2
v_mfma_f32_16x16x32_bf16 v[192:195],a[8:11],a[64:67],v[192:195]
ds_read_b64_tr_b16 a[4:5],v12 offset:30720
ds_read_b64_tr_b16 a[6:7],v12 offset:31232
v_mfma_f32_16x16x32_bf16 v[196:199],a[8:11],a[68:71],v[196:199]
buffer_load_dwordx4 a[200:203],%[vL16],%[srdW],s82 offen offset:0 sc1
v_mfma_f32_16x16x32_bf16 v[200:203],a[8:11],a[72:75],v[200:203]
ds_read_b64_tr_b16 a[240:241],v11 offset:48128
ds_read_b64_tr_b16 a[242:243],v11 offset:48640
v_mfma_f32_16x16x32_bf16 v[204:207],a[8:11],a[76:79],v[204:207]
s_waitcnt lgkmcnt(10)
v_mul_u32_u24 v4,336,v32
v_lshl_add_u32 v4,v33,6,v4
v_mfma_f32_16x16x32_bf16 v[208:211],a[12:15],a[64:67],v[208:211]
buffer_load_dwordx4 a[204:207],%[vL16],%[srdW],s82 offen offset:1024 sc1
v_mfma_f32_16x16x32_bf16 v[212:215],a[12:15],a[68:71],v[212:215]
s_add_u32 s82,s83,8192
ds_read_b64_tr_b16 a[244:245],v11 offset:64512
ds_read_b64_tr_b16 a[246:247],v11 offset:65024
v_mfma_f32_16x16x32_bf16 v[216:219],a[12:15],a[72:75],v[216:219]
v_add_u32 v4,s78,v4
v_and_b32 v34,1,v33
v_mfma_f32_16x16x32_bf16 v[220:223],a[12:15],a[76:79],v[220:223]
s_waitcnt lgkmcnt(8)
buffer_load_dwordx4 a[208:211],%[vL16],%[srdW],s82 offen offset:0 sc1
v_mfma_f32_16x16x32_bf16 v[224:227],a[0:3],a[64:67],v[224:227]
ds_read_b64_tr_b16 a[248:249],v12 offset:15360
ds_read_b64_tr_b16 a[250:251],v12 offset:15872
v_mfma_f32_16x16x32_bf16 v[228:231],a[0:3],a[68:71],v[228:231]
v_lshlrev_b32 v34,4,v34
v_mov_b32 v35,0x3f80
v_mfma_f32_16x16x32_bf16 v[232:235],a[0:3],a[72:75],v[232:235]
buffer_load_dwordx4 a[212:215],%[vL16],%[srdW],s82 offen offset:1024 sc1
v_mfma_f32_16x16x32_bf16 v[236:239],a[0:3],a[76:79],v[236:239]
s_waitcnt lgkmcnt(6)
v_lshlrev_b32 v34,v34,v35
v_cmp_gt_u32 vcc,2,v33
v_mfma_f32_16x16x32_bf16 v[240:243],a[4:7],a[64:67],v[240:243]
ds_read_b64_tr_b16 a[252:253],v12 offset:31744
ds_read_b64_tr_b16 a[254:255],v12 offset:32256
v_mfma_f32_16x16x32_bf16 v[244:247],a[4:7],a[68:71],v[244:247]
v_add_u32 v5,s79,%[vL16]
v_add_u32 v8,s72,%[vL16]
v_mfma_f32_16x16x32_bf16 v[248:251],a[4:7],a[72:75],v[248:251]
v_cndmask_b32 v6,0,v34,vcc
v_cndmask_b32 v7,v34,0,vcc
v_mfma_f32_16x16x32_bf16 v[252:255],a[4:7],a[76:79],v[252:255]
v_add_u32 v20,0x1000,v8
v_and_b32 v32,0x100,%[vL16]
v_cmp_ne_u32 vcc,0,v32
s_waitcnt vmcnt(10)
P4E_%=:
s_waitcnt lgkmcnt(0)
s_barrier
v_mfma_f32_16x16x32_bf16 v[96:99],a[216:219],a[80:83],v[96:99]
s_add_u32 s82,s83,10240
buffer_load_dwordx4 a[32:35],%[vL16],%[srdW],s82 offen offset:0 sc1
v_mfma_f32_16x16x32_bf16 v[112:115],a[220:223],a[80:83],v[112:115]
v_mfma_f32_16x16x32_bf16 v[128:131],a[224:227],a[80:83],v[128:131]
v_mfma_f32_16x16x32_bf16 v[144:147],a[228:231],a[80:83],v[144:147]
v_mfma_f32_16x16x32_bf16 v[160:163],a[232:235],a[80:83],v[160:163]
buffer_load_dwordx4 a[36:39],%[vL16],%[srdW],s82 offen offset:1024 sc1
v_mfma_f32_16x16x32_bf16 v[176:179],a[236:239],a[80:83],v[176:179]
s_add_u32 s82,s83,12288
v_cvt_pk_bf16_f32 v96,v96,v97
v_cvt_pk_bf16_f32 v97,v98,v99
v_mfma_f32_16x16x32_bf16 v[192:195],a[240:243],a[80:83],v[192:195]
ds_write_b64 v2,v[96:97] offset:0
v_cvt_pk_bf16_f32 v112,v112,v113
v_mfma_f32_16x16x32_bf16 v[208:211],a[244:247],a[80:83],v[208:211]
v_cvt_pk_bf16_f32 v113,v114,v115
v_mfma_f32_16x16x32_bf16 v[224:227],a[248:251],a[80:83],v[224:227]
buffer_load_dwordx4 a[40:43],%[vL16],%[srdW],s82 offen offset:0 sc1
v_mfma_f32_16x16x32_bf16 v[240:243],a[252:255],a[80:83],v[240:243]
ds_write_b64 v3,v[112:113] offset:0
v_cvt_pk_bf16_f32 v128,v128,v129
v_mfma_f32_16x16x32_bf16 v[100:103],a[216:219],a[84:87],v[100:103]
v_cvt_pk_bf16_f32 v129,v130,v131
v_mfma_f32_16x16x32_bf16 v[116:119],a[220:223],a[84:87],v[116:119]
ds_write_b64 v2,v[128:129] offset:64
v_cvt_pk_bf16_f32 v144,v144,v145
v_mfma_f32_16x16x32_bf16 v[132:135],a[224:227],a[84:87],v[132:135]
buffer_load_dwordx4 a[44:47],%[vL16],%[srdW],s82 offen offset:1024 sc1
v_mfma_f32_16x16x32_bf16 v[148:151],a[228:231],a[84:87],v[148:151]
s_add_u32 s82,s83,14336
v_cvt_pk_bf16_f32 v145,v146,v147
v_mfma_f32_16x16x32_bf16 v[164:167],a[232:235],a[84:87],v[164:167]
ds_write_b64 v3,v[144:145] offset:64
v_cvt_pk_bf16_f32 v160,v160,v161
v_mfma_f32_16x16x32_bf16 v[180:183],a[236:239],a[84:87],v[180:183]
v_cvt_pk_bf16_f32 v161,v162,v163
v_mfma_f32_16x16x32_bf16 v[196:199],a[240:243],a[84:87],v[196:199]
buffer_load_dwordx4 a[48:51],%[vL16],%[srdW],s82 offen offset:0 sc1
v_mfma_f32_16x16x32_bf16 v[212:215],a[244:247],a[84:87],v[212:215]
ds_write_b64 v2,v[160:161] offset:128
v_cvt_pk_bf16_f32 v176,v176,v177
v_mfma_f32_16x16x32_bf16 v[228:231],a[248:251],a[84:87],v[228:231]
v_cvt_pk_bf16_f32 v177,v178,v179
v_mfma_f32_16x16x32_bf16 v[244:247],a[252:255],a[84:87],v[244:247]
ds_write_b64 v3,v[176:177] offset:128
v_cvt_pk_bf16_f32 v192,v192,v193
v_mfma_f32_16x16x32_bf16 v[104:107],a[216:219],a[88:91],v[104:107]
buffer_load_dwordx4 a[52:55],%[vL16],%[srdW],s82 offen offset:1024 sc1
v_mfma_f32_16x16x32_bf16 v[120:123],a[220:223],a[88:91],v[120:123]
s_add_u32 s82,s74,0
v_cvt_pk_bf16_f32 v193,v194,v195
v_mfma_f32_16x16x32_bf16 v[136:139],a[224:227],a[88:91],v[136:139]
ds_write_b64 v2,v[192:193] offset:192
v_cvt_pk_bf16_f32 v208,v208,v209
v_mfma_f32_16x16x32_bf16 v[152:155],a[228:231],a[88:91],v[152:155]
v_cvt_pk_bf16_f32 v209,v210,v211
v_mfma_f32_16x16x32_bf16 v[168:171],a[232:235],a[88:91],v[168:171]
buffer_load_dwordx4 a[56:59],%[vL16],%[srdW],s82 offen offset:0 sc1
v_mfma_f32_16x16x32_bf16 v[184:187],a[236:239],a[88:91],v[184:187]
ds_write_b64 v3,v[208:209] offset:192
v_cvt_pk_bf16_f32 v224,v224,v225
v_mfma_f32_16x16x32_bf16 v[200:203],a[240:243],a[88:91],v[200:203]
v_cvt_pk_bf16_f32 v225,v226,v227
v_mfma_f32_16x16x32_bf16 v[216:219],a[244:247],a[88:91],v[216:219]
ds_write_b64 v2,v[224:225] offset:256
v_cvt_pk_bf16_f32 v240,v240,v241
v_mfma_f32_16x16x32_bf16 v[232:235],a[248:251],a[88:91],v[232:235]
buffer_load_dwordx4 a[60:63],%[vL16],%[srdW],s82 offen offset:1024 sc1
v_mfma_f32_16x16x32_bf16 v[248:251],a[252:255],a[88:91],v[248:251]
s_add_u32 s82,s74,4096
v_cvt_pk_bf16_f32 v241,v242,v243
v_mfma_f32_16x16x32_bf16 v[108:111],a[216:219],a[92:95],v[108:111]
ds_write_b64 v3,v[240:241] offset:256
ds_read_b128 v[32:35],v4 offset:0
v_mfma_f32_16x16x32_bf16 v[124:127],a[220:223],a[92:95],v[124:127]
ds_read_b128 v[36:39],v4 offset:16
ds_read_b128 v[40:43],v4 offset:32
v_mfma_f32_16x16x32_bf16 v[140:143],a[224:227],a[92:95],v[140:143]
buffer_load_dwordx4 a[64:67],%[vL16],%[srdW],s82 offen offset:0 sc1
v_mfma_f32_16x16x32_bf16 v[156:159],a[228:231],a[92:95],v[156:159]
ds_read_b128 v[44:47],v4 offset:48
ds_read_b128 v[48:51],v4 offset:64
v_mfma_f32_16x16x32_bf16 v[172:175],a[232:235],a[92:95],v[172:175]
ds_read_b128 v[52:55],v4 offset:80
ds_read_b128 v[56:59],v4 offset:96
v_mfma_f32_16x16x32_bf16 v[188:191],a[236:239],a[92:95],v[188:191]
ds_read_b128 v[60:63],v4 offset:112
v_cvt_pk_bf16_f32 v100,v100,v101
v_mfma_f32_16x16x32_bf16 v[204:207],a[240:243],a[92:95],v[204:207]
buffer_load_dwordx4 a[68:71],%[vL16],%[srdW],s82 offen offset:1024 sc1
v_mfma_f32_16x16x32_bf16 v[220:223],a[244:247],a[92:95],v[220:223]
s_add_u32 s82,s74,8192
v_cvt_pk_bf16_f32 v101,v102,v103
v_mfma_f32_16x16x32_bf16 v[236:239],a[248:251],a[92:95],v[236:239]
ds_write_b64 v2,v[100:101] offset:5376
v_cvt_pk_bf16_f32 v116,v116,v117
v_mfma_f32_16x16x32_bf16 v[252:255],a[252:255],a[92:95],v[252:255]
v_cvt_pk_bf16_f32 v117,v118,v119
s_waitcnt lgkmcnt(1)
v_mfma_f32_4x4x4_16b_bf16 v[96:99],v[6:7],v[40:41],0
v_mfma_f32_4x4x4_16b_bf16 v[112:115],v[6:7],v[42:43],0
v_mfma_f32_4x4x4_16b_bf16 v[128:131],v[6:7],v[44:45],0
buffer_load_dwordx4 a[72:75],%[vL16],%[srdW],s82 offen offset:0 sc1
v_mfma_f32_4x4x4_16b_bf16 v[144:147],v[6:7],v[46:47],0
ds_write_b64 v3,v[116:117] offset:5376
v_mfma_f32_4x4x4_16b_bf16 v[96:99],a[96:97],v[32:33],v[96:99]
v_cvt_pk_bf16_f32 v132,v132,v133
v_mfma_f32_4x4x4_16b_bf16 v[112:115],a[96:97],v[34:35],v[112:115]
v_cvt_pk_bf16_f32 v133,v134,v135
v_mfma_f32_4x4x4_16b_bf16 v[128:131],a[96:97],v[36:37],v[128:131]
buffer_load_dwordx4 a[76:79],%[vL16],%[srdW],s82 offen offset:1024 sc1
v_mfma_f32_4x4x4_16b_bf16 v[144:147],a[96:97],v[38:39],v[144:147]
s_add_u32 s82,s74,12288
ds_write_b64 v2,v[132:133] offset:5440
v_mfma_f32_4x4x4_16b_bf16 v[96:99],a[98:99],v[34:35],v[96:99]
v_cvt_pk_bf16_f32 v148,v148,v149
v_mfma_f32_4x4x4_16b_bf16 v[112:115],a[98:99],v[36:37],v[112:115]
v_cvt_pk_bf16_f32 v149,v150,v151
v_mfma_f32_4x4x4_16b_bf16 v[128:131],a[98:99],v[38:39],v[128:131]
buffer_load_dwordx4 a[80:83],%[vL16],%[srdW],s82 offen offset:0 sc1
v_mfma_f32_4x4x4_16b_bf16 v[144:147],a[98:99],v[40:41],v[144:147]
ds_write_b64 v3,v[148:149] offset:5440
v_mfma_f32_4x4x4_16b_bf16 v[96:99],a[100:101],v[36:37],v[96:99]
v_cvt_pk_bf16_f32 v164,v164,v165
v_mfma_f32_4x4x4_16b_bf16 v[112:115],a[100:101],v[38:39],v[112:115]
v_cvt_pk_bf16_f32 v165,v166,v167
v_mfma_f32_4x4x4_16b_bf16 v[128:131],a[100:101],v[40:41],v[128:131]
buffer_load_dwordx4 a[84:87],%[vL16],%[srdW],s82 offen offset:1024 sc1
v_mfma_f32_4x4x4_16b_bf16 v[144:147],a[100:101],v[42:43],v[144:147]
s_add_u32 s82,s75,0
ds_write_b64 v2,v[164:165] offset:5504
v_mfma_f32_4x4x4_16b_bf16 v[96:99],a[102:103],v[38:39],v[96:99]
v_cvt_pk_bf16_f32 v180,v180,v181
v_mfma_f32_4x4x4_16b_bf16 v[112:115],a[102:103],v[40:41],v[112:115]
v_cvt_pk_bf16_f32 v181,v182,v183
v_mfma_f32_4x4x4_16b_bf16 v[128:131],a[102:103],v[42:43],v[128:131]
buffer_load_dwordx4 a[88:91],%[vL16],%[srdW],s82 offen offset:0 sc1
v_mfma_f32_4x4x4_16b_bf16 v[144:147],a[102:103],v[44:45],v[144:147]
ds_write_b64 v3,v[180:181] offset:5504
v_mfma_f32_4x4x4_16b_bf16 v[96:99],a[104:105],v[40:41],v[96:99]
v_cvt_pk_bf16_f32 v196,v196,v197
v_mfma_f32_4x4x4_16b_bf16 v[112:115],a[104:105],v[42:43],v[112:115]
v_cvt_pk_bf16_f32 v197,v198,v199
v_mfma_f32_4x4x4_16b_bf16 v[128:131],a[104:105],v[44:45],v[128:131]
buffer_load_dwordx4 a[92:95],%[vL16],%[srdW],s82 offen offset:1024 sc1
v_mfma_f32_4x4x4_16b_bf16 v[144:147],a[104:105],v[46:47],v[144:147]
ds_write_b64 v2,v[196:197] offset:5568
v_mfma_f32_4x4x4_16b_bf16 v[96:99],a[106:107],v[42:43],v[96:99]
v_cvt_pk_bf16_f32 v212,v212,v213
v_mfma_f32_4x4x4_16b_bf16 v[112:115],a[106:107],v[44:45],v[112:115]
v_cvt_pk_bf16_f32 v213,v214,v215
v_mfma_f32_4x4x4_16b_bf16 v[128:131],a[106:107],v[46:47],v[128:131]
ds_write_b64 v3,v[212:213] offset:5568
v_mfma_f32_4x4x4_16b_bf16 v[144:147],a[106:107],v[48:49],v[144:147]
v_cvt_pk_bf16_f32 v228,v228,v229
v_mfma_f32_4x4x4_16b_bf16 v[96:99],a[108:109],v[44:45],v[96:99]
v_cvt_pk_bf16_f32 v229,v230,v231
v_mfma_f32_4x4x4_16b_bf16 v[112:115],a[108:109],v[46:47],v[112:115]
ds_write_b64 v2,v[228:229] offset:5632
v_mfma_f32_4x4x4_16b_bf16 v[128:131],a[108:109],v[48:49],v[128:131]
v_cvt_pk_bf16_f32 v244,v244,v245
v_mfma_f32_4x4x4_16b_bf16 v[144:147],a[108:109],v[50:51],v[144:147]
v_cvt_pk_bf16_f32 v245,v246,v247
v_mfma_f32_4x4x4_16b_bf16 v[96:99],a[110:111],v[46:47],v[96:99]
ds_write_b64 v3,v[244:245] offset:5632
v_mfma_f32_4x4x4_16b_bf16 v[112:115],a[110:111],v[48:49],v[112:115]
ds_read_b128 v[64:67],v4 offset:5376
v_mfma_f32_4x4x4_16b_bf16 v[128:131],a[110:111],v[50:51],v[128:131]
ds_read_b128 v[68:71],v4 offset:5392
v_mfma_f32_4x4x4_16b_bf16 v[144:147],a[110:111],v[52:53],v[144:147]
ds_read_b128 v[72:75],v4 offset:5408
v_mfma_f32_4x4x4_16b_bf16 v[96:99],a[112:113],v[48:49],v[96:99]
ds_read_b128 v[76:79],v4 offset:5424
v_mfma_f32_4x4x4_16b_bf16 v[112:115],a[112:113],v[50:51],v[112:115]
ds_read_b128 v[80:83],v4 offset:5440
v_mfma_f32_4x4x4_16b_bf16 v[128:131],a[112:113],v[52:53],v[128:131]
ds_read_b128 v[84:87],v4 offset:5456
v_mfma_f32_4x4x4_16b_bf16 v[144:147],a[112:113],v[54:55],v[144:147]
ds_read_b128 v[88:91],v4 offset:5472
ds_read_b128 v[92:95],v4 offset:5488
v_mfma_f32_4x4x4_16b_bf16 v[160:163],v[6:7],v[48:49],0
v_mfma_f32_4x4x4_16b_bf16 v[176:179],v[6:7],v[50:51],0
v_mfma_f32_4x4x4_16b_bf16 v[192:195],v[6:7],v[52:53],0
v_cvt_pk_bf16_f32 v96,v96,v97
v_mfma_f32_4x4x4_16b_bf16 v[208:211],v[6:7],v[54:55],0
v_cvt_pk_bf16_f32 v97,v98,v99
v_mfma_f32_4x4x4_16b_bf16 v[160:163],a[96:97],v[40:41],v[160:163]
v_cvt_pk_bf16_f32 v112,v112,v113
v_mfma_f32_4x4x4_16b_bf16 v[176:179],a[96:97],v[42:43],v[176:179]
v_cvt_pk_bf16_f32 v113,v114,v115
v_mfma_f32_4x4x4_16b_bf16 v[192:195],a[96:97],v[44:45],v[192:195]
v_cndmask_b32 v224,v96,v112,vcc
v_mfma_f32_4x4x4_16b_bf16 v[208:211],a[96:97],v[46:47],v[208:211]
v_cndmask_b32 v225,v97,v113,vcc
v_mfma_f32_4x4x4_16b_bf16 v[160:163],a[98:99],v[42:43],v[160:163]
v_cndmask_b32 v226,v112,v96,vcc
v_mfma_f32_4x4x4_16b_bf16 v[176:179],a[98:99],v[44:45],v[176:179]
v_cndmask_b32 v227,v113,v97,vcc
v_mfma_f32_4x4x4_16b_bf16 v[192:195],a[98:99],v[46:47],v[192:195]
buffer_store_dwordx4 v[224:227],v8,%[srdW],0 offen offset:0 sc1
v_mfma_f32_4x4x4_16b_bf16 v[208:211],a[98:99],v[48:49],v[208:211]
ds_write_b128 v5,v[224:227] offset:0
v_mfma_f32_4x4x4_16b_bf16 v[160:163],a[100:101],v[44:45],v[160:163]
v_cvt_pk_bf16_f32 v128,v128,v129
v_mfma_f32_4x4x4_16b_bf16 v[176:179],a[100:101],v[46:47],v[176:179]
v_cvt_pk_bf16_f32 v129,v130,v131
v_mfma_f32_4x4x4_16b_bf16 v[192:195],a[100:101],v[48:49],v[192:195]
v_cvt_pk_bf16_f32 v144,v144,v145
v_mfma_f32_4x4x4_16b_bf16 v[208:211],a[100:101],v[50:51],v[208:211]
v_cvt_pk_bf16_f32 v145,v146,v147
v_mfma_f32_4x4x4_16b_bf16 v[160:163],a[102:103],v[46:47],v[160:163]
v_cndmask_b32 v240,v128,v144,vcc
)"
R"(v_mfma_f32_4x4x4_16b_bf16 v[176:179],a[102:103],v[48:49],v[176:179]
v_cndmask_b32 v241,v129,v145,vcc
v_mfma_f32_4x4x4_16b_bf16 v[192:195],a[102:103],v[50:51],v[192:195]
v_cndmask_b32 v242,v144,v128,vcc
v_mfma_f32_4x4x4_16b_bf16 v[208:211],a[102:103],v[52:53],v[208:211]
v_cndmask_b32 v243,v145,v129,vcc
v_mfma_f32_4x4x4_16b_bf16 v[160:163],a[104:105],v[48:49],v[160:163]
buffer_store_dwordx4 v[240:243],v8,%[srdW],0 offen offset:2048 sc1
v_mfma_f32_4x4x4_16b_bf16 v[176:179],a[104:105],v[50:51],v[176:179]
ds_write_b128 v5,v[240:243] offset:2048
v_mfma_f32_4x4x4_16b_bf16 v[192:195],a[104:105],v[52:53],v[192:195]
v_cvt_pk_bf16_f32 v104,v104,v105
v_mfma_f32_4x4x4_16b_bf16 v[208:211],a[104:105],v[54:55],v[208:211]
v_cvt_pk_bf16_f32 v105,v106,v107
v_mfma_f32_4x4x4_16b_bf16 v[160:163],a[106:107],v[50:51],v[160:163]
ds_write_b64 v2,v[104:105] offset:10752
v_mfma_f32_4x4x4_16b_bf16 v[176:179],a[106:107],v[52:53],v[176:179]
v_cvt_pk_bf16_f32 v120,v120,v121
v_mfma_f32_4x4x4_16b_bf16 v[192:195],a[106:107],v[54:55],v[192:195]
v_cvt_pk_bf16_f32 v121,v122,v123
v_mfma_f32_4x4x4_16b_bf16 v[208:211],a[106:107],v[56:57],v[208:211]
ds_write_b64 v3,v[120:121] offset:10752
v_mfma_f32_4x4x4_16b_bf16 v[160:163],a[108:109],v[52:53],v[160:163]
v_cvt_pk_bf16_f32 v136,v136,v137
v_mfma_f32_4x4x4_16b_bf16 v[176:179],a[108:109],v[54:55],v[176:179]
v_cvt_pk_bf16_f32 v137,v138,v139
v_mfma_f32_4x4x4_16b_bf16 v[192:195],a[108:109],v[56:57],v[192:195]
ds_write_b64 v2,v[136:137] offset:10816
v_mfma_f32_4x4x4_16b_bf16 v[208:211],a[108:109],v[58:59],v[208:211]
v_cvt_pk_bf16_f32 v152,v152,v153
v_mfma_f32_4x4x4_16b_bf16 v[160:163],a[110:111],v[54:55],v[160:163]
v_cvt_pk_bf16_f32 v153,v154,v155
v_mfma_f32_4x4x4_16b_bf16 v[176:179],a[110:111],v[56:57],v[176:179]
ds_write_b64 v3,v[152:153] offset:10816
v_mfma_f32_4x4x4_16b_bf16 v[192:195],a[110:111],v[58:59],v[192:195]
v_cvt_pk_bf16_f32 v168,v168,v169
v_mfma_f32_4x4x4_16b_bf16 v[208:211],a[110:111],v[60:61],v[208:211]
v_cvt_pk_bf16_f32 v169,v170,v171
v_mfma_f32_4x4x4_16b_bf16 v[160:163],a[112:113],v[56:57],v[160:163]
ds_write_b64 v2,v[168:169] offset:10880
v_mfma_f32_4x4x4_16b_bf16 v[176:179],a[112:113],v[58:59],v[176:179]
v_cvt_pk_bf16_f32 v184,v184,v185
v_mfma_f32_4x4x4_16b_bf16 v[192:195],a[112:113],v[60:61],v[192:195]
v_cvt_pk_bf16_f32 v185,v186,v187
v_mfma_f32_4x4x4_16b_bf16 v[208:211],a[112:113],v[62:63],v[208:211]
ds_write_b64 v3,v[184:185] offset:10880
s_waitcnt lgkmcnt(8)
v_mfma_f32_4x4x4_16b_bf16 v[96:99],v[6:7],v[72:73],0
v_mfma_f32_4x4x4_16b_bf16 v[112:115],v[6:7],v[74:75],0
v_mfma_f32_4x4x4_16b_bf16 v[128:131],v[6:7],v[76:77],0
v_cvt_pk_bf16_f32 v200,v200,v201
v_mfma_f32_4x4x4_16b_bf16 v[144:147],v[6:7],v[78:79],0
v_cvt_pk_bf16_f32 v201,v202,v203
v_mfma_f32_4x4x4_16b_bf16 v[96:99],a[116:117],v[64:65],v[96:99]
ds_write_b64 v2,v[200:201] offset:10944
v_mfma_f32_4x4x4_16b_bf16 v[112:115],a[116:117],v[66:67],v[112:115]
v_cvt_pk_bf16_f32 v216,v216,v217
v_mfma_f32_4x4x4_16b_bf16 v[128:131],a[116:117],v[68:69],v[128:131]
v_cvt_pk_bf16_f32 v217,v218,v219
v_mfma_f32_4x4x4_16b_bf16 v[144:147],a[116:117],v[70:71],v[144:147]
ds_write_b64 v3,v[216:217] offset:10944
v_mfma_f32_4x4x4_16b_bf16 v[96:99],a[118:119],v[66:67],v[96:99]
v_cvt_pk_bf16_f32 v232,v232,v233
v_mfma_f32_4x4x4_16b_bf16 v[112:115],a[118:119],v[68:69],v[112:115]
v_cvt_pk_bf16_f32 v233,v234,v235
v_mfma_f32_4x4x4_16b_bf16 v[128:131],a[118:119],v[70:71],v[128:131]
ds_write_b64 v2,v[232:233] offset:11008
v_mfma_f32_4x4x4_16b_bf16 v[144:147],a[118:119],v[72:73],v[144:147]
v_cvt_pk_bf16_f32 v248,v248,v249
v_mfma_f32_4x4x4_16b_bf16 v[96:99],a[120:121],v[68:69],v[96:99]
v_cvt_pk_bf16_f32 v249,v250,v251
v_mfma_f32_4x4x4_16b_bf16 v[112:115],a[120:121],v[70:71],v[112:115]
ds_write_b64 v3,v[248:249] offset:11008
v_mfma_f32_4x4x4_16b_bf16 v[128:131],a[120:121],v[72:73],v[128:131]
ds_read_b128 v[32:35],v4 offset:10752
v_mfma_f32_4x4x4_16b_bf16 v[144:147],a[120:121],v[74:75],v[144:147]
ds_read_b128 v[36:39],v4 offset:10768
v_mfma_f32_4x4x4_16b_bf16 v[96:99],a[122:123],v[70:71],v[96:99]
ds_read_b128 v[40:43],v4 offset:10784
v_mfma_f32_4x4x4_16b_bf16 v[112:115],a[122:123],v[72:73],v[112:115]
ds_read_b128 v[44:47],v4 offset:10800
v_mfma_f32_4x4x4_16b_bf16 v[128:131],a[122:123],v[74:75],v[128:131]
ds_read_b128 v[48:51],v4 offset:10816
v_mfma_f32_4x4x4_16b_bf16 v[144:147],a[122:123],v[76:77],v[144:147]
ds_read_b128 v[52:55],v4 offset:10832
v_mfma_f32_4x4x4_16b_bf16 v[96:99],a[124:125],v[72:73],v[96:99]
ds_read_b128 v[56:59],v4 offset:10848
v_mfma_f32_4x4x4_16b_bf16 v[112:115],a[124:125],v[74:75],v[112:115]
ds_read_b128 v[60:63],v4 offset:10864
v_mfma_f32_4x4x4_16b_bf16 v[128:131],a[124:125],v[76:77],v[128:131]
v_cvt_pk_bf16_f32 v160,v160,v161
v_mfma_f32_4x4x4_16b_bf16 v[144:147],a[124:125],v[78:79],v[144:147]
v_cvt_pk_bf16_f32 v161,v162,v163
v_mfma_f32_4x4x4_16b_bf16 v[96:99],a[126:127],v[74:75],v[96:99]
v_cvt_pk_bf16_f32 v176,v176,v177
v_mfma_f32_4x4x4_16b_bf16 v[112:115],a[126:127],v[76:77],v[112:115]
v_cvt_pk_bf16_f32 v177,v178,v179
v_mfma_f32_4x4x4_16b_bf16 v[128:131],a[126:127],v[78:79],v[128:131]
v_cndmask_b32 v224,v160,v176,vcc
v_mfma_f32_4x4x4_16b_bf16 v[144:147],a[126:127],v[80:81],v[144:147]
v_cndmask_b32 v225,v161,v177,vcc
v_mfma_f32_4x4x4_16b_bf16 v[96:99],a[128:129],v[76:77],v[96:99]
v_cndmask_b32 v226,v176,v160,vcc
v_mfma_f32_4x4x4_16b_bf16 v[112:115],a[128:129],v[78:79],v[112:115]
v_cndmask_b32 v227,v177,v161,vcc
v_mfma_f32_4x4x4_16b_bf16 v[128:131],a[128:129],v[80:81],v[128:131]
buffer_store_dwordx4 v[224:227],v20,%[srdW],0 offen offset:0 sc1
v_mfma_f32_4x4x4_16b_bf16 v[144:147],a[128:129],v[82:83],v[144:147]
ds_write_b128 v5,v[224:227] offset:4096
v_mfma_f32_4x4x4_16b_bf16 v[96:99],a[130:131],v[78:79],v[96:99]
v_cvt_pk_bf16_f32 v192,v192,v193
v_mfma_f32_4x4x4_16b_bf16 v[112:115],a[130:131],v[80:81],v[112:115]
v_cvt_pk_bf16_f32 v193,v194,v195
v_mfma_f32_4x4x4_16b_bf16 v[128:131],a[130:131],v[82:83],v[128:131]
v_cvt_pk_bf16_f32 v208,v208,v209
v_mfma_f32_4x4x4_16b_bf16 v[144:147],a[130:131],v[84:85],v[144:147]
v_cvt_pk_bf16_f32 v209,v210,v211
v_mfma_f32_4x4x4_16b_bf16 v[96:99],a[132:133],v[80:81],v[96:99]
v_cndmask_b32 v240,v192,v208,vcc
v_mfma_f32_4x4x4_16b_bf16 v[112:115],a[132:133],v[82:83],v[112:115]
v_cndmask_b32 v241,v193,v209,vcc
v_mfma_f32_4x4x4_16b_bf16 v[128:131],a[132:133],v[84:85],v[128:131]
v_cndmask_b32 v242,v208,v192,vcc
v_mfma_f32_4x4x4_16b_bf16 v[144:147],a[132:133],v[86:87],v[144:147]
v_cndmask_b32 v243,v209,v193,vcc
buffer_store_dwordx4 v[240:243],v20,%[srdW],0 offen offset:2048 sc1
ds_write_b128 v5,v[240:243] offset:6144
v_mfma_f32_4x4x4_16b_bf16 v[160:163],v[6:7],v[80:81],0
v_mfma_f32_4x4x4_16b_bf16 v[176:179],v[6:7],v[82:83],0
v_mfma_f32_4x4x4_16b_bf16 v[192:195],v[6:7],v[84:85],0
v_cvt_pk_bf16_f32 v96,v96,v97
v_mfma_f32_4x4x4_16b_bf16 v[208:211],v[6:7],v[86:87],0
v_cvt_pk_bf16_f32 v97,v98,v99
v_mfma_f32_4x4x4_16b_bf16 v[160:163],a[116:117],v[72:73],v[160:163]
v_cvt_pk_bf16_f32 v112,v112,v113
v_mfma_f32_4x4x4_16b_bf16 v[176:179],a[116:117],v[74:75],v[176:179]
v_cvt_pk_bf16_f32 v113,v114,v115
v_mfma_f32_4x4x4_16b_bf16 v[192:195],a[116:117],v[76:77],v[192:195]
v_cndmask_b32 v224,v96,v112,vcc
v_mfma_f32_4x4x4_16b_bf16 v[208:211],a[116:117],v[78:79],v[208:211]
v_cndmask_b32 v225,v97,v113,vcc
v_mfma_f32_4x4x4_16b_bf16 v[160:163],a[118:119],v[74:75],v[160:163]
v_cndmask_b32 v226,v112,v96,vcc
v_mfma_f32_4x4x4_16b_bf16 v[176:179],a[118:119],v[76:77],v[176:179]
v_cndmask_b32 v227,v113,v97,vcc
v_mfma_f32_4x4x4_16b_bf16 v[192:195],a[118:119],v[78:79],v[192:195]
buffer_store_dwordx4 v[224:227],v8,%[srdW],0 offen offset:1024 sc1
v_mfma_f32_4x4x4_16b_bf16 v[208:211],a[118:119],v[80:81],v[208:211]
ds_write_b128 v5,v[224:227] offset:1024
v_mfma_f32_4x4x4_16b_bf16 v[160:163],a[120:121],v[76:77],v[160:163]
v_cvt_pk_bf16_f32 v128,v128,v129
v_mfma_f32_4x4x4_16b_bf16 v[176:179],a[120:121],v[78:79],v[176:179]
v_cvt_pk_bf16_f32 v129,v130,v131
v_mfma_f32_4x4x4_16b_bf16 v[192:195],a[120:121],v[80:81],v[192:195]
v_cvt_pk_bf16_f32 v144,v144,v145
v_mfma_f32_4x4x4_16b_bf16 v[208:211],a[120:121],v[82:83],v[208:211]
v_cvt_pk_bf16_f32 v145,v146,v147
v_mfma_f32_4x4x4_16b_bf16 v[160:163],a[122:123],v[78:79],v[160:163]
v_cndmask_b32 v240,v128,v144,vcc
v_mfma_f32_4x4x4_16b_bf16 v[176:179],a[122:123],v[80:81],v[176:179]
v_cndmask_b32 v241,v129,v145,vcc
v_mfma_f32_4x4x4_16b_bf16 v[192:195],a[122:123],v[82:83],v[192:195]
v_cndmask_b32 v242,v144,v128,vcc
v_mfma_f32_4x4x4_16b_bf16 v[208:211],a[122:123],v[84:85],v[208:211]
v_cndmask_b32 v243,v145,v129,vcc
v_mfma_f32_4x4x4_16b_bf16 v[160:163],a[124:125],v[80:81],v[160:163]
buffer_store_dwordx4 v[240:243],v8,%[srdW],0 offen offset:3072 sc1
v_mfma_f32_4x4x4_16b_bf16 v[176:179],a[124:125],v[82:83],v[176:179]
ds_write_b128 v5,v[240:243] offset:3072
v_mfma_f32_4x4x4_16b_bf16 v[192:195],a[124:125],v[84:85],v[192:195]
v_cvt_pk_bf16_f32 v108,v108,v109
v_mfma_f32_4x4x4_16b_bf16 v[208:211],a[124:125],v[86:87],v[208:211]
v_cvt_pk_bf16_f32 v109,v110,v111
v_mfma_f32_4x4x4_16b_bf16 v[160:163],a[126:127],v[82:83],v[160:163]
ds_write_b64 v2,v[108:109] offset:16128
v_mfma_f32_4x4x4_16b_bf16 v[176:179],a[126:127],v[84:85],v[176:179]
v_cvt_pk_bf16_f32 v124,v124,v125
v_mfma_f32_4x4x4_16b_bf16 v[192:195],a[126:127],v[86:87],v[192:195]
v_cvt_pk_bf16_f32 v125,v126,v127
v_mfma_f32_4x4x4_16b_bf16 v[208:211],a[126:127],v[88:89],v[208:211]
ds_write_b64 v3,v[124:125] offset:16128
v_mfma_f32_4x4x4_16b_bf16 v[160:163],a[128:129],v[84:85],v[160:163]
v_cvt_pk_bf16_f32 v140,v140,v141
v_mfma_f32_4x4x4_16b_bf16 v[176:179],a[128:129],v[86:87],v[176:179]
v_cvt_pk_bf16_f32 v141,v142,v143
v_mfma_f32_4x4x4_16b_bf16 v[192:195],a[128:129],v[88:89],v[192:195]
ds_write_b64 v2,v[140:141] offset:16192
v_mfma_f32_4x4x4_16b_bf16 v[208:211],a[128:129],v[90:91],v[208:211]
v_cvt_pk_bf16_f32 v156,v156,v157
v_mfma_f32_4x4x4_16b_bf16 v[160:163],a[130:131],v[86:87],v[160:163]
v_cvt_pk_bf16_f32 v157,v158,v159
v_mfma_f32_4x4x4_16b_bf16 v[176:179],a[130:131],v[88:89],v[176:179]
ds_write_b64 v3,v[156:157] offset:16192
v_mfma_f32_4x4x4_16b_bf16 v[192:195],a[130:131],v[90:91],v[192:195]
v_cvt_pk_bf16_f32 v172,v172,v173
v_mfma_f32_4x4x4_16b_bf16 v[208:211],a[130:131],v[92:93],v[208:211]
v_cvt_pk_bf16_f32 v173,v174,v175
v_mfma_f32_4x4x4_16b_bf16 v[160:163],a[132:133],v[88:89],v[160:163]
ds_write_b64 v2,v[172:173] offset:16256
v_mfma_f32_4x4x4_16b_bf16 v[176:179],a[132:133],v[90:91],v[176:179]
v_cvt_pk_bf16_f32 v188,v188,v189
v_mfma_f32_4x4x4_16b_bf16 v[192:195],a[132:133],v[92:93],v[192:195]
v_cvt_pk_bf16_f32 v189,v190,v191
v_mfma_f32_4x4x4_16b_bf16 v[208:211],a[132:133],v[94:95],v[208:211]
ds_write_b64 v3,v[188:189] offset:16256
s_waitcnt lgkmcnt(10)
v_mfma_f32_4x4x4_16b_bf16 v[96:99],v[6:7],v[40:41],0
v_mfma_f32_4x4x4_16b_bf16 v[112:115],v[6:7],v[42:43],0
v_mfma_f32_4x4x4_16b_bf16 v[128:131],v[6:7],v[44:45],0
v_cvt_pk_bf16_f32 v204,v204,v205
v_mfma_f32_4x4x4_16b_bf16 v[144:147],v[6:7],v[46:47],0
v_cvt_pk_bf16_f32 v205,v206,v207
v_mfma_f32_4x4x4_16b_bf16 v[96:99],a[136:137],v[32:33],v[96:99]
ds_write_b64 v2,v[204:205] offset:16320
v_mfma_f32_4x4x4_16b_bf16 v[112:115],a[136:137],v[34:35],v[112:115]
v_cvt_pk_bf16_f32 v220,v220,v221
v_mfma_f32_4x4x4_16b_bf16 v[128:131],a[136:137],v[36:37],v[128:131]
v_cvt_pk_bf16_f32 v221,v222,v223
v_mfma_f32_4x4x4_16b_bf16 v[144:147],a[136:137],v[38:39],v[144:147]
ds_write_b64 v3,v[220:221] offset:16320
v_mfma_f32_4x4x4_16b_bf16 v[96:99],a[138:139],v[34:35],v[96:99]
v_cvt_pk_bf16_f32 v236,v236,v237
v_mfma_f32_4x4x4_16b_bf16 v[112:115],a[138:139],v[36:37],v[112:115]
v_cvt_pk_bf16_f32 v237,v238,v239
v_mfma_f32_4x4x4_16b_bf16 v[128:131],a[138:139],v[38:39],v[128:131]
ds_write_b64 v2,v[236:237] offset:16384
v_mfma_f32_4x4x4_16b_bf16 v[144:147],a[138:139],v[40:41],v[144:147]
v_cvt_pk_bf16_f32 v252,v252,v253
v_mfma_f32_4x4x4_16b_bf16 v[96:99],a[140:141],v[36:37],v[96:99]
v_cvt_pk_bf16_f32 v253,v254,v255
v_mfma_f32_4x4x4_16b_bf16 v[112:115],a[140:141],v[38:39],v[112:115]
ds_write_b64 v3,v[252:253] offset:16384
v_mfma_f32_4x4x4_16b_bf16 v[128:131],a[140:141],v[40:41],v[128:131]
ds_read_b128 v[64:67],v4 offset:16128
v_mfma_f32_4x4x4_16b_bf16 v[144:147],a[140:141],v[42:43],v[144:147]
ds_read_b128 v[68:71],v4 offset:16144
v_mfma_f32_4x4x4_16b_bf16 v[96:99],a[142:143],v[38:39],v[96:99]
ds_read_b128 v[72:75],v4 offset:16160
v_mfma_f32_4x4x4_16b_bf16 v[112:115],a[142:143],v[40:41],v[112:115]
ds_read_b128 v[76:79],v4 offset:16176
v_mfma_f32_4x4x4_16b_bf16 v[128:131],a[142:143],v[42:43],v[128:131]
ds_read_b128 v[80:83],v4 offset:16192
v_mfma_f32_4x4x4_16b_bf16 v[144:147],a[142:143],v[44:45],v[144:147]
ds_read_b128 v[84:87],v4 offset:16208
v_mfma_f32_4x4x4_16b_bf16 v[96:99],a[144:145],v[40:41],v[96:99]
ds_read_b128 v[88:91],v4 offset:16224
v_mfma_f32_4x4x4_16b_bf16 v[112:115],a[144:145],v[42:43],v[112:115]
ds_read_b128 v[92:95],v4 offset:16240
v_mfma_f32_4x4x4_16b_bf16 v[128:131],a[144:145],v[44:45],v[128:131]
v_cvt_pk_bf16_f32 v160,v160,v161
v_mfma_f32_4x4x4_16b_bf16 v[144:147],a[144:145],v[46:47],v[144:147]
v_cvt_pk_bf16_f32 v161,v162,v163
v_mfma_f32_4x4x4_16b_bf16 v[96:99],a[146:147],v[42:43],v[96:99]
v_cvt_pk_bf16_f32 v176,v176,v177
v_mfma_f32_4x4x4_16b_bf16 v[112:115],a[146:147],v[44:45],v[112:115]
v_cvt_pk_bf16_f32 v177,v178,v179
v_mfma_f32_4x4x4_16b_bf16 v[128:131],a[146:147],v[46:47],v[128:131]
v_cndmask_b32 v224,v160,v176,vcc
v_mfma_f32_4x4x4_16b_bf16 v[144:147],a[146:147],v[48:49],v[144:147]
v_cndmask_b32 v225,v161,v177,vcc
v_mfma_f32_4x4x4_16b_bf16 v[96:99],a[148:149],v[44:45],v[96:99]
v_cndmask_b32 v226,v176,v160,vcc
v_mfma_f32_4x4x4_16b_bf16 v[112:115],a[148:149],v[46:47],v[112:115]
v_cndmask_b32 v227,v177,v161,vcc
v_mfma_f32_4x4x4_16b_bf16 v[128:131],a[148:149],v[48:49],v[128:131]
buffer_store_dwordx4 v[224:227],v20,%[srdW],0 offen offset:1024 sc1
v_mfma_f32_4x4x4_16b_bf16 v[144:147],a[148:149],v[50:51],v[144:147]
ds_write_b128 v5,v[224:227] offset:5120
v_mfma_f32_4x4x4_16b_bf16 v[96:99],a[150:151],v[46:47],v[96:99]
v_cvt_pk_bf16_f32 v192,v192,v193
v_mfma_f32_4x4x4_16b_bf16 v[112:115],a[150:151],v[48:49],v[112:115]
v_cvt_pk_bf16_f32 v193,v194,v195
v_mfma_f32_4x4x4_16b_bf16 v[128:131],a[150:151],v[50:51],v[128:131]
v_cvt_pk_bf16_f32 v208,v208,v209
v_mfma_f32_4x4x4_16b_bf16 v[144:147],a[150:151],v[52:53],v[144:147]
v_cvt_pk_bf16_f32 v209,v210,v211
v_mfma_f32_4x4x4_16b_bf16 v[96:99],a[152:153],v[48:49],v[96:99]
v_cndmask_b32 v240,v192,v208,vcc
v_mfma_f32_4x4x4_16b_bf16 v[112:115],a[152:153],v[50:51],v[112:115]
v_cndmask_b32 v241,v193,v209,vcc
v_mfma_f32_4x4x4_16b_bf16 v[128:131],a[152:153],v[52:53],v[128:131]
v_cndmask_b32 v242,v208,v192,vcc
v_mfma_f32_4x4x4_16b_bf16 v[144:147],a[152:153],v[54:55],v[144:147]
v_cndmask_b32 v243,v209,v193,vcc
buffer_store_dwordx4 v[240:243],v20,%[srdW],0 offen offset:3072 sc1
ds_write_b128 v5,v[240:243] offset:7168
v_mfma_f32_4x4x4_16b_bf16 v[160:163],v[6:7],v[48:49],0
v_mfma_f32_4x4x4_16b_bf16 v[176:179],v[6:7],v[50:51],0
v_mfma_f32_4x4x4_16b_bf16 v[192:195],v[6:7],v[52:53],0
v_cvt_pk_bf16_f32 v96,v96,v97
v_mfma_f32_4x4x4_16b_bf16 v[208:211],v[6:7],v[54:55],0
v_cvt_pk_bf16_f32 v97,v98,v99
v_mfma_f32_4x4x4_16b_bf16 v[160:163],a[136:137],v[40:41],v[160:163]
v_cvt_pk_bf16_f32 v112,v112,v113
v_mfma_f32_4x4x4_16b_bf16 v[176:179],a[136:137],v[42:43],v[176:179]
v_cvt_pk_bf16_f32 v113,v114,v115
v_mfma_f32_4x4x4_16b_bf16 v[192:195],a[136:137],v[44:45],v[192:195]
v_cndmask_b32 v224,v96,v112,vcc
v_mfma_f32_4x4x4_16b_bf16 v[208:211],a[136:137],v[46:47],v[208:211]
v_cndmask_b32 v225,v97,v113,vcc
v_mfma_f32_4x4x4_16b_bf16 v[160:163],a[138:139],v[42:43],v[160:163]
v_cndmask_b32 v226,v112,v96,vcc
v_mfma_f32_4x4x4_16b_bf16 v[176:179],a[138:139],v[44:45],v[176:179]
v_cndmask_b32 v227,v113,v97,vcc
v_mfma_f32_4x4x4_16b_bf16 v[192:195],a[138:139],v[46:47],v[192:195]
v_add_u32 v8,0x2000,v8
v_mfma_f32_4x4x4_16b_bf16 v[208:211],a[138:139],v[48:49],v[208:211]
v_add_u32 v20,0x2000,v20
v_mfma_f32_4x4x4_16b_bf16 v[160:163],a[140:141],v[44:45],v[160:163]
buffer_store_dwordx4 v[224:227],v8,%[srdW],0 offen offset:0 sc1
v_mfma_f32_4x4x4_16b_bf16 v[176:179],a[140:141],v[46:47],v[176:179]
ds_write_b128 v5,v[224:227] offset:8192
v_mfma_f32_4x4x4_16b_bf16 v[192:195],a[140:141],v[48:49],v[192:195]
v_cvt_pk_bf16_f32 v128,v128,v129
v_mfma_f32_4x4x4_16b_bf16 v[208:211],a[140:141],v[50:51],v[208:211]
v_cvt_pk_bf16_f32 v129,v130,v131
v_mfma_f32_4x4x4_16b_bf16 v[160:163],a[142:143],v[46:47],v[160:163]
v_cvt_pk_bf16_f32 v144,v144,v145
v_mfma_f32_4x4x4_16b_bf16 v[176:179],a[142:143],v[48:49],v[176:179]
v_cvt_pk_bf16_f32 v145,v146,v147
v_mfma_f32_4x4x4_16b_bf16 v[192:195],a[142:143],v[50:51],v[192:195]
v_cndmask_b32 v240,v128,v144,vcc
v_mfma_f32_4x4x4_16b_bf16 v[208:211],a[142:143],v[52:53],v[208:211]
v_cndmask_b32 v241,v129,v145,vcc
v_mfma_f32_4x4x4_16b_bf16 v[160:163],a[144:145],v[48:49],v[160:163]
v_cndmask_b32 v242,v144,v128,vcc
v_mfma_f32_4x4x4_16b_bf16 v[176:179],a[144:145],v[50:51],v[176:179]
v_cndmask_b32 v243,v145,v129,vcc
v_mfma_f32_4x4x4_16b_bf16 v[192:195],a[144:145],v[52:53],v[192:195]
buffer_store_dwordx4 v[240:243],v8,%[srdW],0 offen offset:2048 sc1
v_mfma_f32_4x4x4_16b_bf16 v[208:211],a[144:145],v[54:55],v[208:211]
ds_write_b128 v5,v[240:243] offset:10240
v_mfma_f32_4x4x4_16b_bf16 v[160:163],a[146:147],v[50:51],v[160:163]
v_readlane_b32 s62,%[pv],48
v_mfma_f32_4x4x4_16b_bf16 v[176:179],a[146:147],v[52:53],v[176:179]
v_readlane_b32 s63,%[pv],49
v_mfma_f32_4x4x4_16b_bf16 v[192:195],a[146:147],v[54:55],v[192:195]
v_readlane_b32 s64,%[pv],50
v_mfma_f32_4x4x4_16b_bf16 v[208:211],a[146:147],v[56:57],v[208:211]
v_readlane_b32 s68,%[pv],51
v_mfma_f32_4x4x4_16b_bf16 v[160:163],a[148:149],v[52:53],v[160:163]
v_readlane_b32 s73,%[pv],52
v_mfma_f32_4x4x4_16b_bf16 v[176:179],a[148:149],v[54:55],v[176:179]
v_readlane_b32 s78,%[pv],53
v_mfma_f32_4x4x4_16b_bf16 v[192:195],a[148:149],v[56:57],v[192:195]
v_readlane_b32 s79,%[pv],54
v_mfma_f32_4x4x4_16b_bf16 v[208:211],a[148:149],v[58:59],v[208:211]
v_readlane_b32 s80,%[pv],55
v_mfma_f32_4x4x4_16b_bf16 v[160:163],a[150:151],v[54:55],v[160:163]
v_readlane_b32 s81,%[pv],56
v_mfma_f32_4x4x4_16b_bf16 v[176:179],a[150:151],v[56:57],v[176:179]
v_readlane_b32 s72,%[pv],57
v_mfma_f32_4x4x4_16b_bf16 v[192:195],a[150:151],v[58:59],v[192:195]
v_readlane_b32 s77,%[pv],0
v_mfma_f32_4x4x4_16b_bf16 v[208:211],a[150:151],v[60:61],v[208:211]
v_lshrrev_b32 v16,8,%[vL16]
v_mfma_f32_4x4x4_16b_bf16 v[160:163],a[152:153],v[56:57],v[160:163]
v_bfe_u32 v17,%[vL16],4,4
v_mfma_f32_4x4x4_16b_bf16 v[176:179],a[152:153],v[58:59],v[176:179]
v_mov_b32 v23,s70
v_mfma_f32_4x4x4_16b_bf16 v[192:195],a[152:153],v[60:61],v[192:195]
v_mov_b32 v25,s73
v_mfma_f32_4x4x4_16b_bf16 v[208:211],a[152:153],v[62:63],v[208:211]
v_lshrrev_b32 v18,2,v17
v_lshl_add_u32 v18,v16,2,v18
v_lshlrev_b32 v10,6,v18
s_waitcnt lgkmcnt(4)
v_mfma_f32_4x4x4_16b_bf16 v[96:99],v[6:7],v[72:73],0
v_mfma_f32_4x4x4_16b_bf16 v[112:115],v[6:7],v[74:75],0
v_mfma_f32_4x4x4_16b_bf16 v[128:131],v[6:7],v[76:77],0
v_cvt_pk_bf16_f32 v160,v160,v161
v_mfma_f32_4x4x4_16b_bf16 v[144:147],v[6:7],v[78:79],0
v_cvt_pk_bf16_f32 v161,v162,v163
v_mfma_f32_4x4x4_16b_bf16 v[96:99],a[156:157],v[64:65],v[96:99]
v_cvt_pk_bf16_f32 v176,v176,v177
v_mfma_f32_4x4x4_16b_bf16 v[112:115],a[156:157],v[66:67],v[112:115]
v_cvt_pk_bf16_f32 v177,v178,v179
v_mfma_f32_4x4x4_16b_bf16 v[128:131],a[156:157],v[68:69],v[128:131]
v_cndmask_b32 v224,v160,v176,vcc
v_mfma_f32_4x4x4_16b_bf16 v[144:147],a[156:157],v[70:71],v[144:147]
v_cndmask_b32 v225,v161,v177,vcc
v_mfma_f32_4x4x4_16b_bf16 v[96:99],a[158:159],v[66:67],v[96:99]
v_cndmask_b32 v226,v176,v160,vcc
v_mfma_f32_4x4x4_16b_bf16 v[112:115],a[158:159],v[68:69],v[112:115]
v_cndmask_b32 v227,v177,v161,vcc
v_mfma_f32_4x4x4_16b_bf16 v[128:131],a[158:159],v[70:71],v[128:131]
buffer_store_dwordx4 v[224:227],v20,%[srdW],0 offen offset:0 sc1
v_mfma_f32_4x4x4_16b_bf16 v[144:147],a[158:159],v[72:73],v[144:147]
ds_write_b128 v5,v[224:227] offset:12288
v_mfma_f32_4x4x4_16b_bf16 v[96:99],a[160:161],v[68:69],v[96:99]
v_cvt_pk_bf16_f32 v192,v192,v193
v_mfma_f32_4x4x4_16b_bf16 v[112:115],a[160:161],v[70:71],v[112:115]
v_cvt_pk_bf16_f32 v193,v194,v195
v_mfma_f32_4x4x4_16b_bf16 v[128:131],a[160:161],v[72:73],v[128:131]
v_cvt_pk_bf16_f32 v208,v208,v209
v_mfma_f32_4x4x4_16b_bf16 v[144:147],a[160:161],v[74:75],v[144:147]
v_cvt_pk_bf16_f32 v209,v210,v211
v_mfma_f32_4x4x4_16b_bf16 v[96:99],a[162:163],v[70:71],v[96:99]
v_cndmask_b32 v240,v192,v208,vcc
v_mfma_f32_4x4x4_16b_bf16 v[112:115],a[162:163],v[72:73],v[112:115]
)"
R"(v_cndmask_b32 v241,v193,v209,vcc
v_mfma_f32_4x4x4_16b_bf16 v[128:131],a[162:163],v[74:75],v[128:131]
v_cndmask_b32 v242,v208,v192,vcc
v_mfma_f32_4x4x4_16b_bf16 v[144:147],a[162:163],v[76:77],v[144:147]
v_cndmask_b32 v243,v209,v193,vcc
v_mfma_f32_4x4x4_16b_bf16 v[96:99],a[164:165],v[72:73],v[96:99]
buffer_store_dwordx4 v[240:243],v20,%[srdW],0 offen offset:2048 sc1
v_mfma_f32_4x4x4_16b_bf16 v[112:115],a[164:165],v[74:75],v[112:115]
ds_write_b128 v5,v[240:243] offset:14336
v_mfma_f32_4x4x4_16b_bf16 v[128:131],a[164:165],v[76:77],v[128:131]
v_and_b32 v18,3,v17
v_mfma_f32_4x4x4_16b_bf16 v[144:147],a[164:165],v[78:79],v[144:147]
v_lshl_add_u32 v10,v18,4,v10
v_mfma_f32_4x4x4_16b_bf16 v[96:99],a[166:167],v[74:75],v[96:99]
v_and_b32 v18,1,v16
v_mfma_f32_4x4x4_16b_bf16 v[112:115],a[166:167],v[76:77],v[112:115]
v_lshl_add_u32 v10,v18,3,v10
v_mfma_f32_4x4x4_16b_bf16 v[128:131],a[166:167],v[78:79],v[128:131]
v_add_u32 v11,0x10000,v10
v_mfma_f32_4x4x4_16b_bf16 v[144:147],a[166:167],v[80:81],v[144:147]
v_xor_b32 v12,8,v10
v_mfma_f32_4x4x4_16b_bf16 v[96:99],a[168:169],v[76:77],v[96:99]
v_xor_b32 v13,8,v11
v_mfma_f32_4x4x4_16b_bf16 v[112:115],a[168:169],v[78:79],v[112:115]
v_and_b32 v14,3,v17
v_mfma_f32_4x4x4_16b_bf16 v[128:131],a[168:169],v[80:81],v[128:131]
v_lshlrev_b32 v14,7,v14
v_mfma_f32_4x4x4_16b_bf16 v[144:147],a[168:169],v[82:83],v[144:147]
v_add_u32 v14,s78,v14
v_mfma_f32_4x4x4_16b_bf16 v[96:99],a[170:171],v[78:79],v[96:99]
v_add_u32 v18,s80,v16
v_mfma_f32_4x4x4_16b_bf16 v[112:115],a[170:171],v[80:81],v[112:115]
v_xor_b32 v18,v18,v17
v_mfma_f32_4x4x4_16b_bf16 v[128:131],a[170:171],v[82:83],v[128:131]
v_lshlrev_b32 v18,4,v18
v_mfma_f32_4x4x4_16b_bf16 v[144:147],a[170:171],v[84:85],v[144:147]
v_lshl_add_u32 v15,v16,10,v18
v_mfma_f32_4x4x4_16b_bf16 v[96:99],a[172:173],v[80:81],v[96:99]
v_add_u32 v15,s79,v15
v_mfma_f32_4x4x4_16b_bf16 v[112:115],a[172:173],v[82:83],v[112:115]
v_lshrrev_b32 v19,2,v17
v_mfma_f32_4x4x4_16b_bf16 v[128:131],a[172:173],v[84:85],v[128:131]
v_lshlrev_b32 v19,5,v19
v_mfma_f32_4x4x4_16b_bf16 v[144:147],a[172:173],v[86:87],v[144:147]
v_and_b32 v24,3,v17
s_waitcnt vmcnt(4)
s_barrier
buffer_store_dword v23,v25,%[srdW],0 offen sc1
v_mfma_f32_4x4x4_16b_bf16 v[160:163],v[6:7],v[80:81],0
v_mfma_f32_4x4x4_16b_bf16 v[176:179],v[6:7],v[82:83],0
v_mfma_f32_4x4x4_16b_bf16 v[192:195],v[6:7],v[84:85],0
v_cvt_pk_bf16_f32 v96,v96,v97
v_mfma_f32_4x4x4_16b_bf16 v[208:211],v[6:7],v[86:87],0
v_cvt_pk_bf16_f32 v97,v98,v99
v_mfma_f32_4x4x4_16b_bf16 v[160:163],a[156:157],v[72:73],v[160:163]
v_cvt_pk_bf16_f32 v112,v112,v113
v_mfma_f32_4x4x4_16b_bf16 v[176:179],a[156:157],v[74:75],v[176:179]
v_cvt_pk_bf16_f32 v113,v114,v115
v_mfma_f32_4x4x4_16b_bf16 v[192:195],a[156:157],v[76:77],v[192:195]
v_cndmask_b32 v224,v96,v112,vcc
v_mfma_f32_4x4x4_16b_bf16 v[208:211],a[156:157],v[78:79],v[208:211]
v_cndmask_b32 v225,v97,v113,vcc
v_mfma_f32_4x4x4_16b_bf16 v[160:163],a[158:159],v[74:75],v[160:163]
v_cndmask_b32 v226,v112,v96,vcc
v_mfma_f32_4x4x4_16b_bf16 v[176:179],a[158:159],v[76:77],v[176:179]
v_cndmask_b32 v227,v113,v97,vcc
v_mfma_f32_4x4x4_16b_bf16 v[192:195],a[158:159],v[78:79],v[192:195]
buffer_store_dwordx4 v[224:227],v8,%[srdW],0 offen offset:1024 sc1
v_mfma_f32_4x4x4_16b_bf16 v[208:211],a[158:159],v[80:81],v[208:211]
ds_write_b128 v5,v[224:227] offset:9216
v_mfma_f32_4x4x4_16b_bf16 v[160:163],a[160:161],v[76:77],v[160:163]
v_cvt_pk_bf16_f32 v128,v128,v129
v_mfma_f32_4x4x4_16b_bf16 v[176:179],a[160:161],v[78:79],v[176:179]
v_cvt_pk_bf16_f32 v129,v130,v131
v_mfma_f32_4x4x4_16b_bf16 v[192:195],a[160:161],v[80:81],v[192:195]
v_cvt_pk_bf16_f32 v144,v144,v145
v_mfma_f32_4x4x4_16b_bf16 v[208:211],a[160:161],v[82:83],v[208:211]
v_cvt_pk_bf16_f32 v145,v146,v147
v_mfma_f32_4x4x4_16b_bf16 v[160:163],a[162:163],v[78:79],v[160:163]
v_cndmask_b32 v240,v128,v144,vcc
v_mfma_f32_4x4x4_16b_bf16 v[176:179],a[162:163],v[80:81],v[176:179]
v_cndmask_b32 v241,v129,v145,vcc
v_mfma_f32_4x4x4_16b_bf16 v[192:195],a[162:163],v[82:83],v[192:195]
v_cndmask_b32 v242,v144,v128,vcc
v_mfma_f32_4x4x4_16b_bf16 v[208:211],a[162:163],v[84:85],v[208:211]
v_cndmask_b32 v243,v145,v129,vcc
v_mfma_f32_4x4x4_16b_bf16 v[160:163],a[164:165],v[80:81],v[160:163]
buffer_store_dwordx4 v[240:243],v8,%[srdW],0 offen offset:3072 sc1
v_mfma_f32_4x4x4_16b_bf16 v[176:179],a[164:165],v[82:83],v[176:179]
ds_write_b128 v5,v[240:243] offset:11264
v_mfma_f32_4x4x4_16b_bf16 v[192:195],a[164:165],v[84:85],v[192:195]
v_add_u32 v19,v19,v24
v_mfma_f32_4x4x4_16b_bf16 v[208:211],a[164:165],v[86:87],v[208:211]
v_lshl_add_u32 v21,v19,8,v18
v_mfma_f32_4x4x4_16b_bf16 v[160:163],a[166:167],v[82:83],v[160:163]
v_add_u32 v21,0x20000,v21
v_mfma_f32_4x4x4_16b_bf16 v[176:179],a[166:167],v[84:85],v[176:179]
v_lshlrev_b32 v22,10,v19
v_mfma_f32_4x4x4_16b_bf16 v[192:195],a[166:167],v[86:87],v[192:195]
v_lshl_add_u32 v22,v16,4,v22
v_mfma_f32_4x4x4_16b_bf16 v[208:211],a[166:167],v[88:89],v[208:211]
v_add_u32 v22,s81,v22
v_mfma_f32_4x4x4_16b_bf16 v[160:163],a[168:169],v[84:85],v[160:163]
v_add_u32 v28,64,v14
v_mfma_f32_4x4x4_16b_bf16 v[176:179],a[168:169],v[86:87],v[176:179]
v_mfma_f32_4x4x4_16b_bf16 v[192:195],a[168:169],v[88:89],v[192:195]
v_mfma_f32_4x4x4_16b_bf16 v[208:211],a[168:169],v[90:91],v[208:211]
v_mfma_f32_4x4x4_16b_bf16 v[160:163],a[170:171],v[86:87],v[160:163]
v_mfma_f32_4x4x4_16b_bf16 v[176:179],a[170:171],v[88:89],v[176:179]
v_mfma_f32_4x4x4_16b_bf16 v[192:195],a[170:171],v[90:91],v[192:195]
v_mfma_f32_4x4x4_16b_bf16 v[208:211],a[170:171],v[92:93],v[208:211]
v_mfma_f32_4x4x4_16b_bf16 v[160:163],a[172:173],v[88:89],v[160:163]
v_mfma_f32_4x4x4_16b_bf16 v[176:179],a[172:173],v[90:91],v[176:179]
v_mfma_f32_4x4x4_16b_bf16 v[192:195],a[172:173],v[92:93],v[192:195]
v_mfma_f32_4x4x4_16b_bf16 v[208:211],a[172:173],v[94:95],v[208:211]
s_nop 1
v_cvt_pk_bf16_f32 v160,v160,v161
v_cvt_pk_bf16_f32 v161,v162,v163
v_cvt_pk_bf16_f32 v176,v176,v177
v_cvt_pk_bf16_f32 v177,v178,v179
v_cndmask_b32 v224,v160,v176,vcc
v_cndmask_b32 v225,v161,v177,vcc
v_cndmask_b32 v226,v176,v160,vcc
v_cndmask_b32 v227,v177,v161,vcc
buffer_store_dwordx4 v[224:227],v20,%[srdW],0 offen offset:1024 sc1
ds_write_b128 v5,v[224:227] offset:13312
v_cvt_pk_bf16_f32 v192,v192,v193
v_cvt_pk_bf16_f32 v193,v194,v195
v_cvt_pk_bf16_f32 v208,v208,v209
v_cvt_pk_bf16_f32 v209,v210,v211
v_cndmask_b32 v240,v192,v208,vcc
v_cndmask_b32 v241,v193,v209,vcc
v_cndmask_b32 v242,v208,v192,vcc
v_cndmask_b32 v243,v209,v193,vcc
buffer_store_dwordx4 v[240:243],v20,%[srdW],0 offen offset:3072 sc1
ds_write_b128 v5,v[240:243] offset:15360
s_waitcnt lgkmcnt(0)
s_barrier
ds_read_b64_tr_b16 a[0:1],v10 offset:0
ds_read_b64_tr_b16 a[2:3],v10 offset:1024
ds_read_b64_tr_b16 a[4:5],v12 offset:0
ds_read_b64_tr_b16 a[6:7],v12 offset:1024
ds_read_b64_tr_b16 a[8:9],v10 offset:2048
ds_read_b64_tr_b16 a[10:11],v10 offset:3072
ds_read_b64_tr_b16 a[12:13],v12 offset:2048
ds_read_b64_tr_b16 a[14:15],v12 offset:3072
ds_read_b64_tr_b16 a[16:17],v10 offset:4096
ds_read_b64_tr_b16 a[18:19],v10 offset:5120
ds_read_b64_tr_b16 a[20:21],v12 offset:4096
ds_read_b64_tr_b16 a[22:23],v12 offset:5120
ds_read_b64_tr_b16 a[24:25],v10 offset:6144
ds_read_b64_tr_b16 a[26:27],v10 offset:7168
s_waitcnt lgkmcnt(12)
v_mfma_f32_16x16x32_bf16 v[96:99],a[176:179],a[0:3],0
ds_read_b64_tr_b16 a[28:29],v12 offset:6144
v_mfma_f32_16x16x32_bf16 v[100:103],a[180:183],a[0:3],0
s_waitcnt lgkmcnt(11)
ds_read_b64_tr_b16 a[30:31],v12 offset:7168
v_mfma_f32_16x16x32_bf16 v[104:107],a[176:179],a[4:7],0
ds_read_b64_tr_b16 a[0:1],v10 offset:8192
v_mfma_f32_16x16x32_bf16 v[108:111],a[180:183],a[4:7],0
s_waitcnt lgkmcnt(11)
ds_read_b64_tr_b16 a[2:3],v10 offset:9216
v_mfma_f32_16x16x32_bf16 v[112:115],a[176:179],a[8:11],0
ds_read_b64_tr_b16 a[4:5],v12 offset:8192
v_mfma_f32_16x16x32_bf16 v[116:119],a[180:183],a[8:11],0
s_waitcnt lgkmcnt(11)
ds_read_b64_tr_b16 a[6:7],v12 offset:9216
v_mfma_f32_16x16x32_bf16 v[120:123],a[176:179],a[12:15],0
ds_read_b64_tr_b16 a[8:9],v10 offset:10240
v_mfma_f32_16x16x32_bf16 v[124:127],a[180:183],a[12:15],0
s_waitcnt lgkmcnt(11)
ds_read_b64_tr_b16 a[10:11],v10 offset:11264
v_mfma_f32_16x16x32_bf16 v[128:131],a[176:179],a[16:19],0
ds_read_b64_tr_b16 a[12:13],v12 offset:10240
v_mfma_f32_16x16x32_bf16 v[132:135],a[180:183],a[16:19],0
s_waitcnt lgkmcnt(11)
ds_read_b64_tr_b16 a[14:15],v12 offset:11264
v_mfma_f32_16x16x32_bf16 v[136:139],a[176:179],a[20:23],0
ds_read_b64_tr_b16 a[16:17],v10 offset:12288
v_mfma_f32_16x16x32_bf16 v[140:143],a[180:183],a[20:23],0
s_waitcnt lgkmcnt(11)
ds_read_b64_tr_b16 a[18:19],v10 offset:13312
v_mfma_f32_16x16x32_bf16 v[144:147],a[176:179],a[24:27],0
ds_read_b64_tr_b16 a[20:21],v12 offset:12288
v_mfma_f32_16x16x32_bf16 v[148:151],a[180:183],a[24:27],0
s_waitcnt lgkmcnt(11)
ds_read_b64_tr_b16 a[22:23],v12 offset:13312
v_mfma_f32_16x16x32_bf16 v[152:155],a[176:179],a[28:31],0
ds_read_b64_tr_b16 a[24:25],v10 offset:14336
v_mfma_f32_16x16x32_bf16 v[156:159],a[180:183],a[28:31],0
s_waitcnt lgkmcnt(11)
ds_read_b64_tr_b16 a[26:27],v10 offset:15360
v_mfma_f32_16x16x32_bf16 v[96:99],a[184:187],a[0:3],v[96:99]
ds_read_b64_tr_b16 a[28:29],v12 offset:14336
ds_read_b64_tr_b16 a[30:31],v12 offset:15360
v_mfma_f32_16x16x32_bf16 v[100:103],a[188:191],a[0:3],v[100:103]
s_waitcnt lgkmcnt(12)
buffer_load_dword v29,v14,%[srdW],0 offen sc1
v_mfma_f32_16x16x32_bf16 v[104:107],a[184:187],a[4:7],v[104:107]
ds_read_b64_tr_b16 a[0:1],v10 offset:16384
v_mfma_f32_16x16x32_bf16 v[108:111],a[188:191],a[4:7],v[108:111]
s_waitcnt lgkmcnt(11)
ds_read_b64_tr_b16 a[2:3],v10 offset:17408
v_mfma_f32_16x16x32_bf16 v[112:115],a[184:187],a[8:11],v[112:115]
ds_read_b64_tr_b16 a[4:5],v12 offset:16384
v_mfma_f32_16x16x32_bf16 v[116:119],a[188:191],a[8:11],v[116:119]
s_waitcnt lgkmcnt(11)
ds_read_b64_tr_b16 a[6:7],v12 offset:17408
v_mfma_f32_16x16x32_bf16 v[120:123],a[184:187],a[12:15],v[120:123]
ds_read_b64_tr_b16 a[8:9],v10 offset:18432
v_mfma_f32_16x16x32_bf16 v[124:127],a[188:191],a[12:15],v[124:127]
s_waitcnt lgkmcnt(11)
ds_read_b64_tr_b16 a[10:11],v10 offset:19456
v_mfma_f32_16x16x32_bf16 v[128:131],a[184:187],a[16:19],v[128:131]
ds_read_b64_tr_b16 a[12:13],v12 offset:18432
v_mfma_f32_16x16x32_bf16 v[132:135],a[188:191],a[16:19],v[132:135]
s_waitcnt lgkmcnt(11)
ds_read_b64_tr_b16 a[14:15],v12 offset:19456
v_mfma_f32_16x16x32_bf16 v[136:139],a[184:187],a[20:23],v[136:139]
ds_read_b64_tr_b16 a[16:17],v10 offset:20480
v_mfma_f32_16x16x32_bf16 v[140:143],a[188:191],a[20:23],v[140:143]
s_waitcnt lgkmcnt(11)
ds_read_b64_tr_b16 a[18:19],v10 offset:21504
v_mfma_f32_16x16x32_bf16 v[144:147],a[184:187],a[24:27],v[144:147]
ds_read_b64_tr_b16 a[20:21],v12 offset:20480
v_mfma_f32_16x16x32_bf16 v[148:151],a[188:191],a[24:27],v[148:151]
s_waitcnt lgkmcnt(11)
ds_read_b64_tr_b16 a[22:23],v12 offset:21504
v_mfma_f32_16x16x32_bf16 v[152:155],a[184:187],a[28:31],v[152:155]
ds_read_b64_tr_b16 a[24:25],v10 offset:22528
v_mfma_f32_16x16x32_bf16 v[156:159],a[188:191],a[28:31],v[156:159]
s_waitcnt lgkmcnt(11)
ds_read_b64_tr_b16 a[26:27],v10 offset:23552
v_mfma_f32_16x16x32_bf16 v[96:99],a[192:195],a[0:3],v[96:99]
ds_read_b64_tr_b16 a[28:29],v12 offset:22528
v_mfma_f32_16x16x32_bf16 v[100:103],a[196:199],a[0:3],v[100:103]
s_waitcnt lgkmcnt(11)
ds_read_b64_tr_b16 a[30:31],v12 offset:23552
v_mfma_f32_16x16x32_bf16 v[104:107],a[192:195],a[4:7],v[104:107]
ds_read_b64_tr_b16 a[0:1],v10 offset:24576
v_mfma_f32_16x16x32_bf16 v[108:111],a[196:199],a[4:7],v[108:111]
s_waitcnt lgkmcnt(11)
ds_read_b64_tr_b16 a[2:3],v10 offset:25600
v_mfma_f32_16x16x32_bf16 v[112:115],a[192:195],a[8:11],v[112:115]
ds_read_b64_tr_b16 a[4:5],v12 offset:24576
v_mfma_f32_16x16x32_bf16 v[116:119],a[196:199],a[8:11],v[116:119]
s_waitcnt lgkmcnt(11)
ds_read_b64_tr_b16 a[6:7],v12 offset:25600
v_mfma_f32_16x16x32_bf16 v[120:123],a[192:195],a[12:15],v[120:123]
ds_read_b64_tr_b16 a[8:9],v10 offset:26624
v_mfma_f32_16x16x32_bf16 v[124:127],a[196:199],a[12:15],v[124:127]
s_waitcnt lgkmcnt(11)
ds_read_b64_tr_b16 a[10:11],v10 offset:27648
v_mfma_f32_16x16x32_bf16 v[128:131],a[192:195],a[16:19],v[128:131]
ds_read_b64_tr_b16 a[12:13],v12 offset:26624
v_mfma_f32_16x16x32_bf16 v[132:135],a[196:199],a[16:19],v[132:135]
s_waitcnt lgkmcnt(11)
ds_read_b64_tr_b16 a[14:15],v12 offset:27648
v_mfma_f32_16x16x32_bf16 v[136:139],a[192:195],a[20:23],v[136:139]
ds_read_b64_tr_b16 a[16:17],v10 offset:28672
v_mfma_f32_16x16x32_bf16 v[140:143],a[196:199],a[20:23],v[140:143]
s_waitcnt lgkmcnt(11)
ds_read_b64_tr_b16 a[18:19],v10 offset:29696
v_mfma_f32_16x16x32_bf16 v[144:147],a[192:195],a[24:27],v[144:147]
ds_read_b64_tr_b16 a[20:21],v12 offset:28672
v_mfma_f32_16x16x32_bf16 v[148:151],a[196:199],a[24:27],v[148:151]
s_waitcnt lgkmcnt(11)
ds_read_b64_tr_b16 a[22:23],v12 offset:29696
v_mfma_f32_16x16x32_bf16 v[152:155],a[192:195],a[28:31],v[152:155]
ds_read_b64_tr_b16 a[24:25],v10 offset:30720
v_mfma_f32_16x16x32_bf16 v[156:159],a[196:199],a[28:31],v[156:159]
s_waitcnt lgkmcnt(11)
ds_read_b64_tr_b16 a[26:27],v10 offset:31744
s_waitcnt vmcnt(1)
s_barrier
buffer_store_dword v23,v25,%[srdW],0 offen offset:64 sc1
s_waitcnt vmcnt(1)
v_cmp_ne_u32 vcc,s70,v29
s_nop 0
s_and_b32 vcc_lo,vcc_lo,s77
s_cbranch_scc0 SPB4_%=
s_mov_b32 s97,0x40000
POLL4_%=:
buffer_load_dword v29,v14,%[srdW],0 offen sc1
s_waitcnt vmcnt(0)
v_cmp_ne_u32 vcc,s70,v29
s_cbranch_vccz GO4_%=
s_sub_u32 s97,s97,1
s_cbranch_scc1 GO4_%=
s_sleep 4
s_branch POLL4_%=
GO4_%=:
SPB4_%=:
s_barrier
v_mfma_f32_16x16x32_bf16 v[96:99],a[200:203],a[0:3],v[96:99]
s_add_u32 m0,s68,65536
ds_read_b64_tr_b16 a[28:29],v12 offset:30720
s_add_u32 s61,s62,0
v_mfma_f32_16x16x32_bf16 v[100:103],a[204:207],a[0:3],v[100:103]
s_waitcnt lgkmcnt(11)
ds_read_b64_tr_b16 a[30:31],v12 offset:31744
v_mfma_f32_16x16x32_bf16 v[104:107],a[200:203],a[4:7],v[104:107]
ds_read_b64_tr_b16 a[0:1],v10 offset:32768
ds_read_b64_tr_b16 a[2:3],v10 offset:33792
v_mfma_f32_16x16x32_bf16 v[108:111],a[204:207],a[4:7],v[108:111]
s_waitcnt lgkmcnt(12)
buffer_load_dwordx4 %[vL16],%[srdW],s61 offen sc1 lds
v_mfma_f32_16x16x32_bf16 v[112:115],a[200:203],a[8:11],v[112:115]
s_add_u32 m0,s68,66560
ds_read_b64_tr_b16 a[4:5],v12 offset:32768
s_add_u32 s61,s62,1024
v_mfma_f32_16x16x32_bf16 v[116:119],a[204:207],a[8:11],v[116:119]
s_waitcnt lgkmcnt(11)
ds_read_b64_tr_b16 a[6:7],v12 offset:33792
v_mfma_f32_16x16x32_bf16 v[120:123],a[200:203],a[12:15],v[120:123]
ds_read_b64_tr_b16 a[8:9],v10 offset:34816
ds_read_b64_tr_b16 a[10:11],v10 offset:35840
v_mfma_f32_16x16x32_bf16 v[124:127],a[204:207],a[12:15],v[124:127]
s_waitcnt lgkmcnt(12)
buffer_load_dwordx4 %[vL16],%[srdW],s61 offen sc1 lds
v_mfma_f32_16x16x32_bf16 v[128:131],a[200:203],a[16:19],v[128:131]
s_add_u32 m0,s68,73728
ds_read_b64_tr_b16 a[12:13],v12 offset:34816
s_add_u32 s61,s62,16384
v_mfma_f32_16x16x32_bf16 v[132:135],a[204:207],a[16:19],v[132:135]
s_waitcnt lgkmcnt(11)
ds_read_b64_tr_b16 a[14:15],v12 offset:35840
v_mfma_f32_16x16x32_bf16 v[136:139],a[200:203],a[20:23],v[136:139]
ds_read_b64_tr_b16 a[16:17],v10 offset:36864
ds_read_b64_tr_b16 a[18:19],v10 offset:37888
v_mfma_f32_16x16x32_bf16 v[140:143],a[204:207],a[20:23],v[140:143]
s_waitcnt lgkmcnt(12)
buffer_load_dwordx4 %[vL16],%[srdW],s61 offen sc1 lds
v_mfma_f32_16x16x32_bf16 v[144:147],a[200:203],a[24:27],v[144:147]
s_add_u32 m0,s68,74752
ds_read_b64_tr_b16 a[20:21],v12 offset:36864
s_add_u32 s61,s62,17408
v_mfma_f32_16x16x32_bf16 v[148:151],a[204:207],a[24:27],v[148:151]
s_waitcnt lgkmcnt(11)
ds_read_b64_tr_b16 a[22:23],v12 offset:37888
v_mfma_f32_16x16x32_bf16 v[152:155],a[200:203],a[28:31],v[152:155]
ds_read_b64_tr_b16 a[24:25],v10 offset:38912
ds_read_b64_tr_b16 a[26:27],v10 offset:39936
v_mfma_f32_16x16x32_bf16 v[156:159],a[204:207],a[28:31],v[156:159]
s_waitcnt lgkmcnt(12)
buffer_load_dwordx4 %[vL16],%[srdW],s61 offen sc1 lds
v_mfma_f32_16x16x32_bf16 v[96:99],a[208:211],a[0:3],v[96:99]
s_add_u32 m0,s68,81920
ds_read_b64_tr_b16 a[28:29],v12 offset:38912
s_add_u32 s61,s62,32768
v_mfma_f32_16x16x32_bf16 v[100:103],a[212:215],a[0:3],v[100:103]
s_waitcnt lgkmcnt(11)
ds_read_b64_tr_b16 a[30:31],v12 offset:39936
v_mfma_f32_16x16x32_bf16 v[104:107],a[208:211],a[4:7],v[104:107]
ds_read_b64_tr_b16 a[0:1],v10 offset:40960
ds_read_b64_tr_b16 a[2:3],v10 offset:41984
v_mfma_f32_16x16x32_bf16 v[108:111],a[212:215],a[4:7],v[108:111]
s_waitcnt lgkmcnt(12)
buffer_load_dwordx4 %[vL16],%[srdW],s61 offen sc1 lds
v_mfma_f32_16x16x32_bf16 v[112:115],a[208:211],a[8:11],v[112:115]
s_add_u32 m0,s68,82944
ds_read_b64_tr_b16 a[4:5],v12 offset:40960
s_add_u32 s61,s62,33792
v_mfma_f32_16x16x32_bf16 v[116:119],a[212:215],a[8:11],v[116:119]
s_waitcnt lgkmcnt(11)
ds_read_b64_tr_b16 a[6:7],v12 offset:41984
v_mfma_f32_16x16x32_bf16 v[120:123],a[208:211],a[12:15],v[120:123]
ds_read_b64_tr_b16 a[8:9],v10 offset:43008
ds_read_b64_tr_b16 a[10:11],v10 offset:44032
v_mfma_f32_16x16x32_bf16 v[124:127],a[212:215],a[12:15],v[124:127]
s_waitcnt lgkmcnt(12)
buffer_load_dwordx4 %[vL16],%[srdW],s61 offen sc1 lds
v_mfma_f32_16x16x32_bf16 v[128:131],a[208:211],a[16:19],v[128:131]
s_add_u32 m0,s68,90112
ds_read_b64_tr_b16 a[12:13],v12 offset:43008
s_add_u32 s61,s62,49152
v_mfma_f32_16x16x32_bf16 v[132:135],a[212:215],a[16:19],v[132:135]
s_waitcnt lgkmcnt(11)
ds_read_b64_tr_b16 a[14:15],v12 offset:44032
v_mfma_f32_16x16x32_bf16 v[136:139],a[208:211],a[20:23],v[136:139]
ds_read_b64_tr_b16 a[16:17],v10 offset:45056
ds_read_b64_tr_b16 a[18:19],v10 offset:46080
v_mfma_f32_16x16x32_bf16 v[140:143],a[212:215],a[20:23],v[140:143]
s_waitcnt lgkmcnt(12)
buffer_load_dwordx4 %[vL16],%[srdW],s61 offen sc1 lds
v_mfma_f32_16x16x32_bf16 v[144:147],a[208:211],a[24:27],v[144:147]
s_add_u32 m0,s68,91136
ds_read_b64_tr_b16 a[20:21],v12 offset:45056
s_add_u32 s61,s62,50176
v_mfma_f32_16x16x32_bf16 v[148:151],a[212:215],a[24:27],v[148:151]
s_waitcnt lgkmcnt(11)
ds_read_b64_tr_b16 a[22:23],v12 offset:46080
v_mfma_f32_16x16x32_bf16 v[152:155],a[208:211],a[28:31],v[152:155]
ds_read_b64_tr_b16 a[24:25],v10 offset:47104
ds_read_b64_tr_b16 a[26:27],v10 offset:48128
v_mfma_f32_16x16x32_bf16 v[156:159],a[212:215],a[28:31],v[156:159]
s_waitcnt lgkmcnt(12)
buffer_load_dwordx4 %[vL16],%[srdW],s61 offen sc1 lds
v_mfma_f32_16x16x32_bf16 v[96:99],a[32:35],a[0:3],v[96:99]
)"
R"(s_add_u32 m0,s68,98304
ds_read_b64_tr_b16 a[28:29],v12 offset:47104
s_add_u32 s61,s63,0
v_mfma_f32_16x16x32_bf16 v[100:103],a[36:39],a[0:3],v[100:103]
s_waitcnt lgkmcnt(11)
ds_read_b64_tr_b16 a[30:31],v12 offset:48128
v_mfma_f32_16x16x32_bf16 v[104:107],a[32:35],a[4:7],v[104:107]
ds_read_b64_tr_b16 a[0:1],v10 offset:49152
ds_read_b64_tr_b16 a[2:3],v10 offset:50176
v_mfma_f32_16x16x32_bf16 v[108:111],a[36:39],a[4:7],v[108:111]
s_waitcnt lgkmcnt(12)
buffer_load_dwordx4 %[vL16],%[srdW],s61 offen sc1 lds
v_mfma_f32_16x16x32_bf16 v[112:115],a[32:35],a[8:11],v[112:115]
s_add_u32 m0,s68,99328
ds_read_b64_tr_b16 a[4:5],v12 offset:49152
s_add_u32 s61,s63,1024
v_mfma_f32_16x16x32_bf16 v[116:119],a[36:39],a[8:11],v[116:119]
s_waitcnt lgkmcnt(11)
ds_read_b64_tr_b16 a[6:7],v12 offset:50176
v_mfma_f32_16x16x32_bf16 v[120:123],a[32:35],a[12:15],v[120:123]
ds_read_b64_tr_b16 a[8:9],v10 offset:51200
ds_read_b64_tr_b16 a[10:11],v10 offset:52224
v_mfma_f32_16x16x32_bf16 v[124:127],a[36:39],a[12:15],v[124:127]
s_waitcnt lgkmcnt(12)
buffer_load_dwordx4 %[vL16],%[srdW],s61 offen sc1 lds
v_mfma_f32_16x16x32_bf16 v[128:131],a[32:35],a[16:19],v[128:131]
s_add_u32 m0,s68,106496
ds_read_b64_tr_b16 a[12:13],v12 offset:51200
s_add_u32 s61,s63,16384
v_mfma_f32_16x16x32_bf16 v[132:135],a[36:39],a[16:19],v[132:135]
s_waitcnt lgkmcnt(11)
ds_read_b64_tr_b16 a[14:15],v12 offset:52224
v_mfma_f32_16x16x32_bf16 v[136:139],a[32:35],a[20:23],v[136:139]
ds_read_b64_tr_b16 a[16:17],v10 offset:53248
ds_read_b64_tr_b16 a[18:19],v10 offset:54272
v_mfma_f32_16x16x32_bf16 v[140:143],a[36:39],a[20:23],v[140:143]
s_waitcnt lgkmcnt(12)
buffer_load_dwordx4 %[vL16],%[srdW],s61 offen sc1 lds
v_mfma_f32_16x16x32_bf16 v[144:147],a[32:35],a[24:27],v[144:147]
s_add_u32 m0,s68,107520
ds_read_b64_tr_b16 a[20:21],v12 offset:53248
s_add_u32 s61,s63,17408
v_mfma_f32_16x16x32_bf16 v[148:151],a[36:39],a[24:27],v[148:151]
s_waitcnt lgkmcnt(11)
ds_read_b64_tr_b16 a[22:23],v12 offset:54272
v_mfma_f32_16x16x32_bf16 v[152:155],a[32:35],a[28:31],v[152:155]
ds_read_b64_tr_b16 a[24:25],v10 offset:55296
ds_read_b64_tr_b16 a[26:27],v10 offset:56320
v_mfma_f32_16x16x32_bf16 v[156:159],a[36:39],a[28:31],v[156:159]
s_waitcnt lgkmcnt(12)
buffer_load_dwordx4 %[vL16],%[srdW],s61 offen sc1 lds
v_mfma_f32_16x16x32_bf16 v[96:99],a[40:43],a[0:3],v[96:99]
s_add_u32 m0,s68,114688
ds_read_b64_tr_b16 a[28:29],v12 offset:55296
s_add_u32 s61,s63,32768
v_mfma_f32_16x16x32_bf16 v[100:103],a[44:47],a[0:3],v[100:103]
s_waitcnt lgkmcnt(11)
ds_read_b64_tr_b16 a[30:31],v12 offset:56320
v_mfma_f32_16x16x32_bf16 v[104:107],a[40:43],a[4:7],v[104:107]
ds_read_b64_tr_b16 a[0:1],v10 offset:57344
ds_read_b64_tr_b16 a[2:3],v10 offset:58368
v_mfma_f32_16x16x32_bf16 v[108:111],a[44:47],a[4:7],v[108:111]
s_waitcnt lgkmcnt(12)
buffer_load_dwordx4 %[vL16],%[srdW],s61 offen sc1 lds
v_mfma_f32_16x16x32_bf16 v[112:115],a[40:43],a[8:11],v[112:115]
s_add_u32 m0,s68,115712
ds_read_b64_tr_b16 a[4:5],v12 offset:57344
s_add_u32 s61,s63,33792
v_mfma_f32_16x16x32_bf16 v[116:119],a[44:47],a[8:11],v[116:119]
s_waitcnt lgkmcnt(11)
ds_read_b64_tr_b16 a[6:7],v12 offset:58368
v_mfma_f32_16x16x32_bf16 v[120:123],a[40:43],a[12:15],v[120:123]
ds_read_b64_tr_b16 a[8:9],v10 offset:59392
ds_read_b64_tr_b16 a[10:11],v10 offset:60416
v_mfma_f32_16x16x32_bf16 v[124:127],a[44:47],a[12:15],v[124:127]
s_waitcnt lgkmcnt(12)
buffer_load_dwordx4 %[vL16],%[srdW],s61 offen sc1 lds
v_mfma_f32_16x16x32_bf16 v[128:131],a[40:43],a[16:19],v[128:131]
s_add_u32 m0,s68,122880
ds_read_b64_tr_b16 a[12:13],v12 offset:59392
s_add_u32 s61,s63,49152
v_mfma_f32_16x16x32_bf16 v[132:135],a[44:47],a[16:19],v[132:135]
s_waitcnt lgkmcnt(11)
ds_read_b64_tr_b16 a[14:15],v12 offset:60416
v_mfma_f32_16x16x32_bf16 v[136:139],a[40:43],a[20:23],v[136:139]
ds_read_b64_tr_b16 a[16:17],v10 offset:61440
ds_read_b64_tr_b16 a[18:19],v10 offset:62464
v_mfma_f32_16x16x32_bf16 v[140:143],a[44:47],a[20:23],v[140:143]
s_waitcnt lgkmcnt(12)
buffer_load_dwordx4 %[vL16],%[srdW],s61 offen sc1 lds
v_mfma_f32_16x16x32_bf16 v[144:147],a[40:43],a[24:27],v[144:147]
s_add_u32 m0,s68,123904
ds_read_b64_tr_b16 a[20:21],v12 offset:61440
s_add_u32 s61,s63,50176
v_mfma_f32_16x16x32_bf16 v[148:151],a[44:47],a[24:27],v[148:151]
s_waitcnt lgkmcnt(11)
ds_read_b64_tr_b16 a[22:23],v12 offset:62464
v_mfma_f32_16x16x32_bf16 v[152:155],a[40:43],a[28:31],v[152:155]
ds_read_b64_tr_b16 a[24:25],v10 offset:63488
ds_read_b64_tr_b16 a[26:27],v10 offset:64512
v_mfma_f32_16x16x32_bf16 v[156:159],a[44:47],a[28:31],v[156:159]
s_waitcnt lgkmcnt(12)
buffer_load_dwordx4 %[vL16],%[srdW],s61 offen sc1 lds
v_mfma_f32_16x16x32_bf16 v[96:99],a[48:51],a[0:3],v[96:99]
s_add_u32 s82,s75,4096
ds_read_b64_tr_b16 a[28:29],v12 offset:63488
v_mfma_f32_16x16x32_bf16 v[100:103],a[52:55],a[0:3],v[100:103]
s_waitcnt lgkmcnt(11)
ds_read_b64_tr_b16 a[30:31],v12 offset:64512
v_mfma_f32_16x16x32_bf16 v[104:107],a[48:51],a[4:7],v[104:107]
s_waitcnt vmcnt(8)
s_barrier
ds_read_b64_tr_b16 a[0:1],v11 offset:0
ds_read_b64_tr_b16 a[2:3],v11 offset:1024
v_mfma_f32_16x16x32_bf16 v[108:111],a[52:55],a[4:7],v[108:111]
s_waitcnt lgkmcnt(12)
buffer_load_dwordx4 a[216:219],%[vL16],%[srdW],s82 offen offset:0 sc1
v_mfma_f32_16x16x32_bf16 v[112:115],a[48:51],a[8:11],v[112:115]
ds_read_b64_tr_b16 a[4:5],v13 offset:0
v_mfma_f32_16x16x32_bf16 v[116:119],a[52:55],a[8:11],v[116:119]
s_waitcnt lgkmcnt(11)
ds_read_b64_tr_b16 a[6:7],v13 offset:1024
v_mfma_f32_16x16x32_bf16 v[120:123],a[48:51],a[12:15],v[120:123]
ds_read_b64_tr_b16 a[8:9],v11 offset:2048
ds_read_b64_tr_b16 a[10:11],v11 offset:3072
v_mfma_f32_16x16x32_bf16 v[124:127],a[52:55],a[12:15],v[124:127]
s_waitcnt lgkmcnt(12)
buffer_load_dwordx4 a[220:223],%[vL16],%[srdW],s82 offen offset:1024 sc1
v_mfma_f32_16x16x32_bf16 v[128:131],a[48:51],a[16:19],v[128:131]
s_add_u32 s82,s75,8192
ds_read_b64_tr_b16 a[12:13],v13 offset:2048
v_mfma_f32_16x16x32_bf16 v[132:135],a[52:55],a[16:19],v[132:135]
s_waitcnt lgkmcnt(11)
ds_read_b64_tr_b16 a[14:15],v13 offset:3072
v_mfma_f32_16x16x32_bf16 v[136:139],a[48:51],a[20:23],v[136:139]
ds_read_b64_tr_b16 a[16:17],v11 offset:4096
ds_read_b64_tr_b16 a[18:19],v11 offset:5120
v_mfma_f32_16x16x32_bf16 v[140:143],a[52:55],a[20:23],v[140:143]
s_waitcnt lgkmcnt(12)
buffer_load_dwordx4 a[224:227],%[vL16],%[srdW],s82 offen offset:0 sc1
v_mfma_f32_16x16x32_bf16 v[144:147],a[48:51],a[24:27],v[144:147]
ds_read_b64_tr_b16 a[20:21],v13 offset:4096
v_mfma_f32_16x16x32_bf16 v[148:151],a[52:55],a[24:27],v[148:151]
s_waitcnt lgkmcnt(11)
ds_read_b64_tr_b16 a[22:23],v13 offset:5120
v_mfma_f32_16x16x32_bf16 v[152:155],a[48:51],a[28:31],v[152:155]
ds_read_b64_tr_b16 a[24:25],v11 offset:6144
ds_read_b64_tr_b16 a[26:27],v11 offset:7168
v_mfma_f32_16x16x32_bf16 v[156:159],a[52:55],a[28:31],v[156:159]
s_waitcnt lgkmcnt(12)
buffer_load_dwordx4 a[228:231],%[vL16],%[srdW],s82 offen offset:1024 sc1
v_mfma_f32_16x16x32_bf16 v[96:99],a[56:59],a[0:3],v[96:99]
s_add_u32 s82,s75,12288
ds_read_b64_tr_b16 a[28:29],v13 offset:6144
v_mfma_f32_16x16x32_bf16 v[100:103],a[60:63],a[0:3],v[100:103]
s_waitcnt lgkmcnt(11)
ds_read_b64_tr_b16 a[30:31],v13 offset:7168
v_mfma_f32_16x16x32_bf16 v[104:107],a[56:59],a[4:7],v[104:107]
ds_read_b64_tr_b16 a[0:1],v11 offset:8192
ds_read_b64_tr_b16 a[2:3],v11 offset:9216
v_mfma_f32_16x16x32_bf16 v[108:111],a[60:63],a[4:7],v[108:111]
s_waitcnt lgkmcnt(12)
buffer_load_dwordx4 a[232:235],%[vL16],%[srdW],s82 offen offset:0 sc1
v_mfma_f32_16x16x32_bf16 v[112:115],a[56:59],a[8:11],v[112:115]
ds_read_b64_tr_b16 a[4:5],v13 offset:8192
v_mfma_f32_16x16x32_bf16 v[116:119],a[60:63],a[8:11],v[116:119]
s_waitcnt lgkmcnt(11)
ds_read_b64_tr_b16 a[6:7],v13 offset:9216
v_mfma_f32_16x16x32_bf16 v[120:123],a[56:59],a[12:15],v[120:123]
ds_read_b64_tr_b16 a[8:9],v11 offset:10240
ds_read_b64_tr_b16 a[10:11],v11 offset:11264
v_mfma_f32_16x16x32_bf16 v[124:127],a[60:63],a[12:15],v[124:127]
s_waitcnt lgkmcnt(12)
buffer_load_dwordx4 a[236:239],%[vL16],%[srdW],s82 offen offset:1024 sc1
v_mfma_f32_16x16x32_bf16 v[128:131],a[56:59],a[16:19],v[128:131]
s_add_u32 m0,s68,0
ds_read_b64_tr_b16 a[12:13],v13 offset:10240
s_add_u32 s61,s64,0
v_mfma_f32_16x16x32_bf16 v[132:135],a[60:63],a[16:19],v[132:135]
s_waitcnt lgkmcnt(11)
ds_read_b64_tr_b16 a[14:15],v13 offset:11264
v_mfma_f32_16x16x32_bf16 v[136:139],a[56:59],a[20:23],v[136:139]
ds_read_b64_tr_b16 a[16:17],v11 offset:12288
ds_read_b64_tr_b16 a[18:19],v11 offset:13312
v_mfma_f32_16x16x32_bf16 v[140:143],a[60:63],a[20:23],v[140:143]
s_waitcnt lgkmcnt(12)
buffer_load_dwordx4 %[vL16],%[srdW],s61 offen sc1 lds
v_mfma_f32_16x16x32_bf16 v[144:147],a[56:59],a[24:27],v[144:147]
s_add_u32 m0,s68,1024
ds_read_b64_tr_b16 a[20:21],v13 offset:12288
s_add_u32 s61,s64,1024
v_mfma_f32_16x16x32_bf16 v[148:151],a[60:63],a[24:27],v[148:151]
s_waitcnt lgkmcnt(11)
ds_read_b64_tr_b16 a[22:23],v13 offset:13312
v_mfma_f32_16x16x32_bf16 v[152:155],a[56:59],a[28:31],v[152:155]
ds_read_b64_tr_b16 a[24:25],v11 offset:14336
ds_read_b64_tr_b16 a[26:27],v11 offset:15360
v_mfma_f32_16x16x32_bf16 v[156:159],a[60:63],a[28:31],v[156:159]
s_waitcnt lgkmcnt(12)
buffer_load_dwordx4 %[vL16],%[srdW],s61 offen sc1 lds
v_mfma_f32_16x16x32_bf16 v[96:99],a[64:67],a[0:3],v[96:99]
s_add_u32 m0,s68,8192
ds_read_b64_tr_b16 a[28:29],v13 offset:14336
s_add_u32 s61,s64,16384
v_mfma_f32_16x16x32_bf16 v[100:103],a[68:71],a[0:3],v[100:103]
s_waitcnt lgkmcnt(11)
ds_read_b64_tr_b16 a[30:31],v13 offset:15360
v_mfma_f32_16x16x32_bf16 v[104:107],a[64:67],a[4:7],v[104:107]
ds_read_b64_tr_b16 a[0:1],v11 offset:16384
ds_read_b64_tr_b16 a[2:3],v11 offset:17408
v_mfma_f32_16x16x32_bf16 v[108:111],a[68:71],a[4:7],v[108:111]
s_waitcnt lgkmcnt(12)
buffer_load_dwordx4 %[vL16],%[srdW],s61 offen sc1 lds
v_mfma_f32_16x16x32_bf16 v[112:115],a[64:67],a[8:11],v[112:115]
s_add_u32 m0,s68,9216
ds_read_b64_tr_b16 a[4:5],v13 offset:16384
s_add_u32 s61,s64,17408
v_mfma_f32_16x16x32_bf16 v[116:119],a[68:71],a[8:11],v[116:119]
s_waitcnt lgkmcnt(11)
ds_read_b64_tr_b16 a[6:7],v13 offset:17408
v_mfma_f32_16x16x32_bf16 v[120:123],a[64:67],a[12:15],v[120:123]
ds_read_b64_tr_b16 a[8:9],v11 offset:18432
ds_read_b64_tr_b16 a[10:11],v11 offset:19456
v_mfma_f32_16x16x32_bf16 v[124:127],a[68:71],a[12:15],v[124:127]
s_waitcnt lgkmcnt(12)
buffer_load_dwordx4 %[vL16],%[srdW],s61 offen sc1 lds
v_mfma_f32_16x16x32_bf16 v[128:131],a[64:67],a[16:19],v[128:131]
s_add_u32 m0,s68,16384
ds_read_b64_tr_b16 a[12:13],v13 offset:18432
s_add_u32 s61,s64,32768
v_mfma_f32_16x16x32_bf16 v[132:135],a[68:71],a[16:19],v[132:135]
s_waitcnt lgkmcnt(11)
ds_read_b64_tr_b16 a[14:15],v13 offset:19456
v_mfma_f32_16x16x32_bf16 v[136:139],a[64:67],a[20:23],v[136:139]
ds_read_b64_tr_b16 a[16:17],v11 offset:20480
ds_read_b64_tr_b16 a[18:19],v11 offset:21504
v_mfma_f32_16x16x32_bf16 v[140:143],a[68:71],a[20:23],v[140:143]
s_waitcnt lgkmcnt(12)
buffer_load_dwordx4 %[vL16],%[srdW],s61 offen sc1 lds
v_mfma_f32_16x16x32_bf16 v[144:147],a[64:67],a[24:27],v[144:147]
s_add_u32 m0,s68,17408
ds_read_b64_tr_b16 a[20:21],v13 offset:20480
s_add_u32 s61,s64,33792
v_mfma_f32_16x16x32_bf16 v[148:151],a[68:71],a[24:27],v[148:151]
s_waitcnt lgkmcnt(11)
ds_read_b64_tr_b16 a[22:23],v13 offset:21504
v_mfma_f32_16x16x32_bf16 v[152:155],a[64:67],a[28:31],v[152:155]
ds_read_b64_tr_b16 a[24:25],v11 offset:22528
ds_read_b64_tr_b16 a[26:27],v11 offset:23552
v_mfma_f32_16x16x32_bf16 v[156:159],a[68:71],a[28:31],v[156:159]
s_waitcnt lgkmcnt(12)
buffer_load_dwordx4 %[vL16],%[srdW],s61 offen sc1 lds
v_mfma_f32_16x16x32_bf16 v[96:99],a[72:75],a[0:3],v[96:99]
ds_read_b64_tr_b16 a[28:29],v13 offset:22528
v_mfma_f32_16x16x32_bf16 v[100:103],a[76:79],a[0:3],v[100:103]
s_waitcnt lgkmcnt(11)
ds_read_b64_tr_b16 a[30:31],v13 offset:23552
v_mfma_f32_16x16x32_bf16 v[104:107],a[72:75],a[4:7],v[104:107]
ds_read_b64_tr_b16 a[0:1],v11 offset:24576
ds_read_b64_tr_b16 a[2:3],v11 offset:25600
v_mfma_f32_16x16x32_bf16 v[108:111],a[76:79],a[4:7],v[108:111]
s_waitcnt lgkmcnt(12)
buffer_load_dword v29,v28,%[srdW],0 offen sc1
v_mfma_f32_16x16x32_bf16 v[112:115],a[72:75],a[8:11],v[112:115]
s_add_u32 m0,s68,24576
ds_read_b64_tr_b16 a[4:5],v13 offset:24576
s_add_u32 s61,s64,49152
v_mfma_f32_16x16x32_bf16 v[116:119],a[76:79],a[8:11],v[116:119]
s_waitcnt lgkmcnt(11)
ds_read_b64_tr_b16 a[6:7],v13 offset:25600
v_mfma_f32_16x16x32_bf16 v[120:123],a[72:75],a[12:15],v[120:123]
ds_read_b64_tr_b16 a[8:9],v11 offset:26624
ds_read_b64_tr_b16 a[10:11],v11 offset:27648
v_mfma_f32_16x16x32_bf16 v[124:127],a[76:79],a[12:15],v[124:127]
s_waitcnt lgkmcnt(12)
buffer_load_dwordx4 %[vL16],%[srdW],s61 offen sc1 lds
v_mfma_f32_16x16x32_bf16 v[128:131],a[72:75],a[16:19],v[128:131]
s_add_u32 m0,s68,25600
ds_read_b64_tr_b16 a[12:13],v13 offset:26624
s_add_u32 s61,s64,50176
v_mfma_f32_16x16x32_bf16 v[132:135],a[76:79],a[16:19],v[132:135]
s_waitcnt lgkmcnt(11)
ds_read_b64_tr_b16 a[14:15],v13 offset:27648
v_mfma_f32_16x16x32_bf16 v[136:139],a[72:75],a[20:23],v[136:139]
ds_read_b64_tr_b16 a[16:17],v11 offset:28672
ds_read_b64_tr_b16 a[18:19],v11 offset:29696
v_mfma_f32_16x16x32_bf16 v[140:143],a[76:79],a[20:23],v[140:143]
s_waitcnt lgkmcnt(12)
buffer_load_dwordx4 %[vL16],%[srdW],s61 offen sc1 lds
v_mfma_f32_16x16x32_bf16 v[144:147],a[72:75],a[24:27],v[144:147]
s_add_u32 s82,s76,0
ds_read_b64_tr_b16 a[20:21],v13 offset:28672
v_mfma_f32_16x16x32_bf16 v[148:151],a[76:79],a[24:27],v[148:151]
s_waitcnt lgkmcnt(11)
ds_read_b64_tr_b16 a[22:23],v13 offset:29696
v_mfma_f32_16x16x32_bf16 v[152:155],a[72:75],a[28:31],v[152:155]
ds_read_b64_tr_b16 a[24:25],v11 offset:30720
ds_read_b64_tr_b16 a[26:27],v11 offset:31744
v_mfma_f32_16x16x32_bf16 v[156:159],a[76:79],a[28:31],v[156:159]
s_waitcnt lgkmcnt(12)
buffer_load_dwordx4 a[240:243],%[vL16],%[srdW],s82 offen offset:0 sc1
v_mfma_f32_16x16x32_bf16 v[96:99],a[80:83],a[0:3],v[96:99]
ds_read_b64_tr_b16 a[28:29],v13 offset:30720
v_mfma_f32_16x16x32_bf16 v[100:103],a[84:87],a[0:3],v[100:103]
s_waitcnt lgkmcnt(11)
ds_read_b64_tr_b16 a[30:31],v13 offset:31744
v_mfma_f32_16x16x32_bf16 v[104:107],a[80:83],a[4:7],v[104:107]
s_waitcnt vmcnt(16)
s_barrier
ds_read_b64_tr_b16 a[0:1],v11 offset:32768
ds_read_b64_tr_b16 a[2:3],v11 offset:33792
v_mfma_f32_16x16x32_bf16 v[108:111],a[84:87],a[4:7],v[108:111]
s_waitcnt lgkmcnt(12)
buffer_load_dwordx4 a[244:247],%[vL16],%[srdW],s82 offen offset:1024 sc1
v_mfma_f32_16x16x32_bf16 v[112:115],a[80:83],a[8:11],v[112:115]
s_add_u32 s82,s76,4096
ds_read_b64_tr_b16 a[4:5],v13 offset:32768
v_mfma_f32_16x16x32_bf16 v[116:119],a[84:87],a[8:11],v[116:119]
s_waitcnt lgkmcnt(11)
ds_read_b64_tr_b16 a[6:7],v13 offset:33792
v_mfma_f32_16x16x32_bf16 v[120:123],a[80:83],a[12:15],v[120:123]
ds_read_b64_tr_b16 a[8:9],v11 offset:34816
ds_read_b64_tr_b16 a[10:11],v11 offset:35840
v_mfma_f32_16x16x32_bf16 v[124:127],a[84:87],a[12:15],v[124:127]
s_waitcnt lgkmcnt(12)
buffer_load_dwordx4 a[248:251],%[vL16],%[srdW],s82 offen offset:0 sc1
v_mfma_f32_16x16x32_bf16 v[128:131],a[80:83],a[16:19],v[128:131]
ds_read_b64_tr_b16 a[12:13],v13 offset:34816
v_mfma_f32_16x16x32_bf16 v[132:135],a[84:87],a[16:19],v[132:135]
s_waitcnt lgkmcnt(11)
ds_read_b64_tr_b16 a[14:15],v13 offset:35840
v_mfma_f32_16x16x32_bf16 v[136:139],a[80:83],a[20:23],v[136:139]
ds_read_b64_tr_b16 a[16:17],v11 offset:36864
ds_read_b64_tr_b16 a[18:19],v11 offset:37888
v_mfma_f32_16x16x32_bf16 v[140:143],a[84:87],a[20:23],v[140:143]
s_waitcnt lgkmcnt(12)
buffer_load_dwordx4 a[252:255],%[vL16],%[srdW],s82 offen offset:1024 sc1
v_mfma_f32_16x16x32_bf16 v[144:147],a[80:83],a[24:27],v[144:147]
s_add_u32 s82,s76,8192
ds_read_b64_tr_b16 a[20:21],v13 offset:36864
v_mfma_f32_16x16x32_bf16 v[148:151],a[84:87],a[24:27],v[148:151]
s_waitcnt lgkmcnt(11)
ds_read_b64_tr_b16 a[22:23],v13 offset:37888
v_mfma_f32_16x16x32_bf16 v[152:155],a[80:83],a[28:31],v[152:155]
ds_read_b64_tr_b16 a[24:25],v11 offset:38912
ds_read_b64_tr_b16 a[26:27],v11 offset:39936
v_mfma_f32_16x16x32_bf16 v[156:159],a[84:87],a[28:31],v[156:159]
s_waitcnt lgkmcnt(12)
buffer_load_dwordx4 a[96:99],%[vL16],%[srdW],s82 offen offset:0 sc1
s_waitcnt vmcnt(7)
v_cmp_ne_u32 vcc,s70,v29
s_nop 0
s_and_b32 vcc_lo,vcc_lo,s77
s_cbranch_scc0 SPB5_%=
s_mov_b32 s97,0x40000
POLL5_%=:
buffer_load_dword v29,v28,%[srdW],0 offen sc1
s_waitcnt vmcnt(0)
v_cmp_ne_u32 vcc,s70,v29
s_cbranch_vccz GO5_%=
s_sub_u32 s97,s97,1
s_cbranch_scc1 GO5_%=
s_sleep 4
s_branch POLL5_%=
GO5_%=:
SPB5_%=:
s_barrier
v_mfma_f32_16x16x32_bf16 v[96:99],a[88:91],a[0:3],v[96:99]
ds_read_b64_tr_b16 a[28:29],v13 offset:38912
v_mfma_f32_16x16x32_bf16 v[100:103],a[92:95],a[0:3],v[100:103]
s_waitcnt lgkmcnt(11)
ds_read_b64_tr_b16 a[30:31],v13 offset:39936
v_mfma_f32_16x16x32_bf16 v[104:107],a[88:91],a[4:7],v[104:107]
ds_read_b64_tr_b16 a[0:1],v11 offset:40960
ds_read_b64_tr_b16 a[2:3],v11 offset:41984
v_mfma_f32_16x16x32_bf16 v[108:111],a[92:95],a[4:7],v[108:111]
s_waitcnt lgkmcnt(12)
buffer_load_dwordx4 a[100:103],%[vL16],%[srdW],s82 offen offset:1024 sc1
v_mfma_f32_16x16x32_bf16 v[112:115],a[88:91],a[8:11],v[112:115]
s_add_u32 s82,s76,12288
ds_read_b64_tr_b16 a[4:5],v13 offset:40960
v_mfma_f32_16x16x32_bf16 v[116:119],a[92:95],a[8:11],v[116:119]
s_waitcnt lgkmcnt(11)
ds_read_b64_tr_b16 a[6:7],v13 offset:41984
v_mfma_f32_16x16x32_bf16 v[120:123],a[88:91],a[12:15],v[120:123]
ds_read_b64_tr_b16 a[8:9],v11 offset:43008
ds_read_b64_tr_b16 a[10:11],v11 offset:44032
v_mfma_f32_16x16x32_bf16 v[124:127],a[92:95],a[12:15],v[124:127]
s_waitcnt lgkmcnt(12)
buffer_load_dwordx4 a[104:107],%[vL16],%[srdW],s82 offen offset:0 sc1
v_mfma_f32_16x16x32_bf16 v[128:131],a[88:91],a[16:19],v[128:131]
ds_read_b64_tr_b16 a[12:13],v13 offset:43008
v_mfma_f32_16x16x32_bf16 v[132:135],a[92:95],a[16:19],v[132:135]
s_waitcnt lgkmcnt(11)
ds_read_b64_tr_b16 a[14:15],v13 offset:44032
v_mfma_f32_16x16x32_bf16 v[136:139],a[88:91],a[20:23],v[136:139]
ds_read_b64_tr_b16 a[16:17],v11 offset:45056
ds_read_b64_tr_b16 a[18:19],v11 offset:46080
v_mfma_f32_16x16x32_bf16 v[140:143],a[92:95],a[20:23],v[140:143]
s_waitcnt lgkmcnt(12)
)"
R"(buffer_load_dwordx4 a[108:111],%[vL16],%[srdW],s82 offen offset:1024 sc1
v_mfma_f32_16x16x32_bf16 v[144:147],a[88:91],a[24:27],v[144:147]
s_add_u32 m0,s68,32768
ds_read_b64_tr_b16 a[20:21],v13 offset:45056
s_add_u32 s61,s62,8192
v_mfma_f32_16x16x32_bf16 v[148:151],a[92:95],a[24:27],v[148:151]
s_waitcnt lgkmcnt(11)
ds_read_b64_tr_b16 a[22:23],v13 offset:46080
v_mfma_f32_16x16x32_bf16 v[152:155],a[88:91],a[28:31],v[152:155]
ds_read_b64_tr_b16 a[24:25],v11 offset:47104
ds_read_b64_tr_b16 a[26:27],v11 offset:48128
v_mfma_f32_16x16x32_bf16 v[156:159],a[92:95],a[28:31],v[156:159]
s_waitcnt lgkmcnt(12)
buffer_load_dwordx4 %[vL16],%[srdW],s61 offen sc1 lds
v_mfma_f32_16x16x32_bf16 v[96:99],a[216:219],a[0:3],v[96:99]
s_add_u32 m0,s68,33792
ds_read_b64_tr_b16 a[28:29],v13 offset:47104
s_add_u32 s61,s62,9216
v_mfma_f32_16x16x32_bf16 v[100:103],a[220:223],a[0:3],v[100:103]
s_waitcnt lgkmcnt(11)
ds_read_b64_tr_b16 a[30:31],v13 offset:48128
v_lshrrev_b32 v26,2,v17
v_mfma_f32_16x16x32_bf16 v[104:107],a[216:219],a[4:7],v[104:107]
ds_read_b64_tr_b16 a[0:1],v11 offset:49152
ds_read_b64_tr_b16 a[2:3],v11 offset:50176
v_mfma_f32_16x16x32_bf16 v[108:111],a[220:223],a[4:7],v[108:111]
s_waitcnt lgkmcnt(12)
buffer_load_dwordx4 %[vL16],%[srdW],s61 offen sc1 lds
v_mfma_f32_16x16x32_bf16 v[112:115],a[216:219],a[8:11],v[112:115]
s_add_u32 m0,s68,40960
ds_read_b64_tr_b16 a[4:5],v13 offset:49152
s_add_u32 s61,s62,24576
v_mfma_f32_16x16x32_bf16 v[116:119],a[220:223],a[8:11],v[116:119]
s_waitcnt lgkmcnt(11)
ds_read_b64_tr_b16 a[6:7],v13 offset:50176
v_cmp_eq_u32 vcc,v26,v16
v_mfma_f32_16x16x32_bf16 v[120:123],a[216:219],a[12:15],v[120:123]
ds_read_b64_tr_b16 a[8:9],v11 offset:51200
ds_read_b64_tr_b16 a[10:11],v11 offset:52224
v_mfma_f32_16x16x32_bf16 v[124:127],a[220:223],a[12:15],v[124:127]
s_waitcnt lgkmcnt(12)
buffer_load_dwordx4 %[vL16],%[srdW],s61 offen sc1 lds
v_mfma_f32_16x16x32_bf16 v[128:131],a[216:219],a[16:19],v[128:131]
s_add_u32 m0,s68,41984
ds_read_b64_tr_b16 a[12:13],v13 offset:51200
s_add_u32 s61,s62,25600
v_mfma_f32_16x16x32_bf16 v[132:135],a[220:223],a[16:19],v[132:135]
s_waitcnt lgkmcnt(11)
ds_read_b64_tr_b16 a[14:15],v13 offset:52224
v_and_b32 v26,1,v17
v_mfma_f32_16x16x32_bf16 v[136:139],a[216:219],a[20:23],v[136:139]
ds_read_b64_tr_b16 a[16:17],v11 offset:53248
ds_read_b64_tr_b16 a[18:19],v11 offset:54272
v_mfma_f32_16x16x32_bf16 v[140:143],a[220:223],a[20:23],v[140:143]
s_waitcnt lgkmcnt(12)
buffer_load_dwordx4 %[vL16],%[srdW],s61 offen sc1 lds
v_mfma_f32_16x16x32_bf16 v[144:147],a[216:219],a[24:27],v[144:147]
s_add_u32 m0,s68,49152
ds_read_b64_tr_b16 a[20:21],v13 offset:53248
s_add_u32 s61,s62,40960
v_mfma_f32_16x16x32_bf16 v[148:151],a[220:223],a[24:27],v[148:151]
s_waitcnt lgkmcnt(11)
ds_read_b64_tr_b16 a[22:23],v13 offset:54272
v_lshlrev_b32 v26,4,v26
v_mfma_f32_16x16x32_bf16 v[152:155],a[216:219],a[28:31],v[152:155]
ds_read_b64_tr_b16 a[24:25],v11 offset:55296
ds_read_b64_tr_b16 a[26:27],v11 offset:56320
v_mfma_f32_16x16x32_bf16 v[156:159],a[220:223],a[28:31],v[156:159]
s_waitcnt lgkmcnt(12)
buffer_load_dwordx4 %[vL16],%[srdW],s61 offen sc1 lds
v_mfma_f32_16x16x32_bf16 v[96:99],a[224:227],a[0:3],v[96:99]
s_add_u32 m0,s68,50176
ds_read_b64_tr_b16 a[28:29],v13 offset:55296
s_add_u32 s61,s62,41984
v_mfma_f32_16x16x32_bf16 v[100:103],a[228:231],a[0:3],v[100:103]
s_waitcnt lgkmcnt(11)
ds_read_b64_tr_b16 a[30:31],v13 offset:56320
v_mov_b32 v27,0x3f80
v_mfma_f32_16x16x32_bf16 v[104:107],a[224:227],a[4:7],v[104:107]
ds_read_b64_tr_b16 a[0:1],v11 offset:57344
ds_read_b64_tr_b16 a[2:3],v11 offset:58368
v_mfma_f32_16x16x32_bf16 v[108:111],a[228:231],a[4:7],v[108:111]
s_waitcnt lgkmcnt(12)
buffer_load_dwordx4 %[vL16],%[srdW],s61 offen sc1 lds
v_mfma_f32_16x16x32_bf16 v[112:115],a[224:227],a[8:11],v[112:115]
s_add_u32 m0,s68,57344
ds_read_b64_tr_b16 a[4:5],v13 offset:57344
s_add_u32 s61,s62,57344
v_mfma_f32_16x16x32_bf16 v[116:119],a[228:231],a[8:11],v[116:119]
s_waitcnt lgkmcnt(11)
ds_read_b64_tr_b16 a[6:7],v13 offset:58368
v_lshlrev_b32 v26,v26,v27
v_mfma_f32_16x16x32_bf16 v[120:123],a[224:227],a[12:15],v[120:123]
ds_read_b64_tr_b16 a[8:9],v11 offset:59392
ds_read_b64_tr_b16 a[10:11],v11 offset:60416
v_mfma_f32_16x16x32_bf16 v[124:127],a[228:231],a[12:15],v[124:127]
s_waitcnt lgkmcnt(12)
buffer_load_dwordx4 %[vL16],%[srdW],s61 offen sc1 lds
v_mfma_f32_16x16x32_bf16 v[128:131],a[224:227],a[16:19],v[128:131]
s_add_u32 m0,s68,58368
ds_read_b64_tr_b16 a[12:13],v13 offset:59392
s_add_u32 s61,s62,58368
v_mfma_f32_16x16x32_bf16 v[132:135],a[228:231],a[16:19],v[132:135]
s_waitcnt lgkmcnt(11)
ds_read_b64_tr_b16 a[14:15],v13 offset:60416
v_cndmask_b32 v26,0,v26,vcc
v_mfma_f32_16x16x32_bf16 v[136:139],a[224:227],a[20:23],v[136:139]
ds_read_b64_tr_b16 a[16:17],v11 offset:61440
ds_read_b64_tr_b16 a[18:19],v11 offset:62464
v_mfma_f32_16x16x32_bf16 v[140:143],a[228:231],a[20:23],v[140:143]
s_waitcnt lgkmcnt(12)
buffer_load_dwordx4 %[vL16],%[srdW],s61 offen sc1 lds
v_mfma_f32_16x16x32_bf16 v[144:147],a[224:227],a[24:27],v[144:147]
s_add_u32 s82,s74,2048
ds_read_b64_tr_b16 a[20:21],v13 offset:61440
v_and_b32 v27,2,v17
v_mfma_f32_16x16x32_bf16 v[148:151],a[228:231],a[24:27],v[148:151]
s_waitcnt lgkmcnt(11)
ds_read_b64_tr_b16 a[22:23],v13 offset:62464
v_cmp_eq_u32 vcc,0,v27
v_mfma_f32_16x16x32_bf16 v[152:155],a[224:227],a[28:31],v[152:155]
ds_read_b64_tr_b16 a[24:25],v11 offset:63488
ds_read_b64_tr_b16 a[26:27],v11 offset:64512
v_mfma_f32_16x16x32_bf16 v[156:159],a[228:231],a[28:31],v[156:159]
s_waitcnt lgkmcnt(12)
buffer_load_dwordx4 a[112:115],%[vL16],%[srdW],s82 offen offset:0 sc1
v_mfma_f32_16x16x32_bf16 v[96:99],a[232:235],a[0:3],v[96:99]
ds_read_b64_tr_b16 a[28:29],v13 offset:63488
v_mov_b32 v194,0
v_mfma_f32_16x16x32_bf16 v[100:103],a[236:239],a[0:3],v[100:103]
s_waitcnt lgkmcnt(11)
ds_read_b64_tr_b16 a[30:31],v13 offset:64512
v_mov_b32 v195,0
v_mfma_f32_16x16x32_bf16 v[104:107],a[232:235],a[4:7],v[104:107]
s_waitcnt vmcnt(17)
s_barrier
ds_read_b64_tr_b16 a[0:1],v10 offset:0
ds_read_b64_tr_b16 a[2:3],v10 offset:1024
v_mfma_f32_16x16x32_bf16 v[108:111],a[236:239],a[4:7],v[108:111]
s_waitcnt lgkmcnt(12)
buffer_load_dwordx4 a[116:119],%[vL16],%[srdW],s82 offen offset:1024 sc1
v_mfma_f32_16x16x32_bf16 v[112:115],a[232:235],a[8:11],v[112:115]
s_add_u32 s82,s74,6144
ds_read_b64_tr_b16 a[4:5],v12 offset:0
v_mov_b32 v196,0
v_mfma_f32_16x16x32_bf16 v[116:119],a[236:239],a[8:11],v[116:119]
s_waitcnt lgkmcnt(11)
ds_read_b64_tr_b16 a[6:7],v12 offset:1024
v_mov_b32 v197,0
v_mfma_f32_16x16x32_bf16 v[120:123],a[232:235],a[12:15],v[120:123]
ds_read_b64_tr_b16 a[8:9],v10 offset:2048
ds_read_b64_tr_b16 a[10:11],v10 offset:3072
v_mfma_f32_16x16x32_bf16 v[124:127],a[236:239],a[12:15],v[124:127]
s_waitcnt lgkmcnt(12)
buffer_load_dwordx4 a[120:123],%[vL16],%[srdW],s82 offen offset:0 sc1
v_mfma_f32_16x16x32_bf16 v[128:131],a[232:235],a[16:19],v[128:131]
ds_read_b64_tr_b16 a[12:13],v12 offset:2048
v_cndmask_b32 v192,0,v26,vcc
v_mfma_f32_16x16x32_bf16 v[132:135],a[236:239],a[16:19],v[132:135]
s_waitcnt lgkmcnt(11)
ds_read_b64_tr_b16 a[14:15],v12 offset:3072
v_cndmask_b32 v193,v26,0,vcc
v_mfma_f32_16x16x32_bf16 v[136:139],a[232:235],a[20:23],v[136:139]
ds_read_b64_tr_b16 a[16:17],v10 offset:4096
ds_read_b64_tr_b16 a[18:19],v10 offset:5120
v_mfma_f32_16x16x32_bf16 v[140:143],a[236:239],a[20:23],v[140:143]
s_waitcnt lgkmcnt(12)
buffer_load_dwordx4 a[124:127],%[vL16],%[srdW],s82 offen offset:1024 sc1
v_mfma_f32_16x16x32_bf16 v[144:147],a[232:235],a[24:27],v[144:147]
s_add_u32 m0,s68,65536
ds_read_b64_tr_b16 a[20:21],v12 offset:4096
s_add_u32 s61,s63,8192
v_mfma_f32_16x16x32_bf16 v[148:151],a[236:239],a[24:27],v[148:151]
s_waitcnt lgkmcnt(11)
ds_read_b64_tr_b16 a[22:23],v12 offset:5120
v_mov_b32 v198,v192
v_mfma_f32_16x16x32_bf16 v[152:155],a[232:235],a[28:31],v[152:155]
ds_read_b64_tr_b16 a[24:25],v10 offset:6144
ds_read_b64_tr_b16 a[26:27],v10 offset:7168
v_mfma_f32_16x16x32_bf16 v[156:159],a[236:239],a[28:31],v[156:159]
s_waitcnt lgkmcnt(12)
s_waitcnt vmcnt(19)
v_mov_b32 v199,v193
v_mfma_f32_16x16x32_bf16 v[96:99],a[240:243],a[0:3],v[96:99]
s_waitcnt vmcnt(18)
ds_read_b64_tr_b16 a[28:29],v12 offset:6144
ds_read_b64_tr_b16 a[30:31],v12 offset:7168
v_mfma_f32_16x16x32_bf16 v[100:103],a[244:247],a[0:3],v[100:103]
s_waitcnt lgkmcnt(12)
buffer_load_dwordx4 %[vL16],%[srdW],s61 offen sc1 lds
v_mfma_f32_16x16x32_bf16 v[104:107],a[240:243],a[4:7],v[104:107]
s_add_u32 m0,s68,66560
ds_read_b64_tr_b16 a[0:1],v10 offset:8192
s_add_u32 s61,s63,9216
v_mfma_f32_16x16x32_bf16 v[108:111],a[244:247],a[4:7],v[108:111]
s_waitcnt lgkmcnt(11)
ds_read_b64_tr_b16 a[2:3],v10 offset:9216
v_mfma_f32_16x16x32_bf16 v[112:115],a[240:243],a[8:11],v[112:115]
ds_read_b64_tr_b16 a[4:5],v12 offset:8192
ds_read_b64_tr_b16 a[6:7],v12 offset:9216
v_mfma_f32_16x16x32_bf16 v[116:119],a[244:247],a[8:11],v[116:119]
s_waitcnt lgkmcnt(12)
buffer_load_dwordx4 %[vL16],%[srdW],s61 offen sc1 lds
v_mfma_f32_16x16x32_bf16 v[120:123],a[240:243],a[12:15],v[120:123]
s_add_u32 m0,s68,73728
ds_read_b64_tr_b16 a[8:9],v10 offset:10240
s_add_u32 s61,s63,24576
v_mfma_f32_16x16x32_bf16 v[124:127],a[244:247],a[12:15],v[124:127]
s_waitcnt lgkmcnt(11)
ds_read_b64_tr_b16 a[10:11],v10 offset:11264
v_mfma_f32_16x16x32_bf16 v[128:131],a[240:243],a[16:19],v[128:131]
ds_read_b64_tr_b16 a[12:13],v12 offset:10240
ds_read_b64_tr_b16 a[14:15],v12 offset:11264
v_mfma_f32_16x16x32_bf16 v[132:135],a[244:247],a[16:19],v[132:135]
s_waitcnt lgkmcnt(12)
buffer_load_dwordx4 %[vL16],%[srdW],s61 offen sc1 lds
v_mfma_f32_16x16x32_bf16 v[136:139],a[240:243],a[20:23],v[136:139]
s_add_u32 m0,s68,74752
ds_read_b64_tr_b16 a[16:17],v10 offset:12288
s_add_u32 s61,s63,25600
v_mfma_f32_16x16x32_bf16 v[140:143],a[244:247],a[20:23],v[140:143]
s_waitcnt lgkmcnt(11)
ds_read_b64_tr_b16 a[18:19],v10 offset:13312
v_mfma_f32_16x16x32_bf16 v[144:147],a[240:243],a[24:27],v[144:147]
ds_read_b64_tr_b16 a[20:21],v12 offset:12288
ds_read_b64_tr_b16 a[22:23],v12 offset:13312
v_mfma_f32_16x16x32_bf16 v[148:151],a[244:247],a[24:27],v[148:151]
s_waitcnt lgkmcnt(12)
buffer_load_dwordx4 %[vL16],%[srdW],s61 offen sc1 lds
v_mfma_f32_16x16x32_bf16 v[152:155],a[240:243],a[28:31],v[152:155]
s_add_u32 s82,s74,10240
ds_read_b64_tr_b16 a[24:25],v10 offset:14336
v_mfma_f32_16x16x32_bf16 v[156:159],a[244:247],a[28:31],v[156:159]
s_waitcnt lgkmcnt(11)
s_waitcnt vmcnt(21)
ds_read_b64_tr_b16 a[26:27],v10 offset:15360
v_mfma_f32_16x16x32_bf16 v[96:99],a[248:251],a[0:3],v[96:99]
s_waitcnt vmcnt(20)
ds_read_b64_tr_b16 a[28:29],v12 offset:14336
ds_read_b64_tr_b16 a[30:31],v12 offset:15360
v_mfma_f32_16x16x32_bf16 v[100:103],a[252:255],a[0:3],v[100:103]
s_waitcnt lgkmcnt(12)
buffer_load_dwordx4 a[128:131],%[vL16],%[srdW],s82 offen offset:0 sc1
v_mfma_f32_16x16x32_bf16 v[104:107],a[248:251],a[4:7],v[104:107]
ds_read_b64_tr_b16 a[0:1],v10 offset:16384
v_mfma_f32_16x16x32_bf16 v[108:111],a[252:255],a[4:7],v[108:111]
s_waitcnt lgkmcnt(11)
ds_read_b64_tr_b16 a[2:3],v10 offset:17408
v_mfma_f32_16x16x32_bf16 v[112:115],a[248:251],a[8:11],v[112:115]
ds_read_b64_tr_b16 a[4:5],v12 offset:16384
ds_read_b64_tr_b16 a[6:7],v12 offset:17408
v_mfma_f32_16x16x32_bf16 v[116:119],a[252:255],a[8:11],v[116:119]
s_waitcnt lgkmcnt(12)
buffer_load_dwordx4 a[132:135],%[vL16],%[srdW],s82 offen offset:1024 sc1
v_mfma_f32_16x16x32_bf16 v[120:123],a[248:251],a[12:15],v[120:123]
s_add_u32 s82,s74,14336
ds_read_b64_tr_b16 a[8:9],v10 offset:18432
v_mfma_f32_16x16x32_bf16 v[124:127],a[252:255],a[12:15],v[124:127]
s_waitcnt lgkmcnt(11)
ds_read_b64_tr_b16 a[10:11],v10 offset:19456
v_mfma_f32_16x16x32_bf16 v[128:131],a[248:251],a[16:19],v[128:131]
ds_read_b64_tr_b16 a[12:13],v12 offset:18432
ds_read_b64_tr_b16 a[14:15],v12 offset:19456
v_mfma_f32_16x16x32_bf16 v[132:135],a[252:255],a[16:19],v[132:135]
s_waitcnt lgkmcnt(12)
buffer_load_dwordx4 a[136:139],%[vL16],%[srdW],s82 offen offset:0 sc1
v_mfma_f32_16x16x32_bf16 v[136:139],a[248:251],a[20:23],v[136:139]
ds_read_b64_tr_b16 a[16:17],v10 offset:20480
v_mfma_f32_16x16x32_bf16 v[140:143],a[252:255],a[20:23],v[140:143]
s_waitcnt lgkmcnt(11)
ds_read_b64_tr_b16 a[18:19],v10 offset:21504
v_mfma_f32_16x16x32_bf16 v[144:147],a[248:251],a[24:27],v[144:147]
ds_read_b64_tr_b16 a[20:21],v12 offset:20480
ds_read_b64_tr_b16 a[22:23],v12 offset:21504
v_mfma_f32_16x16x32_bf16 v[148:151],a[252:255],a[24:27],v[148:151]
s_waitcnt lgkmcnt(12)
buffer_load_dwordx4 a[140:143],%[vL16],%[srdW],s82 offen offset:1024 sc1
v_mfma_f32_16x16x32_bf16 v[152:155],a[248:251],a[28:31],v[152:155]
s_add_u32 s82,s75,2048
ds_read_b64_tr_b16 a[24:25],v10 offset:22528
v_mfma_f32_16x16x32_bf16 v[156:159],a[252:255],a[28:31],v[156:159]
s_waitcnt lgkmcnt(11)
s_waitcnt vmcnt(23)
ds_read_b64_tr_b16 a[26:27],v10 offset:23552
v_mfma_f32_16x16x32_bf16 v[96:99],a[96:99],a[0:3],v[96:99]
s_waitcnt vmcnt(22)
ds_read_b64_tr_b16 a[28:29],v12 offset:22528
ds_read_b64_tr_b16 a[30:31],v12 offset:23552
v_mfma_f32_16x16x32_bf16 v[100:103],a[100:103],a[0:3],v[100:103]
s_waitcnt lgkmcnt(12)
buffer_load_dwordx4 a[144:147],%[vL16],%[srdW],s82 offen offset:0 sc1
v_mfma_f32_16x16x32_bf16 v[104:107],a[96:99],a[4:7],v[104:107]
ds_read_b64_tr_b16 a[0:1],v10 offset:24576
v_mfma_f32_16x16x32_bf16 v[108:111],a[100:103],a[4:7],v[108:111]
s_waitcnt lgkmcnt(11)
ds_read_b64_tr_b16 a[2:3],v10 offset:25600
v_mfma_f32_16x16x32_bf16 v[112:115],a[96:99],a[8:11],v[112:115]
ds_read_b64_tr_b16 a[4:5],v12 offset:24576
ds_read_b64_tr_b16 a[6:7],v12 offset:25600
v_mfma_f32_16x16x32_bf16 v[116:119],a[100:103],a[8:11],v[116:119]
s_waitcnt lgkmcnt(12)
buffer_load_dwordx4 a[148:151],%[vL16],%[srdW],s82 offen offset:1024 sc1
v_mfma_f32_16x16x32_bf16 v[120:123],a[96:99],a[12:15],v[120:123]
s_add_u32 s82,s75,6144
ds_read_b64_tr_b16 a[8:9],v10 offset:26624
v_mfma_f32_16x16x32_bf16 v[124:127],a[100:103],a[12:15],v[124:127]
s_waitcnt lgkmcnt(11)
ds_read_b64_tr_b16 a[10:11],v10 offset:27648
v_mfma_f32_16x16x32_bf16 v[128:131],a[96:99],a[16:19],v[128:131]
ds_read_b64_tr_b16 a[12:13],v12 offset:26624
ds_read_b64_tr_b16 a[14:15],v12 offset:27648
v_mfma_f32_16x16x32_bf16 v[132:135],a[100:103],a[16:19],v[132:135]
s_waitcnt lgkmcnt(12)
buffer_load_dwordx4 a[152:155],%[vL16],%[srdW],s82 offen offset:0 sc1
v_mfma_f32_16x16x32_bf16 v[136:139],a[96:99],a[20:23],v[136:139]
ds_read_b64_tr_b16 a[16:17],v10 offset:28672
v_mfma_f32_16x16x32_bf16 v[140:143],a[100:103],a[20:23],v[140:143]
s_waitcnt lgkmcnt(11)
ds_read_b64_tr_b16 a[18:19],v10 offset:29696
v_mfma_f32_16x16x32_bf16 v[144:147],a[96:99],a[24:27],v[144:147]
ds_read_b64_tr_b16 a[20:21],v12 offset:28672
ds_read_b64_tr_b16 a[22:23],v12 offset:29696
v_mfma_f32_16x16x32_bf16 v[148:151],a[100:103],a[24:27],v[148:151]
s_waitcnt lgkmcnt(12)
buffer_load_dwordx4 a[156:159],%[vL16],%[srdW],s82 offen offset:1024 sc1
v_mfma_f32_16x16x32_bf16 v[152:155],a[96:99],a[28:31],v[152:155]
s_add_u32 m0,s68,81920
ds_read_b64_tr_b16 a[24:25],v10 offset:30720
s_add_u32 s61,s63,40960
v_mfma_f32_16x16x32_bf16 v[156:159],a[100:103],a[28:31],v[156:159]
s_waitcnt lgkmcnt(11)
s_waitcnt vmcnt(25)
ds_read_b64_tr_b16 a[26:27],v10 offset:31744
v_mfma_f32_16x16x32_bf16 v[96:99],a[104:107],a[0:3],v[96:99]
s_waitcnt vmcnt(24)
ds_read_b64_tr_b16 a[28:29],v12 offset:30720
ds_read_b64_tr_b16 a[30:31],v12 offset:31744
v_mfma_f32_16x16x32_bf16 v[100:103],a[108:111],a[0:3],v[100:103]
s_waitcnt lgkmcnt(12)
buffer_load_dwordx4 %[vL16],%[srdW],s61 offen sc1 lds
v_mfma_f32_16x16x32_bf16 v[104:107],a[104:107],a[4:7],v[104:107]
s_waitcnt vmcnt(17)
s_barrier
ds_read_b64_tr_b16 a[0:1],v10 offset:32768
s_add_u32 m0,s68,82944
v_mfma_f32_16x16x32_bf16 v[108:111],a[108:111],a[4:7],v[108:111]
s_waitcnt lgkmcnt(11)
ds_read_b64_tr_b16 a[2:3],v10 offset:33792
s_add_u32 s61,s63,41984
v_mfma_f32_16x16x32_bf16 v[112:115],a[104:107],a[8:11],v[112:115]
ds_read_b64_tr_b16 a[4:5],v12 offset:32768
ds_read_b64_tr_b16 a[6:7],v12 offset:33792
v_mfma_f32_16x16x32_bf16 v[116:119],a[108:111],a[8:11],v[116:119]
s_waitcnt lgkmcnt(12)
buffer_load_dwordx4 %[vL16],%[srdW],s61 offen sc1 lds
v_mfma_f32_16x16x32_bf16 v[120:123],a[104:107],a[12:15],v[120:123]
s_add_u32 m0,s68,90112
ds_read_b64_tr_b16 a[8:9],v10 offset:34816
s_add_u32 s61,s63,57344
v_mfma_f32_16x16x32_bf16 v[124:127],a[108:111],a[12:15],v[124:127]
s_waitcnt lgkmcnt(11)
ds_read_b64_tr_b16 a[10:11],v10 offset:35840
v_mfma_f32_16x16x32_bf16 v[128:131],a[104:107],a[16:19],v[128:131]
ds_read_b64_tr_b16 a[12:13],v12 offset:34816
ds_read_b64_tr_b16 a[14:15],v12 offset:35840
v_mfma_f32_16x16x32_bf16 v[132:135],a[108:111],a[16:19],v[132:135]
s_waitcnt lgkmcnt(12)
buffer_load_dwordx4 %[vL16],%[srdW],s61 offen sc1 lds
v_mfma_f32_16x16x32_bf16 v[136:139],a[104:107],a[20:23],v[136:139]
s_add_u32 m0,s68,91136
ds_read_b64_tr_b16 a[16:17],v10 offset:36864
s_add_u32 s61,s63,58368
v_mfma_f32_16x16x32_bf16 v[140:143],a[108:111],a[20:23],v[140:143]
s_waitcnt lgkmcnt(11)
ds_read_b64_tr_b16 a[18:19],v10 offset:37888
v_mfma_f32_16x16x32_bf16 v[144:147],a[104:107],a[24:27],v[144:147]
ds_read_b64_tr_b16 a[20:21],v12 offset:36864
ds_read_b64_tr_b16 a[22:23],v12 offset:37888
v_mfma_f32_16x16x32_bf16 v[148:151],a[108:111],a[24:27],v[148:151]
s_waitcnt lgkmcnt(12)
buffer_load_dwordx4 %[vL16],%[srdW],s61 offen sc1 lds
v_mfma_f32_16x16x32_bf16 v[152:155],a[104:107],a[28:31],v[152:155]
s_add_u32 m0,s68,98304
ds_read_b64_tr_b16 a[24:25],v10 offset:38912
s_add_u32 s61,s64,8192
v_mfma_f32_16x16x32_bf16 v[156:159],a[108:111],a[28:31],v[156:159]
s_waitcnt lgkmcnt(11)
s_waitcnt vmcnt(19)
ds_read_b64_tr_b16 a[26:27],v10 offset:39936
v_mfma_f32_16x16x32_bf16 v[96:99],a[112:115],a[0:3],v[96:99]
s_waitcnt vmcnt(18)
ds_read_b64_tr_b16 a[28:29],v12 offset:38912
ds_read_b64_tr_b16 a[30:31],v12 offset:39936
v_mfma_f32_16x16x32_bf16 v[100:103],a[116:119],a[0:3],v[100:103]
s_waitcnt lgkmcnt(12)
buffer_load_dwordx4 %[vL16],%[srdW],s61 offen sc1 lds
v_mfma_f32_16x16x32_bf16 v[104:107],a[112:115],a[4:7],v[104:107]
s_add_u32 m0,s68,99328
ds_read_b64_tr_b16 a[0:1],v10 offset:40960
s_add_u32 s61,s64,9216
)"
R"(v_mfma_f32_16x16x32_bf16 v[108:111],a[116:119],a[4:7],v[108:111]
s_waitcnt lgkmcnt(11)
ds_read_b64_tr_b16 a[2:3],v10 offset:41984
v_mfma_f32_16x16x32_bf16 v[112:115],a[112:115],a[8:11],v[112:115]
ds_read_b64_tr_b16 a[4:5],v12 offset:40960
ds_read_b64_tr_b16 a[6:7],v12 offset:41984
v_mfma_f32_16x16x32_bf16 v[116:119],a[116:119],a[8:11],v[116:119]
s_waitcnt lgkmcnt(12)
buffer_load_dwordx4 %[vL16],%[srdW],s61 offen sc1 lds
v_mfma_f32_16x16x32_bf16 v[120:123],a[112:115],a[12:15],v[120:123]
s_add_u32 m0,s68,106496
ds_read_b64_tr_b16 a[8:9],v10 offset:43008
s_add_u32 s61,s64,24576
v_mfma_f32_16x16x32_bf16 v[124:127],a[116:119],a[12:15],v[124:127]
s_waitcnt lgkmcnt(11)
ds_read_b64_tr_b16 a[10:11],v10 offset:44032
v_mfma_f32_16x16x32_bf16 v[128:131],a[112:115],a[16:19],v[128:131]
ds_read_b64_tr_b16 a[12:13],v12 offset:43008
ds_read_b64_tr_b16 a[14:15],v12 offset:44032
v_mfma_f32_16x16x32_bf16 v[132:135],a[116:119],a[16:19],v[132:135]
s_waitcnt lgkmcnt(12)
buffer_load_dwordx4 %[vL16],%[srdW],s61 offen sc1 lds
v_mfma_f32_16x16x32_bf16 v[136:139],a[112:115],a[20:23],v[136:139]
s_add_u32 m0,s68,107520
ds_read_b64_tr_b16 a[16:17],v10 offset:45056
s_add_u32 s61,s64,25600
v_mfma_f32_16x16x32_bf16 v[140:143],a[116:119],a[20:23],v[140:143]
s_waitcnt lgkmcnt(11)
ds_read_b64_tr_b16 a[18:19],v10 offset:46080
v_mfma_f32_16x16x32_bf16 v[144:147],a[112:115],a[24:27],v[144:147]
ds_read_b64_tr_b16 a[20:21],v12 offset:45056
ds_read_b64_tr_b16 a[22:23],v12 offset:46080
v_mfma_f32_16x16x32_bf16 v[148:151],a[116:119],a[24:27],v[148:151]
s_waitcnt lgkmcnt(12)
buffer_load_dwordx4 %[vL16],%[srdW],s61 offen sc1 lds
v_mfma_f32_16x16x32_bf16 v[152:155],a[112:115],a[28:31],v[152:155]
s_add_u32 s82,s75,10240
ds_read_b64_tr_b16 a[24:25],v10 offset:47104
v_mfma_f32_16x16x32_bf16 v[156:159],a[116:119],a[28:31],v[156:159]
s_waitcnt lgkmcnt(11)
s_waitcnt vmcnt(21)
ds_read_b64_tr_b16 a[26:27],v10 offset:48128
v_mfma_f32_16x16x32_bf16 v[96:99],a[120:123],a[0:3],v[96:99]
s_waitcnt vmcnt(20)
ds_read_b64_tr_b16 a[28:29],v12 offset:47104
ds_read_b64_tr_b16 a[30:31],v12 offset:48128
v_mfma_f32_16x16x32_bf16 v[100:103],a[124:127],a[0:3],v[100:103]
s_waitcnt lgkmcnt(12)
buffer_load_dwordx4 a[160:163],%[vL16],%[srdW],s82 offen offset:0 sc1
v_mfma_f32_16x16x32_bf16 v[104:107],a[120:123],a[4:7],v[104:107]
ds_read_b64_tr_b16 a[0:1],v10 offset:49152
v_mfma_f32_16x16x32_bf16 v[108:111],a[124:127],a[4:7],v[108:111]
s_waitcnt lgkmcnt(11)
ds_read_b64_tr_b16 a[2:3],v10 offset:50176
v_mfma_f32_16x16x32_bf16 v[112:115],a[120:123],a[8:11],v[112:115]
ds_read_b64_tr_b16 a[4:5],v12 offset:49152
ds_read_b64_tr_b16 a[6:7],v12 offset:50176
v_mfma_f32_16x16x32_bf16 v[116:119],a[124:127],a[8:11],v[116:119]
s_waitcnt lgkmcnt(12)
buffer_load_dwordx4 a[164:167],%[vL16],%[srdW],s82 offen offset:1024 sc1
v_mfma_f32_16x16x32_bf16 v[120:123],a[120:123],a[12:15],v[120:123]
s_add_u32 s82,s75,14336
ds_read_b64_tr_b16 a[8:9],v10 offset:51200
v_mfma_f32_16x16x32_bf16 v[124:127],a[124:127],a[12:15],v[124:127]
s_waitcnt lgkmcnt(11)
ds_read_b64_tr_b16 a[10:11],v10 offset:52224
v_mfma_f32_16x16x32_bf16 v[128:131],a[120:123],a[16:19],v[128:131]
ds_read_b64_tr_b16 a[12:13],v12 offset:51200
ds_read_b64_tr_b16 a[14:15],v12 offset:52224
v_mfma_f32_16x16x32_bf16 v[132:135],a[124:127],a[16:19],v[132:135]
s_waitcnt lgkmcnt(12)
buffer_load_dwordx4 a[168:171],%[vL16],%[srdW],s82 offen offset:0 sc1
v_mfma_f32_16x16x32_bf16 v[136:139],a[120:123],a[20:23],v[136:139]
ds_read_b64_tr_b16 a[16:17],v10 offset:53248
v_mfma_f32_16x16x32_bf16 v[140:143],a[124:127],a[20:23],v[140:143]
s_waitcnt lgkmcnt(11)
ds_read_b64_tr_b16 a[18:19],v10 offset:54272
v_mfma_f32_16x16x32_bf16 v[144:147],a[120:123],a[24:27],v[144:147]
ds_read_b64_tr_b16 a[20:21],v12 offset:53248
ds_read_b64_tr_b16 a[22:23],v12 offset:54272
v_mfma_f32_16x16x32_bf16 v[148:151],a[124:127],a[24:27],v[148:151]
s_waitcnt lgkmcnt(12)
buffer_load_dwordx4 a[172:175],%[vL16],%[srdW],s82 offen offset:1024 sc1
v_mfma_f32_16x16x32_bf16 v[152:155],a[120:123],a[28:31],v[152:155]
s_add_u32 s82,s76,2048
ds_read_b64_tr_b16 a[24:25],v10 offset:55296
v_mfma_f32_16x16x32_bf16 v[156:159],a[124:127],a[28:31],v[156:159]
s_waitcnt lgkmcnt(11)
s_waitcnt vmcnt(19)
ds_read_b64_tr_b16 a[26:27],v10 offset:56320
v_mfma_f32_16x16x32_bf16 v[96:99],a[128:131],a[0:3],v[96:99]
s_waitcnt vmcnt(18)
ds_read_b64_tr_b16 a[28:29],v12 offset:55296
ds_read_b64_tr_b16 a[30:31],v12 offset:56320
v_mfma_f32_16x16x32_bf16 v[100:103],a[132:135],a[0:3],v[100:103]
s_waitcnt lgkmcnt(12)
buffer_load_dwordx4 v[160:163],%[vL16],%[srdW],s82 offen offset:0 sc1
v_mfma_f32_16x16x32_bf16 v[104:107],a[128:131],a[4:7],v[104:107]
ds_read_b64_tr_b16 a[0:1],v10 offset:57344
v_mfma_f32_16x16x32_bf16 v[108:111],a[132:135],a[4:7],v[108:111]
s_waitcnt lgkmcnt(11)
ds_read_b64_tr_b16 a[2:3],v10 offset:58368
v_mfma_f32_16x16x32_bf16 v[112:115],a[128:131],a[8:11],v[112:115]
ds_read_b64_tr_b16 a[4:5],v12 offset:57344
ds_read_b64_tr_b16 a[6:7],v12 offset:58368
v_mfma_f32_16x16x32_bf16 v[116:119],a[132:135],a[8:11],v[116:119]
s_waitcnt lgkmcnt(12)
buffer_load_dwordx4 v[164:167],%[vL16],%[srdW],s82 offen offset:1024 sc1
v_mfma_f32_16x16x32_bf16 v[120:123],a[128:131],a[12:15],v[120:123]
s_add_u32 s82,s76,6144
ds_read_b64_tr_b16 a[8:9],v10 offset:59392
v_mfma_f32_16x16x32_bf16 v[124:127],a[132:135],a[12:15],v[124:127]
s_waitcnt lgkmcnt(11)
ds_read_b64_tr_b16 a[10:11],v10 offset:60416
v_mfma_f32_16x16x32_bf16 v[128:131],a[128:131],a[16:19],v[128:131]
ds_read_b64_tr_b16 a[12:13],v12 offset:59392
ds_read_b64_tr_b16 a[14:15],v12 offset:60416
v_mfma_f32_16x16x32_bf16 v[132:135],a[132:135],a[16:19],v[132:135]
s_waitcnt lgkmcnt(12)
buffer_load_dwordx4 v[168:171],%[vL16],%[srdW],s82 offen offset:0 sc1
v_mfma_f32_16x16x32_bf16 v[136:139],a[128:131],a[20:23],v[136:139]
ds_read_b64_tr_b16 a[16:17],v10 offset:61440
v_mfma_f32_16x16x32_bf16 v[140:143],a[132:135],a[20:23],v[140:143]
s_waitcnt lgkmcnt(11)
ds_read_b64_tr_b16 a[18:19],v10 offset:62464
v_mfma_f32_16x16x32_bf16 v[144:147],a[128:131],a[24:27],v[144:147]
ds_read_b64_tr_b16 a[20:21],v12 offset:61440
ds_read_b64_tr_b16 a[22:23],v12 offset:62464
v_mfma_f32_16x16x32_bf16 v[148:151],a[132:135],a[24:27],v[148:151]
s_waitcnt lgkmcnt(12)
buffer_load_dwordx4 v[172:175],%[vL16],%[srdW],s82 offen offset:1024 sc1
v_mfma_f32_16x16x32_bf16 v[152:155],a[128:131],a[28:31],v[152:155]
s_add_u32 s82,s76,10240
ds_read_b64_tr_b16 a[24:25],v10 offset:63488
v_mfma_f32_16x16x32_bf16 v[156:159],a[132:135],a[28:31],v[156:159]
s_waitcnt lgkmcnt(11)
s_waitcnt vmcnt(21)
ds_read_b64_tr_b16 a[26:27],v10 offset:64512
v_mfma_f32_16x16x32_bf16 v[96:99],a[136:139],a[0:3],v[96:99]
s_waitcnt vmcnt(20)
ds_read_b64_tr_b16 a[28:29],v12 offset:63488
ds_read_b64_tr_b16 a[30:31],v12 offset:64512
v_mfma_f32_16x16x32_bf16 v[100:103],a[140:143],a[0:3],v[100:103]
s_waitcnt lgkmcnt(12)
buffer_load_dwordx4 v[176:179],%[vL16],%[srdW],s82 offen offset:0 sc1
v_mfma_f32_16x16x32_bf16 v[104:107],a[136:139],a[4:7],v[104:107]
s_waitcnt vmcnt(13)
s_barrier
ds_read_b64_tr_b16 a[0:1],v11 offset:0
v_mfma_f32_16x16x32_bf16 v[108:111],a[140:143],a[4:7],v[108:111]
s_waitcnt lgkmcnt(11)
ds_read_b64_tr_b16 a[2:3],v11 offset:1024
v_mfma_f32_16x16x32_bf16 v[112:115],a[136:139],a[8:11],v[112:115]
ds_read_b64_tr_b16 a[4:5],v13 offset:0
ds_read_b64_tr_b16 a[6:7],v13 offset:1024
v_mfma_f32_16x16x32_bf16 v[116:119],a[140:143],a[8:11],v[116:119]
s_waitcnt lgkmcnt(12)
buffer_load_dwordx4 v[180:183],%[vL16],%[srdW],s82 offen offset:1024 sc1
v_mfma_f32_16x16x32_bf16 v[120:123],a[136:139],a[12:15],v[120:123]
s_add_u32 s82,s76,14336
ds_read_b64_tr_b16 a[8:9],v11 offset:2048
v_mfma_f32_16x16x32_bf16 v[124:127],a[140:143],a[12:15],v[124:127]
s_waitcnt lgkmcnt(11)
ds_read_b64_tr_b16 a[10:11],v11 offset:3072
v_mfma_f32_16x16x32_bf16 v[128:131],a[136:139],a[16:19],v[128:131]
ds_read_b64_tr_b16 a[12:13],v13 offset:2048
ds_read_b64_tr_b16 a[14:15],v13 offset:3072
v_mfma_f32_16x16x32_bf16 v[132:135],a[140:143],a[16:19],v[132:135]
s_waitcnt lgkmcnt(12)
buffer_load_dwordx4 v[184:187],%[vL16],%[srdW],s82 offen offset:0 sc1
v_mfma_f32_16x16x32_bf16 v[136:139],a[136:139],a[20:23],v[136:139]
ds_read_b64_tr_b16 a[16:17],v11 offset:4096
v_mfma_f32_16x16x32_bf16 v[140:143],a[140:143],a[20:23],v[140:143]
s_waitcnt lgkmcnt(11)
ds_read_b64_tr_b16 a[18:19],v11 offset:5120
v_mfma_f32_16x16x32_bf16 v[144:147],a[136:139],a[24:27],v[144:147]
ds_read_b64_tr_b16 a[20:21],v13 offset:4096
ds_read_b64_tr_b16 a[22:23],v13 offset:5120
v_mfma_f32_16x16x32_bf16 v[148:151],a[140:143],a[24:27],v[148:151]
s_waitcnt lgkmcnt(12)
buffer_load_dwordx4 v[188:191],%[vL16],%[srdW],s82 offen offset:1024 sc1
v_mfma_f32_16x16x32_bf16 v[152:155],a[136:139],a[28:31],v[152:155]
s_add_u32 m0,s68,114688
ds_read_b64_tr_b16 a[24:25],v11 offset:6144
s_add_u32 s61,s64,40960
v_mfma_f32_16x16x32_bf16 v[156:159],a[140:143],a[28:31],v[156:159]
s_waitcnt lgkmcnt(11)
ds_read_b64_tr_b16 a[26:27],v11 offset:7168
v_mfma_f32_16x16x32_bf16 v[96:99],a[144:147],a[0:3],v[96:99]
ds_read_b64_tr_b16 a[28:29],v13 offset:6144
ds_read_b64_tr_b16 a[30:31],v13 offset:7168
v_mfma_f32_16x16x32_bf16 v[100:103],a[148:151],a[0:3],v[100:103]
s_waitcnt lgkmcnt(12)
buffer_load_dwordx4 %[vL16],%[srdW],s61 offen sc1 lds
v_mfma_f32_16x16x32_bf16 v[104:107],a[144:147],a[4:7],v[104:107]
s_add_u32 m0,s68,115712
ds_read_b64_tr_b16 a[0:1],v11 offset:8192
s_add_u32 s61,s64,41984
v_mfma_f32_16x16x32_bf16 v[108:111],a[148:151],a[4:7],v[108:111]
s_waitcnt lgkmcnt(11)
ds_read_b64_tr_b16 a[2:3],v11 offset:9216
v_mfma_f32_16x16x32_bf16 v[112:115],a[144:147],a[8:11],v[112:115]
ds_read_b64_tr_b16 a[4:5],v13 offset:8192
ds_read_b64_tr_b16 a[6:7],v13 offset:9216
v_mfma_f32_16x16x32_bf16 v[116:119],a[148:151],a[8:11],v[116:119]
s_waitcnt lgkmcnt(12)
buffer_load_dwordx4 %[vL16],%[srdW],s61 offen sc1 lds
v_mfma_f32_16x16x32_bf16 v[120:123],a[144:147],a[12:15],v[120:123]
s_add_u32 m0,s68,122880
ds_read_b64_tr_b16 a[8:9],v11 offset:10240
s_add_u32 s61,s64,57344
v_mfma_f32_16x16x32_bf16 v[124:127],a[148:151],a[12:15],v[124:127]
s_waitcnt lgkmcnt(11)
ds_read_b64_tr_b16 a[10:11],v11 offset:11264
v_mfma_f32_16x16x32_bf16 v[128:131],a[144:147],a[16:19],v[128:131]
ds_read_b64_tr_b16 a[12:13],v13 offset:10240
ds_read_b64_tr_b16 a[14:15],v13 offset:11264
v_mfma_f32_16x16x32_bf16 v[132:135],a[148:151],a[16:19],v[132:135]
s_waitcnt lgkmcnt(12)
buffer_load_dwordx4 %[vL16],%[srdW],s61 offen sc1 lds
v_mfma_f32_16x16x32_bf16 v[136:139],a[144:147],a[20:23],v[136:139]
s_add_u32 m0,s68,123904
ds_read_b64_tr_b16 a[16:17],v11 offset:12288
s_add_u32 s61,s64,58368
v_mfma_f32_16x16x32_bf16 v[140:143],a[148:151],a[20:23],v[140:143]
s_waitcnt lgkmcnt(11)
ds_read_b64_tr_b16 a[18:19],v11 offset:13312
v_mfma_f32_16x16x32_bf16 v[144:147],a[144:147],a[24:27],v[144:147]
ds_read_b64_tr_b16 a[20:21],v13 offset:12288
ds_read_b64_tr_b16 a[22:23],v13 offset:13312
v_mfma_f32_16x16x32_bf16 v[148:151],a[148:151],a[24:27],v[148:151]
s_waitcnt lgkmcnt(12)
buffer_load_dwordx4 %[vL16],%[srdW],s61 offen sc1 lds
v_mfma_f32_16x16x32_bf16 v[152:155],a[144:147],a[28:31],v[152:155]
s_add_u32 m0,s72,0
ds_read_b64_tr_b16 a[24:25],v11 offset:14336
v_add_u32 v24,0,v15
v_mfma_f32_16x16x32_bf16 v[156:159],a[148:151],a[28:31],v[156:159]
s_waitcnt lgkmcnt(11)
ds_read_b64_tr_b16 a[26:27],v11 offset:15360
v_mfma_f32_16x16x32_bf16 v[96:99],a[152:155],a[0:3],v[96:99]
ds_read_b64_tr_b16 a[28:29],v13 offset:14336
ds_read_b64_tr_b16 a[30:31],v13 offset:15360
v_mfma_f32_16x16x32_bf16 v[100:103],a[156:159],a[0:3],v[100:103]
s_waitcnt lgkmcnt(12)
buffer_load_dwordx4 v24,%[srdX],0 offen lds
v_mfma_f32_16x16x32_bf16 v[104:107],a[152:155],a[4:7],v[104:107]
s_add_u32 m0,s72,1024
ds_read_b64_tr_b16 a[0:1],v11 offset:16384
v_add_u32 v24,4096,v15
v_mfma_f32_16x16x32_bf16 v[108:111],a[156:159],a[4:7],v[108:111]
s_waitcnt lgkmcnt(11)
ds_read_b64_tr_b16 a[2:3],v11 offset:17408
v_mfma_f32_16x16x32_bf16 v[112:115],a[152:155],a[8:11],v[112:115]
ds_read_b64_tr_b16 a[4:5],v13 offset:16384
ds_read_b64_tr_b16 a[6:7],v13 offset:17408
v_mfma_f32_16x16x32_bf16 v[116:119],a[156:159],a[8:11],v[116:119]
s_waitcnt lgkmcnt(12)
buffer_load_dwordx4 v24,%[srdX],0 offen lds
v_mfma_f32_16x16x32_bf16 v[120:123],a[152:155],a[12:15],v[120:123]
s_add_u32 m0,s72,2048
ds_read_b64_tr_b16 a[8:9],v11 offset:18432
v_add_u32 v24,8192,v15
v_mfma_f32_16x16x32_bf16 v[124:127],a[156:159],a[12:15],v[124:127]
s_waitcnt lgkmcnt(11)
ds_read_b64_tr_b16 a[10:11],v11 offset:19456
v_mfma_f32_16x16x32_bf16 v[128:131],a[152:155],a[16:19],v[128:131]
ds_read_b64_tr_b16 a[12:13],v13 offset:18432
ds_read_b64_tr_b16 a[14:15],v13 offset:19456
v_mfma_f32_16x16x32_bf16 v[132:135],a[156:159],a[16:19],v[132:135]
s_waitcnt lgkmcnt(12)
buffer_load_dwordx4 v24,%[srdX],0 offen lds
v_mfma_f32_16x16x32_bf16 v[136:139],a[152:155],a[20:23],v[136:139]
s_add_u32 m0,s72,3072
ds_read_b64_tr_b16 a[16:17],v11 offset:20480
v_add_u32 v24,12288,v15
v_mfma_f32_16x16x32_bf16 v[140:143],a[156:159],a[20:23],v[140:143]
s_waitcnt lgkmcnt(11)
ds_read_b64_tr_b16 a[18:19],v11 offset:21504
v_mfma_f32_16x16x32_bf16 v[144:147],a[152:155],a[24:27],v[144:147]
ds_read_b64_tr_b16 a[20:21],v13 offset:20480
ds_read_b64_tr_b16 a[22:23],v13 offset:21504
v_mfma_f32_16x16x32_bf16 v[148:151],a[156:159],a[24:27],v[148:151]
s_waitcnt lgkmcnt(12)
buffer_load_dwordx4 v24,%[srdX],0 offen lds
v_mfma_f32_16x16x32_bf16 v[152:155],a[152:155],a[28:31],v[152:155]
s_add_u32 m0,s72,4096
ds_read_b64_tr_b16 a[24:25],v11 offset:22528
v_add_u32 v24,16384,v15
v_mfma_f32_16x16x32_bf16 v[156:159],a[156:159],a[28:31],v[156:159]
s_waitcnt lgkmcnt(11)
s_waitcnt vmcnt(19)
ds_read_b64_tr_b16 a[26:27],v11 offset:23552
v_mfma_f32_16x16x32_bf16 v[96:99],a[160:163],a[0:3],v[96:99]
s_waitcnt vmcnt(18)
ds_read_b64_tr_b16 a[28:29],v13 offset:22528
ds_read_b64_tr_b16 a[30:31],v13 offset:23552
v_mfma_f32_16x16x32_bf16 v[100:103],a[164:167],a[0:3],v[100:103]
s_waitcnt lgkmcnt(12)
buffer_load_dwordx4 v24,%[srdX],0 offen lds
v_mfma_f32_16x16x32_bf16 v[104:107],a[160:163],a[4:7],v[104:107]
s_add_u32 m0,s72,5120
ds_read_b64_tr_b16 a[0:1],v11 offset:24576
v_add_u32 v24,20480,v15
v_mfma_f32_16x16x32_bf16 v[108:111],a[164:167],a[4:7],v[108:111]
s_waitcnt lgkmcnt(11)
ds_read_b64_tr_b16 a[2:3],v11 offset:25600
v_mfma_f32_16x16x32_bf16 v[112:115],a[160:163],a[8:11],v[112:115]
ds_read_b64_tr_b16 a[4:5],v13 offset:24576
ds_read_b64_tr_b16 a[6:7],v13 offset:25600
v_mfma_f32_16x16x32_bf16 v[116:119],a[164:167],a[8:11],v[116:119]
s_waitcnt lgkmcnt(12)
buffer_load_dwordx4 v24,%[srdX],0 offen lds
v_mfma_f32_16x16x32_bf16 v[120:123],a[160:163],a[12:15],v[120:123]
s_add_u32 m0,s72,6144
ds_read_b64_tr_b16 a[8:9],v11 offset:26624
v_add_u32 v24,24576,v15
v_mfma_f32_16x16x32_bf16 v[124:127],a[164:167],a[12:15],v[124:127]
s_waitcnt lgkmcnt(11)
ds_read_b64_tr_b16 a[10:11],v11 offset:27648
v_mfma_f32_16x16x32_bf16 v[128:131],a[160:163],a[16:19],v[128:131]
ds_read_b64_tr_b16 a[12:13],v13 offset:26624
ds_read_b64_tr_b16 a[14:15],v13 offset:27648
v_mfma_f32_16x16x32_bf16 v[132:135],a[164:167],a[16:19],v[132:135]
s_waitcnt lgkmcnt(12)
buffer_load_dwordx4 v24,%[srdX],0 offen lds
v_mfma_f32_16x16x32_bf16 v[136:139],a[160:163],a[20:23],v[136:139]
s_add_u32 m0,s72,7168
ds_read_b64_tr_b16 a[16:17],v11 offset:28672
v_add_u32 v24,28672,v15
v_mfma_f32_16x16x32_bf16 v[140:143],a[164:167],a[20:23],v[140:143]
s_waitcnt lgkmcnt(11)
ds_read_b64_tr_b16 a[18:19],v11 offset:29696
v_mfma_f32_16x16x32_bf16 v[144:147],a[160:163],a[24:27],v[144:147]
ds_read_b64_tr_b16 a[20:21],v13 offset:28672
ds_read_b64_tr_b16 a[22:23],v13 offset:29696
v_mfma_f32_16x16x32_bf16 v[148:151],a[164:167],a[24:27],v[148:151]
s_waitcnt lgkmcnt(12)
buffer_load_dwordx4 v24,%[srdX],0 offen lds
v_mfma_f32_16x16x32_bf16 v[152:155],a[160:163],a[28:31],v[152:155]
ds_read_b64_tr_b16 a[24:25],v11 offset:30720
v_mfma_f32_16x16x32_bf16 v[156:159],a[164:167],a[28:31],v[156:159]
s_waitcnt lgkmcnt(11)
s_waitcnt vmcnt(21)
ds_read_b64_tr_b16 a[26:27],v11 offset:31744
v_mfma_f32_16x16x32_bf16 v[96:99],a[168:171],a[0:3],v[96:99]
s_waitcnt vmcnt(20)
ds_read_b64_tr_b16 a[28:29],v13 offset:30720
v_mfma_f32_16x16x32_bf16 v[100:103],a[172:175],a[0:3],v[100:103]
s_waitcnt lgkmcnt(11)
ds_read_b64_tr_b16 a[30:31],v13 offset:31744
v_mfma_f32_16x16x32_bf16 v[104:107],a[168:171],a[4:7],v[104:107]
s_waitcnt vmcnt(8)
s_barrier
ds_read_b64_tr_b16 a[0:1],v11 offset:32768
v_mfma_f32_16x16x32_bf16 v[108:111],a[172:175],a[4:7],v[108:111]
s_waitcnt lgkmcnt(11)
ds_read_b64_tr_b16 a[2:3],v11 offset:33792
v_mfma_f32_16x16x32_bf16 v[112:115],a[168:171],a[8:11],v[112:115]
ds_read_b64_tr_b16 a[4:5],v11 offset:40960
v_mfma_f32_16x16x32_bf16 v[116:119],a[172:175],a[8:11],v[116:119]
s_waitcnt lgkmcnt(11)
ds_read_b64_tr_b16 a[6:7],v11 offset:41984
v_mfma_f32_16x16x32_bf16 v[120:123],a[168:171],a[12:15],v[120:123]
ds_read_b64_tr_b16 a[8:9],v11 offset:49152
v_mfma_f32_16x16x32_bf16 v[124:127],a[172:175],a[12:15],v[124:127]
s_waitcnt lgkmcnt(11)
ds_read_b64_tr_b16 a[10:11],v11 offset:50176
v_mfma_f32_16x16x32_bf16 v[128:131],a[168:171],a[16:19],v[128:131]
ds_read_b64_tr_b16 a[12:13],v11 offset:57344
v_mfma_f32_16x16x32_bf16 v[132:135],a[172:175],a[16:19],v[132:135]
s_waitcnt lgkmcnt(11)
ds_read_b64_tr_b16 a[14:15],v11 offset:58368
v_mfma_f32_16x16x32_bf16 v[136:139],a[168:171],a[20:23],v[136:139]
s_waitcnt vmcnt(0)
s_barrier
ds_read_b128 a[16:19],v21 offset:0
v_mfma_f32_16x16x32_bf16 v[140:143],a[172:175],a[20:23],v[140:143]
s_waitcnt lgkmcnt(11)
v_mfma_f32_16x16x32_bf16 v[144:147],a[168:171],a[24:27],v[144:147]
ds_read_b64_tr_b16 a[20:21],v13 offset:32768
v_mfma_f32_16x16x32_bf16 v[148:151],a[172:175],a[24:27],v[148:151]
s_waitcnt lgkmcnt(10)
ds_read_b64_tr_b16 a[22:23],v13 offset:33792
v_mfma_f32_16x16x32_bf16 v[152:155],a[168:171],a[28:31],v[152:155]
ds_read_b64_tr_b16 a[24:25],v13 offset:40960
v_mfma_f32_16x16x32_bf16 v[156:159],a[172:175],a[28:31],v[156:159]
s_waitcnt lgkmcnt(10)
ds_read_b64_tr_b16 a[26:27],v13 offset:41984
v_mfma_f32_16x16x32_bf16 v[96:99],v[160:163],a[0:3],v[96:99]
ds_read_b64_tr_b16 a[28:29],v13 offset:49152
v_mfma_f32_16x16x32_bf16 v[100:103],v[164:167],a[0:3],v[100:103]
s_waitcnt lgkmcnt(10)
ds_read_b64_tr_b16 a[30:31],v13 offset:50176
v_mfma_f32_16x16x32_bf16 v[96:99],v[168:171],a[4:7],v[96:99]
ds_read_b64_tr_b16 a[0:1],v13 offset:57344
v_mfma_f32_16x16x32_bf16 v[100:103],v[172:175],a[4:7],v[100:103]
s_waitcnt lgkmcnt(10)
ds_read_b64_tr_b16 a[2:3],v13 offset:58368
v_mfma_f32_16x16x32_bf16 v[96:99],v[176:179],a[8:11],v[96:99]
)"
R"(ds_read_b128 a[4:7],v21 offset:1024
v_mfma_f32_16x16x32_bf16 v[100:103],v[180:183],a[8:11],v[100:103]
s_waitcnt lgkmcnt(10)
v_mfma_f32_16x16x32_bf16 v[96:99],v[184:187],a[12:15],v[96:99]
ds_read_b64_tr_b16 a[8:9],v11 offset:34816
v_mfma_f32_16x16x32_bf16 v[100:103],v[188:191],a[12:15],v[100:103]
s_waitcnt lgkmcnt(10)
ds_read_b64_tr_b16 a[10:11],v11 offset:35840
v_mfma_f32_16x16x32_bf16 v[96:99],v[192:195],a[16:19],v[96:99]
ds_read_b64_tr_b16 a[12:13],v11 offset:43008
v_mfma_f32_16x16x32_bf16 v[100:103],v[196:199],a[16:19],v[100:103]
s_waitcnt lgkmcnt(10)
ds_read_b64_tr_b16 a[14:15],v11 offset:44032
v_mfma_f32_16x16x32_bf16 v[104:107],v[160:163],a[20:23],v[104:107]
ds_read_b64_tr_b16 a[16:17],v11 offset:51200
v_mfma_f32_16x16x32_bf16 v[108:111],v[164:167],a[20:23],v[108:111]
s_waitcnt lgkmcnt(10)
ds_read_b64_tr_b16 a[18:19],v11 offset:52224
v_mfma_f32_16x16x32_bf16 v[104:107],v[168:171],a[24:27],v[104:107]
ds_read_b64_tr_b16 a[20:21],v11 offset:59392
v_mfma_f32_16x16x32_bf16 v[108:111],v[172:175],a[24:27],v[108:111]
s_waitcnt lgkmcnt(10)
ds_read_b64_tr_b16 a[22:23],v11 offset:60416
v_mfma_f32_16x16x32_bf16 v[104:107],v[176:179],a[28:31],v[104:107]
ds_read_b128 a[24:27],v21 offset:2048
v_cvt_pk_bf16_f32 v96,v96,v97
v_mfma_f32_16x16x32_bf16 v[108:111],v[180:183],a[28:31],v[108:111]
s_waitcnt lgkmcnt(10)
v_cvt_pk_bf16_f32 v97,v98,v99
v_mfma_f32_16x16x32_bf16 v[104:107],v[184:187],a[0:3],v[104:107]
ds_read_b64_tr_b16 a[28:29],v13 offset:34816
v_cvt_pk_bf16_f32 v98,v100,v101
v_mfma_f32_16x16x32_bf16 v[108:111],v[188:191],a[0:3],v[108:111]
s_waitcnt lgkmcnt(10)
ds_read_b64_tr_b16 a[30:31],v13 offset:35840
v_cvt_pk_bf16_f32 v99,v102,v103
v_mfma_f32_16x16x32_bf16 v[104:107],v[192:195],a[4:7],v[104:107]
ds_read_b64_tr_b16 a[0:1],v13 offset:43008
v_mfma_f32_16x16x32_bf16 v[108:111],v[196:199],a[4:7],v[108:111]
s_waitcnt lgkmcnt(10)
ds_read_b64_tr_b16 a[2:3],v13 offset:44032
v_mfma_f32_16x16x32_bf16 v[112:115],v[160:163],a[8:11],v[112:115]
ds_read_b64_tr_b16 a[4:5],v13 offset:51200
ds_read_b64_tr_b16 a[6:7],v13 offset:52224
v_mfma_f32_16x16x32_bf16 v[116:119],v[164:167],a[8:11],v[116:119]
s_waitcnt lgkmcnt(11)
buffer_store_dwordx4 v[96:99],v22,%[srdO],0 offen sc1
v_mfma_f32_16x16x32_bf16 v[112:115],v[168:171],a[12:15],v[112:115]
ds_read_b64_tr_b16 a[8:9],v13 offset:59392
v_mfma_f32_16x16x32_bf16 v[116:119],v[172:175],a[12:15],v[116:119]
s_waitcnt lgkmcnt(10)
ds_read_b64_tr_b16 a[10:11],v13 offset:60416
v_mfma_f32_16x16x32_bf16 v[112:115],v[176:179],a[16:19],v[112:115]
ds_read_b128 a[12:15],v21 offset:3072
v_cvt_pk_bf16_f32 v104,v104,v105
v_mfma_f32_16x16x32_bf16 v[116:119],v[180:183],a[16:19],v[116:119]
s_waitcnt lgkmcnt(10)
v_cvt_pk_bf16_f32 v105,v106,v107
v_mfma_f32_16x16x32_bf16 v[112:115],v[184:187],a[20:23],v[112:115]
ds_read_b64_tr_b16 a[16:17],v11 offset:36864
v_cvt_pk_bf16_f32 v106,v108,v109
v_mfma_f32_16x16x32_bf16 v[116:119],v[188:191],a[20:23],v[116:119]
s_waitcnt lgkmcnt(10)
ds_read_b64_tr_b16 a[18:19],v11 offset:37888
v_cvt_pk_bf16_f32 v107,v110,v111
v_mfma_f32_16x16x32_bf16 v[112:115],v[192:195],a[24:27],v[112:115]
ds_read_b64_tr_b16 a[20:21],v11 offset:45056
v_add_u32 v22,0x1000,v22
v_mfma_f32_16x16x32_bf16 v[116:119],v[196:199],a[24:27],v[116:119]
s_waitcnt lgkmcnt(10)
ds_read_b64_tr_b16 a[22:23],v11 offset:46080
v_mfma_f32_16x16x32_bf16 v[120:123],v[160:163],a[28:31],v[120:123]
ds_read_b64_tr_b16 a[24:25],v11 offset:53248
ds_read_b64_tr_b16 a[26:27],v11 offset:54272
v_mfma_f32_16x16x32_bf16 v[124:127],v[164:167],a[28:31],v[124:127]
s_waitcnt lgkmcnt(11)
buffer_store_dwordx4 v[104:107],v22,%[srdO],0 offen sc1
v_mfma_f32_16x16x32_bf16 v[120:123],v[168:171],a[0:3],v[120:123]
ds_read_b64_tr_b16 a[28:29],v11 offset:61440
v_mfma_f32_16x16x32_bf16 v[124:127],v[172:175],a[0:3],v[124:127]
s_waitcnt lgkmcnt(10)
ds_read_b64_tr_b16 a[30:31],v11 offset:62464
v_mfma_f32_16x16x32_bf16 v[120:123],v[176:179],a[4:7],v[120:123]
ds_read_b128 a[0:3],v21 offset:4096
v_cvt_pk_bf16_f32 v112,v112,v113
v_mfma_f32_16x16x32_bf16 v[124:127],v[180:183],a[4:7],v[124:127]
s_waitcnt lgkmcnt(10)
v_cvt_pk_bf16_f32 v113,v114,v115
v_mfma_f32_16x16x32_bf16 v[120:123],v[184:187],a[8:11],v[120:123]
ds_read_b64_tr_b16 a[4:5],v13 offset:36864
v_cvt_pk_bf16_f32 v114,v116,v117
v_mfma_f32_16x16x32_bf16 v[124:127],v[188:191],a[8:11],v[124:127]
s_waitcnt lgkmcnt(10)
ds_read_b64_tr_b16 a[6:7],v13 offset:37888
v_cvt_pk_bf16_f32 v115,v118,v119
v_mfma_f32_16x16x32_bf16 v[120:123],v[192:195],a[12:15],v[120:123]
ds_read_b64_tr_b16 a[8:9],v13 offset:45056
v_add_u32 v22,0x1000,v22
v_mfma_f32_16x16x32_bf16 v[124:127],v[196:199],a[12:15],v[124:127]
s_waitcnt lgkmcnt(10)
ds_read_b64_tr_b16 a[10:11],v13 offset:46080
v_mfma_f32_16x16x32_bf16 v[128:131],v[160:163],a[16:19],v[128:131]
ds_read_b64_tr_b16 a[12:13],v13 offset:53248
ds_read_b64_tr_b16 a[14:15],v13 offset:54272
v_mfma_f32_16x16x32_bf16 v[132:135],v[164:167],a[16:19],v[132:135]
s_waitcnt lgkmcnt(11)
buffer_store_dwordx4 v[112:115],v22,%[srdO],0 offen sc1
v_mfma_f32_16x16x32_bf16 v[128:131],v[168:171],a[20:23],v[128:131]
ds_read_b64_tr_b16 a[16:17],v13 offset:61440
v_mfma_f32_16x16x32_bf16 v[132:135],v[172:175],a[20:23],v[132:135]
s_waitcnt lgkmcnt(10)
ds_read_b64_tr_b16 a[18:19],v13 offset:62464
v_mfma_f32_16x16x32_bf16 v[128:131],v[176:179],a[24:27],v[128:131]
ds_read_b128 a[20:23],v21 offset:5120
v_cvt_pk_bf16_f32 v120,v120,v121
v_mfma_f32_16x16x32_bf16 v[132:135],v[180:183],a[24:27],v[132:135]
s_waitcnt lgkmcnt(10)
v_cvt_pk_bf16_f32 v121,v122,v123
v_mfma_f32_16x16x32_bf16 v[128:131],v[184:187],a[28:31],v[128:131]
ds_read_b64_tr_b16 a[24:25],v11 offset:38912
v_cvt_pk_bf16_f32 v122,v124,v125
v_mfma_f32_16x16x32_bf16 v[132:135],v[188:191],a[28:31],v[132:135]
s_waitcnt lgkmcnt(10)
ds_read_b64_tr_b16 a[26:27],v11 offset:39936
v_cvt_pk_bf16_f32 v123,v126,v127
v_mfma_f32_16x16x32_bf16 v[128:131],v[192:195],a[0:3],v[128:131]
ds_read_b64_tr_b16 a[28:29],v11 offset:47104
v_add_u32 v22,0x1000,v22
v_mfma_f32_16x16x32_bf16 v[132:135],v[196:199],a[0:3],v[132:135]
s_waitcnt lgkmcnt(10)
ds_read_b64_tr_b16 a[30:31],v11 offset:48128
v_mfma_f32_16x16x32_bf16 v[136:139],v[160:163],a[4:7],v[136:139]
ds_read_b64_tr_b16 a[0:1],v11 offset:55296
ds_read_b64_tr_b16 a[2:3],v11 offset:56320
v_mfma_f32_16x16x32_bf16 v[140:143],v[164:167],a[4:7],v[140:143]
s_waitcnt lgkmcnt(11)
buffer_store_dwordx4 v[120:123],v22,%[srdO],0 offen sc1
v_mfma_f32_16x16x32_bf16 v[136:139],v[168:171],a[8:11],v[136:139]
ds_read_b64_tr_b16 a[4:5],v11 offset:63488
v_mfma_f32_16x16x32_bf16 v[140:143],v[172:175],a[8:11],v[140:143]
s_waitcnt lgkmcnt(10)
ds_read_b64_tr_b16 a[6:7],v11 offset:64512
v_mfma_f32_16x16x32_bf16 v[136:139],v[176:179],a[12:15],v[136:139]
ds_read_b128 a[8:11],v21 offset:6144
v_cvt_pk_bf16_f32 v128,v128,v129
v_mfma_f32_16x16x32_bf16 v[140:143],v[180:183],a[12:15],v[140:143]
s_waitcnt lgkmcnt(10)
v_cvt_pk_bf16_f32 v129,v130,v131
v_mfma_f32_16x16x32_bf16 v[136:139],v[184:187],a[16:19],v[136:139]
ds_read_b64_tr_b16 a[12:13],v13 offset:38912
v_cvt_pk_bf16_f32 v130,v132,v133
v_mfma_f32_16x16x32_bf16 v[140:143],v[188:191],a[16:19],v[140:143]
s_waitcnt lgkmcnt(10)
ds_read_b64_tr_b16 a[14:15],v13 offset:39936
v_cvt_pk_bf16_f32 v131,v134,v135
v_mfma_f32_16x16x32_bf16 v[136:139],v[192:195],a[20:23],v[136:139]
ds_read_b64_tr_b16 a[16:17],v13 offset:47104
v_add_u32 v22,0x1000,v22
v_mfma_f32_16x16x32_bf16 v[140:143],v[196:199],a[20:23],v[140:143]
s_waitcnt lgkmcnt(10)
ds_read_b64_tr_b16 a[18:19],v13 offset:48128
v_mfma_f32_16x16x32_bf16 v[144:147],v[160:163],a[24:27],v[144:147]
ds_read_b64_tr_b16 a[20:21],v13 offset:55296
ds_read_b64_tr_b16 a[22:23],v13 offset:56320
v_mfma_f32_16x16x32_bf16 v[148:151],v[164:167],a[24:27],v[148:151]
s_waitcnt lgkmcnt(11)
buffer_store_dwordx4 v[128:131],v22,%[srdO],0 offen sc1
v_mfma_f32_16x16x32_bf16 v[144:147],v[168:171],a[28:31],v[144:147]
ds_read_b64_tr_b16 a[24:25],v13 offset:63488
v_mfma_f32_16x16x32_bf16 v[148:151],v[172:175],a[28:31],v[148:151]
s_waitcnt lgkmcnt(10)
ds_read_b64_tr_b16 a[26:27],v13 offset:64512
v_mfma_f32_16x16x32_bf16 v[144:147],v[176:179],a[0:3],v[144:147]
ds_read_b128 a[28:31],v21 offset:7168
v_cvt_pk_bf16_f32 v136,v136,v137
v_mfma_f32_16x16x32_bf16 v[148:151],v[180:183],a[0:3],v[148:151]
s_waitcnt lgkmcnt(10)
v_cvt_pk_bf16_f32 v137,v138,v139
v_mfma_f32_16x16x32_bf16 v[144:147],v[184:187],a[4:7],v[144:147]
v_cvt_pk_bf16_f32 v138,v140,v141
v_cvt_pk_bf16_f32 v139,v142,v143
v_mfma_f32_16x16x32_bf16 v[148:151],v[188:191],a[4:7],v[148:151]
s_waitcnt lgkmcnt(9)
v_add_u32 v22,0x1000,v22
v_mfma_f32_16x16x32_bf16 v[144:147],v[192:195],a[8:11],v[144:147]
buffer_store_dwordx4 v[136:139],v22,%[srdO],0 offen sc1
v_mfma_f32_16x16x32_bf16 v[148:151],v[196:199],a[8:11],v[148:151]
s_waitcnt lgkmcnt(7)
v_mfma_f32_16x16x32_bf16 v[152:155],v[160:163],a[12:15],v[152:155]
v_mfma_f32_16x16x32_bf16 v[156:159],v[164:167],a[12:15],v[156:159]
s_waitcnt lgkmcnt(5)
v_mfma_f32_16x16x32_bf16 v[152:155],v[168:171],a[16:19],v[152:155]
v_mfma_f32_16x16x32_bf16 v[156:159],v[172:175],a[16:19],v[156:159]
s_waitcnt lgkmcnt(3)
v_mfma_f32_16x16x32_bf16 v[152:155],v[176:179],a[20:23],v[152:155]
v_cvt_pk_bf16_f32 v144,v144,v145
v_cvt_pk_bf16_f32 v145,v146,v147
v_mfma_f32_16x16x32_bf16 v[156:159],v[180:183],a[20:23],v[156:159]
s_waitcnt lgkmcnt(1)
v_cvt_pk_bf16_f32 v146,v148,v149
v_cvt_pk_bf16_f32 v147,v150,v151
v_mfma_f32_16x16x32_bf16 v[152:155],v[184:187],a[24:27],v[152:155]
v_add_u32 v22,0x1000,v22
v_mfma_f32_16x16x32_bf16 v[156:159],v[188:191],a[24:27],v[156:159]
s_waitcnt lgkmcnt(0)
buffer_store_dwordx4 v[144:147],v22,%[srdO],0 offen sc1
v_mfma_f32_16x16x32_bf16 v[152:155],v[192:195],a[28:31],v[152:155]
v_mfma_f32_16x16x32_bf16 v[156:159],v[196:199],a[28:31],v[156:159]
s_nop 6
v_cvt_pk_bf16_f32 v152,v152,v153
v_cvt_pk_bf16_f32 v153,v154,v155
v_cvt_pk_bf16_f32 v154,v156,v157
v_cvt_pk_bf16_f32 v155,v158,v159
v_add_u32 v22,0x1000,v22
buffer_store_dwordx4 v[152:155],v22,%[srdO],0 offen sc1
s_branch END_%=
END_%=:
EBF_K5_END:
)"
    :: [srdW] "s"(rw), [srdX] "s"(RSRC(x + (size_t)b * T * 512, T * 1024)), [srdO] "s"(RSRC(out + (size_t)b * T * 512, T * 1024)), [vL16] "{v0}"(lane * 16), [pv] "{v1}"(pv),
       "{v2}"(lv[0]), "{v3}"(lv[1]), "{v4}"(lv[2]), "{v5}"(lv[3]), "{v6}"(lv[4]), "{v7}"(lv[5]), "{v8}"(lv[6]), "{v9}"(dg), "{v10}"(lv[7]), "{v11}"(lv[8]), "{v13}"(lv[9]), "{v15}"(pv2)
    : "v12","v14","v16","v17","v18","v19","v20","v21","v22","v23","v24","v25","v26","v27","v28","v29","v30","v31","v32","v33","v34","v35","v36","v37","v38","v39","v40","v41","v42","v43","v44","v45","v46","v47","v48","v49","v50","v51","v52","v53","v54","v55","v56","v57","v58","v59","v60","v61","v62","v63","v64","v65","v66","v67","v68","v69","v70","v71","v72","v73","v74","v75","v76","v77","v78","v79","v80","v81","v82","v83","v84","v85","v86","v87","v88","v89","v90","v91","v92","v93","v94","v95","v96","v97","v98","v99","v100","v101","v102","v103","v104","v105","v106","v107","v108","v109","v110","v111","v112","v113","v114","v115","v116","v117","v118","v119","v120","v121","v122","v123","v124","v125","v126","v127","v128","v129","v130","v131","v132","v133","v134","v135","v136","v137","v138","v139","v140","v141","v142","v143","v144","v145","v146","v147","v148","v149","v150","v151","v152","v153","v154","v155","v156","v157","v158","v159","v160","v161","v162","v163","v164","v165","v166","v167","v168","v169","v170","v171","v172","v173","v174","v175","v176","v177","v178","v179","v180","v181","v182","v183","v184","v185","v186","v187","v188","v189","v190","v191","v192","v193","v194","v195","v196","v197","v198","v199","v200","v201","v202","v203","v204","v205","v206","v207","v208","v209","v210","v211","v212","v213","v214","v215","v216","v217","v218","v219","v220","v221","v222","v223","v224","v225","v226","v227","v228","v229","v230","v231","v232","v233","v234","v235","v236","v237","v238","v239","v240","v241","v242","v243","v244","v245","v246","v247","v248","v249","v250","v251","v252","v253","v254","v255","a0","a1","a2","a3","a4","a5","a6","a7","a8","a9","a10","a11","a12","a13","a14","a15","a16","a17","a18","a19","a20","a21","a22","a23","a24","a25","a26","a27","a28","a29","a30","a31","a32","a33","a34","a35","a36","a37","a38","a39","a40","a41","a42","a43","a44","a45","a46","a47","a48","a49","a50","a51","a52","a53","a54","a55","a56","a57","a58","a59","a60","a61","a62","a63","a64","a65","a66","a67","a68","a69","a70","a71","a72","a73","a74","a75","a76","a77","a78","a79","a80","a81","a82","a83","a84","a85","a86","a87","a88","a89","a90","a91","a92","a93","a94","a95","a96","a97","a98","a99","a100","a101","a102","a103","a104","a105","a106","a107","a108","a109","a110","a111","a112","a113","a114","a115","a116","a117","a118","a119","a120","a121","a122","a123","a124","a125","a126","a127","a128","a129","a130","a131","a132","a133","a134","a135","a136","a137","a138","a139","a140","a141","a142","a143","a144","a145","a146","a147","a148","a149","a150","a151","a152","a153","a154","a155","a156","a157","a158","a159","a160","a161","a162","a163","a164","a165","a166","a167","a168","a169","a170","a171","a172","a173","a174","a175","a176","a177","a178","a179","a180","a181","a182","a183","a184","a185","a186","a187","a188","a189","a190","a191","a192","a193","a194","a195","a196","a197","a198","a199","a200","a201","a202","a203","a204","a205","a206","a207","a208","a209","a210","a211","a212","a213","a214","a215","a216","a217","a218","a219","a220","a221","a222","a223","a224","a225","a226","a227","a228","a229","a230","a231","a232","a233","a234","a235","a236","a237","a238","a239","a240","a241","a242","a243","a244","a245","a246","a247","a248","a249","a250","a251","a252","a253","a254","a255","s40","s41","s42","s43","s44","s45","s46","s47","s48","s49","s50","s51","s52","s53","s54","s55","s56","s57","s58","s59","s60","s61","s62","s63","s64","s65","s66","s67","s68","s69","s70","s71","s72","s73","s74","s75","s76","s77","s78","s79","s80","s81","s82","s83","s84","s85","s86","s87","s88","s89","s90","s91","s92","s93","s94","s95","s96","s97","s98","s99","vcc","scc","memory");
}

extern "C" void kernel_launch(void* const* d_in, const int* in_sizes, int n_in, void* d_out, int out_size, void* d_ws, size_t ws_size, hipStream_t stream) {
  (void)hipFuncSetAttribute((const void*)ebf_block, hipFuncAttributeMaxDynamicSharedMemorySize, 163840);
  ebf_block<<<256, 256, 163840, stream>>>((const u16*)d_in[0], (const u16*)d_in[1], (const u16*)d_in[2], (const u16*)d_in[3], (const u16*)d_in[4], (const u16*)d_in[5], (const u16*)d_in[6],
                                          (const u16*)d_in[7], (char*)d_ws, (u16*)d_out);
}
```
